```python
import torch

def _softmax(x, axis=-1):
    return torch.softmax(x, dim=axis)
def _gelu(x):
    return torch.nn.functional.gelu(x, approximate="tanh")
def _silu(x):
    return torch.nn.functional.silu(x)
def _sigmoid(x):
    return torch.sigmoid(x)
def _layernorm(x, axis=-1, eps=1e-5):
    m = x.mean(dim=axis, keepdim=True)
    v = x.var(dim=axis, keepdim=True, unbiased=False)
    return (x - m) / torch.sqrt(v + eps)
def _scatter_add(tgt, idx, src, axis=0):
    return tgt.clone().index_add_(axis, idx, src)
def _segment_sum(src, seg, n):
    out = torch.zeros((n,) + src.shape[1:], dtype=src.dtype)
    return out.index_add_(0, seg, src)
def _pad(x, pw):
    flat = tuple(v for pair in reversed(pw) for v in pair)
    return torch.nn.functional.pad(x, flat)

kernel_name = 'standalone__gqa_softmax__near_ridge'

def setup_inputs():

    _w = (lambda a: (lambda t: t.to(torch.bfloat16) if t.is_floating_point() else t)(torch.as_tensor(a)))
    B, Hq, Hkv, Sq, Sk, D = 8, 32, 32, 336, 2048, 128
    return {
        "q": _w((lambda *s: torch.randn(*s, dtype=torch.bfloat16) * 0.8)(B, Hq, Sq, D)),
        "k": _w((lambda *s: torch.randn(*s, dtype=torch.bfloat16) * 0.8)(B, Hkv, Sk, D)),
        "v": _w((lambda *s: torch.randn(*s, dtype=torch.bfloat16) * 0.05)(B, Hkv, Sk, D)),
    }

def reference(q, k, v):
    B, Hq, Sq, D = q.shape
    Hkv = k.shape[1]
    rep = Hq // Hkv
    k = (lambda x, n, axis: torch.repeat_interleave(x, n, dim=axis))(k, rep, axis=1)
    v = (lambda x, n, axis: torch.repeat_interleave(x, n, dim=axis))(v, rep, axis=1)
    scale = D ** -0.5
    scores = torch.einsum("bhqd,bhkd->bhqk", q, k) * scale
    p = _softmax(scores, axis=-1)
    return torch.einsum("bhqk,bhkd->bhqd", p, v)
```

```cpp
#include <hip/hip_runtime.h>
#include <hip/hip_bf16.h>
#include <cstdio>

using bf16 = __hip_bfloat16;
constexpr int   D = 128, NW = 8, QBLK = 32, KVBLK = 64;
constexpr float SCALE = 0.088388347648318440f;
#define ATTN_THR 8.f
constexpr float THR = ATTN_THR;
#define ATTN_LAYOUT 0
constexpr int LAYOUT = ATTN_LAYOUT;
#define ATTN_SDEPTH 2
constexpr int SDEPTH = ATTN_SDEPTH;
static_assert(SDEPTH == 1 || SDEPTH == 2, "SDEPTH is 1 or 2");
#define ATTN_B 8
#define ATTN_HQ 32
#define ATTN_HKV 32
#define ATTN_S 336
#define ATTN_SKV 2048
constexpr int B = ATTN_B, HQ = ATTN_HQ, HKV = ATTN_HKV, S = ATTN_S, SKV = ATTN_SKV;
constexpr int LDQ = LAYOUT ? HQ * D : D, LDK = LAYOUT ? HKV * D : D, LDO = LDQ;

constexpr size_t SHM_V = KVBLK * D * 2, SHM_K = KVBLK * D * 2, SHM_ATTN = 2 * SHM_V + 2 * SHM_K + NW * 64 * 4;

using bf16x8 = __attribute__((ext_vector_type(8))) short;
using s16x4  = __attribute__((ext_vector_type(4))) short;
using f32x16 = __attribute__((ext_vector_type(16))) float;
using f32x8  = __attribute__((ext_vector_type(8))) float;
using u32x4  = __attribute__((ext_vector_type(4))) unsigned;
#define KSWZ(row, colB) ((row) * 256 + ((colB) ^ (((row) & 7) << 4)))
#define SBAR() __builtin_amdgcn_sched_barrier(0)
__device__ __forceinline__ int crow(int r, int hi) { return (r & 3) + 8 * (r >> 2) + 4 * hi; }
__device__ __forceinline__ unsigned cvtpk(float lo, float hi) {
  unsigned r; asm volatile("v_cvt_pk_bf16_f32 %0, %1, %2" : "=v"(r) : "v"(lo), "v"(hi)); return r;
}
template <typename TIn> struct Stage;
template <> struct Stage<bf16>  { using T = bf16x8;
  __device__ static __forceinline__ T ld8(const bf16* p) { return *reinterpret_cast<const bf16x8*>(p); }
  __device__ static __forceinline__ bf16x8 tobf(T x) { return x; } };
template <> struct Stage<float> { using T = f32x8;
  __device__ static __forceinline__ T ld8(const float* p) { return *reinterpret_cast<const f32x8*>(p); }
  __device__ static __forceinline__ bf16x8 tobf(T x) {
    u32x4 w = {cvtpk(x[0], x[1]), cvtpk(x[2], x[3]), cvtpk(x[4], x[5]), cvtpk(x[6], x[7])}; return *reinterpret_cast<bf16x8*>(&w); } };

__device__ __forceinline__ void partialSM(f32x16& p0, f32x16& p1, float& m_reg, float& mn, float& alpha) {
  constexpr float C = SCALE * 1.4426950408889634f;
  float pmax = p0[0]; for (int r = 1; r < 16; ++r) pmax = fmaxf(pmax, p0[r]); for (int r = 0; r < 16; ++r) pmax = fmaxf(pmax, p1[r]);
  { auto rr = __builtin_amdgcn_permlane32_swap(__float_as_uint(pmax), __float_as_uint(pmax), false, false);
    pmax = fmaxf(__uint_as_float(rr[0]), __uint_as_float(rr[1])); }
  if (__builtin_expect(__all(pmax - m_reg <= THR / SCALE), 1)) { mn = m_reg; alpha = 1.f; }
  else { mn = fmaxf(m_reg, pmax); alpha = __builtin_amdgcn_exp2f((m_reg - mn) * C); m_reg = mn; }
  float mnC = -mn * C;
  for (int r = 0; r < 16; ++r) p0[r] = fmaf(p0[r], C, mnC); for (int r = 0; r < 16; ++r) p1[r] = fmaf(p1[r], C, mnC);
  for (int r = 0; r < 16; ++r) p0[r] = __builtin_amdgcn_exp2f(p0[r]);
}
__device__ __forceinline__ void finishSM(f32x16& p0, f32x16& p1, float alpha, float& l_reg, bf16x8& pa0, bf16x8& pa1, bf16x8& pa2, bf16x8& pa3) {
  for (int r = 0; r < 16; ++r) p1[r] = __builtin_amdgcn_exp2f(p1[r]);
  float ps = 0; for (int r = 0; r < 16; ++r) ps += p0[r]; for (int r = 0; r < 16; ++r) ps += p1[r];
  { auto rr = __builtin_amdgcn_permlane32_swap(__float_as_uint(ps), __float_as_uint(ps), false, false);
    ps = __uint_as_float(rr[0]) + __uint_as_float(rr[1]); }
  l_reg = l_reg * alpha + ps;
#define PK4(P, BASE, OUT) do { unsigned a0 = cvtpk(P[BASE + 0], P[BASE + 1]), a1 = cvtpk(P[BASE + 2], P[BASE + 3]);   \
    unsigned b0 = cvtpk(P[BASE + 4], P[BASE + 5]), b1 = cvtpk(P[BASE + 6], P[BASE + 7]);                              \
    auto r0 = __builtin_amdgcn_permlane32_swap(a0, b0, false, false); auto r1 = __builtin_amdgcn_permlane32_swap(a1, b1, false, false); \
    u32x4 w = {r0[0], r1[0], r0[1], r1[1]}; OUT = *reinterpret_cast<bf16x8*>(&w); } while (0)
  PK4(p0, 0, pa0); PK4(p0, 8, pa1); PK4(p1, 0, pa2); PK4(p1, 8, pa3);
#undef PK4
}
__device__ __forceinline__ void qkt(f32x16& p0, f32x16& p1, const bf16* Ks, const bf16x8* qr, int r32, int hi) {
  p0 = f32x16{}; p1 = f32x16{};
  for (int d0 = 0; d0 < 8; ++d0) { int cb = (d0 * 16 + hi * 8) * 2;
    bf16x8 b0 = *reinterpret_cast<const bf16x8*>((const char*)Ks + KSWZ(r32, cb));
    bf16x8 b1 = *reinterpret_cast<const bf16x8*>((const char*)Ks + KSWZ(32 + r32, cb));
    p0 = __builtin_amdgcn_mfma_f32_32x32x16_bf16(b0, qr[d0], p0, 0, 0, 0);
    p1 = __builtin_amdgcn_mfma_f32_32x32x16_bf16(b1, qr[d0], p1, 0, 0, 0); }
}
__device__ __forceinline__ int v_st(int k, int c) { const int kk = (k & ~0xC) | ((k & 4) << 1) | ((k & 8) >> 1); return ((kk >> 3) * 4 + (c >> 5)) * 512 + ((kk & 7) * 32 + (c & 31)) * 2; }
__device__ __forceinline__ int v_rd_base(int lane) { return ((lane & 3) << 3) | (((lane >> 2) & 3) << 6) | (((lane >> 4) & 1) << 5) | (((lane >> 5) & 1) << 8); }
constexpr int v_rd_off(int d0, int ks, int half) { return d0 * 512 + ks * 4096 + half * 2048; }
template <int OFF> __device__ __forceinline__ s16x4 tr_read(int vb) {
  s16x4 r; asm volatile("ds_read_b64_tr_b16 %0, %1 offset:%2" : "=&v"(r) : "v"(vb), "i"(OFF) : "memory"); return r;
}
template <int D0> __device__ __forceinline__ void pv_one(f32x16& od, int vb, bf16x8 pa0, bf16x8 pa1, bf16x8 pa2, bf16x8 pa3) {
  const s16x4 l0 = tr_read<v_rd_off(D0, 0, 0)>(vb), h0 = tr_read<v_rd_off(D0, 0, 1)>(vb), l1 = tr_read<v_rd_off(D0, 1, 0)>(vb), h1 = tr_read<v_rd_off(D0, 1, 1)>(vb);
  const s16x4 l2 = tr_read<v_rd_off(D0, 2, 0)>(vb), h2 = tr_read<v_rd_off(D0, 2, 1)>(vb), l3 = tr_read<v_rd_off(D0, 3, 0)>(vb), h3 = tr_read<v_rd_off(D0, 3, 1)>(vb);
  asm volatile("s_waitcnt lgkmcnt(0)" ::: "memory"); SBAR();
#define PK(L, H) (bf16x8){L[0], L[1], L[2], L[3], H[0], H[1], H[2], H[3]}
  od = __builtin_amdgcn_mfma_f32_32x32x16_bf16(pa0, PK(l0, h0), od, 0, 0, 0);
  od = __builtin_amdgcn_mfma_f32_32x32x16_bf16(pa1, PK(l1, h1), od, 0, 0, 0);
  od = __builtin_amdgcn_mfma_f32_32x32x16_bf16(pa2, PK(l2, h2), od, 0, 0, 0);
  od = __builtin_amdgcn_mfma_f32_32x32x16_bf16(pa3, PK(l3, h3), od, 0, 0, 0);
#undef PK
}
__device__ __forceinline__ void pv_d0(f32x16* o, int vb, bf16x8 pa0, bf16x8 pa1, bf16x8 pa2, bf16x8 pa3) {
  pv_one<0>(o[0], vb, pa0, pa1, pa2, pa3); pv_one<1>(o[1], vb, pa0, pa1, pa2, pa3); pv_one<2>(o[2], vb, pa0, pa1, pa2, pa3); pv_one<3>(o[3], vb, pa0, pa1, pa2, pa3);
}

template <typename TQ>
__device__ __forceinline__ void attn_dense_body(const TQ* __restrict__ Qb, const bf16* __restrict__ Kh, const bf16* __restrict__ Vh,
                                                bf16* __restrict__ Ob, int seq, char* lds, int nrows) {
  using St = Stage<bf16>; using SQ = Stage<TQ>;
  const int tid = threadIdx.x, wid = tid >> 6, lane = tid & 63, r32 = lane & 31, hi = lane >> 5;
  bf16* V_lds = (bf16*)lds; bf16* K_lds = (bf16*)(lds + 2 * SHM_V);
  float* ws = (float*)(lds + 2 * SHM_V + 2 * SHM_K) + wid * 64; float* li_l = ws; float* al_l = ws + 32;
  float m_reg = -1e30f, l_reg = 0; f32x16 o[4] = {}; bf16x8 qr[8];
  int qrow = wid * QBLK + r32; if (qrow >= nrows) qrow = nrows - 1; const TQ* Qw = Qb + (long)qrow * LDQ + hi * 8;
#pragma unroll
  for (int d0 = 0; d0 < 8; ++d0) qr[d0] = SQ::tobf(SQ::ld8(Qw + d0 * 16));
  const int sr = tid >> 4, sc = (tid & 15) * 8, vst0 = v_st(sr, sc), vst1 = v_st(32 + sr, sc);
  const int vb0 = (int)(uintptr_t)V_lds + v_rd_base(lane);
  struct { typename St::T vs0, vs1, ks0, ks1; } sr_[SDEPTH];
#define SLOAD(i, k0) do { sr_[i].vs0 = St::ld8(&Vh[(long)((k0) + sr) * LDK + sc]); sr_[i].vs1 = St::ld8(&Vh[(long)((k0) + 32 + sr) * LDK + sc]); \
    sr_[i].ks0 = St::ld8(&Kh[(long)((k0) + sr) * LDK + sc]); sr_[i].ks1 = St::ld8(&Kh[(long)((k0) + 32 + sr) * LDK + sc]); } while (0)
#define SWRITE(b, i) do { *(bf16x8*)((char*)V_lds + (b) * SHM_V + vst0) = St::tobf(sr_[i].vs0);          \
    *(bf16x8*)((char*)V_lds + (b) * SHM_V + vst1) = St::tobf(sr_[i].vs1); int kc = sc * 2;               \
    *(bf16x8*)((char*)K_lds + (b) * SHM_K + KSWZ(sr, kc)) = St::tobf(sr_[i].ks0);                       \
    *(bf16x8*)((char*)K_lds + (b) * SHM_K + KSWZ(32 + sr, kc)) = St::tobf(sr_[i].ks1); } while (0)
#define SWAIT() do { if constexpr (SDEPTH == 2) asm volatile("s_waitcnt vmcnt(4)" ::: "memory"); else asm volatile("s_waitcnt vmcnt(0)" ::: "memory"); } while (0)
#define RESC(a) do { if (__any((a) < 1.f)) { if (hi == 0) al_l[r32] = (a); asm volatile("s_waitcnt lgkmcnt(0)" ::: "memory"); \
    for (int d = 0; d < 4; ++d) for (int r = 0; r < 16; ++r) o[d][r] *= al_l[crow(r, hi)]; } } while (0)
  f32x16 pA0, pA1, pB0, pB1; float mnA, mnB, alA, alB; bf16x8 pa0, pa1, pa2, pa3; const int NT = seq / KVBLK;
  constexpr int SE = 0, SO = SDEPTH - 1;
  SLOAD(SE, 0); asm volatile("s_waitcnt vmcnt(0)" ::: "memory"); SWRITE(0, SE); __syncthreads();
  qkt(pA0, pA1, K_lds, qr, r32, hi); partialSM(pA0, pA1, m_reg, mnA, alA);
  SLOAD(SO, KVBLK); if constexpr (SDEPTH == 2) { if (2 < NT) SLOAD(SE, 2 * KVBLK); }
  SWAIT(); SWRITE(1, SO); __syncthreads();
  for (int j = 1; j + 1 < NT; j += 2) {
    SBAR(); qkt(pB0, pB1, (bf16*)((char*)K_lds + SHM_K), qr, r32, hi);
    finishSM(pA0, pA1, alA, l_reg, pa0, pa1, pa2, pa3); SBAR();
    SLOAD(SO, (j + SDEPTH) * KVBLK); SBAR();
    pv_d0(o, vb0, pa0, pa1, pa2, pa3); partialSM(pB0, pB1, m_reg, mnB, alB);
    __syncthreads(); SWAIT(); SWRITE(0, SE);
    RESC(alB); __syncthreads();
    SBAR(); qkt(pA0, pA1, K_lds, qr, r32, hi);
    finishSM(pB0, pB1, alB, l_reg, pa0, pa1, pa2, pa3); SBAR();
    if (SDEPTH == 1 || j + 3 < NT) SLOAD(SE, (j + 1 + SDEPTH) * KVBLK); SBAR();
    pv_d0(o, vb0 + (int)SHM_V, pa0, pa1, pa2, pa3); partialSM(pA0, pA1, m_reg, mnA, alA);
    __syncthreads(); SWAIT(); SWRITE(1, SO);
    RESC(alA); __syncthreads();
  }
  SBAR(); qkt(pB0, pB1, (bf16*)((char*)K_lds + SHM_K), qr, r32, hi);
  finishSM(pA0, pA1, alA, l_reg, pa0, pa1, pa2, pa3); SBAR();
  pv_d0(o, vb0, pa0, pa1, pa2, pa3); partialSM(pB0, pB1, m_reg, mnB, alB);
  __syncthreads(); RESC(alB);
  finishSM(pB0, pB1, alB, l_reg, pa0, pa1, pa2, pa3); SBAR();
  pv_d0(o, vb0 + (int)SHM_V, pa0, pa1, pa2, pa3);
  if (hi == 0) li_l[r32] = l_reg; asm volatile("s_waitcnt lgkmcnt(0)" ::: "memory");
  float rli[16];
#pragma unroll
  for (int r = 0; r < 16; ++r) rli[r] = __builtin_amdgcn_rcpf(li_l[crow(r, hi)]);
  bf16* Ow = Ob + (long)(wid * QBLK) * LDO;
#pragma unroll
  for (int r = 0; r < 16; ++r) { int orow = crow(r, hi);
    if (wid * QBLK + orow < nrows) for (int d0 = 0; d0 < 4; ++d0) Ow[(long)orow * LDO + d0 * 32 + r32] = __float2bfloat16(o[d0][r] * rli[r]); }
#undef SLOAD
#undef SWRITE
#undef SWAIT
#undef RESC
}

__global__ __launch_bounds__(64 * NW, 1)
void attn_fwd_dense(const bf16* __restrict__ Q, const bf16* __restrict__ K, const bf16* __restrict__ V, bf16* __restrict__ O) {
  extern __shared__ __attribute__((aligned(16))) char shm[];
  const long bh = blockIdx.y, i0 = (long)blockIdx.x * (QBLK * NW);
  const long q0 = (bh * S + i0) * D, k0 = bh * SKV * (long)D;
  attn_dense_body<bf16>(Q + q0, K + k0, V + k0, O + q0, SKV, shm, S - (int)i0);
}
extern "C" void kernel_launch(void* const* d_in, const int* in_sizes, int n_in, void* d_out, int out_size, void* d_ws, size_t ws_size, hipStream_t stream) {
  (void)hipFuncSetAttribute((const void*)attn_fwd_dense, hipFuncAttributeMaxDynamicSharedMemorySize, (int)SHM_ATTN);
  hipLaunchKernelGGL(attn_fwd_dense, dim3(2, 256), dim3(64 * NW), SHM_ATTN, stream,
                     (const bf16*)d_in[0], (const bf16*)d_in[1], (const bf16*)d_in[2], (bf16*)d_out);
}
```

```cpp
#include <hip/hip_runtime.h>
#include <hip/hip_bf16.h>

__global__ __launch_bounds__(256, 1) void standalone__gqa_softmax__near_ridge_kernel(
    const void* __restrict__ q, const void* __restrict__ k, const void* __restrict__ v, void* __restrict__ o) {
  __shared__ __attribute__((aligned(16))) char lds[163840];
  const unsigned wid = __builtin_amdgcn_readfirstlane(threadIdx.x >> 6);
  const unsigned bh = blockIdx.x;
  const unsigned ldsbase = (unsigned)(size_t)lds;
  asm volatile(
      "s_mov_b32 s52, %5\n"
      "v_mbcnt_lo_u32_b32 v13, -1, 0\n"
      "v_mbcnt_hi_u32_b32 v13, -1, v13\n"
      "s_mov_b32 s53, %4\n"
      "s_lshr_b32 s58, s53, 3\n"
      "s_and_b32 s58, s58, 3\n"
      "L_stag:\n"
      "s_cmp_eq_u32 s58, 0\n"
      "s_cbranch_scc1 L_stag_done\n"
      "s_sleep 20\n"
      "s_sub_u32 s58, s58, 1\n"
      "s_branch L_stag\n"
      "L_stag_done:\n"
      "v_and_b32 v20, 15, v13\n"
      "v_lshrrev_b32 v21, 4, v13\n"
      "v_xor_b32 v22, v20, v21\n"
      "v_lshlrev_b32 v22, 4, v22\n"
      "v_lshl_or_b32 v6, v21, 8, v22\n"
      "v_xor_b32 v7, 64, v6\n"
      "v_xor_b32 v8, 128, v6\n"
      "v_xor_b32 v9, 192, v6\n"
      "s_mul_i32 s62, s52, 0x5000\n"
      "s_mul_i32 s64, s52, 0x5000\n"
      "s_add_u32 s63, s64, 0x13000\n"
      "s_cmp_eq_u32 s52, 0\n"
      "s_cselect_b32 s63, 0xa000, s63\n"
      "s_mov_b64 s[58:59], %1\n"
      "s_mul_i32 s60, s53, 0x80000\n"
      "s_add_u32 s40, s58, s60\n"
      "s_addc_u32 s59, s59, 0\n"
      "s_and_b32 s41, s59, 0xffff\n"
      "s_mov_b32 s42, 0x80000\n"
      "s_mov_b32 s43, 0x20000\n"
      "s_mov_b64 s[58:59], %0\n"
      "s_mul_i32 s60, s53, 0x15000\n"
      "s_add_u32 s36, s58, s60\n"
      "s_addc_u32 s59, s59, 0\n"
      "s_and_b32 s37, s59, 0xffff\n"
      "s_mov_b32 s38, 0x15000\n"
      "s_mov_b32 s39, 0x20000\n"
      "s_cmp_eq_u32 s52, 1\n"
      "s_cbranch_scc1 L_path_1\n"
      "s_cmp_eq_u32 s52, 2\n"
      "s_cbranch_scc1 L_path_2\n"
      "s_cmp_eq_u32 s52, 3\n"
      "s_cbranch_scc1 L_path_3\n"
      "L_path_0:\n"
      "s_mov_b32 s58, 0x14000\n"
      "s_mov_b32 m0, 0xf000\n"
      "s_nop 0\n"
      "buffer_load_dwordx4 v6, s[36:39], s58 offen offset:0 nt lds\n"
      "s_add_u32 s58, s62, 0\n"
      "s_add_u32 s59, s63, 0\n"
      "s_mov_b32 m0, s59\n"
      "s_nop 0\n"
      "buffer_load_dwordx4 v6, s[36:39], s58 offen offset:0 nt lds\n"
      "buffer_load_dwordx4 v7, s[36:39], s58 offen offset:1024 nt lds\n"
      "buffer_load_dwordx4 v8, s[36:39], s58 offen offset:2048 nt lds\n"
      "buffer_load_dwordx4 v9, s[36:39], s58 offen offset:3072 nt lds\n"
      "s_add_u32 s58, s62, 4096\n"
      "s_add_u32 s59, s63, 4096\n"
      "s_mov_b32 m0, s59\n"
      "s_nop 0\n"
      "buffer_load_dwordx4 v6, s[36:39], s58 offen offset:0 nt lds\n"
      "buffer_load_dwordx4 v7, s[36:39], s58 offen offset:1024 nt lds\n"
      "buffer_load_dwordx4 v8, s[36:39], s58 offen offset:2048 nt lds\n"
      "buffer_load_dwordx4 v9, s[36:39], s58 offen offset:3072 nt lds\n"
      "s_add_u32 s58, s62, 8192\n"
      "s_add_u32 s59, s63, 8192\n"
      "s_mov_b32 m0, s59\n"
      "s_nop 0\n"
      "buffer_load_dwordx4 v6, s[36:39], s58 offen offset:0 nt lds\n"
      "buffer_load_dwordx4 v7, s[36:39], s58 offen offset:1024 nt lds\n"
      "buffer_load_dwordx4 v8, s[36:39], s58 offen offset:2048 nt lds\n"
      "buffer_load_dwordx4 v9, s[36:39], s58 offen offset:3072 nt lds\n"
      "s_mov_b64 s[58:59], %2\n"
      "s_mul_i32 s60, s53, 0x80000\n"
      "s_add_u32 s44, s58, s60\n"
      "s_addc_u32 s59, s59, 0\n"
      "s_and_b32 s45, s59, 0xffff\n"
      "s_mov_b32 s46, 0x80000\n"
      "s_mov_b32 s47, 0x20000\n"
      "s_mov_b64 s[58:59], %3\n"
      "s_mul_i32 s60, s53, 0x15000\n"
      "s_add_u32 s48, s58, s60\n"
      "s_addc_u32 s59, s59, 0\n"
      "s_and_b32 s49, s59, 0xffff\n"
      "s_mov_b32 s50, 0x15000\n"
      "s_mov_b32 s51, 0x20000\n"
      "s_mov_b32 s54, 0x3e0293ee\n"
      "s_mov_b32 s65, 0\n"
      "s_mov_b32 s96, 0x57800000\n"
      "s_mov_b32 s97, 0xffff0000\n"
      "v_lshl_or_b32 v0, v20, 8, v22\n"
      "v_xor_b32 v1, 64, v0\n"
      "v_xor_b32 v2, 128, v0\n"
      "v_xor_b32 v3, 192, v0\n"
      "v_bfe_u32 v23, v13, 2, 2\n"
      "v_and_b32 v24, 3, v13\n"
      "v_lshlrev_b32 v25, 4, v24\n"
      "v_lshl_or_b32 v25, v21, 6, v25\n"
      "v_lshl_or_b32 v10, v23, 8, v25\n"
      "v_xor_b32 v11, 32, v10\n"
      "v_lshlrev_b32 v25, 3, v24\n"
      "v_lshl_or_b32 v25, v23, 6, v25\n"
      "v_lshl_or_b32 v25, v21, 10, v25\n"
      "v_and_b32 v23, 1, v21\n"
      "v_lshl_or_b32 v25, v23, 5, v25\n"
      "v_or_b32 v4, 0x10000, v25\n"
      "v_xor_b32 v5, 32, v4\n"
      "v_lshlrev_b32 v12, 4, v13\n"
      "v_add_u32 v248, s63, v0\n"
      "v_add_u32 v249, s63, v1\n"
      "v_add_u32 v250, s63, v2\n"
      "v_add_u32 v251, s63, v3\n"
      "v_mov_b32 v64, 0\n"
      "v_mov_b32 v65, 0\n"
      "v_mov_b32 v66, 0\n"
      "v_mov_b32 v67, 0\n"
      "v_mov_b32 v14, 0\n"
      "v_mov_b32 v15, 0\n"
      "v_mov_b32 v16, 0\n"
      "v_mov_b32 v17, 0\n"
      "v_mov_b32 v18, 0\n"
      "v_mov_b32 v19, 0\n"
      "v_mfma_f32_32x32x16_bf16 a[0:15], v[64:67], v[64:67], 0\n"
      "v_mfma_f32_32x32x16_bf16 a[16:31], v[64:67], v[64:67], 0\n"
      "v_mfma_f32_32x32x16_bf16 a[32:47], v[64:67], v[64:67], 0\n"
      "v_mfma_f32_32x32x16_bf16 a[48:63], v[64:67], v[64:67], 0\n"
      "v_mfma_f32_32x32x16_bf16 a[64:79], v[64:67], v[64:67], 0\n"
      "v_mfma_f32_32x32x16_bf16 a[80:95], v[64:67], v[64:67], 0\n"
      "v_mfma_f32_32x32x16_bf16 a[96:111], v[64:67], v[64:67], 0\n"
      "v_mfma_f32_32x32x16_bf16 a[112:127], v[64:67], v[64:67], 0\n"
      "v_mfma_f32_32x32x16_bf16 a[128:143], v[64:67], v[64:67], 0\n"
      "v_mfma_f32_32x32x16_bf16 a[144:159], v[64:67], v[64:67], 0\n"
      "v_mfma_f32_32x32x16_bf16 a[160:175], v[64:67], v[64:67], 0\n"
      "v_mfma_f32_32x32x16_bf16 a[176:191], v[64:67], v[64:67], 0\n"
      "s_waitcnt vmcnt(8)\n"
      "ds_read_b128 v[136:139], v248 offset:0\n"
      "ds_read_b128 v[140:143], v249 offset:0\n"
      "ds_read_b128 v[144:147], v250 offset:0\n"
      "ds_read_b128 v[148:151], v251 offset:0\n"
      "s_add_u32 s58, s62, 12288\n"
      "s_add_u32 s59, s63, 12288\n"
      "s_mov_b32 m0, s59\n"
      "s_nop 0\n"
      "buffer_load_dwordx4 v6, s[36:39], s58 offen offset:0 nt lds\n"
      "buffer_load_dwordx4 v7, s[36:39], s58 offen offset:1024 nt lds\n"
      "buffer_load_dwordx4 v8, s[36:39], s58 offen offset:2048 nt lds\n"
      "buffer_load_dwordx4 v9, s[36:39], s58 offen offset:3072 nt lds\n"
      "s_waitcnt lgkmcnt(0)\n"
      "v_lshlrev_b32 v20, 16, v136\n"
      "v_and_b32 v21, s97, v136\n"
      "v_lshlrev_b32 v22, 16, v137\n"
      "v_and_b32 v23, s97, v137\n"
      "v_lshlrev_b32 v24, 16, v138\n"
      "v_and_b32 v25, s97, v138\n"
      "v_lshlrev_b32 v26, 16, v139\n"
      "v_and_b32 v27, s97, v139\n"
      "v_pk_mul_f32 v[20:21], v[20:21], s[54:55] op_sel_hi:[1,0]\n"
      "v_pk_mul_f32 v[22:23], v[22:23], s[54:55] op_sel_hi:[1,0]\n"
      "v_pk_mul_f32 v[24:25], v[24:25], s[54:55] op_sel_hi:[1,0]\n"
      "v_pk_mul_f32 v[26:27], v[26:27], s[54:55] op_sel_hi:[1,0]\n"
      "v_cvt_pk_bf16_f32 v136, v20, v21\n"
      "v_cvt_pk_bf16_f32 v137, v22, v23\n"
      "v_cvt_pk_bf16_f32 v138, v24, v25\n"
      "v_cvt_pk_bf16_f32 v139, v26, v27\n"
      "v_accvgpr_write_b32 a192, v136\n"
      "v_accvgpr_write_b32 a193, v137\n"
      "v_accvgpr_write_b32 a194, v138\n"
      "v_accvgpr_write_b32 a195, v139\n"
      "v_lshlrev_b32 v20, 16, v140\n"
      "v_and_b32 v21, s97, v140\n"
      "v_lshlrev_b32 v22, 16, v141\n"
      "v_and_b32 v23, s97, v141\n"
      "v_lshlrev_b32 v24, 16, v142\n"
      "v_and_b32 v25, s97, v142\n"
      "v_lshlrev_b32 v26, 16, v143\n"
      "v_and_b32 v27, s97, v143\n"
      "v_pk_mul_f32 v[20:21], v[20:21], s[54:55] op_sel_hi:[1,0]\n"
      "v_pk_mul_f32 v[22:23], v[22:23], s[54:55] op_sel_hi:[1,0]\n"
      "v_pk_mul_f32 v[24:25], v[24:25], s[54:55] op_sel_hi:[1,0]\n"
      "v_pk_mul_f32 v[26:27], v[26:27], s[54:55] op_sel_hi:[1,0]\n"
      "v_cvt_pk_bf16_f32 v140, v20, v21\n"
      "v_cvt_pk_bf16_f32 v141, v22, v23\n"
      "v_cvt_pk_bf16_f32 v142, v24, v25\n"
      "v_cvt_pk_bf16_f32 v143, v26, v27\n"
      "v_accvgpr_write_b32 a196, v140\n"
      "v_accvgpr_write_b32 a197, v141\n"
      "v_accvgpr_write_b32 a198, v142\n"
      "v_accvgpr_write_b32 a199, v143\n"
      "v_lshlrev_b32 v20, 16, v144\n"
      "v_and_b32 v21, s97, v144\n"
      "v_lshlrev_b32 v22, 16, v145\n"
      "v_and_b32 v23, s97, v145\n"
      "v_lshlrev_b32 v24, 16, v146\n"
      "v_and_b32 v25, s97, v146\n"
      "v_lshlrev_b32 v26, 16, v147\n"
      "v_and_b32 v27, s97, v147\n"
      "v_pk_mul_f32 v[20:21], v[20:21], s[54:55] op_sel_hi:[1,0]\n"
      "v_pk_mul_f32 v[22:23], v[22:23], s[54:55] op_sel_hi:[1,0]\n"
      "v_pk_mul_f32 v[24:25], v[24:25], s[54:55] op_sel_hi:[1,0]\n"
      "v_pk_mul_f32 v[26:27], v[26:27], s[54:55] op_sel_hi:[1,0]\n"
      "v_cvt_pk_bf16_f32 v144, v20, v21\n"
      "v_cvt_pk_bf16_f32 v145, v22, v23\n"
      "v_cvt_pk_bf16_f32 v146, v24, v25\n"
      "v_cvt_pk_bf16_f32 v147, v26, v27\n"
      "v_accvgpr_write_b32 a200, v144\n"
      "v_accvgpr_write_b32 a201, v145\n"
      "v_accvgpr_write_b32 a202, v146\n"
      "v_accvgpr_write_b32 a203, v147\n"
      "v_lshlrev_b32 v20, 16, v148\n"
      "v_and_b32 v21, s97, v148\n"
      "v_lshlrev_b32 v22, 16, v149\n"
      "v_and_b32 v23, s97, v149\n"
      "v_lshlrev_b32 v24, 16, v150\n"
      "v_and_b32 v25, s97, v150\n"
      "v_lshlrev_b32 v26, 16, v151\n"
      "v_and_b32 v27, s97, v151\n"
      "v_pk_mul_f32 v[20:21], v[20:21], s[54:55] op_sel_hi:[1,0]\n"
      "v_pk_mul_f32 v[22:23], v[22:23], s[54:55] op_sel_hi:[1,0]\n"
      "v_pk_mul_f32 v[24:25], v[24:25], s[54:55] op_sel_hi:[1,0]\n"
      "v_pk_mul_f32 v[26:27], v[26:27], s[54:55] op_sel_hi:[1,0]\n"
      "v_cvt_pk_bf16_f32 v148, v20, v21\n"
      "v_cvt_pk_bf16_f32 v149, v22, v23\n"
      "v_cvt_pk_bf16_f32 v150, v24, v25\n"
      "v_cvt_pk_bf16_f32 v151, v26, v27\n"
      "v_accvgpr_write_b32 a204, v148\n"
      "v_accvgpr_write_b32 a205, v149\n"
      "v_accvgpr_write_b32 a206, v150\n"
      "v_accvgpr_write_b32 a207, v151\n"
      "s_waitcnt vmcnt(8)\n"
      "ds_read_b128 v[152:155], v248 offset:4096\n"
      "ds_read_b128 v[156:159], v249 offset:4096\n"
      "ds_read_b128 v[160:163], v250 offset:4096\n"
      "ds_read_b128 v[164:167], v251 offset:4096\n"
      "s_add_u32 s58, s62, 16384\n"
      "s_add_u32 s59, s63, 16384\n"
      "s_mov_b32 m0, s59\n"
      "s_nop 0\n"
      "buffer_load_dwordx4 v6, s[36:39], s58 offen offset:0 nt lds\n"
      "buffer_load_dwordx4 v7, s[36:39], s58 offen offset:1024 nt lds\n"
      "buffer_load_dwordx4 v8, s[36:39], s58 offen offset:2048 nt lds\n"
      "buffer_load_dwordx4 v9, s[36:39], s58 offen offset:3072 nt lds\n"
      "s_waitcnt lgkmcnt(0)\n"
      "v_lshlrev_b32 v20, 16, v152\n"
      "v_and_b32 v21, s97, v152\n"
      "v_lshlrev_b32 v22, 16, v153\n"
      "v_and_b32 v23, s97, v153\n"
      "v_lshlrev_b32 v24, 16, v154\n"
      "v_and_b32 v25, s97, v154\n"
      "v_lshlrev_b32 v26, 16, v155\n"
      "v_and_b32 v27, s97, v155\n"
      "v_pk_mul_f32 v[20:21], v[20:21], s[54:55] op_sel_hi:[1,0]\n"
      "v_pk_mul_f32 v[22:23], v[22:23], s[54:55] op_sel_hi:[1,0]\n"
      "v_pk_mul_f32 v[24:25], v[24:25], s[54:55] op_sel_hi:[1,0]\n"
      "v_pk_mul_f32 v[26:27], v[26:27], s[54:55] op_sel_hi:[1,0]\n"
      "v_cvt_pk_bf16_f32 v152, v20, v21\n"
      "v_cvt_pk_bf16_f32 v153, v22, v23\n"
      "v_cvt_pk_bf16_f32 v154, v24, v25\n"
      "v_cvt_pk_bf16_f32 v155, v26, v27\n"
      "v_accvgpr_write_b32 a208, v152\n"
      "v_accvgpr_write_b32 a209, v153\n"
      "v_accvgpr_write_b32 a210, v154\n"
      "v_accvgpr_write_b32 a211, v155\n"
      "v_lshlrev_b32 v20, 16, v156\n"
      "v_and_b32 v21, s97, v156\n"
      "v_lshlrev_b32 v22, 16, v157\n"
      "v_and_b32 v23, s97, v157\n"
      "v_lshlrev_b32 v24, 16, v158\n"
      "v_and_b32 v25, s97, v158\n"
      "v_lshlrev_b32 v26, 16, v159\n"
      "v_and_b32 v27, s97, v159\n"
      "v_pk_mul_f32 v[20:21], v[20:21], s[54:55] op_sel_hi:[1,0]\n"
      "v_pk_mul_f32 v[22:23], v[22:23], s[54:55] op_sel_hi:[1,0]\n"
      "v_pk_mul_f32 v[24:25], v[24:25], s[54:55] op_sel_hi:[1,0]\n"
      "v_pk_mul_f32 v[26:27], v[26:27], s[54:55] op_sel_hi:[1,0]\n"
      "v_cvt_pk_bf16_f32 v156, v20, v21\n"
      "v_cvt_pk_bf16_f32 v157, v22, v23\n"
      "v_cvt_pk_bf16_f32 v158, v24, v25\n"
      "v_cvt_pk_bf16_f32 v159, v26, v27\n"
      "v_accvgpr_write_b32 a212, v156\n"
      "v_accvgpr_write_b32 a213, v157\n"
      "v_accvgpr_write_b32 a214, v158\n"
      "v_accvgpr_write_b32 a215, v159\n"
      "v_lshlrev_b32 v20, 16, v160\n"
      "v_and_b32 v21, s97, v160\n"
      "v_lshlrev_b32 v22, 16, v161\n"
      "v_and_b32 v23, s97, v161\n"
      "v_lshlrev_b32 v24, 16, v162\n"
      "v_and_b32 v25, s97, v162\n"
      "v_lshlrev_b32 v26, 16, v163\n"
      "v_and_b32 v27, s97, v163\n"
      "v_pk_mul_f32 v[20:21], v[20:21], s[54:55] op_sel_hi:[1,0]\n"
      "v_pk_mul_f32 v[22:23], v[22:23], s[54:55] op_sel_hi:[1,0]\n"
      "v_pk_mul_f32 v[24:25], v[24:25], s[54:55] op_sel_hi:[1,0]\n"
      "v_pk_mul_f32 v[26:27], v[26:27], s[54:55] op_sel_hi:[1,0]\n"
      "v_cvt_pk_bf16_f32 v160, v20, v21\n"
      "v_cvt_pk_bf16_f32 v161, v22, v23\n"
      "v_cvt_pk_bf16_f32 v162, v24, v25\n"
      "v_cvt_pk_bf16_f32 v163, v26, v27\n"
      "v_accvgpr_write_b32 a216, v160\n"
      "v_accvgpr_write_b32 a217, v161\n"
      "v_accvgpr_write_b32 a218, v162\n"
      "v_accvgpr_write_b32 a219, v163\n"
      "v_lshlrev_b32 v20, 16, v164\n"
      "v_and_b32 v21, s97, v164\n"
      "v_lshlrev_b32 v22, 16, v165\n"
      "v_and_b32 v23, s97, v165\n"
      "v_lshlrev_b32 v24, 16, v166\n"
      "v_and_b32 v25, s97, v166\n"
      "v_lshlrev_b32 v26, 16, v167\n"
      "v_and_b32 v27, s97, v167\n"
      "v_pk_mul_f32 v[20:21], v[20:21], s[54:55] op_sel_hi:[1,0]\n"
      "v_pk_mul_f32 v[22:23], v[22:23], s[54:55] op_sel_hi:[1,0]\n"
      "v_pk_mul_f32 v[24:25], v[24:25], s[54:55] op_sel_hi:[1,0]\n"
      "v_pk_mul_f32 v[26:27], v[26:27], s[54:55] op_sel_hi:[1,0]\n"
      "v_cvt_pk_bf16_f32 v164, v20, v21\n"
      "v_cvt_pk_bf16_f32 v165, v22, v23\n"
      "v_cvt_pk_bf16_f32 v166, v24, v25\n"
      "v_cvt_pk_bf16_f32 v167, v26, v27\n"
      "v_accvgpr_write_b32 a220, v164\n"
      "v_accvgpr_write_b32 a221, v165\n"
      "v_accvgpr_write_b32 a222, v166\n"
      "v_accvgpr_write_b32 a223, v167\n"
      "s_waitcnt vmcnt(8)\n"
      "ds_read_b128 v[136:139], v248 offset:8192\n"
      "ds_read_b128 v[140:143], v249 offset:8192\n"
      "ds_read_b128 v[144:147], v250 offset:8192\n"
      "ds_read_b128 v[148:151], v251 offset:8192\n"
      "s_mov_b32 s58, 0x0\n"
      "s_mov_b32 m0, 0x10000\n"
      "s_nop 0\n"
      "buffer_load_dwordx4 v10, s[44:47], s58 offen offset:0 nt lds\n"
      "buffer_load_dwordx4 v11, s[44:47], s58 offen offset:1024 nt lds\n"
      "buffer_load_dwordx4 v10, s[44:47], s58 offen offset:2048 nt lds\n"
      "buffer_load_dwordx4 v11, s[44:47], s58 offen offset:3072 nt lds\n"
      "s_waitcnt lgkmcnt(0)\n"
      "v_lshlrev_b32 v20, 16, v136\n"
      "v_and_b32 v21, s97, v136\n"
      "v_lshlrev_b32 v22, 16, v137\n"
      "v_and_b32 v23, s97, v137\n"
      "v_lshlrev_b32 v24, 16, v138\n"
      "v_and_b32 v25, s97, v138\n"
      "v_lshlrev_b32 v26, 16, v139\n"
      "v_and_b32 v27, s97, v139\n"
      "v_pk_mul_f32 v[20:21], v[20:21], s[54:55] op_sel_hi:[1,0]\n"
      "v_pk_mul_f32 v[22:23], v[22:23], s[54:55] op_sel_hi:[1,0]\n"
      "v_pk_mul_f32 v[24:25], v[24:25], s[54:55] op_sel_hi:[1,0]\n"
      "v_pk_mul_f32 v[26:27], v[26:27], s[54:55] op_sel_hi:[1,0]\n"
      "v_cvt_pk_bf16_f32 v136, v20, v21\n"
      "v_cvt_pk_bf16_f32 v137, v22, v23\n"
      "v_cvt_pk_bf16_f32 v138, v24, v25\n"
      "v_cvt_pk_bf16_f32 v139, v26, v27\n"
      "v_accvgpr_write_b32 a224, v136\n"
      "v_accvgpr_write_b32 a225, v137\n"
      "v_accvgpr_write_b32 a226, v138\n"
      "v_accvgpr_write_b32 a227, v139\n"
      "v_lshlrev_b32 v20, 16, v140\n"
      "v_and_b32 v21, s97, v140\n"
      "v_lshlrev_b32 v22, 16, v141\n"
      "v_and_b32 v23, s97, v141\n"
      "v_lshlrev_b32 v24, 16, v142\n"
      "v_and_b32 v25, s97, v142\n"
      "v_lshlrev_b32 v26, 16, v143\n"
      "v_and_b32 v27, s97, v143\n"
      "v_pk_mul_f32 v[20:21], v[20:21], s[54:55] op_sel_hi:[1,0]\n"
      "v_pk_mul_f32 v[22:23], v[22:23], s[54:55] op_sel_hi:[1,0]\n"
      "v_pk_mul_f32 v[24:25], v[24:25], s[54:55] op_sel_hi:[1,0]\n"
      "v_pk_mul_f32 v[26:27], v[26:27], s[54:55] op_sel_hi:[1,0]\n"
      "v_cvt_pk_bf16_f32 v140, v20, v21\n"
      "v_cvt_pk_bf16_f32 v141, v22, v23\n"
      "v_cvt_pk_bf16_f32 v142, v24, v25\n"
      "v_cvt_pk_bf16_f32 v143, v26, v27\n"
      "v_accvgpr_write_b32 a228, v140\n"
      "v_accvgpr_write_b32 a229, v141\n"
      "v_accvgpr_write_b32 a230, v142\n"
      "v_accvgpr_write_b32 a231, v143\n"
      "v_lshlrev_b32 v20, 16, v144\n"
      "v_and_b32 v21, s97, v144\n"
      "v_lshlrev_b32 v22, 16, v145\n"
      "v_and_b32 v23, s97, v145\n"
      "v_lshlrev_b32 v24, 16, v146\n"
      "v_and_b32 v25, s97, v146\n"
      "v_lshlrev_b32 v26, 16, v147\n"
      "v_and_b32 v27, s97, v147\n"
      "v_pk_mul_f32 v[20:21], v[20:21], s[54:55] op_sel_hi:[1,0]\n"
      "v_pk_mul_f32 v[22:23], v[22:23], s[54:55] op_sel_hi:[1,0]\n"
      "v_pk_mul_f32 v[24:25], v[24:25], s[54:55] op_sel_hi:[1,0]\n"
      "v_pk_mul_f32 v[26:27], v[26:27], s[54:55] op_sel_hi:[1,0]\n"
      "v_cvt_pk_bf16_f32 v144, v20, v21\n"
      "v_cvt_pk_bf16_f32 v145, v22, v23\n"
      "v_cvt_pk_bf16_f32 v146, v24, v25\n"
      "v_cvt_pk_bf16_f32 v147, v26, v27\n"
      "v_accvgpr_write_b32 a232, v144\n"
      "v_accvgpr_write_b32 a233, v145\n"
      "v_accvgpr_write_b32 a234, v146\n"
      "v_accvgpr_write_b32 a235, v147\n"
      "v_lshlrev_b32 v20, 16, v148\n"
      "v_and_b32 v21, s97, v148\n"
      "v_lshlrev_b32 v22, 16, v149\n"
      "v_and_b32 v23, s97, v149\n"
      "v_lshlrev_b32 v24, 16, v150\n"
      "v_and_b32 v25, s97, v150\n"
      "v_lshlrev_b32 v26, 16, v151\n"
      "v_and_b32 v27, s97, v151\n"
      "v_pk_mul_f32 v[20:21], v[20:21], s[54:55] op_sel_hi:[1,0]\n"
      "v_pk_mul_f32 v[22:23], v[22:23], s[54:55] op_sel_hi:[1,0]\n"
      "v_pk_mul_f32 v[24:25], v[24:25], s[54:55] op_sel_hi:[1,0]\n"
      "v_pk_mul_f32 v[26:27], v[26:27], s[54:55] op_sel_hi:[1,0]\n"
      "v_cvt_pk_bf16_f32 v148, v20, v21\n"
      "v_cvt_pk_bf16_f32 v149, v22, v23\n"
      "v_cvt_pk_bf16_f32 v150, v24, v25\n"
      "v_cvt_pk_bf16_f32 v151, v26, v27\n"
      "v_accvgpr_write_b32 a236, v148\n"
      "v_accvgpr_write_b32 a237, v149\n"
      "v_accvgpr_write_b32 a238, v150\n"
      "v_accvgpr_write_b32 a239, v151\n"
      "s_waitcnt vmcnt(24)\n"
      "s_barrier\n"
      "ds_read_b128 v[152:155], v0 offset:61440\n"
      "ds_read_b128 v[156:159], v1 offset:61440\n"
      "ds_read_b128 v[160:163], v2 offset:61440\n"
      "ds_read_b128 v[164:167], v3 offset:61440\n"
      "ds_read_b128 v[32:35], v0 offset:0\n"
      "ds_read_b128 v[36:39], v1 offset:0\n"
      "ds_read_b128 v[40:43], v2 offset:0\n"
      "ds_read_b128 v[44:47], v3 offset:0\n"
      "ds_read_b128 v[48:51], v0 offset:4096\n"
      "ds_read_b128 v[52:55], v1 offset:4096\n"
      "ds_read_b128 v[56:59], v2 offset:4096\n"
      "ds_read_b128 v[60:63], v3 offset:4096\n"
      "s_mov_b32 s58, 0x2000\n"
      "s_mov_b32 m0, 0x12000\n"
      "s_nop 0\n"
      "buffer_load_dwordx4 v10, s[44:47], s58 offen offset:0 nt lds\n"
      "buffer_load_dwordx4 v11, s[44:47], s58 offen offset:1024 nt lds\n"
      "buffer_load_dwordx4 v10, s[44:47], s58 offen offset:2048 nt lds\n"
      "buffer_load_dwordx4 v11, s[44:47], s58 offen offset:3072 nt lds\n"
      "s_waitcnt lgkmcnt(0)\n"
      "v_mfma_f32_16x16x32_bf16 v[96:99], v[32:35], a[192:195], 0\n"
      "v_lshlrev_b32 v20, 16, v152\n"
      "v_and_b32 v21, s97, v152\n"
      "v_lshlrev_b32 v22, 16, v153\n"
      "v_mfma_f32_16x16x32_bf16 v[96:99], v[36:39], a[196:199], v[96:99]\n"
      "v_and_b32 v23, s97, v153\n"
      "v_lshlrev_b32 v24, 16, v154\n"
      "v_and_b32 v25, s97, v154\n"
      "v_mfma_f32_16x16x32_bf16 v[96:99], v[40:43], a[200:203], v[96:99]\n"
      "v_lshlrev_b32 v26, 16, v155\n"
      "v_and_b32 v27, s97, v155\n"
      "v_pk_mul_f32 v[20:21], v[20:21], s[54:55] op_sel_hi:[1,0]\n"
      "v_mfma_f32_16x16x32_bf16 v[96:99], v[44:47], a[204:207], v[96:99]\n"
      "v_pk_mul_f32 v[22:23], v[22:23], s[54:55] op_sel_hi:[1,0]\n"
      "v_pk_mul_f32 v[24:25], v[24:25], s[54:55] op_sel_hi:[1,0]\n"
      "v_pk_mul_f32 v[26:27], v[26:27], s[54:55] op_sel_hi:[1,0]\n"
      "v_mfma_f32_16x16x32_bf16 v[100:103], v[48:51], a[192:195], 0\n"
      "v_cvt_pk_bf16_f32 v232, v20, v21\n"
      "v_cvt_pk_bf16_f32 v233, v22, v23\n"
      "v_cvt_pk_bf16_f32 v234, v24, v25\n"
      "v_mfma_f32_16x16x32_bf16 v[100:103], v[52:55], a[196:199], v[100:103]\n"
      "v_cvt_pk_bf16_f32 v235, v26, v27\n"
      "v_lshlrev_b32 v20, 16, v156\n"
      "v_and_b32 v21, s97, v156\n"
      "v_mfma_f32_16x16x32_bf16 v[100:103], v[56:59], a[200:203], v[100:103]\n"
      "v_lshlrev_b32 v22, 16, v157\n"
      "v_and_b32 v23, s97, v157\n"
      "v_lshlrev_b32 v24, 16, v158\n"
      "v_mfma_f32_16x16x32_bf16 v[100:103], v[60:63], a[204:207], v[100:103]\n"
      "v_and_b32 v25, s97, v158\n"
      "v_lshlrev_b32 v26, 16, v159\n"
      "v_and_b32 v27, s97, v159\n"
      "v_mfma_f32_16x16x32_bf16 v[104:107], v[32:35], a[208:211], 0\n"
      "v_pk_mul_f32 v[20:21], v[20:21], s[54:55] op_sel_hi:[1,0]\n"
      "v_pk_mul_f32 v[22:23], v[22:23], s[54:55] op_sel_hi:[1,0]\n"
      "v_pk_mul_f32 v[24:25], v[24:25], s[54:55] op_sel_hi:[1,0]\n"
      "v_mfma_f32_16x16x32_bf16 v[104:107], v[36:39], a[212:215], v[104:107]\n"
      "v_pk_mul_f32 v[26:27], v[26:27], s[54:55] op_sel_hi:[1,0]\n"
      "v_cvt_pk_bf16_f32 v236, v20, v21\n"
      "v_cvt_pk_bf16_f32 v237, v22, v23\n"
      "v_mfma_f32_16x16x32_bf16 v[104:107], v[40:43], a[216:219], v[104:107]\n"
      "v_cvt_pk_bf16_f32 v238, v24, v25\n"
      "v_cvt_pk_bf16_f32 v239, v26, v27\n"
      "v_lshlrev_b32 v20, 16, v160\n"
      "v_mfma_f32_16x16x32_bf16 v[104:107], v[44:47], a[220:223], v[104:107]\n"
      "v_and_b32 v21, s97, v160\n"
      "v_lshlrev_b32 v22, 16, v161\n"
      "v_and_b32 v23, s97, v161\n"
      "v_mfma_f32_16x16x32_bf16 v[108:111], v[48:51], a[208:211], 0\n"
      "v_lshlrev_b32 v24, 16, v162\n"
      "v_and_b32 v25, s97, v162\n"
      "v_lshlrev_b32 v26, 16, v163\n"
      "v_mfma_f32_16x16x32_bf16 v[108:111], v[52:55], a[212:215], v[108:111]\n"
      "v_and_b32 v27, s97, v163\n"
      "v_pk_mul_f32 v[20:21], v[20:21], s[54:55] op_sel_hi:[1,0]\n"
      "v_pk_mul_f32 v[22:23], v[22:23], s[54:55] op_sel_hi:[1,0]\n"
      "v_mfma_f32_16x16x32_bf16 v[108:111], v[56:59], a[216:219], v[108:111]\n"
      "v_pk_mul_f32 v[24:25], v[24:25], s[54:55] op_sel_hi:[1,0]\n"
      "v_pk_mul_f32 v[26:27], v[26:27], s[54:55] op_sel_hi:[1,0]\n"
      "v_cvt_pk_bf16_f32 v240, v20, v21\n"
      "v_mfma_f32_16x16x32_bf16 v[108:111], v[60:63], a[220:223], v[108:111]\n"
      "v_cvt_pk_bf16_f32 v241, v22, v23\n"
      "v_cvt_pk_bf16_f32 v242, v24, v25\n"
      "v_cvt_pk_bf16_f32 v243, v26, v27\n"
      "v_mfma_f32_16x16x32_bf16 v[112:115], v[32:35], a[224:227], 0\n"
      "v_lshlrev_b32 v20, 16, v164\n"
      "v_and_b32 v21, s97, v164\n"
      "v_lshlrev_b32 v22, 16, v165\n"
      "v_mfma_f32_16x16x32_bf16 v[112:115], v[36:39], a[228:231], v[112:115]\n"
      "v_and_b32 v23, s97, v165\n"
      "v_lshlrev_b32 v24, 16, v166\n"
      "v_and_b32 v25, s97, v166\n"
      "v_mfma_f32_16x16x32_bf16 v[112:115], v[40:43], a[232:235], v[112:115]\n"
      "v_lshlrev_b32 v26, 16, v167\n"
      "v_and_b32 v27, s97, v167\n"
      "v_pk_mul_f32 v[20:21], v[20:21], s[54:55] op_sel_hi:[1,0]\n"
      "v_mfma_f32_16x16x32_bf16 v[112:115], v[44:47], a[236:239], v[112:115]\n"
      "v_pk_mul_f32 v[22:23], v[22:23], s[54:55] op_sel_hi:[1,0]\n"
      "v_pk_mul_f32 v[24:25], v[24:25], s[54:55] op_sel_hi:[1,0]\n"
      "v_pk_mul_f32 v[26:27], v[26:27], s[54:55] op_sel_hi:[1,0]\n"
      "v_mfma_f32_16x16x32_bf16 v[116:119], v[48:51], a[224:227], 0\n"
      "v_cvt_pk_bf16_f32 v244, v20, v21\n"
      "v_cvt_pk_bf16_f32 v245, v22, v23\n"
      "v_cvt_pk_bf16_f32 v246, v24, v25\n"
      "v_mfma_f32_16x16x32_bf16 v[116:119], v[52:55], a[228:231], v[116:119]\n"
      "v_cvt_pk_bf16_f32 v247, v26, v27\n"
      "v_mfma_f32_16x16x32_bf16 v[116:119], v[56:59], a[232:235], v[116:119]\n"
      "v_mfma_f32_16x16x32_bf16 v[116:119], v[60:63], a[236:239], v[116:119]\n"
      "s_nop 1\n"
      "v_mfma_f32_16x16x32_bf16 v[176:179], v[32:35], v[232:235], 0\n"
      "v_mfma_f32_16x16x32_bf16 v[176:179], v[36:39], v[236:239], v[176:179]\n"
      "v_mfma_f32_16x16x32_bf16 v[176:179], v[40:43], v[240:243], v[176:179]\n"
      "v_mfma_f32_16x16x32_bf16 v[176:179], v[44:47], v[244:247], v[176:179]\n"
      "v_mfma_f32_16x16x32_bf16 v[168:171], v[48:51], v[232:235], 0\n"
      "v_mfma_f32_16x16x32_bf16 v[168:171], v[52:55], v[236:239], v[168:171]\n"
      "v_mfma_f32_16x16x32_bf16 v[168:171], v[56:59], v[240:243], v[168:171]\n"
      "v_mfma_f32_16x16x32_bf16 v[168:171], v[60:63], v[244:247], v[168:171]\n"
      "v_max3_f32 v28, v96, v97, v98\n"
      "v_max3_f32 v29, v99, v100, v101\n"
      "v_max3_f32 v28, v28, v102, v103\n"
      "v_max_f32 v28, v28, v29\n"
      "v_mov_b32 v29, v28\n"
      "s_nop 1\n"
      "v_permlane16_swap_b32 v28, v29\n"
      "s_nop 0\n"
      "v_max_f32 v28, v28, v29\n"
      "v_mov_b32 v29, v28\n"
      "s_nop 1\n"
      "v_permlane32_swap_b32 v28, v29\n"
      "s_nop 0\n"
      "v_max_f32 v28, v28, v29\n"
      "v_sub_f32 v192, 0, v28\n"
      "v_mov_b32 v193, v192\n"
      "v_mov_b32 v194, v192\n"
      "v_mov_b32 v195, v192\n"
      "v_add_f32 v96, v96, v192\n"
      "v_add_f32 v97, v97, v192\n"
      "v_add_f32 v98, v98, v192\n"
      "v_add_f32 v99, v99, v192\n"
      "v_add_f32 v100, v100, v192\n"
      "v_add_f32 v101, v101, v192\n"
      "v_add_f32 v102, v102, v192\n"
      "v_add_f32 v103, v103, v192\n"
      "v_max3_f32 v28, v104, v105, v106\n"
      "v_max3_f32 v29, v107, v108, v109\n"
      "v_max3_f32 v28, v28, v110, v111\n"
      "v_max_f32 v28, v28, v29\n"
      "v_mov_b32 v29, v28\n"
      "s_nop 1\n"
      "v_permlane16_swap_b32 v28, v29\n"
      "s_nop 0\n"
      "v_max_f32 v28, v28, v29\n"
      "v_mov_b32 v29, v28\n"
      "s_nop 1\n"
      "v_permlane32_swap_b32 v28, v29\n"
      "s_nop 0\n"
      "v_max_f32 v28, v28, v29\n"
      "v_sub_f32 v196, 0, v28\n"
      "v_mov_b32 v197, v196\n"
      "v_mov_b32 v198, v196\n"
      "v_mov_b32 v199, v196\n"
      "v_add_f32 v104, v104, v196\n"
      "v_add_f32 v105, v105, v196\n"
      "v_add_f32 v106, v106, v196\n"
      "v_add_f32 v107, v107, v196\n"
      "v_add_f32 v108, v108, v196\n"
      "v_add_f32 v109, v109, v196\n"
      "v_add_f32 v110, v110, v196\n"
      "v_add_f32 v111, v111, v196\n"
      "v_max3_f32 v28, v112, v113, v114\n"
      "v_max3_f32 v29, v115, v116, v117\n"
      "v_max3_f32 v28, v28, v118, v119\n"
      "v_max_f32 v28, v28, v29\n"
      "v_mov_b32 v29, v28\n"
      "s_nop 1\n"
      "v_permlane16_swap_b32 v28, v29\n"
      "s_nop 0\n"
      "v_max_f32 v28, v28, v29\n"
      "v_mov_b32 v29, v28\n"
      "s_nop 1\n"
      "v_permlane32_swap_b32 v28, v29\n"
      "s_nop 0\n"
      "v_max_f32 v28, v28, v29\n"
      "v_sub_f32 v200, 0, v28\n"
      "v_mov_b32 v201, v200\n"
      "v_mov_b32 v202, v200\n"
      "v_mov_b32 v203, v200\n"
      "v_add_f32 v112, v112, v200\n"
      "v_add_f32 v113, v113, v200\n"
      "v_add_f32 v114, v114, v200\n"
      "v_add_f32 v115, v115, v200\n"
      "v_add_f32 v116, v116, v200\n"
      "v_add_f32 v117, v117, v200\n"
      "s_waitcnt vmcnt(12)\n"
      "ds_read_b128 v[136:139], v248 offset:12288\n"
      "ds_read_b128 v[140:143], v249 offset:12288\n"
      "ds_read_b128 v[144:147], v250 offset:12288\n"
      "ds_read_b128 v[148:151], v251 offset:12288\n"
      "s_mov_b32 s58, 0x4000\n"
      "s_mov_b32 m0, 0x14000\n"
      "s_nop 0\n"
      "buffer_load_dwordx4 v10, s[44:47], s58 offen offset:0 nt lds\n"
      "buffer_load_dwordx4 v11, s[44:47], s58 offen offset:1024 nt lds\n"
      "buffer_load_dwordx4 v10, s[44:47], s58 offen offset:2048 nt lds\n"
      "buffer_load_dwordx4 v11, s[44:47], s58 offen offset:3072 nt lds\n"
      "v_add_f32 v118, v118, v200\n"
      "v_add_f32 v119, v119, v200\n"
      "v_max3_f32 v28, v176, v177, v178\n"
      "v_max3_f32 v29, v179, v168, v169\n"
      "v_max3_f32 v28, v28, v170, v171\n"
      "v_max_f32 v28, v28, v29\n"
      "v_mov_b32 v29, v28\n"
      "s_nop 1\n"
      "s_waitcnt lgkmcnt(0)\n"
      "v_permlane16_swap_b32 v28, v29\n"
      "v_lshlrev_b32 v20, 16, v136\n"
      "v_max_f32 v28, v28, v29\n"
      "v_mov_b32 v29, v28\n"
      "v_and_b32 v21, s97, v136\n"
      "v_lshlrev_b32 v22, 16, v137\n"
      "v_permlane32_swap_b32 v28, v29\n"
      "v_and_b32 v23, s97, v137\n"
      "v_max_f32 v28, v28, v29\n"
      "v_sub_f32 v212, 0, v28\n"
      "v_mov_b32 v213, v212\n"
      "v_mov_b32 v214, v212\n"
      "v_mov_b32 v215, v212\n"
      "v_add_f32 v176, v176, v212\n"
      "v_add_f32 v177, v177, v212\n"
      "v_add_f32 v178, v178, v212\n"
      "v_add_f32 v179, v179, v212\n"
      "v_lshlrev_b32 v24, 16, v138\n"
      "v_and_b32 v25, s97, v138\n"
      "v_lshlrev_b32 v26, 16, v139\n"
      "v_and_b32 v27, s97, v139\n"
      "v_pk_mul_f32 v[20:21], v[20:21], s[54:55] op_sel_hi:[1,0]\n"
      "v_pk_mul_f32 v[22:23], v[22:23], s[54:55] op_sel_hi:[1,0]\n"
      "v_pk_mul_f32 v[24:25], v[24:25], s[54:55] op_sel_hi:[1,0]\n"
      "v_pk_mul_f32 v[26:27], v[26:27], s[54:55] op_sel_hi:[1,0]\n"
      "v_cvt_pk_bf16_f32 v136, v20, v21\n"
      "v_cvt_pk_bf16_f32 v137, v22, v23\n"
      "v_cvt_pk_bf16_f32 v138, v24, v25\n"
      "v_cvt_pk_bf16_f32 v139, v26, v27\n"
      "v_accvgpr_write_b32 a240, v136\n"
      "v_accvgpr_write_b32 a241, v137\n"
      "v_accvgpr_write_b32 a242, v138\n"
      "v_accvgpr_write_b32 a243, v139\n"
      "v_lshlrev_b32 v20, 16, v140\n"
      "v_and_b32 v21, s97, v140\n"
      "v_lshlrev_b32 v22, 16, v141\n"
      "v_and_b32 v23, s97, v141\n"
      "v_lshlrev_b32 v24, 16, v142\n"
      "v_and_b32 v25, s97, v142\n"
      "v_lshlrev_b32 v26, 16, v143\n"
      "v_and_b32 v27, s97, v143\n"
      "v_pk_mul_f32 v[20:21], v[20:21], s[54:55] op_sel_hi:[1,0]\n"
      "v_pk_mul_f32 v[22:23], v[22:23], s[54:55] op_sel_hi:[1,0]\n"
      "v_pk_mul_f32 v[24:25], v[24:25], s[54:55] op_sel_hi:[1,0]\n"
      "v_pk_mul_f32 v[26:27], v[26:27], s[54:55] op_sel_hi:[1,0]\n"
      "v_cvt_pk_bf16_f32 v140, v20, v21\n"
      "v_cvt_pk_bf16_f32 v141, v22, v23\n"
      "v_cvt_pk_bf16_f32 v142, v24, v25\n"
      "v_cvt_pk_bf16_f32 v143, v26, v27\n"
      "v_accvgpr_write_b32 a244, v140\n"
      "v_accvgpr_write_b32 a245, v141\n"
      "v_accvgpr_write_b32 a246, v142\n"
      "v_accvgpr_write_b32 a247, v143\n"
      "v_lshlrev_b32 v20, 16, v144\n"
      "v_and_b32 v21, s97, v144\n"
      "v_lshlrev_b32 v22, 16, v145\n"
      "v_and_b32 v23, s97, v145\n"
      "v_lshlrev_b32 v24, 16, v146\n"
      "v_and_b32 v25, s97, v146\n"
      "v_lshlrev_b32 v26, 16, v147\n"
      "v_and_b32 v27, s97, v147\n"
      "v_pk_mul_f32 v[20:21], v[20:21], s[54:55] op_sel_hi:[1,0]\n"
      "v_pk_mul_f32 v[22:23], v[22:23], s[54:55] op_sel_hi:[1,0]\n"
      "v_pk_mul_f32 v[24:25], v[24:25], s[54:55] op_sel_hi:[1,0]\n"
      "v_pk_mul_f32 v[26:27], v[26:27], s[54:55] op_sel_hi:[1,0]\n"
      "v_cvt_pk_bf16_f32 v144, v20, v21\n"
      "v_cvt_pk_bf16_f32 v145, v22, v23\n"
      "v_cvt_pk_bf16_f32 v146, v24, v25\n"
      "v_cvt_pk_bf16_f32 v147, v26, v27\n"
      "v_accvgpr_write_b32 a248, v144\n"
      "v_accvgpr_write_b32 a249, v145\n"
      "v_accvgpr_write_b32 a250, v146\n"
      "v_accvgpr_write_b32 a251, v147\n"
      "v_lshlrev_b32 v20, 16, v148\n"
      "v_and_b32 v21, s97, v148\n"
      "v_lshlrev_b32 v22, 16, v149\n"
      "v_and_b32 v23, s97, v149\n"
      "v_lshlrev_b32 v24, 16, v150\n"
      "v_and_b32 v25, s97, v150\n"
      "v_lshlrev_b32 v26, 16, v151\n"
      "v_and_b32 v27, s97, v151\n"
      "v_pk_mul_f32 v[20:21], v[20:21], s[54:55] op_sel_hi:[1,0]\n"
      "v_pk_mul_f32 v[22:23], v[22:23], s[54:55] op_sel_hi:[1,0]\n"
      "v_pk_mul_f32 v[24:25], v[24:25], s[54:55] op_sel_hi:[1,0]\n"
      "v_pk_mul_f32 v[26:27], v[26:27], s[54:55] op_sel_hi:[1,0]\n"
      "v_cvt_pk_bf16_f32 v148, v20, v21\n"
      "v_cvt_pk_bf16_f32 v149, v22, v23\n"
      "v_cvt_pk_bf16_f32 v150, v24, v25\n"
      "v_cvt_pk_bf16_f32 v151, v26, v27\n"
      "v_accvgpr_write_b32 a252, v148\n"
      "v_accvgpr_write_b32 a253, v149\n"
      "v_accvgpr_write_b32 a254, v150\n"
      "v_accvgpr_write_b32 a255, v151\n"
      "s_nop 1\n"
      "v_mfma_f32_16x16x32_bf16 v[120:123], v[32:35], a[240:243], 0\n"
      "v_mfma_f32_16x16x32_bf16 v[120:123], v[36:39], a[244:247], v[120:123]\n"
      "v_mfma_f32_16x16x32_bf16 v[120:123], v[40:43], a[248:251], v[120:123]\n"
      "v_mfma_f32_16x16x32_bf16 v[120:123], v[44:47], a[252:255], v[120:123]\n"
      "v_mfma_f32_16x16x32_bf16 v[124:127], v[48:51], a[240:243], 0\n"
      "v_mfma_f32_16x16x32_bf16 v[124:127], v[52:55], a[244:247], v[124:127]\n"
      "v_mfma_f32_16x16x32_bf16 v[124:127], v[56:59], a[248:251], v[124:127]\n"
      "v_mfma_f32_16x16x32_bf16 v[124:127], v[60:63], a[252:255], v[124:127]\n"
      "s_nop 3\n"
      "s_waitcnt vmcnt(12)\n"
      "ds_read_b128 v[152:155], v248 offset:16384\n"
      "ds_read_b128 v[156:159], v249 offset:16384\n"
      "ds_read_b128 v[160:163], v250 offset:16384\n"
      "ds_read_b128 v[164:167], v251 offset:16384\n"
      "s_mov_b32 s58, 0x6000\n"
      "s_mov_b32 m0, 0x16000\n"
      "s_nop 0\n"
      "buffer_load_dwordx4 v10, s[44:47], s58 offen offset:0 nt lds\n"
      "buffer_load_dwordx4 v11, s[44:47], s58 offen offset:1024 nt lds\n"
      "buffer_load_dwordx4 v10, s[44:47], s58 offen offset:2048 nt lds\n"
      "buffer_load_dwordx4 v11, s[44:47], s58 offen offset:3072 nt lds\n"
      "v_max3_f32 v28, v120, v121, v122\n"
      "v_max3_f32 v29, v123, v124, v125\n"
      "v_max3_f32 v28, v28, v126, v127\n"
      "v_max_f32 v28, v28, v29\n"
      "v_mov_b32 v29, v28\n"
      "s_nop 1\n"
      "v_permlane16_swap_b32 v28, v29\n"
      "s_nop 0\n"
      "s_waitcnt lgkmcnt(0)\n"
      "v_max_f32 v28, v28, v29\n"
      "v_mov_b32 v29, v28\n"
      "v_lshlrev_b32 v20, 16, v152\n"
      "v_and_b32 v21, s97, v152\n"
      "v_permlane32_swap_b32 v28, v29\n"
      "v_lshlrev_b32 v22, 16, v153\n"
      "v_max_f32 v28, v28, v29\n"
      "v_sub_f32 v204, 0, v28\n"
      "v_mov_b32 v205, v204\n"
      "v_mov_b32 v206, v204\n"
      "v_mov_b32 v207, v204\n"
      "v_add_f32 v120, v120, v204\n"
      "v_add_f32 v121, v121, v204\n"
      "v_add_f32 v122, v122, v204\n"
      "v_add_f32 v123, v123, v204\n"
      "v_add_f32 v124, v124, v204\n"
      "v_add_f32 v125, v125, v204\n"
      "v_add_f32 v126, v126, v204\n"
      "v_add_f32 v127, v127, v204\n"
      "v_and_b32 v23, s97, v153\n"
      "v_lshlrev_b32 v24, 16, v154\n"
      "v_and_b32 v25, s97, v154\n"
      "v_lshlrev_b32 v26, 16, v155\n"
      "v_and_b32 v27, s97, v155\n"
      "v_pk_mul_f32 v[20:21], v[20:21], s[54:55] op_sel_hi:[1,0]\n"
      "v_pk_mul_f32 v[22:23], v[22:23], s[54:55] op_sel_hi:[1,0]\n"
      "v_pk_mul_f32 v[24:25], v[24:25], s[54:55] op_sel_hi:[1,0]\n"
      "v_pk_mul_f32 v[26:27], v[26:27], s[54:55] op_sel_hi:[1,0]\n"
      "v_cvt_pk_bf16_f32 v216, v20, v21\n"
      "v_cvt_pk_bf16_f32 v217, v22, v23\n"
      "v_cvt_pk_bf16_f32 v218, v24, v25\n"
      "v_cvt_pk_bf16_f32 v219, v26, v27\n"
      "v_lshlrev_b32 v20, 16, v156\n"
      "v_and_b32 v21, s97, v156\n"
      "v_lshlrev_b32 v22, 16, v157\n"
      "v_and_b32 v23, s97, v157\n"
      "v_lshlrev_b32 v24, 16, v158\n"
      "v_and_b32 v25, s97, v158\n"
      "v_lshlrev_b32 v26, 16, v159\n"
      "v_and_b32 v27, s97, v159\n"
      "v_pk_mul_f32 v[20:21], v[20:21], s[54:55] op_sel_hi:[1,0]\n"
      "v_pk_mul_f32 v[22:23], v[22:23], s[54:55] op_sel_hi:[1,0]\n"
      "v_pk_mul_f32 v[24:25], v[24:25], s[54:55] op_sel_hi:[1,0]\n"
      "v_pk_mul_f32 v[26:27], v[26:27], s[54:55] op_sel_hi:[1,0]\n"
      "v_cvt_pk_bf16_f32 v220, v20, v21\n"
      "v_cvt_pk_bf16_f32 v221, v22, v23\n"
      "v_cvt_pk_bf16_f32 v222, v24, v25\n"
      "v_cvt_pk_bf16_f32 v223, v26, v27\n"
      "v_lshlrev_b32 v20, 16, v160\n"
      "v_and_b32 v21, s97, v160\n"
      "v_lshlrev_b32 v22, 16, v161\n"
      "v_and_b32 v23, s97, v161\n"
      "v_lshlrev_b32 v24, 16, v162\n"
      "v_and_b32 v25, s97, v162\n"
      "v_lshlrev_b32 v26, 16, v163\n"
      "v_and_b32 v27, s97, v163\n"
      "v_pk_mul_f32 v[20:21], v[20:21], s[54:55] op_sel_hi:[1,0]\n"
      "v_pk_mul_f32 v[22:23], v[22:23], s[54:55] op_sel_hi:[1,0]\n"
      "v_pk_mul_f32 v[24:25], v[24:25], s[54:55] op_sel_hi:[1,0]\n"
      "v_pk_mul_f32 v[26:27], v[26:27], s[54:55] op_sel_hi:[1,0]\n"
      "v_cvt_pk_bf16_f32 v224, v20, v21\n"
      "v_cvt_pk_bf16_f32 v225, v22, v23\n"
      "v_cvt_pk_bf16_f32 v226, v24, v25\n"
      "v_cvt_pk_bf16_f32 v227, v26, v27\n"
      "v_lshlrev_b32 v20, 16, v164\n"
      "v_and_b32 v21, s97, v164\n"
      "v_lshlrev_b32 v22, 16, v165\n"
      "v_and_b32 v23, s97, v165\n"
      "v_lshlrev_b32 v24, 16, v166\n"
      "v_and_b32 v25, s97, v166\n"
      "v_lshlrev_b32 v26, 16, v167\n"
      "v_and_b32 v27, s97, v167\n"
      "v_pk_mul_f32 v[20:21], v[20:21], s[54:55] op_sel_hi:[1,0]\n"
      "v_pk_mul_f32 v[22:23], v[22:23], s[54:55] op_sel_hi:[1,0]\n"
      "v_pk_mul_f32 v[24:25], v[24:25], s[54:55] op_sel_hi:[1,0]\n"
      "v_pk_mul_f32 v[26:27], v[26:27], s[54:55] op_sel_hi:[1,0]\n"
      "v_cvt_pk_bf16_f32 v228, v20, v21\n"
      "v_cvt_pk_bf16_f32 v229, v22, v23\n"
      "v_cvt_pk_bf16_f32 v230, v24, v25\n"
      "v_cvt_pk_bf16_f32 v231, v26, v27\n"
      "s_nop 1\n"
      "v_mfma_f32_16x16x32_bf16 v[128:131], v[32:35], v[216:219], 0\n"
      "v_mfma_f32_16x16x32_bf16 v[128:131], v[36:39], v[220:223], v[128:131]\n"
      "v_mfma_f32_16x16x32_bf16 v[128:131], v[40:43], v[224:227], v[128:131]\n"
      "v_mfma_f32_16x16x32_bf16 v[128:131], v[44:47], v[228:231], v[128:131]\n"
      "v_mfma_f32_16x16x32_bf16 v[132:135], v[48:51], v[216:219], 0\n"
      "v_mfma_f32_16x16x32_bf16 v[132:135], v[52:55], v[220:223], v[132:135]\n"
      "v_mfma_f32_16x16x32_bf16 v[132:135], v[56:59], v[224:227], v[132:135]\n"
      "v_mfma_f32_16x16x32_bf16 v[132:135], v[60:63], v[228:231], v[132:135]\n"
      "s_nop 3\n"
      "s_nop 3\n"
      "v_max3_f32 v28, v128, v129, v130\n"
      "v_max3_f32 v29, v131, v132, v133\n"
      "v_max3_f32 v28, v28, v134, v135\n"
      "v_max_f32 v28, v28, v29\n"
      "v_mov_b32 v29, v28\n"
      "s_nop 1\n"
      "v_permlane16_swap_b32 v28, v29\n"
      "s_nop 0\n"
      "v_max_f32 v28, v28, v29\n"
      "v_mov_b32 v29, v28\n"
      "s_nop 1\n"
      "v_permlane32_swap_b32 v28, v29\n"
      "s_nop 0\n"
      "v_max_f32 v28, v28, v29\n"
      "v_sub_f32 v208, 0, v28\n"
      "v_mov_b32 v209, v208\n"
      "v_mov_b32 v210, v208\n"
      "v_mov_b32 v211, v208\n"
      "v_add_f32 v128, v128, v208\n"
      "v_add_f32 v129, v129, v208\n"
      "v_add_f32 v130, v130, v208\n"
      "v_add_f32 v131, v131, v208\n"
      "v_add_f32 v132, v132, v208\n"
      "v_add_f32 v133, v133, v208\n"
      "v_add_f32 v134, v134, v208\n"
      "v_add_f32 v135, v135, v208\n"
      "L_restart_w0:\n"
      "s_mov_b32 s55, 0x8000\n"
      "s_mov_b32 s56, 0\n"
      "s_mov_b32 s57, 16\n"
      "s_waitcnt vmcnt(12)\n"
      "s_barrier\n"
      "s_xor_b32 m0, s56, 0x18000\n"
      "s_nop 0\n"
      "buffer_load_dwordx4 v10, s[44:47], s55 offen offset:0 nt lds\n"
      "buffer_load_dwordx4 v11, s[44:47], s55 offen offset:1024 nt lds\n"
      "v_exp_f32 v96, v96\n"
      "v_exp_f32 v97, v97\n"
      "v_add_f32 v14, v14, v96\n"
      "v_add_f32 v14, v14, v97\n"
      "v_cvt_pk_bf16_f32 v96, v96, v97\n"
      "buffer_load_dwordx4 v10, s[44:47], s55 offen offset:2048 nt lds\n"
      "v_exp_f32 v98, v98\n"
      "v_exp_f32 v99, v99\n"
      "v_add_f32 v14, v14, v98\n"
      "v_add_f32 v14, v14, v99\n"
      "v_cvt_pk_bf16_f32 v97, v98, v99\n"
      "v_exp_f32 v100, v100\n"
      "v_exp_f32 v101, v101\n"
      "v_add_f32 v14, v14, v100\n"
      "v_add_f32 v14, v14, v101\n"
      "v_cvt_pk_bf16_f32 v98, v100, v101\n"
      "buffer_load_dwordx4 v11, s[44:47], s55 offen offset:3072 nt lds\n"
      "v_exp_f32 v102, v102\n"
      "s_add_u32 s55, s55, 0x2000\n"
      "v_add_f32 v14, v14, v102\n"
      "v_exp_f32 v103, v103\n"
      "v_exp_f32 v104, v104\n"
      "v_add_f32 v14, v14, v103\n"
      "v_cvt_pk_bf16_f32 v99, v102, v103\n"
      "v_add_f32 v15, v15, v104\n"
      "v_exp_f32 v105, v105\n"
      "v_exp_f32 v106, v106\n"
      "v_add_f32 v15, v15, v105\n"
      "v_cvt_pk_bf16_f32 v104, v104, v105\n"
      "v_add_f32 v15, v15, v106\n"
      "v_exp_f32 v107, v107\n"
      "v_exp_f32 v108, v108\n"
      "v_add_f32 v15, v15, v107\n"
      "v_cvt_pk_bf16_f32 v105, v106, v107\n"
      "v_add_f32 v15, v15, v108\n"
      "v_exp_f32 v109, v109\n"
      "s_nop 0\n"
      "v_add_f32 v15, v15, v109\n"
      "v_cvt_pk_bf16_f32 v106, v108, v109\n"
      "s_xor_b32 m0, s56, 0x1a000\n"
      "s_nop 0\n"
      "buffer_load_dwordx4 v10, s[44:47], s55 offen offset:0 nt lds\n"
      "v_exp_f32 v110, v110\n"
      "v_exp_f32 v111, v111\n"
      "v_add_f32 v15, v15, v110\n"
      "v_add_f32 v15, v15, v111\n"
      "v_cvt_pk_bf16_f32 v107, v110, v111\n"
      "v_exp_f32 v112, v112\n"
      "ds_read_b128 v[32:35], v0 offset:8192\n"
      "v_add_f32 v16, v16, v112\n"
      "v_exp_f32 v113, v113\n"
      "s_nop 0\n"
      "v_add_f32 v16, v16, v113\n"
      "v_cvt_pk_bf16_f32 v112, v112, v113\n"
      "buffer_load_dwordx4 v11, s[44:47], s55 offen offset:1024 nt lds\n"
      "v_exp_f32 v114, v114\n"
      "ds_read_b128 v[36:39], v1 offset:8192\n"
      "v_add_f32 v16, v16, v114\n"
      "v_exp_f32 v115, v115\n"
      "v_exp_f32 v116, v116\n"
      "v_add_f32 v16, v16, v115\n"
      "v_cvt_pk_bf16_f32 v113, v114, v115\n"
      "ds_read_b128 v[40:43], v2 offset:8192\n"
      "v_add_f32 v16, v16, v116\n"
      "v_exp_f32 v117, v117\n"
      "s_nop 0\n"
      "v_add_f32 v16, v16, v117\n"
      "v_cvt_pk_bf16_f32 v114, v116, v117\n"
      "buffer_load_dwordx4 v10, s[44:47], s55 offen offset:2048 nt lds\n"
      "v_exp_f32 v118, v118\n"
      "ds_read_b128 v[44:47], v3 offset:8192\n"
      "v_add_f32 v16, v16, v118\n"
      "v_exp_f32 v119, v119\n"
      "v_exp_f32 v120, v120\n"
      "v_add_f32 v16, v16, v119\n"
      "v_cvt_pk_bf16_f32 v115, v118, v119\n"
      "ds_read_b128 v[48:51], v0 offset:12288\n"
      "v_add_f32 v17, v17, v120\n"
      "v_exp_f32 v121, v121\n"
      "s_nop 0\n"
      "v_add_f32 v17, v17, v121\n"
      "v_cvt_pk_bf16_f32 v120, v120, v121\n"
      "buffer_load_dwordx4 v11, s[44:47], s55 offen offset:3072 nt lds\n"
      "v_exp_f32 v122, v122\n"
      "s_add_u32 s55, s55, 0x2000\n"
      "ds_read_b128 v[52:55], v1 offset:12288\n"
      "v_add_f32 v17, v17, v122\n"
      "v_exp_f32 v123, v123\n"
      "v_exp_f32 v124, v124\n"
      "v_add_f32 v17, v17, v123\n"
      "v_cvt_pk_bf16_f32 v121, v122, v123\n"
      "ds_read_b128 v[56:59], v2 offset:12288\n"
      "v_add_f32 v17, v17, v124\n"
      "v_exp_f32 v125, v125\n"
      "v_exp_f32 v126, v126\n"
      "v_add_f32 v17, v17, v125\n"
      "v_cvt_pk_bf16_f32 v122, v124, v125\n"
      "ds_read_b128 v[60:63], v3 offset:12288\n"
      "v_add_f32 v17, v17, v126\n"
      "v_exp_f32 v127, v127\n"
      "v_exp_f32 v128, v128\n"
      "v_add_f32 v17, v17, v127\n"
      "v_cvt_pk_bf16_f32 v123, v126, v127\n"
      "v_add_f32 v18, v18, v128\n"
      "v_exp_f32 v129, v129\n"
      "v_exp_f32 v130, v130\n"
      "v_add_f32 v18, v18, v129\n"
      "v_cvt_pk_bf16_f32 v128, v128, v129\n"
      "v_add_f32 v18, v18, v130\n"
      "s_waitcnt lgkmcnt(0)\n"
      "s_waitcnt vmcnt(16)\n"
      "s_barrier\n"
      "s_branch L_entry_w0\n"
      "L_loop_w0:\n"
      "v_mfma_f32_16x16x32_bf16 v[96:99], v[32:35], a[192:195], v[192:195]\n"
      "v_exp_f32 v170, v170\n"
      "v_mfma_f32_16x16x32_bf16 v[96:99], v[36:39], a[196:199], v[96:99]\n"
      "ds_read_b64_tr_b16 v[64:65], v4 offset:24576\n"
      "v_add_f32 v18, v18, v170\n"
      "v_mfma_f32_16x16x32_bf16 v[96:99], v[40:43], a[200:203], v[96:99]\n"
      "v_exp_f32 v171, v171\n"
      "v_mfma_f32_16x16x32_bf16 v[96:99], v[44:47], a[204:207], v[96:99]\n"
      "v_add_f32 v18, v18, v171\n"
      "v_cvt_pk_bf16_f32 v169, v170, v171\n"
      "v_mfma_f32_16x16x32_bf16 v[100:103], v[48:51], a[192:195], v[192:195]\n"
      "v_exp_f32 v172, v172\n"
      "v_mfma_f32_16x16x32_bf16 v[100:103], v[52:55], a[196:199], v[100:103]\n"
      "ds_read_b64_tr_b16 v[66:67], v4 offset:28672\n"
      "v_add_f32 v18, v18, v172\n"
      "v_mfma_f32_16x16x32_bf16 v[100:103], v[56:59], a[200:203], v[100:103]\n"
      "v_exp_f32 v173, v173\n"
      "v_mfma_f32_16x16x32_bf16 v[100:103], v[60:63], a[204:207], v[100:103]\n"
      "v_add_f32 v18, v18, v173\n"
      "v_cvt_pk_bf16_f32 v170, v172, v173\n"
      "v_mfma_f32_16x16x32_bf16 v[104:107], v[32:35], a[208:211], v[196:199]\n"
      "v_exp_f32 v174, v174\n"
      "v_mfma_f32_16x16x32_bf16 v[104:107], v[36:39], a[212:215], v[104:107]\n"
      "ds_read_b64_tr_b16 v[68:69], v5 offset:24576\n"
      "v_add_f32 v18, v18, v174\n"
      "v_mfma_f32_16x16x32_bf16 v[104:107], v[40:43], a[216:219], v[104:107]\n"
      "v_exp_f32 v175, v175\n"
      "v_mfma_f32_16x16x32_bf16 v[104:107], v[44:47], a[220:223], v[104:107]\n"
      "v_add_f32 v18, v18, v175\n"
      "v_cvt_pk_bf16_f32 v171, v174, v175\n"
      "v_mfma_f32_16x16x32_bf16 v[108:111], v[48:51], a[208:211], v[196:199]\n"
      "ds_read_b64_tr_b16 v[70:71], v5 offset:28672\n"
      "ds_read_b64_tr_b16 v[72:73], v4 offset:24832\n"
      "v_mfma_f32_16x16x32_bf16 v[108:111], v[52:55], a[212:215], v[108:111]\n"
      "v_mfma_f32_16x16x32_bf16 v[108:111], v[56:59], a[216:219], v[108:111]\n"
      "v_exp_f32 v96, v96\n"
      "v_mfma_f32_16x16x32_bf16 v[108:111], v[60:63], a[220:223], v[108:111]\n"
      "ds_read_b64_tr_b16 v[74:75], v4 offset:28928\n"
      "v_add_f32 v14, v14, v96\n"
      "v_mfma_f32_16x16x32_bf16 v[112:115], v[32:35], a[224:227], v[200:203]\n"
      "v_exp_f32 v97, v97\n"
      "v_mfma_f32_16x16x32_bf16 v[112:115], v[36:39], a[228:231], v[112:115]\n"
      "v_add_f32 v14, v14, v97\n"
      "v_cvt_pk_bf16_f32 v96, v96, v97\n"
      "v_mfma_f32_16x16x32_bf16 v[112:115], v[40:43], a[232:235], v[112:115]\n"
      "v_exp_f32 v98, v98\n"
      "v_mfma_f32_16x16x32_bf16 v[112:115], v[44:47], a[236:239], v[112:115]\n"
      "ds_read_b64_tr_b16 v[76:77], v5 offset:24832\n"
      "v_add_f32 v14, v14, v98\n"
      "v_mfma_f32_16x16x32_bf16 v[116:119], v[48:51], a[224:227], v[200:203]\n"
      "v_exp_f32 v99, v99\n"
      "v_mfma_f32_16x16x32_bf16 v[116:119], v[52:55], a[228:231], v[116:119]\n"
      "v_add_f32 v14, v14, v99\n"
      "v_cvt_pk_bf16_f32 v97, v98, v99\n"
      "v_mfma_f32_16x16x32_bf16 v[116:119], v[56:59], a[232:235], v[116:119]\n"
      "v_exp_f32 v100, v100\n"
      "v_mfma_f32_16x16x32_bf16 v[116:119], v[60:63], a[236:239], v[116:119]\n"
      "ds_read_b64_tr_b16 v[78:79], v5 offset:28928\n"
      "v_add_f32 v14, v14, v100\n"
      "v_mfma_f32_16x16x32_bf16 v[120:123], v[32:35], a[240:243], v[204:207]\n"
      "v_exp_f32 v101, v101\n"
      "v_mfma_f32_16x16x32_bf16 v[120:123], v[36:39], a[244:247], v[120:123]\n"
      "v_add_f32 v14, v14, v101\n"
      "v_cvt_pk_bf16_f32 v98, v100, v101\n"
      "v_mfma_f32_16x16x32_bf16 v[120:123], v[40:43], a[248:251], v[120:123]\n"
      "v_exp_f32 v102, v102\n"
      "v_mfma_f32_16x16x32_bf16 v[120:123], v[44:47], a[252:255], v[120:123]\n"
      "ds_read_b64_tr_b16 v[80:81], v4 offset:25088\n"
      "ds_read_b64_tr_b16 v[82:83], v4 offset:29184\n"
      "v_add_f32 v14, v14, v102\n"
      "v_mfma_f32_16x16x32_bf16 v[124:127], v[48:51], a[240:243], v[204:207]\n"
      "v_exp_f32 v103, v103\n"
      "v_mfma_f32_16x16x32_bf16 v[124:127], v[52:55], a[244:247], v[124:127]\n"
      "v_add_f32 v14, v14, v103\n"
      "v_cvt_pk_bf16_f32 v99, v102, v103\n"
      "v_mfma_f32_16x16x32_bf16 v[124:127], v[56:59], a[248:251], v[124:127]\n"
      "v_exp_f32 v104, v104\n"
      "v_mfma_f32_16x16x32_bf16 v[124:127], v[60:63], a[252:255], v[124:127]\n"
      "ds_read_b64_tr_b16 v[84:85], v5 offset:25088\n"
      "ds_read_b64_tr_b16 v[86:87], v5 offset:29184\n"
      "v_add_f32 v15, v15, v104\n"
      "v_mfma_f32_16x16x32_bf16 v[128:131], v[32:35], v[216:219], v[208:211]\n"
      "v_exp_f32 v105, v105\n"
      "v_mfma_f32_16x16x32_bf16 v[128:131], v[36:39], v[220:223], v[128:131]\n"
      "v_add_f32 v15, v15, v105\n"
      "v_cvt_pk_bf16_f32 v104, v104, v105\n"
      "v_mfma_f32_16x16x32_bf16 v[128:131], v[40:43], v[224:227], v[128:131]\n"
      "v_exp_f32 v106, v106\n"
      "v_mfma_f32_16x16x32_bf16 v[128:131], v[44:47], v[228:231], v[128:131]\n"
      "ds_read_b64_tr_b16 v[88:89], v4 offset:25344\n"
      "ds_read_b64_tr_b16 v[90:91], v4 offset:29440\n"
      "v_add_f32 v15, v15, v106\n"
      "v_mfma_f32_16x16x32_bf16 v[132:135], v[48:51], v[216:219], v[208:211]\n"
      "v_exp_f32 v107, v107\n"
      "v_mfma_f32_16x16x32_bf16 v[132:135], v[52:55], v[220:223], v[132:135]\n"
      "v_add_f32 v15, v15, v107\n"
      "v_cvt_pk_bf16_f32 v105, v106, v107\n"
      "v_mfma_f32_16x16x32_bf16 v[132:135], v[56:59], v[224:227], v[132:135]\n"
      "v_exp_f32 v108, v108\n"
      "v_mfma_f32_16x16x32_bf16 v[132:135], v[60:63], v[228:231], v[132:135]\n"
      "ds_read_b64_tr_b16 v[92:93], v5 offset:25344\n"
      "ds_read_b64_tr_b16 v[94:95], v5 offset:29440\n"
      "v_add_f32 v15, v15, v108\n"
      "v_xor_b32 v4, 0x8000, v4\n"
      "v_mfma_f32_16x16x32_bf16 v[176:179], v[32:35], v[232:235], v[212:215]\n"
      "v_exp_f32 v109, v109\n"
      "v_xor_b32 v5, 0x8000, v5\n"
      "v_mfma_f32_16x16x32_bf16 v[176:179], v[36:39], v[236:239], v[176:179]\n"
      "v_add_f32 v15, v15, v109\n"
      "v_cvt_pk_bf16_f32 v106, v108, v109\n"
      "v_mfma_f32_16x16x32_bf16 v[176:179], v[40:43], v[240:243], v[176:179]\n"
      "v_exp_f32 v110, v110\n"
      "v_mfma_f32_16x16x32_bf16 v[176:179], v[44:47], v[244:247], v[176:179]\n"
      "v_add_f32 v15, v15, v110\n"
      "s_waitcnt lgkmcnt(0)\n"
      "v_mfma_f32_16x16x32_bf16 a[0:3], v[136:139], v[64:67], a[0:3]\n"
      "v_exp_f32 v111, v111\n"
      "v_mfma_f32_16x16x32_bf16 a[4:7], v[136:139], v[68:71], a[4:7]\n"
      "v_add_f32 v15, v15, v111\n"
      "v_cvt_pk_bf16_f32 v107, v110, v111\n"
      "s_xor_b32 m0, s56, 0x1a000\n"
      "v_mfma_f32_16x16x32_bf16 a[8:11], v[136:139], v[72:75], a[8:11]\n"
      "buffer_load_dwordx4 v10, s[44:47], s55 offen offset:0 nt lds\n"
      "v_exp_f32 v112, v112\n"
      "v_mfma_f32_16x16x32_bf16 a[12:15], v[136:139], v[76:79], a[12:15]\n"
      "ds_read_b128 v[32:35], v0 offset:8192\n"
      "v_add_f32 v16, v16, v112\n"
      "v_mfma_f32_16x16x32_bf16 a[16:19], v[136:139], v[80:83], a[16:19]\n"
      "v_exp_f32 v113, v113\n"
      "v_mfma_f32_16x16x32_bf16 a[20:23], v[136:139], v[84:87], a[20:23]\n"
      "v_add_f32 v16, v16, v113\n"
      "v_cvt_pk_bf16_f32 v112, v112, v113\n"
      "v_mfma_f32_16x16x32_bf16 a[24:27], v[136:139], v[88:91], a[24:27]\n"
      "v_exp_f32 v114, v114\n"
      "v_mfma_f32_16x16x32_bf16 a[28:31], v[136:139], v[92:95], a[28:31]\n"
      "ds_read_b128 v[36:39], v1 offset:8192\n"
      "v_add_f32 v16, v16, v114\n"
      "v_mfma_f32_16x16x32_bf16 a[32:35], v[144:147], v[64:67], a[32:35]\n"
      "v_exp_f32 v115, v115\n"
      "v_mfma_f32_16x16x32_bf16 a[36:39], v[144:147], v[68:71], a[36:39]\n"
      "v_add_f32 v16, v16, v115\n"
      "v_cvt_pk_bf16_f32 v113, v114, v115\n"
      "v_mfma_f32_16x16x32_bf16 a[40:43], v[144:147], v[72:75], a[40:43]\n"
      "buffer_load_dwordx4 v11, s[44:47], s55 offen offset:1024 nt lds\n"
      "v_exp_f32 v116, v116\n"
      "v_mfma_f32_16x16x32_bf16 a[44:47], v[144:147], v[76:79], a[44:47]\n"
      "ds_read_b128 v[40:43], v2 offset:8192\n"
      "v_add_f32 v16, v16, v116\n"
      "v_mfma_f32_16x16x32_bf16 a[48:51], v[144:147], v[80:83], a[48:51]\n"
      "v_exp_f32 v117, v117\n"
      "v_mfma_f32_16x16x32_bf16 a[52:55], v[144:147], v[84:87], a[52:55]\n"
      "v_add_f32 v16, v16, v117\n"
      "v_cvt_pk_bf16_f32 v114, v116, v117\n"
      "v_mfma_f32_16x16x32_bf16 a[56:59], v[144:147], v[88:91], a[56:59]\n"
      "v_exp_f32 v118, v118\n"
      "v_mfma_f32_16x16x32_bf16 a[60:63], v[144:147], v[92:95], a[60:63]\n"
      "ds_read_b128 v[44:47], v3 offset:8192\n"
      "v_add_f32 v16, v16, v118\n"
      "v_mfma_f32_16x16x32_bf16 a[64:67], v[152:155], v[64:67], a[64:67]\n"
      "v_exp_f32 v119, v119\n"
      "v_mfma_f32_16x16x32_bf16 a[68:71], v[152:155], v[68:71], a[68:71]\n"
      "v_add_f32 v16, v16, v119\n"
      "v_cvt_pk_bf16_f32 v115, v118, v119\n"
      "v_mfma_f32_16x16x32_bf16 a[72:75], v[152:155], v[72:75], a[72:75]\n"
      "buffer_load_dwordx4 v10, s[44:47], s55 offen offset:2048 nt lds\n"
      "v_exp_f32 v120, v120\n"
      "v_mfma_f32_16x16x32_bf16 a[76:79], v[152:155], v[76:79], a[76:79]\n"
      "ds_read_b128 v[48:51], v0 offset:12288\n"
      "v_add_f32 v17, v17, v120\n"
      "v_mfma_f32_16x16x32_bf16 a[80:83], v[152:155], v[80:83], a[80:83]\n"
      "v_exp_f32 v121, v121\n"
      "v_mfma_f32_16x16x32_bf16 a[84:87], v[152:155], v[84:87], a[84:87]\n"
      "v_add_f32 v17, v17, v121\n"
      "v_cvt_pk_bf16_f32 v120, v120, v121\n"
      "v_mfma_f32_16x16x32_bf16 a[88:91], v[152:155], v[88:91], a[88:91]\n"
      "v_exp_f32 v122, v122\n"
      "v_mfma_f32_16x16x32_bf16 a[92:95], v[152:155], v[92:95], a[92:95]\n"
      "ds_read_b128 v[52:55], v1 offset:12288\n"
      "v_add_f32 v17, v17, v122\n"
      "v_mfma_f32_16x16x32_bf16 a[96:99], v[160:163], v[64:67], a[96:99]\n"
      "v_exp_f32 v123, v123\n"
      "v_mfma_f32_16x16x32_bf16 a[100:103], v[160:163], v[68:71], a[100:103]\n"
      "v_add_f32 v17, v17, v123\n"
      "v_cvt_pk_bf16_f32 v121, v122, v123\n"
      "v_mfma_f32_16x16x32_bf16 a[104:107], v[160:163], v[72:75], a[104:107]\n"
      "buffer_load_dwordx4 v11, s[44:47], s55 offen offset:3072 nt lds\n"
      "v_exp_f32 v124, v124\n"
      "s_add_u32 s55, s55, 0x2000\n"
      "v_mfma_f32_16x16x32_bf16 a[108:111], v[160:163], v[76:79], a[108:111]\n"
      "ds_read_b128 v[56:59], v2 offset:12288\n"
      "v_add_f32 v17, v17, v124\n"
      "v_mfma_f32_16x16x32_bf16 a[112:115], v[160:163], v[80:83], a[112:115]\n"
      "v_exp_f32 v125, v125\n"
      "v_mfma_f32_16x16x32_bf16 a[116:119], v[160:163], v[84:87], a[116:119]\n"
      "v_add_f32 v17, v17, v125\n"
      "v_cvt_pk_bf16_f32 v122, v124, v125\n"
      "v_mfma_f32_16x16x32_bf16 a[120:123], v[160:163], v[88:91], a[120:123]\n"
      "v_exp_f32 v126, v126\n"
      "v_mfma_f32_16x16x32_bf16 a[124:127], v[160:163], v[92:95], a[124:127]\n"
      "ds_read_b128 v[60:63], v3 offset:12288\n"
      "v_add_f32 v17, v17, v126\n"
      "v_mfma_f32_16x16x32_bf16 a[128:131], v[168:171], v[64:67], a[128:131]\n"
      "v_exp_f32 v127, v127\n"
      "v_mfma_f32_16x16x32_bf16 a[132:135], v[168:171], v[68:71], a[132:135]\n"
      "v_add_f32 v17, v17, v127\n"
      "v_cvt_pk_bf16_f32 v123, v126, v127\n"
      "v_mfma_f32_16x16x32_bf16 a[136:139], v[168:171], v[72:75], a[136:139]\n"
      "v_exp_f32 v128, v128\n"
      "v_mfma_f32_16x16x32_bf16 a[140:143], v[168:171], v[76:79], a[140:143]\n"
      "v_add_f32 v18, v18, v128\n"
      "v_mfma_f32_16x16x32_bf16 a[144:147], v[168:171], v[80:83], a[144:147]\n"
      "v_exp_f32 v129, v129\n"
      "v_mfma_f32_16x16x32_bf16 a[148:151], v[168:171], v[84:87], a[148:151]\n"
      "v_add_f32 v18, v18, v129\n"
      "v_cvt_pk_bf16_f32 v128, v128, v129\n"
      "v_mfma_f32_16x16x32_bf16 a[152:155], v[168:171], v[88:91], a[152:155]\n"
      "v_exp_f32 v130, v130\n"
      "v_mfma_f32_16x16x32_bf16 a[156:159], v[168:171], v[92:95], a[156:159]\n"
      "v_add_f32 v18, v18, v130\n"
      "s_waitcnt lgkmcnt(0)\n"
      "L_entry_w0:\n"
      "v_mfma_f32_16x16x32_bf16 v[136:139], v[32:35], a[192:195], v[192:195]\n"
      "v_exp_f32 v131, v131\n"
      "v_mfma_f32_16x16x32_bf16 v[136:139], v[36:39], a[196:199], v[136:139]\n"
      "v_add_f32 v18, v18, v131\n"
      "v_cvt_pk_bf16_f32 v129, v130, v131\n"
      "v_mfma_f32_16x16x32_bf16 v[136:139], v[40:43], a[200:203], v[136:139]\n"
      "v_exp_f32 v132, v132\n"
      "v_mfma_f32_16x16x32_bf16 v[136:139], v[44:47], a[204:207], v[136:139]\n"
      "ds_read_b64_tr_b16 v[64:65], v4 offset:0\n"
      "v_add_f32 v18, v18, v132\n"
      "v_mfma_f32_16x16x32_bf16 v[140:143], v[48:51], a[192:195], v[192:195]\n"
      "v_exp_f32 v133, v133\n"
      "v_mfma_f32_16x16x32_bf16 v[140:143], v[52:55], a[196:199], v[140:143]\n"
      "v_add_f32 v18, v18, v133\n"
      "v_cvt_pk_bf16_f32 v130, v132, v133\n"
      "v_mfma_f32_16x16x32_bf16 v[140:143], v[56:59], a[200:203], v[140:143]\n"
      "v_exp_f32 v134, v134\n"
      "v_mfma_f32_16x16x32_bf16 v[140:143], v[60:63], a[204:207], v[140:143]\n"
      "ds_read_b64_tr_b16 v[66:67], v4 offset:4096\n"
      "v_add_f32 v18, v18, v134\n"
      "v_mfma_f32_16x16x32_bf16 v[144:147], v[32:35], a[208:211], v[196:199]\n"
      "v_exp_f32 v135, v135\n"
      "v_mfma_f32_16x16x32_bf16 v[144:147], v[36:39], a[212:215], v[144:147]\n"
      "v_add_f32 v18, v18, v135\n"
      "v_cvt_pk_bf16_f32 v131, v134, v135\n"
      "v_mfma_f32_16x16x32_bf16 v[144:147], v[40:43], a[216:219], v[144:147]\n"
      "v_exp_f32 v176, v176\n"
      "v_mfma_f32_16x16x32_bf16 v[144:147], v[44:47], a[220:223], v[144:147]\n"
      "ds_read_b64_tr_b16 v[68:69], v5 offset:0\n"
      "v_add_f32 v19, v19, v176\n"
      "v_mfma_f32_16x16x32_bf16 v[148:151], v[48:51], a[208:211], v[196:199]\n"
      "v_exp_f32 v177, v177\n"
      "v_mfma_f32_16x16x32_bf16 v[148:151], v[52:55], a[212:215], v[148:151]\n"
      "v_add_f32 v19, v19, v177\n"
      "v_cvt_pk_bf16_f32 v176, v176, v177\n"
      "v_mfma_f32_16x16x32_bf16 v[148:151], v[56:59], a[216:219], v[148:151]\n"
      "v_exp_f32 v178, v178\n"
      "v_mfma_f32_16x16x32_bf16 v[148:151], v[60:63], a[220:223], v[148:151]\n"
      "ds_read_b64_tr_b16 v[70:71], v5 offset:4096\n"
      "v_add_f32 v19, v19, v178\n"
      "v_mfma_f32_16x16x32_bf16 v[152:155], v[32:35], a[224:227], v[200:203]\n"
      "v_exp_f32 v179, v179\n"
      "v_mfma_f32_16x16x32_bf16 v[152:155], v[36:39], a[228:231], v[152:155]\n"
      "v_add_f32 v19, v19, v179\n"
      "v_cvt_pk_bf16_f32 v177, v178, v179\n"
      "v_mfma_f32_16x16x32_bf16 v[152:155], v[40:43], a[232:235], v[152:155]\n"
      "ds_read_b64_tr_b16 v[72:73], v4 offset:256\n"
      "ds_read_b64_tr_b16 v[74:75], v4 offset:4352\n"
      "v_mfma_f32_16x16x32_bf16 v[152:155], v[44:47], a[236:239], v[152:155]\n"
      "ds_read_b64_tr_b16 v[76:77], v5 offset:256\n"
      "ds_read_b64_tr_b16 v[78:79], v5 offset:4352\n"
      "v_mfma_f32_16x16x32_bf16 v[156:159], v[48:51], a[224:227], v[200:203]\n"
      "v_exp_f32 v136, v136\n"
      "v_mfma_f32_16x16x32_bf16 v[156:159], v[52:55], a[228:231], v[156:159]\n"
      "ds_read_b64_tr_b16 v[80:81], v4 offset:512\n"
      "v_add_f32 v14, v14, v136\n"
      "v_mfma_f32_16x16x32_bf16 v[156:159], v[56:59], a[232:235], v[156:159]\n"
      "v_exp_f32 v137, v137\n"
      "v_mfma_f32_16x16x32_bf16 v[156:159], v[60:63], a[236:239], v[156:159]\n"
      "v_add_f32 v14, v14, v137\n"
      "v_cvt_pk_bf16_f32 v136, v136, v137\n"
      "v_mfma_f32_16x16x32_bf16 v[160:163], v[32:35], a[240:243], v[204:207]\n"
      "v_exp_f32 v138, v138\n"
      "v_mfma_f32_16x16x32_bf16 v[160:163], v[36:39], a[244:247], v[160:163]\n"
      "ds_read_b64_tr_b16 v[82:83], v4 offset:4608\n"
      "v_add_f32 v14, v14, v138\n"
      "v_mfma_f32_16x16x32_bf16 v[160:163], v[40:43], a[248:251], v[160:163]\n"
      "v_exp_f32 v139, v139\n"
      "v_mfma_f32_16x16x32_bf16 v[160:163], v[44:47], a[252:255], v[160:163]\n"
      "v_add_f32 v14, v14, v139\n"
      "v_cvt_pk_bf16_f32 v137, v138, v139\n"
      "v_mfma_f32_16x16x32_bf16 v[164:167], v[48:51], a[240:243], v[204:207]\n"
      "v_exp_f32 v140, v140\n"
      "v_mfma_f32_16x16x32_bf16 v[164:167], v[52:55], a[244:247], v[164:167]\n"
      "ds_read_b64_tr_b16 v[84:85], v5 offset:512\n"
      "ds_read_b64_tr_b16 v[86:87], v5 offset:4608\n"
      "v_add_f32 v14, v14, v140\n"
      "v_mfma_f32_16x16x32_bf16 v[164:167], v[56:59], a[248:251], v[164:167]\n"
      "v_exp_f32 v141, v141\n"
      "v_mfma_f32_16x16x32_bf16 v[164:167], v[60:63], a[252:255], v[164:167]\n"
      "v_add_f32 v14, v14, v141\n"
      "v_cvt_pk_bf16_f32 v138, v140, v141\n"
      "v_mfma_f32_16x16x32_bf16 v[168:171], v[32:35], v[216:219], v[208:211]\n"
      "v_exp_f32 v142, v142\n"
      "v_mfma_f32_16x16x32_bf16 v[168:171], v[36:39], v[220:223], v[168:171]\n"
      "ds_read_b64_tr_b16 v[88:89], v4 offset:768\n"
      "ds_read_b64_tr_b16 v[90:91], v4 offset:4864\n"
      "v_add_f32 v14, v14, v142\n"
      "v_mfma_f32_16x16x32_bf16 v[168:171], v[40:43], v[224:227], v[168:171]\n"
      "v_exp_f32 v143, v143\n"
      "v_mfma_f32_16x16x32_bf16 v[168:171], v[44:47], v[228:231], v[168:171]\n"
      "v_add_f32 v14, v14, v143\n"
      "v_cvt_pk_bf16_f32 v139, v142, v143\n"
      "v_mfma_f32_16x16x32_bf16 v[172:175], v[48:51], v[216:219], v[208:211]\n"
      "v_exp_f32 v144, v144\n"
      "v_mfma_f32_16x16x32_bf16 v[172:175], v[52:55], v[220:223], v[172:175]\n"
      "ds_read_b64_tr_b16 v[92:93], v5 offset:768\n"
      "ds_read_b64_tr_b16 v[94:95], v5 offset:4864\n"
      "v_add_f32 v15, v15, v144\n"
      "v_mfma_f32_16x16x32_bf16 v[172:175], v[56:59], v[224:227], v[172:175]\n"
      "v_exp_f32 v145, v145\n"
      "v_mfma_f32_16x16x32_bf16 v[172:175], v[60:63], v[228:231], v[172:175]\n"
      "v_add_f32 v15, v15, v145\n"
      "v_cvt_pk_bf16_f32 v144, v144, v145\n"
      "s_waitcnt lgkmcnt(0)\n"
      "v_mfma_f32_16x16x32_bf16 a[0:3], v[96:99], v[64:67], a[0:3]\n"
      "v_exp_f32 v146, v146\n"
      "v_mfma_f32_16x16x32_bf16 a[4:7], v[96:99], v[68:71], a[4:7]\n"
      "ds_read_b128 v[32:35], v0 offset:16384\n"
      "v_add_f32 v15, v15, v146\n"
      "v_mfma_f32_16x16x32_bf16 a[8:11], v[96:99], v[72:75], a[8:11]\n"
      "v_exp_f32 v147, v147\n"
      "v_mfma_f32_16x16x32_bf16 a[12:15], v[96:99], v[76:79], a[12:15]\n"
      "v_add_f32 v15, v15, v147\n"
      "v_cvt_pk_bf16_f32 v145, v146, v147\n"
      "v_mfma_f32_16x16x32_bf16 a[16:19], v[96:99], v[80:83], a[16:19]\n"
      "v_exp_f32 v148, v148\n"
      "v_mfma_f32_16x16x32_bf16 a[20:23], v[96:99], v[84:87], a[20:23]\n"
      "v_add_f32 v15, v15, v148\n"
      "s_xor_b32 m0, s56, 0x1c000\n"
      "v_mfma_f32_16x16x32_bf16 a[24:27], v[96:99], v[88:91], a[24:27]\n"
      "buffer_load_dwordx4 v10, s[44:47], s55 offen offset:0 nt lds\n"
      "v_exp_f32 v149, v149\n"
      "v_mfma_f32_16x16x32_bf16 a[28:31], v[96:99], v[92:95], a[28:31]\n"
      "v_add_f32 v15, v15, v149\n"
      "v_cvt_pk_bf16_f32 v146, v148, v149\n"
      "v_mfma_f32_16x16x32_bf16 a[32:35], v[104:107], v[64:67], a[32:35]\n"
      "v_exp_f32 v150, v150\n"
      "v_mfma_f32_16x16x32_bf16 a[36:39], v[104:107], v[68:71], a[36:39]\n"
      "ds_read_b128 v[36:39], v1 offset:16384\n"
      "v_add_f32 v15, v15, v150\n"
      "v_mfma_f32_16x16x32_bf16 a[40:43], v[104:107], v[72:75], a[40:43]\n"
      "v_exp_f32 v151, v151\n"
      "v_mfma_f32_16x16x32_bf16 a[44:47], v[104:107], v[76:79], a[44:47]\n"
      "v_add_f32 v15, v15, v151\n"
      "v_cvt_pk_bf16_f32 v147, v150, v151\n"
      "v_mfma_f32_16x16x32_bf16 a[48:51], v[104:107], v[80:83], a[48:51]\n"
      "v_exp_f32 v152, v152\n"
      "v_mfma_f32_16x16x32_bf16 a[52:55], v[104:107], v[84:87], a[52:55]\n"
      "ds_read_b128 v[40:43], v2 offset:16384\n"
      "v_add_f32 v16, v16, v152\n"
      "v_mfma_f32_16x16x32_bf16 a[56:59], v[104:107], v[88:91], a[56:59]\n"
      "buffer_load_dwordx4 v11, s[44:47], s55 offen offset:1024 nt lds\n"
      "v_exp_f32 v153, v153\n"
      "v_mfma_f32_16x16x32_bf16 a[60:63], v[104:107], v[92:95], a[60:63]\n"
      "v_add_f32 v16, v16, v153\n"
      "v_cvt_pk_bf16_f32 v152, v152, v153\n"
      "v_mfma_f32_16x16x32_bf16 a[64:67], v[112:115], v[64:67], a[64:67]\n"
      "v_exp_f32 v154, v154\n"
      "v_mfma_f32_16x16x32_bf16 a[68:71], v[112:115], v[68:71], a[68:71]\n"
      "ds_read_b128 v[44:47], v3 offset:16384\n"
      "v_add_f32 v16, v16, v154\n"
      "v_mfma_f32_16x16x32_bf16 a[72:75], v[112:115], v[72:75], a[72:75]\n"
      "v_exp_f32 v155, v155\n"
      "v_mfma_f32_16x16x32_bf16 a[76:79], v[112:115], v[76:79], a[76:79]\n"
      "v_add_f32 v16, v16, v155\n"
      "v_cvt_pk_bf16_f32 v153, v154, v155\n"
      "v_mfma_f32_16x16x32_bf16 a[80:83], v[112:115], v[80:83], a[80:83]\n"
      "v_exp_f32 v156, v156\n"
      "v_mfma_f32_16x16x32_bf16 a[84:87], v[112:115], v[84:87], a[84:87]\n"
      "ds_read_b128 v[48:51], v0 offset:20480\n"
      "v_add_f32 v16, v16, v156\n"
      "v_mfma_f32_16x16x32_bf16 a[88:91], v[112:115], v[88:91], a[88:91]\n"
      "buffer_load_dwordx4 v10, s[44:47], s55 offen offset:2048 nt lds\n"
      "v_exp_f32 v157, v157\n"
      "v_mfma_f32_16x16x32_bf16 a[92:95], v[112:115], v[92:95], a[92:95]\n"
      "v_add_f32 v16, v16, v157\n"
      "v_cvt_pk_bf16_f32 v154, v156, v157\n"
      "v_mfma_f32_16x16x32_bf16 a[96:99], v[120:123], v[64:67], a[96:99]\n"
      "v_exp_f32 v158, v158\n"
      "v_mfma_f32_16x16x32_bf16 a[100:103], v[120:123], v[68:71], a[100:103]\n"
      "v_add_f32 v16, v16, v158\n"
      "v_mfma_f32_16x16x32_bf16 a[104:107], v[120:123], v[72:75], a[104:107]\n"
      "v_exp_f32 v159, v159\n"
      "v_mfma_f32_16x16x32_bf16 a[108:111], v[120:123], v[76:79], a[108:111]\n"
      "v_add_f32 v16, v16, v159\n"
      "v_cvt_pk_bf16_f32 v155, v158, v159\n"
      "v_mfma_f32_16x16x32_bf16 a[112:115], v[120:123], v[80:83], a[112:115]\n"
      "v_exp_f32 v160, v160\n"
      "v_mfma_f32_16x16x32_bf16 a[116:119], v[120:123], v[84:87], a[116:119]\n"
      "ds_read_b128 v[52:55], v1 offset:20480\n"
      "v_add_f32 v17, v17, v160\n"
      "v_mfma_f32_16x16x32_bf16 a[120:123], v[120:123], v[88:91], a[120:123]\n"
      "buffer_load_dwordx4 v11, s[44:47], s55 offen offset:3072 nt lds\n"
      "v_exp_f32 v161, v161\n"
      "s_add_u32 s55, s55, 0x2000\n"
      "v_mfma_f32_16x16x32_bf16 a[124:127], v[120:123], v[92:95], a[124:127]\n"
      "v_add_f32 v17, v17, v161\n"
      "v_cvt_pk_bf16_f32 v160, v160, v161\n"
      "v_mfma_f32_16x16x32_bf16 a[128:131], v[128:131], v[64:67], a[128:131]\n"
      "v_exp_f32 v162, v162\n"
      "v_mfma_f32_16x16x32_bf16 a[132:135], v[128:131], v[68:71], a[132:135]\n"
      "ds_read_b128 v[56:59], v2 offset:20480\n"
      "v_add_f32 v17, v17, v162\n"
      "v_mfma_f32_16x16x32_bf16 a[136:139], v[128:131], v[72:75], a[136:139]\n"
      "v_exp_f32 v163, v163\n"
      "v_mfma_f32_16x16x32_bf16 a[140:143], v[128:131], v[76:79], a[140:143]\n"
      "v_add_f32 v17, v17, v163\n"
      "v_cvt_pk_bf16_f32 v161, v162, v163\n"
      "v_mfma_f32_16x16x32_bf16 a[144:147], v[128:131], v[80:83], a[144:147]\n"
      "v_exp_f32 v164, v164\n"
      "v_mfma_f32_16x16x32_bf16 a[148:151], v[128:131], v[84:87], a[148:151]\n"
      "ds_read_b128 v[60:63], v3 offset:20480\n"
      "v_add_f32 v17, v17, v164\n"
      "v_mfma_f32_16x16x32_bf16 a[152:155], v[128:131], v[88:91], a[152:155]\n"
      "v_exp_f32 v165, v165\n"
      "v_mfma_f32_16x16x32_bf16 a[156:159], v[128:131], v[92:95], a[156:159]\n"
      "v_add_f32 v17, v17, v165\n"
      "v_cvt_pk_bf16_f32 v162, v164, v165\n"
      "v_mfma_f32_16x16x16_bf16 a[160:163], v[176:177], v[64:65], a[160:163]\n"
      "v_exp_f32 v166, v166\n"
      "v_mfma_f32_16x16x16_bf16 a[164:167], v[176:177], v[68:69], a[164:167]\n"
      "v_add_f32 v17, v17, v166\n"
      "v_mfma_f32_16x16x16_bf16 a[168:171], v[176:177], v[72:73], a[168:171]\n"
      "v_exp_f32 v167, v167\n"
      "v_mfma_f32_16x16x16_bf16 a[172:175], v[176:177], v[76:77], a[172:175]\n"
      "v_add_f32 v17, v17, v167\n"
      "v_cvt_pk_bf16_f32 v163, v166, v167\n"
      "v_mfma_f32_16x16x16_bf16 a[176:179], v[176:177], v[80:81], a[176:179]\n"
      "v_exp_f32 v168, v168\n"
      "v_mfma_f32_16x16x16_bf16 a[180:183], v[176:177], v[84:85], a[180:183]\n"
      "v_add_f32 v18, v18, v168\n"
      "v_mfma_f32_16x16x16_bf16 a[184:187], v[176:177], v[88:89], a[184:187]\n"
      "v_exp_f32 v169, v169\n"
      "v_mfma_f32_16x16x16_bf16 a[188:191], v[176:177], v[92:93], a[188:191]\n"
      "v_add_f32 v18, v18, v169\n"
      "v_cvt_pk_bf16_f32 v168, v168, v169\n"
      "s_waitcnt lgkmcnt(0)\n"
      "s_waitcnt vmcnt(12)\n"
      "s_barrier\n"
      "v_mfma_f32_16x16x32_bf16 v[96:99], v[32:35], a[192:195], v[192:195]\n"
      "v_exp_f32 v170, v170\n"
      "v_mfma_f32_16x16x32_bf16 v[96:99], v[36:39], a[196:199], v[96:99]\n"
      "ds_read_b64_tr_b16 v[64:65], v4 offset:8192\n"
      "v_add_f32 v18, v18, v170\n"
      "v_mfma_f32_16x16x32_bf16 v[96:99], v[40:43], a[200:203], v[96:99]\n"
      "v_exp_f32 v171, v171\n"
      "v_mfma_f32_16x16x32_bf16 v[96:99], v[44:47], a[204:207], v[96:99]\n"
      "v_add_f32 v18, v18, v171\n"
      "v_cvt_pk_bf16_f32 v169, v170, v171\n"
      "v_mfma_f32_16x16x32_bf16 v[100:103], v[48:51], a[192:195], v[192:195]\n"
      "v_exp_f32 v172, v172\n"
      "v_mfma_f32_16x16x32_bf16 v[100:103], v[52:55], a[196:199], v[100:103]\n"
      "ds_read_b64_tr_b16 v[66:67], v4 offset:12288\n"
      "v_add_f32 v18, v18, v172\n"
      "v_mfma_f32_16x16x32_bf16 v[100:103], v[56:59], a[200:203], v[100:103]\n"
      "v_exp_f32 v173, v173\n"
      "v_mfma_f32_16x16x32_bf16 v[100:103], v[60:63], a[204:207], v[100:103]\n"
      "v_add_f32 v18, v18, v173\n"
      "v_cvt_pk_bf16_f32 v170, v172, v173\n"
      "v_mfma_f32_16x16x32_bf16 v[104:107], v[32:35], a[208:211], v[196:199]\n"
      "v_exp_f32 v174, v174\n"
      "v_mfma_f32_16x16x32_bf16 v[104:107], v[36:39], a[212:215], v[104:107]\n"
      "ds_read_b64_tr_b16 v[68:69], v5 offset:8192\n"
      "v_add_f32 v18, v18, v174\n"
      "v_mfma_f32_16x16x32_bf16 v[104:107], v[40:43], a[216:219], v[104:107]\n"
      "v_exp_f32 v175, v175\n"
      "v_mfma_f32_16x16x32_bf16 v[104:107], v[44:47], a[220:223], v[104:107]\n"
      "v_add_f32 v18, v18, v175\n"
      "v_cvt_pk_bf16_f32 v171, v174, v175\n"
      "v_mfma_f32_16x16x32_bf16 v[108:111], v[48:51], a[208:211], v[196:199]\n"
      "ds_read_b64_tr_b16 v[70:71], v5 offset:12288\n"
      "ds_read_b64_tr_b16 v[72:73], v4 offset:8448\n"
      "v_mfma_f32_16x16x32_bf16 v[108:111], v[52:55], a[212:215], v[108:111]\n"
      "v_mfma_f32_16x16x32_bf16 v[108:111], v[56:59], a[216:219], v[108:111]\n"
      "v_exp_f32 v96, v96\n"
      "v_mfma_f32_16x16x32_bf16 v[108:111], v[60:63], a[220:223], v[108:111]\n"
      "ds_read_b64_tr_b16 v[74:75], v4 offset:12544\n"
      "v_add_f32 v14, v14, v96\n"
      "v_mfma_f32_16x16x32_bf16 v[112:115], v[32:35], a[224:227], v[200:203]\n"
      "v_exp_f32 v97, v97\n"
      "v_mfma_f32_16x16x32_bf16 v[112:115], v[36:39], a[228:231], v[112:115]\n"
      "v_add_f32 v14, v14, v97\n"
      "v_cvt_pk_bf16_f32 v96, v96, v97\n"
      "v_mfma_f32_16x16x32_bf16 v[112:115], v[40:43], a[232:235], v[112:115]\n"
      "v_exp_f32 v98, v98\n"
      "v_mfma_f32_16x16x32_bf16 v[112:115], v[44:47], a[236:239], v[112:115]\n"
      "ds_read_b64_tr_b16 v[76:77], v5 offset:8448\n"
      "v_add_f32 v14, v14, v98\n"
      "v_mfma_f32_16x16x32_bf16 v[116:119], v[48:51], a[224:227], v[200:203]\n"
      "v_exp_f32 v99, v99\n"
      "v_mfma_f32_16x16x32_bf16 v[116:119], v[52:55], a[228:231], v[116:119]\n"
      "v_add_f32 v14, v14, v99\n"
      "v_cvt_pk_bf16_f32 v97, v98, v99\n"
      "v_mfma_f32_16x16x32_bf16 v[116:119], v[56:59], a[232:235], v[116:119]\n"
      "v_exp_f32 v100, v100\n"
      "v_mfma_f32_16x16x32_bf16 v[116:119], v[60:63], a[236:239], v[116:119]\n"
      "ds_read_b64_tr_b16 v[78:79], v5 offset:12544\n"
      "v_add_f32 v14, v14, v100\n"
      "v_mfma_f32_16x16x32_bf16 v[120:123], v[32:35], a[240:243], v[204:207]\n"
      "v_exp_f32 v101, v101\n"
      "v_mfma_f32_16x16x32_bf16 v[120:123], v[36:39], a[244:247], v[120:123]\n"
      "v_add_f32 v14, v14, v101\n"
      "v_cvt_pk_bf16_f32 v98, v100, v101\n"
      "v_mfma_f32_16x16x32_bf16 v[120:123], v[40:43], a[248:251], v[120:123]\n"
      "v_exp_f32 v102, v102\n"
      "v_mfma_f32_16x16x32_bf16 v[120:123], v[44:47], a[252:255], v[120:123]\n"
      "ds_read_b64_tr_b16 v[80:81], v4 offset:8704\n"
      "ds_read_b64_tr_b16 v[82:83], v4 offset:12800\n"
      "v_add_f32 v14, v14, v102\n"
      "v_mfma_f32_16x16x32_bf16 v[124:127], v[48:51], a[240:243], v[204:207]\n"
      "v_exp_f32 v103, v103\n"
      "v_mfma_f32_16x16x32_bf16 v[124:127], v[52:55], a[244:247], v[124:127]\n"
      "v_add_f32 v14, v14, v103\n"
      "v_cvt_pk_bf16_f32 v99, v102, v103\n"
      "v_mfma_f32_16x16x32_bf16 v[124:127], v[56:59], a[248:251], v[124:127]\n"
      "v_exp_f32 v104, v104\n"
      "v_mfma_f32_16x16x32_bf16 v[124:127], v[60:63], a[252:255], v[124:127]\n"
      "ds_read_b64_tr_b16 v[84:85], v5 offset:8704\n"
      "ds_read_b64_tr_b16 v[86:87], v5 offset:12800\n"
      "v_add_f32 v15, v15, v104\n"
      "v_mfma_f32_16x16x32_bf16 v[128:131], v[32:35], v[216:219], v[208:211]\n"
      "v_exp_f32 v105, v105\n"
      "v_mfma_f32_16x16x32_bf16 v[128:131], v[36:39], v[220:223], v[128:131]\n"
      "v_add_f32 v15, v15, v105\n"
      "v_cvt_pk_bf16_f32 v104, v104, v105\n"
      "v_mfma_f32_16x16x32_bf16 v[128:131], v[40:43], v[224:227], v[128:131]\n"
      "v_exp_f32 v106, v106\n"
      "v_mfma_f32_16x16x32_bf16 v[128:131], v[44:47], v[228:231], v[128:131]\n"
      "ds_read_b64_tr_b16 v[88:89], v4 offset:8960\n"
      "ds_read_b64_tr_b16 v[90:91], v4 offset:13056\n"
      "v_add_f32 v15, v15, v106\n"
      "v_mfma_f32_16x16x32_bf16 v[132:135], v[48:51], v[216:219], v[208:211]\n"
      "v_exp_f32 v107, v107\n"
      "v_mfma_f32_16x16x32_bf16 v[132:135], v[52:55], v[220:223], v[132:135]\n"
      "v_add_f32 v15, v15, v107\n"
      "v_cvt_pk_bf16_f32 v105, v106, v107\n"
      "v_mfma_f32_16x16x32_bf16 v[132:135], v[56:59], v[224:227], v[132:135]\n"
      "v_exp_f32 v108, v108\n"
      "v_mfma_f32_16x16x32_bf16 v[132:135], v[60:63], v[228:231], v[132:135]\n"
      "ds_read_b64_tr_b16 v[92:93], v5 offset:8960\n"
      "ds_read_b64_tr_b16 v[94:95], v5 offset:13056\n"
      "v_add_f32 v15, v15, v108\n"
      "v_mfma_f32_16x16x32_bf16 v[176:179], v[32:35], v[232:235], v[212:215]\n"
      "v_exp_f32 v109, v109\n"
      "v_mfma_f32_16x16x32_bf16 v[176:179], v[36:39], v[236:239], v[176:179]\n"
      "v_add_f32 v15, v15, v109\n"
      "v_cvt_pk_bf16_f32 v106, v108, v109\n"
      "v_mfma_f32_16x16x32_bf16 v[176:179], v[40:43], v[240:243], v[176:179]\n"
      "v_exp_f32 v110, v110\n"
      "v_mfma_f32_16x16x32_bf16 v[176:179], v[44:47], v[244:247], v[176:179]\n"
      "v_add_f32 v15, v15, v110\n"
      "s_waitcnt lgkmcnt(0)\n"
      "v_mfma_f32_16x16x32_bf16 a[0:3], v[136:139], v[64:67], a[0:3]\n"
      "v_exp_f32 v111, v111\n"
      "v_mfma_f32_16x16x32_bf16 a[4:7], v[136:139], v[68:71], a[4:7]\n"
      "v_add_f32 v15, v15, v111\n"
      "v_cvt_pk_bf16_f32 v107, v110, v111\n"
      "s_xor_b32 m0, s56, 0x1e000\n"
      "v_mfma_f32_16x16x32_bf16 a[8:11], v[136:139], v[72:75], a[8:11]\n"
      "buffer_load_dwordx4 v10, s[44:47], s55 offen offset:0 nt lds\n"
      "v_exp_f32 v112, v112\n"
      "v_mfma_f32_16x16x32_bf16 a[12:15], v[136:139], v[76:79], a[12:15]\n"
      "ds_read_b128 v[32:35], v0 offset:24576\n"
      "v_add_f32 v16, v16, v112\n"
      "v_mfma_f32_16x16x32_bf16 a[16:19], v[136:139], v[80:83], a[16:19]\n"
      "v_exp_f32 v113, v113\n"
      "v_mfma_f32_16x16x32_bf16 a[20:23], v[136:139], v[84:87], a[20:23]\n"
      "v_add_f32 v16, v16, v113\n"
      "v_cvt_pk_bf16_f32 v112, v112, v113\n"
      "v_mfma_f32_16x16x32_bf16 a[24:27], v[136:139], v[88:91], a[24:27]\n"
      "v_exp_f32 v114, v114\n"
      "v_mfma_f32_16x16x32_bf16 a[28:31], v[136:139], v[92:95], a[28:31]\n"
      "ds_read_b128 v[36:39], v1 offset:24576\n"
      "v_add_f32 v16, v16, v114\n"
      "v_mfma_f32_16x16x32_bf16 a[32:35], v[144:147], v[64:67], a[32:35]\n"
      "v_exp_f32 v115, v115\n"
      "v_mfma_f32_16x16x32_bf16 a[36:39], v[144:147], v[68:71], a[36:39]\n"
      "v_add_f32 v16, v16, v115\n"
      "v_cvt_pk_bf16_f32 v113, v114, v115\n"
      "v_mfma_f32_16x16x32_bf16 a[40:43], v[144:147], v[72:75], a[40:43]\n"
      "buffer_load_dwordx4 v11, s[44:47], s55 offen offset:1024 nt lds\n"
      "v_exp_f32 v116, v116\n"
      "v_mfma_f32_16x16x32_bf16 a[44:47], v[144:147], v[76:79], a[44:47]\n"
      "ds_read_b128 v[40:43], v2 offset:24576\n"
      "v_add_f32 v16, v16, v116\n"
      "v_mfma_f32_16x16x32_bf16 a[48:51], v[144:147], v[80:83], a[48:51]\n"
      "v_exp_f32 v117, v117\n"
      "v_mfma_f32_16x16x32_bf16 a[52:55], v[144:147], v[84:87], a[52:55]\n"
      "v_add_f32 v16, v16, v117\n"
      "v_cvt_pk_bf16_f32 v114, v116, v117\n"
      "v_mfma_f32_16x16x32_bf16 a[56:59], v[144:147], v[88:91], a[56:59]\n"
      "v_exp_f32 v118, v118\n"
      "v_mfma_f32_16x16x32_bf16 a[60:63], v[144:147], v[92:95], a[60:63]\n"
      "ds_read_b128 v[44:47], v3 offset:24576\n"
      "v_add_f32 v16, v16, v118\n"
      "v_mfma_f32_16x16x32_bf16 a[64:67], v[152:155], v[64:67], a[64:67]\n"
      "v_exp_f32 v119, v119\n"
      "v_mfma_f32_16x16x32_bf16 a[68:71], v[152:155], v[68:71], a[68:71]\n"
      "v_add_f32 v16, v16, v119\n"
      "v_cvt_pk_bf16_f32 v115, v118, v119\n"
      "v_mfma_f32_16x16x32_bf16 a[72:75], v[152:155], v[72:75], a[72:75]\n"
      "buffer_load_dwordx4 v10, s[44:47], s55 offen offset:2048 nt lds\n"
      "v_exp_f32 v120, v120\n"
      "v_mfma_f32_16x16x32_bf16 a[76:79], v[152:155], v[76:79], a[76:79]\n"
      "ds_read_b128 v[48:51], v0 offset:28672\n"
      "v_add_f32 v17, v17, v120\n"
      "v_mfma_f32_16x16x32_bf16 a[80:83], v[152:155], v[80:83], a[80:83]\n"
      "v_exp_f32 v121, v121\n"
      "v_mfma_f32_16x16x32_bf16 a[84:87], v[152:155], v[84:87], a[84:87]\n"
      "v_add_f32 v17, v17, v121\n"
      "v_cvt_pk_bf16_f32 v120, v120, v121\n"
      "v_mfma_f32_16x16x32_bf16 a[88:91], v[152:155], v[88:91], a[88:91]\n"
      "v_exp_f32 v122, v122\n"
      "v_mfma_f32_16x16x32_bf16 a[92:95], v[152:155], v[92:95], a[92:95]\n"
      "ds_read_b128 v[52:55], v1 offset:28672\n"
      "v_add_f32 v17, v17, v122\n"
      "v_mfma_f32_16x16x32_bf16 a[96:99], v[160:163], v[64:67], a[96:99]\n"
      "v_exp_f32 v123, v123\n"
      "v_mfma_f32_16x16x32_bf16 a[100:103], v[160:163], v[68:71], a[100:103]\n"
      "v_add_f32 v17, v17, v123\n"
      "v_cvt_pk_bf16_f32 v121, v122, v123\n"
      "v_mfma_f32_16x16x32_bf16 a[104:107], v[160:163], v[72:75], a[104:107]\n"
      "buffer_load_dwordx4 v11, s[44:47], s55 offen offset:3072 nt lds\n"
      "v_exp_f32 v124, v124\n"
      "s_add_u32 s55, s55, 0x2000\n"
      "v_mfma_f32_16x16x32_bf16 a[108:111], v[160:163], v[76:79], a[108:111]\n"
      "ds_read_b128 v[56:59], v2 offset:28672\n"
      "v_add_f32 v17, v17, v124\n"
      "v_mfma_f32_16x16x32_bf16 a[112:115], v[160:163], v[80:83], a[112:115]\n"
      "v_exp_f32 v125, v125\n"
      "v_mfma_f32_16x16x32_bf16 a[116:119], v[160:163], v[84:87], a[116:119]\n"
      "v_add_f32 v17, v17, v125\n"
      "v_cvt_pk_bf16_f32 v122, v124, v125\n"
      "v_mfma_f32_16x16x32_bf16 a[120:123], v[160:163], v[88:91], a[120:123]\n"
      "v_exp_f32 v126, v126\n"
      "v_mfma_f32_16x16x32_bf16 a[124:127], v[160:163], v[92:95], a[124:127]\n"
      "ds_read_b128 v[60:63], v3 offset:28672\n"
      "v_add_f32 v17, v17, v126\n"
      "v_mfma_f32_16x16x32_bf16 a[128:131], v[168:171], v[64:67], a[128:131]\n"
      "v_exp_f32 v127, v127\n"
      "v_mfma_f32_16x16x32_bf16 a[132:135], v[168:171], v[68:71], a[132:135]\n"
      "v_add_f32 v17, v17, v127\n"
      "v_cvt_pk_bf16_f32 v123, v126, v127\n"
      "v_mfma_f32_16x16x32_bf16 a[136:139], v[168:171], v[72:75], a[136:139]\n"
      "v_exp_f32 v128, v128\n"
      "v_mfma_f32_16x16x32_bf16 a[140:143], v[168:171], v[76:79], a[140:143]\n"
      "v_add_f32 v18, v18, v128\n"
      "v_mfma_f32_16x16x32_bf16 a[144:147], v[168:171], v[80:83], a[144:147]\n"
      "v_exp_f32 v129, v129\n"
      "v_mfma_f32_16x16x32_bf16 a[148:151], v[168:171], v[84:87], a[148:151]\n"
      "v_add_f32 v18, v18, v129\n"
      "v_cvt_pk_bf16_f32 v128, v128, v129\n"
      "v_mfma_f32_16x16x32_bf16 a[152:155], v[168:171], v[88:91], a[152:155]\n"
      "v_exp_f32 v130, v130\n"
      "v_mfma_f32_16x16x32_bf16 a[156:159], v[168:171], v[92:95], a[156:159]\n"
      "v_add_f32 v18, v18, v130\n"
      "s_waitcnt lgkmcnt(0)\n"
      "v_mfma_f32_16x16x32_bf16 v[136:139], v[32:35], a[192:195], v[192:195]\n"
      "v_exp_f32 v131, v131\n"
      "v_mfma_f32_16x16x32_bf16 v[136:139], v[36:39], a[196:199], v[136:139]\n"
      "v_add_f32 v18, v18, v131\n"
      "v_cvt_pk_bf16_f32 v129, v130, v131\n"
      "v_mfma_f32_16x16x32_bf16 v[136:139], v[40:43], a[200:203], v[136:139]\n"
      "v_exp_f32 v132, v132\n"
      "v_mfma_f32_16x16x32_bf16 v[136:139], v[44:47], a[204:207], v[136:139]\n"
      "ds_read_b64_tr_b16 v[64:65], v4 offset:16384\n"
      "v_add_f32 v18, v18, v132\n"
      "v_mfma_f32_16x16x32_bf16 v[140:143], v[48:51], a[192:195], v[192:195]\n"
      "v_exp_f32 v133, v133\n"
      "v_mfma_f32_16x16x32_bf16 v[140:143], v[52:55], a[196:199], v[140:143]\n"
      "v_add_f32 v18, v18, v133\n"
      "v_cvt_pk_bf16_f32 v130, v132, v133\n"
      "v_mfma_f32_16x16x32_bf16 v[140:143], v[56:59], a[200:203], v[140:143]\n"
      "v_exp_f32 v134, v134\n"
      "v_mfma_f32_16x16x32_bf16 v[140:143], v[60:63], a[204:207], v[140:143]\n"
      "ds_read_b64_tr_b16 v[66:67], v4 offset:20480\n"
      "v_add_f32 v18, v18, v134\n"
      "v_mfma_f32_16x16x32_bf16 v[144:147], v[32:35], a[208:211], v[196:199]\n"
      "v_exp_f32 v135, v135\n"
      "v_mfma_f32_16x16x32_bf16 v[144:147], v[36:39], a[212:215], v[144:147]\n"
      "v_add_f32 v18, v18, v135\n"
      "v_cvt_pk_bf16_f32 v131, v134, v135\n"
      "v_mfma_f32_16x16x32_bf16 v[144:147], v[40:43], a[216:219], v[144:147]\n"
      "v_exp_f32 v176, v176\n"
      "v_mfma_f32_16x16x32_bf16 v[144:147], v[44:47], a[220:223], v[144:147]\n"
      "ds_read_b64_tr_b16 v[68:69], v5 offset:16384\n"
      "v_add_f32 v19, v19, v176\n"
      "v_mfma_f32_16x16x32_bf16 v[148:151], v[48:51], a[208:211], v[196:199]\n"
      "v_exp_f32 v177, v177\n"
      "v_mfma_f32_16x16x32_bf16 v[148:151], v[52:55], a[212:215], v[148:151]\n"
      "v_add_f32 v19, v19, v177\n"
      "v_cvt_pk_bf16_f32 v176, v176, v177\n"
      "v_mfma_f32_16x16x32_bf16 v[148:151], v[56:59], a[216:219], v[148:151]\n"
      "v_exp_f32 v178, v178\n"
      "v_mfma_f32_16x16x32_bf16 v[148:151], v[60:63], a[220:223], v[148:151]\n"
      "ds_read_b64_tr_b16 v[70:71], v5 offset:20480\n"
      "v_add_f32 v19, v19, v178\n"
      "v_mfma_f32_16x16x32_bf16 v[152:155], v[32:35], a[224:227], v[200:203]\n"
      "v_exp_f32 v179, v179\n"
      "v_mfma_f32_16x16x32_bf16 v[152:155], v[36:39], a[228:231], v[152:155]\n"
      "v_add_f32 v19, v19, v179\n"
      "v_cvt_pk_bf16_f32 v177, v178, v179\n"
      "v_mfma_f32_16x16x32_bf16 v[152:155], v[40:43], a[232:235], v[152:155]\n"
      "ds_read_b64_tr_b16 v[72:73], v4 offset:16640\n"
      "ds_read_b64_tr_b16 v[74:75], v4 offset:20736\n"
      "v_mfma_f32_16x16x32_bf16 v[152:155], v[44:47], a[236:239], v[152:155]\n"
      "ds_read_b64_tr_b16 v[76:77], v5 offset:16640\n"
      "ds_read_b64_tr_b16 v[78:79], v5 offset:20736\n"
      "v_mfma_f32_16x16x32_bf16 v[156:159], v[48:51], a[224:227], v[200:203]\n"
      "v_exp_f32 v136, v136\n"
      "v_mfma_f32_16x16x32_bf16 v[156:159], v[52:55], a[228:231], v[156:159]\n"
      "ds_read_b64_tr_b16 v[80:81], v4 offset:16896\n"
      "v_add_f32 v14, v14, v136\n"
      "v_mfma_f32_16x16x32_bf16 v[156:159], v[56:59], a[232:235], v[156:159]\n"
      "v_exp_f32 v137, v137\n"
      "v_mfma_f32_16x16x32_bf16 v[156:159], v[60:63], a[236:239], v[156:159]\n"
      "v_add_f32 v14, v14, v137\n"
      "v_cvt_pk_bf16_f32 v136, v136, v137\n"
      "v_mfma_f32_16x16x32_bf16 v[160:163], v[32:35], a[240:243], v[204:207]\n"
      "v_exp_f32 v138, v138\n"
      "v_mfma_f32_16x16x32_bf16 v[160:163], v[36:39], a[244:247], v[160:163]\n"
      "ds_read_b64_tr_b16 v[82:83], v4 offset:20992\n"
      "v_add_f32 v14, v14, v138\n"
      "v_mfma_f32_16x16x32_bf16 v[160:163], v[40:43], a[248:251], v[160:163]\n"
      "v_exp_f32 v139, v139\n"
      "v_mfma_f32_16x16x32_bf16 v[160:163], v[44:47], a[252:255], v[160:163]\n"
      "v_add_f32 v14, v14, v139\n"
      "v_cvt_pk_bf16_f32 v137, v138, v139\n"
      "v_mfma_f32_16x16x32_bf16 v[164:167], v[48:51], a[240:243], v[204:207]\n"
      "v_exp_f32 v140, v140\n"
      "v_mfma_f32_16x16x32_bf16 v[164:167], v[52:55], a[244:247], v[164:167]\n"
      "ds_read_b64_tr_b16 v[84:85], v5 offset:16896\n"
      "ds_read_b64_tr_b16 v[86:87], v5 offset:20992\n"
      "v_add_f32 v14, v14, v140\n"
      "v_mfma_f32_16x16x32_bf16 v[164:167], v[56:59], a[248:251], v[164:167]\n"
      "v_exp_f32 v141, v141\n"
      "v_mfma_f32_16x16x32_bf16 v[164:167], v[60:63], a[252:255], v[164:167]\n"
      "v_add_f32 v14, v14, v141\n"
      "v_cvt_pk_bf16_f32 v138, v140, v141\n"
      "v_mfma_f32_16x16x32_bf16 v[168:171], v[32:35], v[216:219], v[208:211]\n"
      "v_exp_f32 v142, v142\n"
      "v_mfma_f32_16x16x32_bf16 v[168:171], v[36:39], v[220:223], v[168:171]\n"
      "ds_read_b64_tr_b16 v[88:89], v4 offset:17152\n"
      "ds_read_b64_tr_b16 v[90:91], v4 offset:21248\n"
      "v_add_f32 v14, v14, v142\n"
      "v_mfma_f32_16x16x32_bf16 v[168:171], v[40:43], v[224:227], v[168:171]\n"
      "v_exp_f32 v143, v143\n"
      "v_xor_b32 v3, 0x8000, v3\n"
      "v_mfma_f32_16x16x32_bf16 v[168:171], v[44:47], v[228:231], v[168:171]\n"
      "v_add_f32 v14, v14, v143\n"
      "v_cvt_pk_bf16_f32 v139, v142, v143\n"
      "v_mfma_f32_16x16x32_bf16 v[172:175], v[48:51], v[216:219], v[208:211]\n"
      "v_exp_f32 v144, v144\n"
      "v_xor_b32 v2, 0x8000, v2\n"
      "v_mfma_f32_16x16x32_bf16 v[172:175], v[52:55], v[220:223], v[172:175]\n"
      "ds_read_b64_tr_b16 v[92:93], v5 offset:17152\n"
      "ds_read_b64_tr_b16 v[94:95], v5 offset:21248\n"
      "v_add_f32 v15, v15, v144\n"
      "v_mfma_f32_16x16x32_bf16 v[172:175], v[56:59], v[224:227], v[172:175]\n"
      "v_exp_f32 v145, v145\n"
      "v_xor_b32 v1, 0x8000, v1\n"
      "v_mfma_f32_16x16x32_bf16 v[172:175], v[60:63], v[228:231], v[172:175]\n"
      "v_add_f32 v15, v15, v145\n"
      "v_cvt_pk_bf16_f32 v144, v144, v145\n"
      "s_waitcnt lgkmcnt(0)\n"
      "v_mfma_f32_16x16x32_bf16 a[0:3], v[96:99], v[64:67], a[0:3]\n"
      "v_exp_f32 v146, v146\n"
      "v_xor_b32 v0, 0x8000, v0\n"
      "v_mfma_f32_16x16x32_bf16 a[4:7], v[96:99], v[68:71], a[4:7]\n"
      "ds_read_b128 v[32:35], v0 offset:0\n"
      "v_add_f32 v15, v15, v146\n"
      "v_mfma_f32_16x16x32_bf16 a[8:11], v[96:99], v[72:75], a[8:11]\n"
      "v_exp_f32 v147, v147\n"
      "v_mfma_f32_16x16x32_bf16 a[12:15], v[96:99], v[76:79], a[12:15]\n"
      "v_add_f32 v15, v15, v147\n"
      "v_cvt_pk_bf16_f32 v145, v146, v147\n"
      "v_mfma_f32_16x16x32_bf16 a[16:19], v[96:99], v[80:83], a[16:19]\n"
      "v_exp_f32 v148, v148\n"
      "v_mfma_f32_16x16x32_bf16 a[20:23], v[96:99], v[84:87], a[20:23]\n"
      "v_add_f32 v15, v15, v148\n"
      "s_xor_b32 m0, s56, 0x10000\n"
      "v_mfma_f32_16x16x32_bf16 a[24:27], v[96:99], v[88:91], a[24:27]\n"
      "buffer_load_dwordx4 v10, s[44:47], s55 offen offset:0 nt lds\n"
      "v_exp_f32 v149, v149\n"
      "v_mfma_f32_16x16x32_bf16 a[28:31], v[96:99], v[92:95], a[28:31]\n"
      "v_add_f32 v15, v15, v149\n"
      "v_cvt_pk_bf16_f32 v146, v148, v149\n"
      "v_mfma_f32_16x16x32_bf16 a[32:35], v[104:107], v[64:67], a[32:35]\n"
      "v_exp_f32 v150, v150\n"
      "v_mfma_f32_16x16x32_bf16 a[36:39], v[104:107], v[68:71], a[36:39]\n"
      "ds_read_b128 v[36:39], v1 offset:0\n"
      "v_add_f32 v15, v15, v150\n"
      "v_mfma_f32_16x16x32_bf16 a[40:43], v[104:107], v[72:75], a[40:43]\n"
      "v_exp_f32 v151, v151\n"
      "v_mfma_f32_16x16x32_bf16 a[44:47], v[104:107], v[76:79], a[44:47]\n"
      "v_add_f32 v15, v15, v151\n"
      "v_cvt_pk_bf16_f32 v147, v150, v151\n"
      "v_mfma_f32_16x16x32_bf16 a[48:51], v[104:107], v[80:83], a[48:51]\n"
      "v_exp_f32 v152, v152\n"
      "v_mfma_f32_16x16x32_bf16 a[52:55], v[104:107], v[84:87], a[52:55]\n"
      "ds_read_b128 v[40:43], v2 offset:0\n"
      "v_add_f32 v16, v16, v152\n"
      "v_mfma_f32_16x16x32_bf16 a[56:59], v[104:107], v[88:91], a[56:59]\n"
      "buffer_load_dwordx4 v11, s[44:47], s55 offen offset:1024 nt lds\n"
      "v_exp_f32 v153, v153\n"
      "v_mfma_f32_16x16x32_bf16 a[60:63], v[104:107], v[92:95], a[60:63]\n"
      "v_add_f32 v16, v16, v153\n"
      "v_cvt_pk_bf16_f32 v152, v152, v153\n"
      "v_mfma_f32_16x16x32_bf16 a[64:67], v[112:115], v[64:67], a[64:67]\n"
      "v_exp_f32 v154, v154\n"
      "v_mfma_f32_16x16x32_bf16 a[68:71], v[112:115], v[68:71], a[68:71]\n"
      "ds_read_b128 v[44:47], v3 offset:0\n"
      "v_add_f32 v16, v16, v154\n"
      "v_mfma_f32_16x16x32_bf16 a[72:75], v[112:115], v[72:75], a[72:75]\n"
      "v_exp_f32 v155, v155\n"
      "v_mfma_f32_16x16x32_bf16 a[76:79], v[112:115], v[76:79], a[76:79]\n"
      "v_add_f32 v16, v16, v155\n"
      "v_cvt_pk_bf16_f32 v153, v154, v155\n"
      "v_mfma_f32_16x16x32_bf16 a[80:83], v[112:115], v[80:83], a[80:83]\n"
      "v_exp_f32 v156, v156\n"
      "v_mfma_f32_16x16x32_bf16 a[84:87], v[112:115], v[84:87], a[84:87]\n"
      "ds_read_b128 v[48:51], v0 offset:4096\n"
      "v_add_f32 v16, v16, v156\n"
      "v_mfma_f32_16x16x32_bf16 a[88:91], v[112:115], v[88:91], a[88:91]\n"
      "buffer_load_dwordx4 v10, s[44:47], s55 offen offset:2048 nt lds\n"
      "v_exp_f32 v157, v157\n"
      "v_mfma_f32_16x16x32_bf16 a[92:95], v[112:115], v[92:95], a[92:95]\n"
      "v_add_f32 v16, v16, v157\n"
      "v_cvt_pk_bf16_f32 v154, v156, v157\n"
      "v_mfma_f32_16x16x32_bf16 a[96:99], v[120:123], v[64:67], a[96:99]\n"
      "v_exp_f32 v158, v158\n"
      "v_mfma_f32_16x16x32_bf16 a[100:103], v[120:123], v[68:71], a[100:103]\n"
      "v_add_f32 v16, v16, v158\n"
      "v_mfma_f32_16x16x32_bf16 a[104:107], v[120:123], v[72:75], a[104:107]\n"
      "v_exp_f32 v159, v159\n"
      "v_mfma_f32_16x16x32_bf16 a[108:111], v[120:123], v[76:79], a[108:111]\n"
      "v_add_f32 v16, v16, v159\n"
      "v_cvt_pk_bf16_f32 v155, v158, v159\n"
      "v_mfma_f32_16x16x32_bf16 a[112:115], v[120:123], v[80:83], a[112:115]\n"
      "v_exp_f32 v160, v160\n"
      "v_mfma_f32_16x16x32_bf16 a[116:119], v[120:123], v[84:87], a[116:119]\n"
      "ds_read_b128 v[52:55], v1 offset:4096\n"
      "v_add_f32 v17, v17, v160\n"
      "v_mfma_f32_16x16x32_bf16 a[120:123], v[120:123], v[88:91], a[120:123]\n"
      "buffer_load_dwordx4 v11, s[44:47], s55 offen offset:3072 nt lds\n"
      "v_exp_f32 v161, v161\n"
      "s_add_u32 s55, s55, 0x2000\n"
      "s_xor_b32 s56, s56, 0x8000\n"
      "v_mfma_f32_16x16x32_bf16 a[124:127], v[120:123], v[92:95], a[124:127]\n"
      "v_add_f32 v17, v17, v161\n"
      "v_cvt_pk_bf16_f32 v160, v160, v161\n"
      "v_mfma_f32_16x16x32_bf16 a[128:131], v[128:131], v[64:67], a[128:131]\n"
      "v_exp_f32 v162, v162\n"
      "v_mfma_f32_16x16x32_bf16 a[132:135], v[128:131], v[68:71], a[132:135]\n"
      "ds_read_b128 v[56:59], v2 offset:4096\n"
      "v_add_f32 v17, v17, v162\n"
      "v_mfma_f32_16x16x32_bf16 a[136:139], v[128:131], v[72:75], a[136:139]\n"
      "v_exp_f32 v163, v163\n"
      "v_mfma_f32_16x16x32_bf16 a[140:143], v[128:131], v[76:79], a[140:143]\n"
      "v_add_f32 v17, v17, v163\n"
      "v_cvt_pk_bf16_f32 v161, v162, v163\n"
      "v_mfma_f32_16x16x32_bf16 a[144:147], v[128:131], v[80:83], a[144:147]\n"
      "v_exp_f32 v164, v164\n"
      "v_mfma_f32_16x16x32_bf16 a[148:151], v[128:131], v[84:87], a[148:151]\n"
      "ds_read_b128 v[60:63], v3 offset:4096\n"
      "v_add_f32 v17, v17, v164\n"
      "v_mfma_f32_16x16x32_bf16 a[152:155], v[128:131], v[88:91], a[152:155]\n"
      "v_exp_f32 v165, v165\n"
      "v_mfma_f32_16x16x32_bf16 a[156:159], v[128:131], v[92:95], a[156:159]\n"
      "v_add_f32 v17, v17, v165\n"
      "v_cvt_pk_bf16_f32 v162, v164, v165\n"
      "v_mfma_f32_16x16x16_bf16 a[160:163], v[176:177], v[64:65], a[160:163]\n"
      "v_exp_f32 v166, v166\n"
      "v_mfma_f32_16x16x16_bf16 a[164:167], v[176:177], v[68:69], a[164:167]\n"
      "v_add_f32 v17, v17, v166\n"
      "v_mfma_f32_16x16x16_bf16 a[168:171], v[176:177], v[72:73], a[168:171]\n"
      "v_exp_f32 v167, v167\n"
      "v_mfma_f32_16x16x16_bf16 a[172:175], v[176:177], v[76:77], a[172:175]\n"
      "v_add_f32 v17, v17, v167\n"
      "v_cvt_pk_bf16_f32 v163, v166, v167\n"
      "v_mfma_f32_16x16x16_bf16 a[176:179], v[176:177], v[80:81], a[176:179]\n"
      "v_exp_f32 v168, v168\n"
      "v_mfma_f32_16x16x16_bf16 a[180:183], v[176:177], v[84:85], a[180:183]\n"
      "v_add_f32 v18, v18, v168\n"
      "v_mfma_f32_16x16x16_bf16 a[184:187], v[176:177], v[88:89], a[184:187]\n"
      "v_exp_f32 v169, v169\n"
      "v_mfma_f32_16x16x16_bf16 a[188:191], v[176:177], v[92:93], a[188:191]\n"
      "v_add_f32 v18, v18, v169\n"
      "v_cvt_pk_bf16_f32 v168, v168, v169\n"
      "s_waitcnt lgkmcnt(0)\n"
      "s_waitcnt vmcnt(12)\n"
      "s_barrier\n"
      "s_sub_u32 s57, s57, 1\n"
      "s_cmp_lg_u32 s57, 0\n"
      "s_cbranch_scc1 L_loop_w0\n"
      "v_exp_f32 v170, v170\n"
      "ds_read_b64_tr_b16 v[64:65], v4 offset:24576\n"
      "v_add_f32 v18, v18, v170\n"
      "v_exp_f32 v171, v171\n"
      "v_exp_f32 v172, v172\n"
      "v_add_f32 v18, v18, v171\n"
      "v_cvt_pk_bf16_f32 v169, v170, v171\n"
      "ds_read_b64_tr_b16 v[66:67], v4 offset:28672\n"
      "v_add_f32 v18, v18, v172\n"
      "v_exp_f32 v173, v173\n"
      "v_exp_f32 v174, v174\n"
      "v_add_f32 v18, v18, v173\n"
      "v_cvt_pk_bf16_f32 v170, v172, v173\n"
      "ds_read_b64_tr_b16 v[68:69], v5 offset:24576\n"
      "v_add_f32 v18, v18, v174\n"
      "v_exp_f32 v175, v175\n"
      "s_nop 0\n"
      "v_add_f32 v18, v18, v175\n"
      "v_cvt_pk_bf16_f32 v171, v174, v175\n"
      "ds_read_b64_tr_b16 v[70:71], v5 offset:28672\n"
      "ds_read_b64_tr_b16 v[72:73], v4 offset:24832\n"
      "ds_read_b64_tr_b16 v[74:75], v4 offset:28928\n"
      "ds_read_b64_tr_b16 v[76:77], v5 offset:24832\n"
      "ds_read_b64_tr_b16 v[78:79], v5 offset:28928\n"
      "ds_read_b64_tr_b16 v[80:81], v4 offset:25088\n"
      "ds_read_b64_tr_b16 v[82:83], v4 offset:29184\n"
      "ds_read_b64_tr_b16 v[84:85], v5 offset:25088\n"
      "ds_read_b64_tr_b16 v[86:87], v5 offset:29184\n"
      "ds_read_b64_tr_b16 v[88:89], v4 offset:25344\n"
      "ds_read_b64_tr_b16 v[90:91], v4 offset:29440\n"
      "ds_read_b64_tr_b16 v[92:93], v5 offset:25344\n"
      "ds_read_b64_tr_b16 v[94:95], v5 offset:29440\n"
      "v_and_b32 v22, 15, v13\n"
      "v_lshrrev_b32 v23, 4, v13\n"
      "v_lshrrev_b32 v29, 2, v22\n"
      "v_xor_b32 v29, v29, v23\n"
      "v_lshlrev_b32 v29, 3, v29\n"
      "v_lshl_or_b32 v29, v22, 5, v29\n"
      "v_add_u32 v24, s64, v29\n"
      "v_bfe_u32 v29, v13, 2, 2\n"
      "v_lshl_or_b32 v29, v23, 3, v29\n"
      "v_lshlrev_b32 v29, 5, v29\n"
      "v_add_u32 v29, s64, v29\n"
      "v_lshlrev_b32 v30, 1, v23\n"
      "v_xor_b32 v30, v30, v13\n"
      "v_and_b32 v30, 3, v30\n"
      "v_lshl_add_u32 v25, v30, 3, v29\n"
      "v_xor_b32 v30, 1, v30\n"
      "v_lshl_add_u32 v26, v30, 3, v29\n"
      "v_add_u32 v26, 128, v26\n"
      "v_lshlrev_b32 v29, 4, v23\n"
      "v_lshl_or_b32 v253, v22, 8, v29\n"
      "v_add_u32 v27, 0x20c00, v29\n"
      "v_add_u32 v250, 0x20000, v29\n"
      "v_lshlrev_b32 v29, 2, v22\n"
      "v_add_u32 v251, 0x20c00, v29\n"
      "v_add_u32 v248, 0x14000, v12\n"
      "v_add_u32 v249, 0x14000, v12\n"
      "v_lshlrev_b32 v21, 2, v13\n"
      "v_add_u32 v21, 0x20000, v21\n"
      "v_add_u32 v0, s64, v0\n"
      "v_add_u32 v6, s64, v6\n"
      "v_add_u32 v1, s64, v1\n"
      "v_add_u32 v7, s64, v7\n"
      "v_add_u32 v2, s64, v2\n"
      "v_add_u32 v8, s64, v8\n"
      "v_add_u32 v3, s64, v3\n"
      "v_add_u32 v9, s64, v9\n"
      "s_waitcnt vmcnt(0)\n"
      "s_barrier\n"
      "s_waitcnt lgkmcnt(0)\n"
      "v_mfma_f32_16x16x32_bf16 a[0:3], v[136:139], v[64:67], a[0:3]\n"
      "v_mov_b32 v50, v14\n"
      "v_mov_b32 v51, v15\n"
      "v_mfma_f32_16x16x32_bf16 a[4:7], v[136:139], v[68:71], a[4:7]\n"
      "v_mov_b32 v52, v16\n"
      "v_mov_b32 v53, v17\n"
      "v_mfma_f32_16x16x32_bf16 a[8:11], v[136:139], v[72:75], a[8:11]\n"
      "v_mov_b32 v54, v18\n"
      "v_mov_b32 v55, v19\n"
      "v_mfma_f32_16x16x32_bf16 a[12:15], v[136:139], v[76:79], a[12:15]\n"
      "s_nop 1\n"
      "v_mfma_f32_16x16x32_bf16 a[16:19], v[136:139], v[80:83], a[16:19]\n"
      "v_permlane16_swap_b32 v14, v50\n"
      "v_permlane16_swap_b32 v15, v51\n"
      "v_mfma_f32_16x16x32_bf16 a[20:23], v[136:139], v[84:87], a[20:23]\n"
      "v_permlane16_swap_b32 v16, v52\n"
      "v_permlane16_swap_b32 v17, v53\n"
      "v_mfma_f32_16x16x32_bf16 a[24:27], v[136:139], v[88:91], a[24:27]\n"
      "v_permlane16_swap_b32 v18, v54\n"
      "v_permlane16_swap_b32 v19, v55\n"
      "v_mfma_f32_16x16x32_bf16 a[28:31], v[136:139], v[92:95], a[28:31]\n"
      "s_nop 1\n"
      "v_mfma_f32_16x16x32_bf16 a[32:35], v[144:147], v[64:67], a[32:35]\n"
      "v_add_f32 v14, v14, v50\n"
      "v_add_f32 v15, v15, v51\n"
      "v_mfma_f32_16x16x32_bf16 a[36:39], v[144:147], v[68:71], a[36:39]\n"
      "v_add_f32 v16, v16, v52\n"
      "v_add_f32 v17, v17, v53\n"
      "v_mfma_f32_16x16x32_bf16 a[40:43], v[144:147], v[72:75], a[40:43]\n"
      "v_add_f32 v18, v18, v54\n"
      "v_add_f32 v19, v19, v55\n"
      "v_mfma_f32_16x16x32_bf16 a[44:47], v[144:147], v[76:79], a[44:47]\n"
      "v_mov_b32 v50, v14\n"
      "v_mov_b32 v51, v15\n"
      "v_mfma_f32_16x16x32_bf16 a[48:51], v[144:147], v[80:83], a[48:51]\n"
      "v_mov_b32 v52, v16\n"
      "v_mov_b32 v53, v17\n"
      "v_mfma_f32_16x16x32_bf16 a[52:55], v[144:147], v[84:87], a[52:55]\n"
      "v_mov_b32 v54, v18\n"
      "v_mov_b32 v55, v19\n"
      "v_mfma_f32_16x16x32_bf16 a[56:59], v[144:147], v[88:91], a[56:59]\n"
      "s_nop 1\n"
      "v_mfma_f32_16x16x32_bf16 a[60:63], v[144:147], v[92:95], a[60:63]\n"
      "v_permlane32_swap_b32 v14, v50\n"
      "v_permlane32_swap_b32 v15, v51\n"
      "v_mfma_f32_16x16x32_bf16 a[64:67], v[152:155], v[64:67], a[64:67]\n"
      "v_permlane32_swap_b32 v16, v52\n"
      "v_permlane32_swap_b32 v17, v53\n"
      "v_mfma_f32_16x16x32_bf16 a[68:71], v[152:155], v[68:71], a[68:71]\n"
      "v_permlane32_swap_b32 v18, v54\n"
      "v_permlane32_swap_b32 v19, v55\n"
      "v_mfma_f32_16x16x32_bf16 a[72:75], v[152:155], v[72:75], a[72:75]\n"
      "s_nop 1\n"
      "v_mfma_f32_16x16x32_bf16 a[76:79], v[152:155], v[76:79], a[76:79]\n"
      "v_add_f32 v14, v14, v50\n"
      "v_add_f32 v15, v15, v51\n"
      "v_mfma_f32_16x16x32_bf16 a[80:83], v[152:155], v[80:83], a[80:83]\n"
      "v_add_f32 v16, v16, v52\n"
      "v_add_f32 v17, v17, v53\n"
      "v_mfma_f32_16x16x32_bf16 a[84:87], v[152:155], v[84:87], a[84:87]\n"
      "v_add_f32 v18, v18, v54\n"
      "v_add_f32 v19, v19, v55\n"
      "v_mfma_f32_16x16x32_bf16 a[88:91], v[152:155], v[88:91], a[88:91]\n"
      "s_mov_b64 s[98:99], 0\n"
      "v_cmp_nge_f32 vcc, s96, v14\n"
      "v_mfma_f32_16x16x32_bf16 a[92:95], v[152:155], v[92:95], a[92:95]\n"
      "v_rcp_f32 v40, v14\n"
      "s_nop 1\n"
      "v_mfma_f32_16x16x32_bf16 a[96:99], v[160:163], v[64:67], a[96:99]\n"
      "s_or_b64 s[98:99], s[98:99], vcc\n"
      "v_cmp_nge_f32 vcc, s96, v15\n"
      "v_mfma_f32_16x16x32_bf16 a[100:103], v[160:163], v[68:71], a[100:103]\n"
      "v_rcp_f32 v41, v15\n"
      "s_nop 1\n"
      "v_mfma_f32_16x16x32_bf16 a[104:107], v[160:163], v[72:75], a[104:107]\n"
      "s_or_b64 s[98:99], s[98:99], vcc\n"
      "v_cmp_nge_f32 vcc, s96, v16\n"
      "v_mfma_f32_16x16x32_bf16 a[108:111], v[160:163], v[76:79], a[108:111]\n"
      "v_rcp_f32 v42, v16\n"
      "s_nop 1\n"
      "v_mfma_f32_16x16x32_bf16 a[112:115], v[160:163], v[80:83], a[112:115]\n"
      "s_or_b64 s[98:99], s[98:99], vcc\n"
      "v_cmp_nge_f32 vcc, s96, v17\n"
      "v_mfma_f32_16x16x32_bf16 a[116:119], v[160:163], v[84:87], a[116:119]\n"
      "v_rcp_f32 v43, v17\n"
      "s_nop 1\n"
      "v_mfma_f32_16x16x32_bf16 a[120:123], v[160:163], v[88:91], a[120:123]\n"
      "s_or_b64 s[98:99], s[98:99], vcc\n"
      "v_cmp_nge_f32 vcc, s96, v18\n"
      "v_mfma_f32_16x16x32_bf16 a[124:127], v[160:163], v[92:95], a[124:127]\n"
      "v_rcp_f32 v44, v18\n"
      "s_nop 1\n"
      "v_mfma_f32_16x16x32_bf16 a[128:131], v[168:171], v[64:67], a[128:131]\n"
      "s_or_b64 s[98:99], s[98:99], vcc\n"
      "v_cmp_nge_f32 vcc, s96, v19\n"
      "v_mfma_f32_16x16x32_bf16 a[132:135], v[168:171], v[68:71], a[132:135]\n"
      "s_nop 1\n"
      "v_mfma_f32_16x16x32_bf16 a[136:139], v[168:171], v[72:75], a[136:139]\n"
      "s_or_b64 s[98:99], s[98:99], vcc\n"
      "ds_write_b32 v251, v40 offset:0\n"
      "v_mfma_f32_16x16x32_bf16 a[140:143], v[168:171], v[76:79], a[140:143]\n"
      "ds_write_b32 v251, v41 offset:64\n"
      "ds_write_b32 v251, v42 offset:128\n"
      "v_mfma_f32_16x16x32_bf16 a[144:147], v[168:171], v[80:83], a[144:147]\n"
      "ds_write_b32 v251, v43 offset:192\n"
      "ds_write_b32 v251, v44 offset:256\n"
      "v_mfma_f32_16x16x32_bf16 a[148:151], v[168:171], v[84:87], a[148:151]\n"
      "ds_read_b128 v[196:199], v27 offset:0\n"
      "ds_read_b128 v[200:203], v27 offset:64\n"
      "v_mfma_f32_16x16x32_bf16 a[152:155], v[168:171], v[88:91], a[152:155]\n"
      "ds_read_b128 v[204:207], v27 offset:128\n"
      "ds_read_b128 v[208:211], v27 offset:192\n"
      "v_mfma_f32_16x16x32_bf16 a[156:159], v[168:171], v[92:95], a[156:159]\n"
      "ds_read_b128 v[212:215], v27 offset:256\n"
      "s_cmp_lg_u64 s[98:99], 0\n"
      "s_cselect_b32 s59, 1, 0\n"
      "v_mov_b32 v28, s59\n"
      "v_mov_b32 v20, 0x20800\n"
      "ds_write_b128 v248, a[160:163] offset:0\n"
      "ds_write_b128 v248, a[164:167] offset:1024\n"
      "ds_write_b128 v248, a[168:171] offset:2048\n"
      "ds_write_b128 v248, a[172:175] offset:3072\n"
      "ds_write_b128 v248, a[176:179] offset:4096\n"
      "ds_write_b128 v248, a[180:183] offset:5120\n"
      "ds_write_b128 v248, a[184:187] offset:6144\n"
      "ds_write_b128 v248, a[188:191] offset:7168\n"
      "ds_write_b32 v21, v19 offset:0\n"
      "ds_write_b32 v20, v28 offset:0\n"
      "s_waitcnt lgkmcnt(10)\n"
      "v_accvgpr_read_b32 v96, a0\n"
      "v_accvgpr_read_b32 v97, a1\n"
      "v_accvgpr_read_b32 v98, a2\n"
      "v_accvgpr_read_b32 v99, a3\n"
      "v_mul_f32 v96, v196, v96\n"
      "v_mul_f32 v97, v197, v97\n"
      "v_mul_f32 v98, v198, v98\n"
      "v_mul_f32 v99, v199, v99\n"
      "v_cvt_pk_bf16_f32 v96, v96, v97\n"
      "v_cvt_pk_bf16_f32 v97, v98, v99\n"
      "ds_write_b64 v24, v[96:97] offset:0\n"
      "v_accvgpr_read_b32 v100, a4\n"
      "v_accvgpr_read_b32 v101, a5\n"
      "v_accvgpr_read_b32 v102, a6\n"
      "v_accvgpr_read_b32 v103, a7\n"
      "v_mul_f32 v100, v196, v100\n"
      "v_mul_f32 v101, v197, v101\n"
      "v_mul_f32 v102, v198, v102\n"
      "v_mul_f32 v103, v199, v103\n"
      "v_cvt_pk_bf16_f32 v100, v100, v101\n"
      "v_cvt_pk_bf16_f32 v101, v102, v103\n"
      "ds_write_b64 v24, v[100:101] offset:512\n"
      "v_accvgpr_read_b32 v104, a8\n"
      "v_accvgpr_read_b32 v105, a9\n"
      "v_accvgpr_read_b32 v106, a10\n"
      "v_accvgpr_read_b32 v107, a11\n"
      "v_mul_f32 v104, v196, v104\n"
      "v_mul_f32 v105, v197, v105\n"
      "v_mul_f32 v106, v198, v106\n"
      "v_mul_f32 v107, v199, v107\n"
      "v_cvt_pk_bf16_f32 v104, v104, v105\n"
      "v_cvt_pk_bf16_f32 v105, v106, v107\n"
      "ds_write_b64 v24, v[104:105] offset:1024\n"
      "v_accvgpr_read_b32 v108, a12\n"
      "v_accvgpr_read_b32 v109, a13\n"
      "v_accvgpr_read_b32 v110, a14\n"
      "v_accvgpr_read_b32 v111, a15\n"
      "v_mul_f32 v108, v196, v108\n"
      "v_mul_f32 v109, v197, v109\n"
      "v_mul_f32 v110, v198, v110\n"
      "v_mul_f32 v111, v199, v111\n"
      "v_cvt_pk_bf16_f32 v108, v108, v109\n"
      "v_cvt_pk_bf16_f32 v109, v110, v111\n"
      "ds_write_b64 v24, v[108:109] offset:1536\n"
      "v_accvgpr_read_b32 v112, a16\n"
      "v_accvgpr_read_b32 v113, a17\n"
      "v_accvgpr_read_b32 v114, a18\n"
      "v_accvgpr_read_b32 v115, a19\n"
      "v_mul_f32 v112, v196, v112\n"
      "v_mul_f32 v113, v197, v113\n"
      "v_mul_f32 v114, v198, v114\n"
      "v_mul_f32 v115, v199, v115\n"
      "v_cvt_pk_bf16_f32 v112, v112, v113\n"
      "v_cvt_pk_bf16_f32 v113, v114, v115\n"
      "ds_write_b64 v24, v[112:113] offset:2048\n"
      "v_accvgpr_read_b32 v116, a20\n"
      "v_accvgpr_read_b32 v117, a21\n"
      "v_accvgpr_read_b32 v118, a22\n"
      "v_accvgpr_read_b32 v119, a23\n"
      "v_mul_f32 v116, v196, v116\n"
      "v_mul_f32 v117, v197, v117\n"
      "v_mul_f32 v118, v198, v118\n"
      "v_mul_f32 v119, v199, v119\n"
      "v_cvt_pk_bf16_f32 v116, v116, v117\n"
      "v_cvt_pk_bf16_f32 v117, v118, v119\n"
      "ds_write_b64 v24, v[116:117] offset:2560\n"
      "v_accvgpr_read_b32 v120, a24\n"
      "v_accvgpr_read_b32 v121, a25\n"
      "v_accvgpr_read_b32 v122, a26\n"
      "v_accvgpr_read_b32 v123, a27\n"
      "v_mul_f32 v120, v196, v120\n"
      "v_mul_f32 v121, v197, v121\n"
      "v_mul_f32 v122, v198, v122\n"
      "v_mul_f32 v123, v199, v123\n"
      "v_cvt_pk_bf16_f32 v120, v120, v121\n"
      "v_cvt_pk_bf16_f32 v121, v122, v123\n"
      "ds_write_b64 v24, v[120:121] offset:3072\n"
      "v_accvgpr_read_b32 v124, a28\n"
      "v_accvgpr_read_b32 v125, a29\n"
      "v_accvgpr_read_b32 v126, a30\n"
      "v_accvgpr_read_b32 v127, a31\n"
      "v_mul_f32 v124, v196, v124\n"
      "v_mul_f32 v125, v197, v125\n"
      "v_mul_f32 v126, v198, v126\n"
      "v_mul_f32 v127, v199, v127\n"
      "v_cvt_pk_bf16_f32 v124, v124, v125\n"
      "v_cvt_pk_bf16_f32 v125, v126, v127\n"
      "ds_write_b64 v24, v[124:125] offset:3584\n"
      "ds_read_b64_tr_b16 v[64:65], v25 offset:0\n"
      "ds_read_b64_tr_b16 v[66:67], v26 offset:0\n"
      "ds_read_b64_tr_b16 v[68:69], v25 offset:1024\n"
      "ds_read_b64_tr_b16 v[70:71], v26 offset:1024\n"
      "ds_read_b64_tr_b16 v[72:73], v25 offset:2048\n"
      "ds_read_b64_tr_b16 v[74:75], v26 offset:2048\n"
      "ds_read_b64_tr_b16 v[76:77], v25 offset:3072\n"
      "ds_read_b64_tr_b16 v[78:79], v26 offset:3072\n"
      "v_accvgpr_read_b32 v128, a32\n"
      "v_accvgpr_read_b32 v129, a33\n"
      "v_accvgpr_read_b32 v130, a34\n"
      "v_accvgpr_read_b32 v131, a35\n"
      "v_pk_mul_f32 v[128:129], v[128:129], v[200:201]\n"
      "v_pk_mul_f32 v[130:131], v[130:131], v[202:203]\n"
      "v_cvt_pk_bf16_f32 v128, v128, v129\n"
      "v_cvt_pk_bf16_f32 v129, v130, v131\n"
      "ds_write_b64 v24, v[128:129] offset:4096\n"
      "v_accvgpr_read_b32 v132, a36\n"
      "v_accvgpr_read_b32 v133, a37\n"
      "v_accvgpr_read_b32 v134, a38\n"
      "v_accvgpr_read_b32 v135, a39\n"
      "v_pk_mul_f32 v[132:133], v[132:133], v[200:201]\n"
      "v_pk_mul_f32 v[134:135], v[134:135], v[202:203]\n"
      "v_cvt_pk_bf16_f32 v132, v132, v133\n"
      "v_cvt_pk_bf16_f32 v133, v134, v135\n"
      "ds_write_b64 v24, v[132:133] offset:4608\n"
      "v_accvgpr_read_b32 v136, a40\n"
      "v_accvgpr_read_b32 v137, a41\n"
      "v_accvgpr_read_b32 v138, a42\n"
      "v_accvgpr_read_b32 v139, a43\n"
      "v_pk_mul_f32 v[136:137], v[136:137], v[200:201]\n"
      "v_pk_mul_f32 v[138:139], v[138:139], v[202:203]\n"
      "v_cvt_pk_bf16_f32 v136, v136, v137\n"
      "v_cvt_pk_bf16_f32 v137, v138, v139\n"
      "ds_write_b64 v24, v[136:137] offset:5120\n"
      "v_accvgpr_read_b32 v140, a44\n"
      "v_accvgpr_read_b32 v141, a45\n"
      "v_accvgpr_read_b32 v142, a46\n"
      "v_accvgpr_read_b32 v143, a47\n"
      "v_pk_mul_f32 v[140:141], v[140:141], v[200:201]\n"
      "v_pk_mul_f32 v[142:143], v[142:143], v[202:203]\n"
      "v_cvt_pk_bf16_f32 v140, v140, v141\n"
      "v_cvt_pk_bf16_f32 v141, v142, v143\n"
      "ds_write_b64 v24, v[140:141] offset:5632\n"
      "s_waitcnt lgkmcnt(4)\n"
      "ds_write_b128 v0, v[64:67] offset:0\n"
      "ds_write_b128 v1, v[68:71] offset:0\n"
      "ds_write_b128 v2, v[72:75] offset:0\n"
      "ds_write_b128 v3, v[76:79] offset:0\n"
      "ds_read_b128 v[64:67], v6 offset:0\n"
      "ds_read_b128 v[68:71], v7 offset:1024\n"
      "ds_read_b128 v[72:75], v8 offset:2048\n"
      "ds_read_b128 v[76:79], v9 offset:3072\n"
      "v_accvgpr_read_b32 v144, a48\n"
      "v_accvgpr_read_b32 v145, a49\n"
      "v_accvgpr_read_b32 v146, a50\n"
      "v_accvgpr_read_b32 v147, a51\n"
      "v_pk_mul_f32 v[144:145], v[144:145], v[200:201]\n"
      "v_pk_mul_f32 v[146:147], v[146:147], v[202:203]\n"
      "v_cvt_pk_bf16_f32 v144, v144, v145\n"
      "v_cvt_pk_bf16_f32 v145, v146, v147\n"
      "ds_write_b64 v24, v[144:145] offset:6144\n"
      "v_accvgpr_read_b32 v148, a52\n"
      "v_accvgpr_read_b32 v149, a53\n"
      "v_accvgpr_read_b32 v150, a54\n"
      "v_accvgpr_read_b32 v151, a55\n"
      "v_pk_mul_f32 v[148:149], v[148:149], v[200:201]\n"
      "v_pk_mul_f32 v[150:151], v[150:151], v[202:203]\n"
      "v_cvt_pk_bf16_f32 v148, v148, v149\n"
      "v_cvt_pk_bf16_f32 v149, v150, v151\n"
      "ds_write_b64 v24, v[148:149] offset:6656\n"
      "v_accvgpr_read_b32 v152, a56\n"
      "v_accvgpr_read_b32 v153, a57\n"
      "v_accvgpr_read_b32 v154, a58\n"
      "v_accvgpr_read_b32 v155, a59\n"
      "v_pk_mul_f32 v[152:153], v[152:153], v[200:201]\n"
      "v_pk_mul_f32 v[154:155], v[154:155], v[202:203]\n"
      "v_cvt_pk_bf16_f32 v152, v152, v153\n"
      "v_cvt_pk_bf16_f32 v153, v154, v155\n"
      "ds_write_b64 v24, v[152:153] offset:7168\n"
      "v_accvgpr_read_b32 v156, a60\n"
      "v_accvgpr_read_b32 v157, a61\n"
      "v_accvgpr_read_b32 v158, a62\n"
      "v_accvgpr_read_b32 v159, a63\n"
      "v_pk_mul_f32 v[156:157], v[156:157], v[200:201]\n"
      "v_pk_mul_f32 v[158:159], v[158:159], v[202:203]\n"
      "v_cvt_pk_bf16_f32 v156, v156, v157\n"
      "v_cvt_pk_bf16_f32 v157, v158, v159\n"
      "ds_write_b64 v24, v[156:157] offset:7680\n"
      "s_waitcnt lgkmcnt(4)\n"
      "s_add_u32 s58, s62, 0\n"
      "buffer_store_dwordx4 v[64:67], v12, s[48:51], s58 offen offset:0 nt\n"
      "buffer_store_dwordx4 v[68:71], v12, s[48:51], s58 offen offset:1024 nt\n"
      "buffer_store_dwordx4 v[72:75], v12, s[48:51], s58 offen offset:2048 nt\n"
      "buffer_store_dwordx4 v[76:79], v12, s[48:51], s58 offen offset:3072 nt\n"
      "v_mov_b32 v20, 0x20800\n"
      "ds_read_b64_tr_b16 v[80:81], v25 offset:4096\n"
      "ds_read_b64_tr_b16 v[82:83], v26 offset:4096\n"
      "ds_read_b64_tr_b16 v[84:85], v25 offset:5120\n"
      "ds_read_b64_tr_b16 v[86:87], v26 offset:5120\n"
      "ds_read_b64_tr_b16 v[88:89], v25 offset:6144\n"
      "ds_read_b64_tr_b16 v[90:91], v26 offset:6144\n"
      "ds_read_b64_tr_b16 v[92:93], v25 offset:7168\n"
      "ds_read_b64_tr_b16 v[94:95], v26 offset:7168\n"
      "v_accvgpr_read_b32 v96, a64\n"
      "v_accvgpr_read_b32 v97, a65\n"
      "v_accvgpr_read_b32 v98, a66\n"
      "v_accvgpr_read_b32 v99, a67\n"
      "v_pk_mul_f32 v[96:97], v[96:97], v[204:205]\n"
      "v_pk_mul_f32 v[98:99], v[98:99], v[206:207]\n"
      "v_cvt_pk_bf16_f32 v96, v96, v97\n"
      "v_cvt_pk_bf16_f32 v97, v98, v99\n"
      "ds_write_b64 v24, v[96:97] offset:8192\n"
      "v_accvgpr_read_b32 v100, a68\n"
      "v_accvgpr_read_b32 v101, a69\n"
      "v_accvgpr_read_b32 v102, a70\n"
      "v_accvgpr_read_b32 v103, a71\n"
      "v_pk_mul_f32 v[100:101], v[100:101], v[204:205]\n"
      "v_pk_mul_f32 v[102:103], v[102:103], v[206:207]\n"
      "v_cvt_pk_bf16_f32 v100, v100, v101\n"
      "v_cvt_pk_bf16_f32 v101, v102, v103\n"
      "ds_write_b64 v24, v[100:101] offset:8704\n"
      "v_accvgpr_read_b32 v104, a72\n"
      "v_accvgpr_read_b32 v105, a73\n"
      "v_accvgpr_read_b32 v106, a74\n"
      "v_accvgpr_read_b32 v107, a75\n"
      "v_pk_mul_f32 v[104:105], v[104:105], v[204:205]\n"
      "v_pk_mul_f32 v[106:107], v[106:107], v[206:207]\n"
      "v_cvt_pk_bf16_f32 v104, v104, v105\n"
      "v_cvt_pk_bf16_f32 v105, v106, v107\n"
      "ds_write_b64 v24, v[104:105] offset:9216\n"
      "v_accvgpr_read_b32 v108, a76\n"
      "v_accvgpr_read_b32 v109, a77\n"
      "v_accvgpr_read_b32 v110, a78\n"
      "v_accvgpr_read_b32 v111, a79\n"
      "v_pk_mul_f32 v[108:109], v[108:109], v[204:205]\n"
      "v_pk_mul_f32 v[110:111], v[110:111], v[206:207]\n"
      "v_cvt_pk_bf16_f32 v108, v108, v109\n"
      "v_cvt_pk_bf16_f32 v109, v110, v111\n"
      "ds_write_b64 v24, v[108:109] offset:9728\n"
      "s_waitcnt lgkmcnt(4)\n"
      "ds_write_b128 v0, v[80:83] offset:4096\n"
      "ds_write_b128 v1, v[84:87] offset:4096\n"
      "ds_write_b128 v2, v[88:91] offset:4096\n"
      "ds_write_b128 v3, v[92:95] offset:4096\n"
      "ds_read_b128 v[80:83], v6 offset:4096\n"
      "ds_read_b128 v[84:87], v7 offset:5120\n"
      "ds_read_b128 v[88:91], v8 offset:6144\n"
      "ds_read_b128 v[92:95], v9 offset:7168\n"
      "v_accvgpr_read_b32 v112, a80\n"
      "v_accvgpr_read_b32 v113, a81\n"
      "v_accvgpr_read_b32 v114, a82\n"
      "v_accvgpr_read_b32 v115, a83\n"
      "v_pk_mul_f32 v[112:113], v[112:113], v[204:205]\n"
      "v_pk_mul_f32 v[114:115], v[114:115], v[206:207]\n"
      "v_cvt_pk_bf16_f32 v112, v112, v113\n"
      "v_cvt_pk_bf16_f32 v113, v114, v115\n"
      "ds_write_b64 v24, v[112:113] offset:10240\n"
      "v_accvgpr_read_b32 v116, a84\n"
      "v_accvgpr_read_b32 v117, a85\n"
      "v_accvgpr_read_b32 v118, a86\n"
      "v_accvgpr_read_b32 v119, a87\n"
      "v_pk_mul_f32 v[116:117], v[116:117], v[204:205]\n"
      "v_pk_mul_f32 v[118:119], v[118:119], v[206:207]\n"
      "v_cvt_pk_bf16_f32 v116, v116, v117\n"
      "v_cvt_pk_bf16_f32 v117, v118, v119\n"
      "ds_write_b64 v24, v[116:117] offset:10752\n"
      "v_accvgpr_read_b32 v120, a88\n"
      "v_accvgpr_read_b32 v121, a89\n"
      "v_accvgpr_read_b32 v122, a90\n"
      "v_accvgpr_read_b32 v123, a91\n"
      "v_pk_mul_f32 v[120:121], v[120:121], v[204:205]\n"
      "v_pk_mul_f32 v[122:123], v[122:123], v[206:207]\n"
      "v_cvt_pk_bf16_f32 v120, v120, v121\n"
      "v_cvt_pk_bf16_f32 v121, v122, v123\n"
      "ds_write_b64 v24, v[120:121] offset:11264\n"
      "v_accvgpr_read_b32 v124, a92\n"
      "v_accvgpr_read_b32 v125, a93\n"
      "v_accvgpr_read_b32 v126, a94\n"
      "v_accvgpr_read_b32 v127, a95\n"
      "v_pk_mul_f32 v[124:125], v[124:125], v[204:205]\n"
      "v_pk_mul_f32 v[126:127], v[126:127], v[206:207]\n"
      "v_cvt_pk_bf16_f32 v124, v124, v125\n"
      "v_cvt_pk_bf16_f32 v125, v126, v127\n"
      "ds_write_b64 v24, v[124:125] offset:11776\n"
      "s_waitcnt lgkmcnt(4)\n"
      "s_add_u32 s58, s62, 4096\n"
      "buffer_store_dwordx4 v[80:83], v12, s[48:51], s58 offen offset:0 nt\n"
      "buffer_store_dwordx4 v[84:87], v12, s[48:51], s58 offen offset:1024 nt\n"
      "buffer_store_dwordx4 v[88:91], v12, s[48:51], s58 offen offset:2048 nt\n"
      "buffer_store_dwordx4 v[92:95], v12, s[48:51], s58 offen offset:3072 nt\n"
      "s_barrier\n"
      "ds_read_b64_tr_b16 v[64:65], v25 offset:8192\n"
      "ds_read_b64_tr_b16 v[66:67], v26 offset:8192\n"
      "ds_read_b64_tr_b16 v[68:69], v25 offset:9216\n"
      "ds_read_b64_tr_b16 v[70:71], v26 offset:9216\n"
      "ds_read_b64_tr_b16 v[72:73], v25 offset:10240\n"
      "ds_read_b64_tr_b16 v[74:75], v26 offset:10240\n"
      "ds_read_b64_tr_b16 v[76:77], v25 offset:11264\n"
      "ds_read_b64_tr_b16 v[78:79], v26 offset:11264\n"
      "ds_read_b128 v[160:163], v249 offset:0\n"
      "ds_read_b128 v[164:167], v249 offset:1024\n"
      "ds_read_b128 v[168:171], v249 offset:8192\n"
      "ds_read_b128 v[172:175], v249 offset:9216\n"
      "ds_read_b128 v[176:179], v249 offset:16384\n"
      "ds_read_b128 v[180:183], v249 offset:17408\n"
      "ds_read_b128 v[184:187], v249 offset:24576\n"
      "ds_read_b128 v[188:191], v249 offset:25600\n"
      "ds_read_b128 v[32:35], v250 offset:0\n"
      "ds_read_b128 v[36:39], v250 offset:256\n"
      "ds_read_b128 v[40:43], v250 offset:512\n"
      "ds_read_b128 v[44:47], v250 offset:768\n"
      "ds_read_b128 v[48:51], v20 offset:0\n"
      "v_accvgpr_read_b32 v128, a96\n"
      "v_accvgpr_read_b32 v129, a97\n"
      "v_accvgpr_read_b32 v130, a98\n"
      "v_accvgpr_read_b32 v131, a99\n"
      "v_pk_mul_f32 v[128:129], v[128:129], v[208:209]\n"
      "v_pk_mul_f32 v[130:131], v[130:131], v[210:211]\n"
      "v_cvt_pk_bf16_f32 v128, v128, v129\n"
      "v_cvt_pk_bf16_f32 v129, v130, v131\n"
      "ds_write_b64 v24, v[128:129] offset:12288\n"
      "v_accvgpr_read_b32 v132, a100\n"
      "v_accvgpr_read_b32 v133, a101\n"
      "v_accvgpr_read_b32 v134, a102\n"
      "v_accvgpr_read_b32 v135, a103\n"
      "v_pk_mul_f32 v[132:133], v[132:133], v[208:209]\n"
      "v_pk_mul_f32 v[134:135], v[134:135], v[210:211]\n"
      "v_cvt_pk_bf16_f32 v132, v132, v133\n"
      "v_cvt_pk_bf16_f32 v133, v134, v135\n"
      "ds_write_b64 v24, v[132:133] offset:12800\n"
      "v_accvgpr_read_b32 v136, a104\n"
      "v_accvgpr_read_b32 v137, a105\n"
      "v_accvgpr_read_b32 v138, a106\n"
      "v_accvgpr_read_b32 v139, a107\n"
      "v_pk_mul_f32 v[136:137], v[136:137], v[208:209]\n"
      "v_pk_mul_f32 v[138:139], v[138:139], v[210:211]\n"
      "v_cvt_pk_bf16_f32 v136, v136, v137\n"
      "v_cvt_pk_bf16_f32 v137, v138, v139\n"
      "ds_write_b64 v24, v[136:137] offset:13312\n"
      "v_accvgpr_read_b32 v140, a108\n"
      "v_accvgpr_read_b32 v141, a109\n"
      "v_accvgpr_read_b32 v142, a110\n"
      "v_accvgpr_read_b32 v143, a111\n"
      "v_pk_mul_f32 v[140:141], v[140:141], v[208:209]\n"
      "v_pk_mul_f32 v[142:143], v[142:143], v[210:211]\n"
      "v_cvt_pk_bf16_f32 v140, v140, v141\n"
      "v_cvt_pk_bf16_f32 v141, v142, v143\n"
      "ds_write_b64 v24, v[140:141] offset:13824\n"
      "s_waitcnt lgkmcnt(15)\n"
      "ds_write_b128 v0, v[64:67] offset:8192\n"
      "ds_write_b128 v1, v[68:71] offset:8192\n"
      "ds_write_b128 v2, v[72:75] offset:8192\n"
      "ds_write_b128 v3, v[76:79] offset:8192\n"
      "ds_read_b128 v[64:67], v6 offset:8192\n"
      "ds_read_b128 v[68:71], v7 offset:9216\n"
      "ds_read_b128 v[72:75], v8 offset:10240\n"
      "ds_read_b128 v[76:79], v9 offset:11264\n"
      "v_accvgpr_read_b32 v144, a112\n"
      "v_accvgpr_read_b32 v145, a113\n"
      "v_accvgpr_read_b32 v146, a114\n"
      "v_accvgpr_read_b32 v147, a115\n"
      "v_pk_mul_f32 v[144:145], v[144:145], v[208:209]\n"
      "v_pk_mul_f32 v[146:147], v[146:147], v[210:211]\n"
      "v_cvt_pk_bf16_f32 v144, v144, v145\n"
      "v_cvt_pk_bf16_f32 v145, v146, v147\n"
      "ds_write_b64 v24, v[144:145] offset:14336\n"
      "v_accvgpr_read_b32 v148, a116\n"
      "v_accvgpr_read_b32 v149, a117\n"
      "v_accvgpr_read_b32 v150, a118\n"
      "v_accvgpr_read_b32 v151, a119\n"
      "v_pk_mul_f32 v[148:149], v[148:149], v[208:209]\n"
      "v_pk_mul_f32 v[150:151], v[150:151], v[210:211]\n"
      "v_cvt_pk_bf16_f32 v148, v148, v149\n"
      "v_cvt_pk_bf16_f32 v149, v150, v151\n"
      "ds_write_b64 v24, v[148:149] offset:14848\n"
      "v_accvgpr_read_b32 v152, a120\n"
      "v_accvgpr_read_b32 v153, a121\n"
      "v_accvgpr_read_b32 v154, a122\n"
      "v_accvgpr_read_b32 v155, a123\n"
      "v_pk_mul_f32 v[152:153], v[152:153], v[208:209]\n"
      "v_pk_mul_f32 v[154:155], v[154:155], v[210:211]\n"
      "v_cvt_pk_bf16_f32 v152, v152, v153\n"
      "v_cvt_pk_bf16_f32 v153, v154, v155\n"
      "ds_write_b64 v24, v[152:153] offset:15360\n"
      "v_accvgpr_read_b32 v156, a124\n"
      "v_accvgpr_read_b32 v157, a125\n"
      "v_accvgpr_read_b32 v158, a126\n"
      "v_accvgpr_read_b32 v159, a127\n"
      "v_pk_mul_f32 v[156:157], v[156:157], v[208:209]\n"
      "v_pk_mul_f32 v[158:159], v[158:159], v[210:211]\n"
      "v_cvt_pk_bf16_f32 v156, v156, v157\n"
      "v_cvt_pk_bf16_f32 v157, v158, v159\n"
      "ds_write_b64 v24, v[156:157] offset:15872\n"
      "s_waitcnt lgkmcnt(4)\n"
      "s_add_u32 s58, s62, 8192\n"
      "buffer_store_dwordx4 v[64:67], v12, s[48:51], s58 offen offset:0 nt\n"
      "buffer_store_dwordx4 v[68:71], v12, s[48:51], s58 offen offset:1024 nt\n"
      "buffer_store_dwordx4 v[72:75], v12, s[48:51], s58 offen offset:2048 nt\n"
      "buffer_store_dwordx4 v[76:79], v12, s[48:51], s58 offen offset:3072 nt\n"
      "v_add_f32 v32, v32, v36\n"
      "v_add_f32 v40, v40, v44\n"
      "v_add_f32 v33, v33, v37\n"
      "v_add_f32 v41, v41, v45\n"
      "v_add_f32 v34, v34, v38\n"
      "v_add_f32 v42, v42, v46\n"
      "v_add_f32 v35, v35, v39\n"
      "v_add_f32 v43, v43, v47\n"
      "v_add_f32 v160, v160, v168\n"
      "v_add_f32 v176, v176, v184\n"
      "v_add_f32 v161, v161, v169\n"
      "v_add_f32 v177, v177, v185\n"
      "v_add_f32 v162, v162, v170\n"
      "v_add_f32 v178, v178, v186\n"
      "v_add_f32 v163, v163, v171\n"
      "v_add_f32 v179, v179, v187\n"
      "v_add_f32 v164, v164, v172\n"
      "v_add_f32 v180, v180, v188\n"
      "v_add_f32 v165, v165, v173\n"
      "v_add_f32 v181, v181, v189\n"
      "v_add_f32 v166, v166, v174\n"
      "v_add_f32 v182, v182, v190\n"
      "v_add_f32 v167, v167, v175\n"
      "v_add_f32 v183, v183, v191\n"
      "v_add_f32 v32, v32, v40\n"
      "v_add_f32 v33, v33, v41\n"
      "v_add_f32 v34, v34, v42\n"
      "v_add_f32 v35, v35, v43\n"
      "v_add_f32 v160, v160, v176\n"
      "v_add_f32 v161, v161, v177\n"
      "v_add_f32 v162, v162, v178\n"
      "v_add_f32 v163, v163, v179\n"
      "v_add_f32 v164, v164, v180\n"
      "v_add_f32 v165, v165, v181\n"
      "v_add_f32 v166, v166, v182\n"
      "v_add_f32 v167, v167, v183\n"
      "v_rcp_f32 v52, v32\n"
      "v_rcp_f32 v53, v33\n"
      "v_rcp_f32 v54, v34\n"
      "v_rcp_f32 v55, v35\n"
      "v_or3_b32 v48, v48, v49, v50\n"
      "v_or_b32 v48, v48, v51\n"
      "v_mul_f32 v160, v52, v160\n"
      "v_mul_f32 v161, v53, v161\n"
      "v_mul_f32 v162, v54, v162\n"
      "v_mul_f32 v163, v55, v163\n"
      "v_mul_f32 v164, v52, v164\n"
      "v_mul_f32 v165, v53, v165\n"
      "v_mul_f32 v166, v54, v166\n"
      "v_mul_f32 v167, v55, v167\n"
      "v_cvt_pk_bf16_f32 v56, v160, v161\n"
      "v_cvt_pk_bf16_f32 v57, v162, v163\n"
      "v_cvt_pk_bf16_f32 v58, v164, v165\n"
      "v_cvt_pk_bf16_f32 v59, v166, v167\n"
      "ds_write_b64 v24, v[56:57] offset:0\n"
      "ds_write_b64 v24, v[58:59] offset:512\n"
      "ds_read_b64_tr_b16 v[60:61], v25 offset:0\n"
      "ds_read_b64_tr_b16 v[62:63], v26 offset:0\n"
      "v_readfirstlane_b32 s60, v48\n"
      "s_mov_b32 s59, 0x14000\n"
      "s_waitcnt lgkmcnt(0)\n"
      "buffer_store_dwordx4 v[60:63], v253, s[48:51], s59 offen offset:0 nt\n"
      "ds_read_b64_tr_b16 v[80:81], v25 offset:12288\n"
      "ds_read_b64_tr_b16 v[82:83], v26 offset:12288\n"
      "ds_read_b64_tr_b16 v[84:85], v25 offset:13312\n"
      "ds_read_b64_tr_b16 v[86:87], v26 offset:13312\n"
      "ds_read_b64_tr_b16 v[88:89], v25 offset:14336\n"
      "ds_read_b64_tr_b16 v[90:91], v26 offset:14336\n"
      "ds_read_b64_tr_b16 v[92:93], v25 offset:15360\n"
      "ds_read_b64_tr_b16 v[94:95], v26 offset:15360\n"
      "v_accvgpr_read_b32 v96, a128\n"
      "v_accvgpr_read_b32 v97, a129\n"
      "v_accvgpr_read_b32 v98, a130\n"
      "v_accvgpr_read_b32 v99, a131\n"
      "v_pk_mul_f32 v[96:97], v[96:97], v[212:213]\n"
      "v_pk_mul_f32 v[98:99], v[98:99], v[214:215]\n"
      "v_cvt_pk_bf16_f32 v96, v96, v97\n"
      "v_cvt_pk_bf16_f32 v97, v98, v99\n"
      "ds_write_b64 v24, v[96:97] offset:16384\n"
      "v_accvgpr_read_b32 v100, a132\n"
      "v_accvgpr_read_b32 v101, a133\n"
      "v_accvgpr_read_b32 v102, a134\n"
      "v_accvgpr_read_b32 v103, a135\n"
      "v_pk_mul_f32 v[100:101], v[100:101], v[212:213]\n"
      "v_pk_mul_f32 v[102:103], v[102:103], v[214:215]\n"
      "v_cvt_pk_bf16_f32 v100, v100, v101\n"
      "v_cvt_pk_bf16_f32 v101, v102, v103\n"
      "ds_write_b64 v24, v[100:101] offset:16896\n"
      "v_accvgpr_read_b32 v104, a136\n"
      "v_accvgpr_read_b32 v105, a137\n"
      "v_accvgpr_read_b32 v106, a138\n"
      "v_accvgpr_read_b32 v107, a139\n"
      "v_pk_mul_f32 v[104:105], v[104:105], v[212:213]\n"
      "v_pk_mul_f32 v[106:107], v[106:107], v[214:215]\n"
      "v_cvt_pk_bf16_f32 v104, v104, v105\n"
      "v_cvt_pk_bf16_f32 v105, v106, v107\n"
      "ds_write_b64 v24, v[104:105] offset:17408\n"
      "v_accvgpr_read_b32 v108, a140\n"
      "v_accvgpr_read_b32 v109, a141\n"
      "v_accvgpr_read_b32 v110, a142\n"
      "v_accvgpr_read_b32 v111, a143\n"
      "v_pk_mul_f32 v[108:109], v[108:109], v[212:213]\n"
      "v_pk_mul_f32 v[110:111], v[110:111], v[214:215]\n"
      "v_cvt_pk_bf16_f32 v108, v108, v109\n"
      "v_cvt_pk_bf16_f32 v109, v110, v111\n"
      "ds_write_b64 v24, v[108:109] offset:17920\n"
      "s_waitcnt lgkmcnt(4)\n"
      "ds_write_b128 v0, v[80:83] offset:12288\n"
      "ds_write_b128 v1, v[84:87] offset:12288\n"
      "ds_write_b128 v2, v[88:91] offset:12288\n"
      "ds_write_b128 v3, v[92:95] offset:12288\n"
      "ds_read_b128 v[80:83], v6 offset:12288\n"
      "ds_read_b128 v[84:87], v7 offset:13312\n"
      "ds_read_b128 v[88:91], v8 offset:14336\n"
      "ds_read_b128 v[92:95], v9 offset:15360\n"
      "v_accvgpr_read_b32 v112, a144\n"
      "v_accvgpr_read_b32 v113, a145\n"
      "v_accvgpr_read_b32 v114, a146\n"
      "v_accvgpr_read_b32 v115, a147\n"
      "v_pk_mul_f32 v[112:113], v[112:113], v[212:213]\n"
      "v_pk_mul_f32 v[114:115], v[114:115], v[214:215]\n"
      "v_cvt_pk_bf16_f32 v112, v112, v113\n"
      "v_cvt_pk_bf16_f32 v113, v114, v115\n"
      "ds_write_b64 v24, v[112:113] offset:18432\n"
      "v_accvgpr_read_b32 v116, a148\n"
      "v_accvgpr_read_b32 v117, a149\n"
      "v_accvgpr_read_b32 v118, a150\n"
      "v_accvgpr_read_b32 v119, a151\n"
      "v_pk_mul_f32 v[116:117], v[116:117], v[212:213]\n"
      "v_pk_mul_f32 v[118:119], v[118:119], v[214:215]\n"
      "v_cvt_pk_bf16_f32 v116, v116, v117\n"
      "v_cvt_pk_bf16_f32 v117, v118, v119\n"
      "ds_write_b64 v24, v[116:117] offset:18944\n"
      "v_accvgpr_read_b32 v120, a152\n"
      "v_accvgpr_read_b32 v121, a153\n"
      "v_accvgpr_read_b32 v122, a154\n"
      "v_accvgpr_read_b32 v123, a155\n"
      "v_pk_mul_f32 v[120:121], v[120:121], v[212:213]\n"
      "v_pk_mul_f32 v[122:123], v[122:123], v[214:215]\n"
      "v_cvt_pk_bf16_f32 v120, v120, v121\n"
      "v_cvt_pk_bf16_f32 v121, v122, v123\n"
      "ds_write_b64 v24, v[120:121] offset:19456\n"
      "v_accvgpr_read_b32 v124, a156\n"
      "v_accvgpr_read_b32 v125, a157\n"
      "v_accvgpr_read_b32 v126, a158\n"
      "v_accvgpr_read_b32 v127, a159\n"
      "v_pk_mul_f32 v[124:125], v[124:125], v[212:213]\n"
      "v_pk_mul_f32 v[126:127], v[126:127], v[214:215]\n"
      "v_cvt_pk_bf16_f32 v124, v124, v125\n"
      "v_cvt_pk_bf16_f32 v125, v126, v127\n"
      "ds_write_b64 v24, v[124:125] offset:19968\n"
      "s_waitcnt lgkmcnt(4)\n"
      "s_add_u32 s58, s62, 12288\n"
      "buffer_store_dwordx4 v[80:83], v12, s[48:51], s58 offen offset:0 nt\n"
      "buffer_store_dwordx4 v[84:87], v12, s[48:51], s58 offen offset:1024 nt\n"
      "buffer_store_dwordx4 v[88:91], v12, s[48:51], s58 offen offset:2048 nt\n"
      "buffer_store_dwordx4 v[92:95], v12, s[48:51], s58 offen offset:3072 nt\n"
      "ds_read_b64_tr_b16 v[64:65], v25 offset:16384\n"
      "ds_read_b64_tr_b16 v[66:67], v26 offset:16384\n"
      "ds_read_b64_tr_b16 v[68:69], v25 offset:17408\n"
      "ds_read_b64_tr_b16 v[70:71], v26 offset:17408\n"
      "ds_read_b64_tr_b16 v[72:73], v25 offset:18432\n"
      "ds_read_b64_tr_b16 v[74:75], v26 offset:18432\n"
      "ds_read_b64_tr_b16 v[76:77], v25 offset:19456\n"
      "ds_read_b64_tr_b16 v[78:79], v26 offset:19456\n"
      "s_waitcnt lgkmcnt(0)\n"
      "s_add_u32 s58, s62, 16384\n"
      "ds_write_b128 v0, v[64:67] offset:16384\n"
      "ds_write_b128 v1, v[68:71] offset:16384\n"
      "ds_write_b128 v2, v[72:75] offset:16384\n"
      "ds_write_b128 v3, v[76:79] offset:16384\n"
      "ds_read_b128 v[64:67], v6 offset:16384\n"
      "ds_read_b128 v[68:71], v7 offset:17408\n"
      "ds_read_b128 v[72:75], v8 offset:18432\n"
      "ds_read_b128 v[76:79], v9 offset:19456\n"
      "s_waitcnt lgkmcnt(0)\n"
      "buffer_store_dwordx4 v[64:67], v12, s[48:51], s58 offen offset:0 nt\n"
      "buffer_store_dwordx4 v[68:71], v12, s[48:51], s58 offen offset:1024 nt\n"
      "buffer_store_dwordx4 v[72:75], v12, s[48:51], s58 offen offset:2048 nt\n"
      "buffer_store_dwordx4 v[76:79], v12, s[48:51], s58 offen offset:3072 nt\n"
      "s_andn2_b32 s60, s60, s65\n"
      "s_cmp_lg_u32 s60, 0\n"
      "s_cbranch_scc1 L_fallback_w0\n"
      "s_branch L_end\n"
      "L_fallback_w0:\n"
      "s_mov_b32 s65, 1\n"
      "s_waitcnt vmcnt(0) lgkmcnt(0)\n"
      "s_barrier\n"
      "v_subrev_u32 v0, s64, v0\n"
      "v_subrev_u32 v6, s64, v6\n"
      "v_subrev_u32 v1, s64, v1\n"
      "v_subrev_u32 v7, s64, v7\n"
      "v_subrev_u32 v2, s64, v2\n"
      "v_subrev_u32 v8, s64, v8\n"
      "v_subrev_u32 v3, s64, v3\n"
      "v_subrev_u32 v9, s64, v9\n"
      "v_and_b32 v20, 15, v13\n"
      "v_lshrrev_b32 v21, 4, v13\n"
      "v_lshlrev_b32 v22, 4, v21\n"
      "v_lshl_or_b32 v22, v20, 8, v22\n"
      "v_mov_b32 v64, 0xff800000\n"
      "v_mov_b32 v65, 0xff800000\n"
      "v_mov_b32 v66, 0xff800000\n"
      "v_mov_b32 v67, 0xff800000\n"
      "v_mov_b32 v68, 0xff800000\n"
      "v_mov_b32 v69, 0xff800000\n"
      "s_mov_b32 s60, 0\n"
      "L_fb_loop_w0:\n"
      "s_add_u32 s61, s60, 4096\n"
      "buffer_load_dwordx4 v[32:35], v22, s[40:43], s60 offen offset:0\n"
      "buffer_load_dwordx4 v[36:39], v22, s[40:43], s60 offen offset:64\n"
      "buffer_load_dwordx4 v[40:43], v22, s[40:43], s60 offen offset:128\n"
      "buffer_load_dwordx4 v[44:47], v22, s[40:43], s60 offen offset:192\n"
      "buffer_load_dwordx4 v[48:51], v22, s[40:43], s61 offen offset:0\n"
      "buffer_load_dwordx4 v[52:55], v22, s[40:43], s61 offen offset:64\n"
      "buffer_load_dwordx4 v[56:59], v22, s[40:43], s61 offen offset:128\n"
      "buffer_load_dwordx4 v[60:63], v22, s[40:43], s61 offen offset:192\n"
      "s_waitcnt vmcnt(0)\n"
      "v_mfma_f32_16x16x32_bf16 v[96:99], v[32:35], a[192:195], 0\n"
      "v_mfma_f32_16x16x32_bf16 v[96:99], v[36:39], a[196:199], v[96:99]\n"
      "v_mfma_f32_16x16x32_bf16 v[96:99], v[40:43], a[200:203], v[96:99]\n"
      "v_mfma_f32_16x16x32_bf16 v[96:99], v[44:47], a[204:207], v[96:99]\n"
      "v_mfma_f32_16x16x32_bf16 v[100:103], v[48:51], a[192:195], 0\n"
      "v_mfma_f32_16x16x32_bf16 v[100:103], v[52:55], a[196:199], v[100:103]\n"
      "v_mfma_f32_16x16x32_bf16 v[100:103], v[56:59], a[200:203], v[100:103]\n"
      "v_mfma_f32_16x16x32_bf16 v[100:103], v[60:63], a[204:207], v[100:103]\n"
      "s_nop 7\n"
      "v_max3_f32 v64, v64, v96, v97\n"
      "v_max3_f32 v64, v64, v98, v99\n"
      "v_max3_f32 v64, v64, v100, v101\n"
      "v_max3_f32 v64, v64, v102, v103\n"
      "v_mfma_f32_16x16x32_bf16 v[96:99], v[32:35], a[208:211], 0\n"
      "v_mfma_f32_16x16x32_bf16 v[96:99], v[36:39], a[212:215], v[96:99]\n"
      "v_mfma_f32_16x16x32_bf16 v[96:99], v[40:43], a[216:219], v[96:99]\n"
      "v_mfma_f32_16x16x32_bf16 v[96:99], v[44:47], a[220:223], v[96:99]\n"
      "v_mfma_f32_16x16x32_bf16 v[100:103], v[48:51], a[208:211], 0\n"
      "v_mfma_f32_16x16x32_bf16 v[100:103], v[52:55], a[212:215], v[100:103]\n"
      "v_mfma_f32_16x16x32_bf16 v[100:103], v[56:59], a[216:219], v[100:103]\n"
      "v_mfma_f32_16x16x32_bf16 v[100:103], v[60:63], a[220:223], v[100:103]\n"
      "s_nop 7\n"
      "v_max3_f32 v65, v65, v96, v97\n"
      "v_max3_f32 v65, v65, v98, v99\n"
      "v_max3_f32 v65, v65, v100, v101\n"
      "v_max3_f32 v65, v65, v102, v103\n"
      "v_mfma_f32_16x16x32_bf16 v[96:99], v[32:35], a[224:227], 0\n"
      "v_mfma_f32_16x16x32_bf16 v[96:99], v[36:39], a[228:231], v[96:99]\n"
      "v_mfma_f32_16x16x32_bf16 v[96:99], v[40:43], a[232:235], v[96:99]\n"
      "v_mfma_f32_16x16x32_bf16 v[96:99], v[44:47], a[236:239], v[96:99]\n"
      "v_mfma_f32_16x16x32_bf16 v[100:103], v[48:51], a[224:227], 0\n"
      "v_mfma_f32_16x16x32_bf16 v[100:103], v[52:55], a[228:231], v[100:103]\n"
      "v_mfma_f32_16x16x32_bf16 v[100:103], v[56:59], a[232:235], v[100:103]\n"
      "v_mfma_f32_16x16x32_bf16 v[100:103], v[60:63], a[236:239], v[100:103]\n"
      "s_nop 7\n"
      "v_max3_f32 v66, v66, v96, v97\n"
      "v_max3_f32 v66, v66, v98, v99\n"
      "v_max3_f32 v66, v66, v100, v101\n"
      "v_max3_f32 v66, v66, v102, v103\n"
      "v_mfma_f32_16x16x32_bf16 v[96:99], v[32:35], a[240:243], 0\n"
      "v_mfma_f32_16x16x32_bf16 v[96:99], v[36:39], a[244:247], v[96:99]\n"
      "v_mfma_f32_16x16x32_bf16 v[96:99], v[40:43], a[248:251], v[96:99]\n"
      "v_mfma_f32_16x16x32_bf16 v[96:99], v[44:47], a[252:255], v[96:99]\n"
      "v_mfma_f32_16x16x32_bf16 v[100:103], v[48:51], a[240:243], 0\n"
      "v_mfma_f32_16x16x32_bf16 v[100:103], v[52:55], a[244:247], v[100:103]\n"
      "v_mfma_f32_16x16x32_bf16 v[100:103], v[56:59], a[248:251], v[100:103]\n"
      "v_mfma_f32_16x16x32_bf16 v[100:103], v[60:63], a[252:255], v[100:103]\n"
      "s_nop 7\n"
      "v_max3_f32 v67, v67, v96, v97\n"
      "v_max3_f32 v67, v67, v98, v99\n"
      "v_max3_f32 v67, v67, v100, v101\n"
      "v_max3_f32 v67, v67, v102, v103\n"
      "v_mfma_f32_16x16x32_bf16 v[96:99], v[32:35], v[216:219], 0\n"
      "v_mfma_f32_16x16x32_bf16 v[96:99], v[36:39], v[220:223], v[96:99]\n"
      "v_mfma_f32_16x16x32_bf16 v[96:99], v[40:43], v[224:227], v[96:99]\n"
      "v_mfma_f32_16x16x32_bf16 v[96:99], v[44:47], v[228:231], v[96:99]\n"
      "v_mfma_f32_16x16x32_bf16 v[100:103], v[48:51], v[216:219], 0\n"
      "v_mfma_f32_16x16x32_bf16 v[100:103], v[52:55], v[220:223], v[100:103]\n"
      "v_mfma_f32_16x16x32_bf16 v[100:103], v[56:59], v[224:227], v[100:103]\n"
      "v_mfma_f32_16x16x32_bf16 v[100:103], v[60:63], v[228:231], v[100:103]\n"
      "s_nop 7\n"
      "v_max3_f32 v68, v68, v96, v97\n"
      "v_max3_f32 v68, v68, v98, v99\n"
      "v_max3_f32 v68, v68, v100, v101\n"
      "v_max3_f32 v68, v68, v102, v103\n"
      "v_mfma_f32_16x16x32_bf16 v[96:99], v[32:35], v[232:235], 0\n"
      "v_mfma_f32_16x16x32_bf16 v[96:99], v[36:39], v[236:239], v[96:99]\n"
      "v_mfma_f32_16x16x32_bf16 v[96:99], v[40:43], v[240:243], v[96:99]\n"
      "v_mfma_f32_16x16x32_bf16 v[96:99], v[44:47], v[244:247], v[96:99]\n"
      "v_mfma_f32_16x16x32_bf16 v[100:103], v[48:51], v[232:235], 0\n"
      "v_mfma_f32_16x16x32_bf16 v[100:103], v[52:55], v[236:239], v[100:103]\n"
      "v_mfma_f32_16x16x32_bf16 v[100:103], v[56:59], v[240:243], v[100:103]\n"
      "v_mfma_f32_16x16x32_bf16 v[100:103], v[60:63], v[244:247], v[100:103]\n"
      "s_nop 7\n"
      "v_max3_f32 v69, v69, v96, v97\n"
      "v_max3_f32 v69, v69, v98, v99\n"
      "v_max3_f32 v69, v69, v100, v101\n"
      "v_max3_f32 v69, v69, v102, v103\n"
      "s_add_u32 s60, s60, 8192\n"
      "s_cmp_lt_u32 s60, 0x80000\n"
      "s_cbranch_scc1 L_fb_loop_w0\n"
      "v_mov_b32 v29, v64\n"
      "s_nop 1\n"
      "v_permlane16_swap_b32 v64, v29\n"
      "s_nop 0\n"
      "v_max_f32 v64, v64, v29\n"
      "v_mov_b32 v29, v64\n"
      "s_nop 1\n"
      "v_permlane32_swap_b32 v64, v29\n"
      "s_nop 0\n"
      "v_max_f32 v64, v64, v29\n"
      "v_sub_f32 v192, 0, v64\n"
      "v_mov_b32 v193, v192\n"
      "v_mov_b32 v194, v192\n"
      "v_mov_b32 v195, v192\n"
      "v_mov_b32 v29, v65\n"
      "s_nop 1\n"
      "v_permlane16_swap_b32 v65, v29\n"
      "s_nop 0\n"
      "v_max_f32 v65, v65, v29\n"
      "v_mov_b32 v29, v65\n"
      "s_nop 1\n"
      "v_permlane32_swap_b32 v65, v29\n"
      "s_nop 0\n"
      "v_max_f32 v65, v65, v29\n"
      "v_sub_f32 v196, 0, v65\n"
      "v_mov_b32 v197, v196\n"
      "v_mov_b32 v198, v196\n"
      "v_mov_b32 v199, v196\n"
      "v_mov_b32 v29, v66\n"
      "s_nop 1\n"
      "v_permlane16_swap_b32 v66, v29\n"
      "s_nop 0\n"
      "v_max_f32 v66, v66, v29\n"
      "v_mov_b32 v29, v66\n"
      "s_nop 1\n"
      "v_permlane32_swap_b32 v66, v29\n"
      "s_nop 0\n"
      "v_max_f32 v66, v66, v29\n"
      "v_sub_f32 v200, 0, v66\n"
      "v_mov_b32 v201, v200\n"
      "v_mov_b32 v202, v200\n"
      "v_mov_b32 v203, v200\n"
      "v_mov_b32 v29, v67\n"
      "s_nop 1\n"
      "v_permlane16_swap_b32 v67, v29\n"
      "s_nop 0\n"
      "v_max_f32 v67, v67, v29\n"
      "v_mov_b32 v29, v67\n"
      "s_nop 1\n"
      "v_permlane32_swap_b32 v67, v29\n"
      "s_nop 0\n"
      "v_max_f32 v67, v67, v29\n"
      "v_sub_f32 v204, 0, v67\n"
      "v_mov_b32 v205, v204\n"
      "v_mov_b32 v206, v204\n"
      "v_mov_b32 v207, v204\n"
      "v_mov_b32 v29, v68\n"
      "s_nop 1\n"
      "v_permlane16_swap_b32 v68, v29\n"
      "s_nop 0\n"
      "v_max_f32 v68, v68, v29\n"
      "v_mov_b32 v29, v68\n"
      "s_nop 1\n"
      "v_permlane32_swap_b32 v68, v29\n"
      "s_nop 0\n"
      "v_max_f32 v68, v68, v29\n"
      "v_sub_f32 v208, 0, v68\n"
      "v_mov_b32 v209, v208\n"
      "v_mov_b32 v210, v208\n"
      "v_mov_b32 v211, v208\n"
      "v_mov_b32 v29, v69\n"
      "s_nop 1\n"
      "v_permlane16_swap_b32 v69, v29\n"
      "s_nop 0\n"
      "v_max_f32 v69, v69, v29\n"
      "v_mov_b32 v29, v69\n"
      "s_nop 1\n"
      "v_permlane32_swap_b32 v69, v29\n"
      "s_nop 0\n"
      "v_max_f32 v69, v69, v29\n"
      "v_sub_f32 v212, 0, v69\n"
      "v_mov_b32 v213, v212\n"
      "v_mov_b32 v214, v212\n"
      "v_mov_b32 v215, v212\n"
      "v_mov_b32 v64, 0\n"
      "v_mov_b32 v65, 0\n"
      "v_mov_b32 v66, 0\n"
      "v_mov_b32 v67, 0\n"
      "v_mov_b32 v14, 0\n"
      "v_mov_b32 v15, 0\n"
      "v_mov_b32 v16, 0\n"
      "v_mov_b32 v17, 0\n"
      "v_mov_b32 v18, 0\n"
      "v_mov_b32 v19, 0\n"
      "v_mfma_f32_32x32x16_bf16 a[0:15], v[64:67], v[64:67], 0\n"
      "v_mfma_f32_32x32x16_bf16 a[16:31], v[64:67], v[64:67], 0\n"
      "v_mfma_f32_32x32x16_bf16 a[32:47], v[64:67], v[64:67], 0\n"
      "v_mfma_f32_32x32x16_bf16 a[48:63], v[64:67], v[64:67], 0\n"
      "v_mfma_f32_32x32x16_bf16 a[64:79], v[64:67], v[64:67], 0\n"
      "v_mfma_f32_32x32x16_bf16 a[80:95], v[64:67], v[64:67], 0\n"
      "v_mfma_f32_32x32x16_bf16 a[96:111], v[64:67], v[64:67], 0\n"
      "v_mfma_f32_32x32x16_bf16 a[112:127], v[64:67], v[64:67], 0\n"
      "v_mfma_f32_32x32x16_bf16 a[128:143], v[64:67], v[64:67], 0\n"
      "v_mfma_f32_32x32x16_bf16 a[144:159], v[64:67], v[64:67], 0\n"
      "v_mfma_f32_32x32x16_bf16 a[160:175], v[64:67], v[64:67], 0\n"
      "v_mfma_f32_32x32x16_bf16 a[176:191], v[64:67], v[64:67], 0\n"
      "v_xor_b32 v4, 0x8000, v4\n"
      "v_xor_b32 v5, 0x8000, v5\n"
      "s_mov_b32 s58, 0x0\n"
      "s_mov_b32 m0, 0x10000\n"
      "s_nop 0\n"
      "buffer_load_dwordx4 v10, s[44:47], s58 offen offset:0 nt lds\n"
      "buffer_load_dwordx4 v11, s[44:47], s58 offen offset:1024 nt lds\n"
      "buffer_load_dwordx4 v10, s[44:47], s58 offen offset:2048 nt lds\n"
      "buffer_load_dwordx4 v11, s[44:47], s58 offen offset:3072 nt lds\n"
      "s_mov_b32 s58, 0x2000\n"
      "s_mov_b32 m0, 0x12000\n"
      "s_nop 0\n"
      "buffer_load_dwordx4 v10, s[44:47], s58 offen offset:0 nt lds\n"
      "buffer_load_dwordx4 v11, s[44:47], s58 offen offset:1024 nt lds\n"
      "buffer_load_dwordx4 v10, s[44:47], s58 offen offset:2048 nt lds\n"
      "buffer_load_dwordx4 v11, s[44:47], s58 offen offset:3072 nt lds\n"
      "s_mov_b32 s58, 0x4000\n"
      "s_mov_b32 m0, 0x14000\n"
      "s_nop 0\n"
      "buffer_load_dwordx4 v10, s[44:47], s58 offen offset:0 nt lds\n"
      "buffer_load_dwordx4 v11, s[44:47], s58 offen offset:1024 nt lds\n"
      "buffer_load_dwordx4 v10, s[44:47], s58 offen offset:2048 nt lds\n"
      "buffer_load_dwordx4 v11, s[44:47], s58 offen offset:3072 nt lds\n"
      "s_mov_b32 s58, 0x6000\n"
      "s_mov_b32 m0, 0x16000\n"
      "s_nop 0\n"
      "buffer_load_dwordx4 v10, s[44:47], s58 offen offset:0 nt lds\n"
      "buffer_load_dwordx4 v11, s[44:47], s58 offen offset:1024 nt lds\n"
      "buffer_load_dwordx4 v10, s[44:47], s58 offen offset:2048 nt lds\n"
      "buffer_load_dwordx4 v11, s[44:47], s58 offen offset:3072 nt lds\n"
      "s_waitcnt vmcnt(8)\n"
      "s_barrier\n"
      "ds_read_b128 v[32:35], v0 offset:0\n"
      "ds_read_b128 v[36:39], v1 offset:0\n"
      "ds_read_b128 v[40:43], v2 offset:0\n"
      "ds_read_b128 v[44:47], v3 offset:0\n"
      "ds_read_b128 v[48:51], v0 offset:4096\n"
      "ds_read_b128 v[52:55], v1 offset:4096\n"
      "ds_read_b128 v[56:59], v2 offset:4096\n"
      "ds_read_b128 v[60:63], v3 offset:4096\n"
      "s_waitcnt lgkmcnt(0)\n"
      "v_mfma_f32_16x16x32_bf16 v[96:99], v[32:35], a[192:195], v[192:195]\n"
      "v_mfma_f32_16x16x32_bf16 v[96:99], v[36:39], a[196:199], v[96:99]\n"
      "v_mfma_f32_16x16x32_bf16 v[96:99], v[40:43], a[200:203], v[96:99]\n"
      "v_mfma_f32_16x16x32_bf16 v[96:99], v[44:47], a[204:207], v[96:99]\n"
      "v_mfma_f32_16x16x32_bf16 v[100:103], v[48:51], a[192:195], v[192:195]\n"
      "v_mfma_f32_16x16x32_bf16 v[100:103], v[52:55], a[196:199], v[100:103]\n"
      "v_mfma_f32_16x16x32_bf16 v[100:103], v[56:59], a[200:203], v[100:103]\n"
      "v_mfma_f32_16x16x32_bf16 v[100:103], v[60:63], a[204:207], v[100:103]\n"
      "v_mfma_f32_16x16x32_bf16 v[104:107], v[32:35], a[208:211], v[196:199]\n"
      "v_mfma_f32_16x16x32_bf16 v[104:107], v[36:39], a[212:215], v[104:107]\n"
      "v_mfma_f32_16x16x32_bf16 v[104:107], v[40:43], a[216:219], v[104:107]\n"
      "v_mfma_f32_16x16x32_bf16 v[104:107], v[44:47], a[220:223], v[104:107]\n"
      "v_mfma_f32_16x16x32_bf16 v[108:111], v[48:51], a[208:211], v[196:199]\n"
      "v_mfma_f32_16x16x32_bf16 v[108:111], v[52:55], a[212:215], v[108:111]\n"
      "v_mfma_f32_16x16x32_bf16 v[108:111], v[56:59], a[216:219], v[108:111]\n"
      "v_mfma_f32_16x16x32_bf16 v[108:111], v[60:63], a[220:223], v[108:111]\n"
      "v_mfma_f32_16x16x32_bf16 v[112:115], v[32:35], a[224:227], v[200:203]\n"
      "v_mfma_f32_16x16x32_bf16 v[112:115], v[36:39], a[228:231], v[112:115]\n"
      "v_mfma_f32_16x16x32_bf16 v[112:115], v[40:43], a[232:235], v[112:115]\n"
      "v_mfma_f32_16x16x32_bf16 v[112:115], v[44:47], a[236:239], v[112:115]\n"
      "v_mfma_f32_16x16x32_bf16 v[116:119], v[48:51], a[224:227], v[200:203]\n"
      "v_mfma_f32_16x16x32_bf16 v[116:119], v[52:55], a[228:231], v[116:119]\n"
      "v_mfma_f32_16x16x32_bf16 v[116:119], v[56:59], a[232:235], v[116:119]\n"
      "v_mfma_f32_16x16x32_bf16 v[116:119], v[60:63], a[236:239], v[116:119]\n"
      "v_mfma_f32_16x16x32_bf16 v[120:123], v[32:35], a[240:243], v[204:207]\n"
      "v_mfma_f32_16x16x32_bf16 v[120:123], v[36:39], a[244:247], v[120:123]\n"
      "v_mfma_f32_16x16x32_bf16 v[120:123], v[40:43], a[248:251], v[120:123]\n"
      "v_mfma_f32_16x16x32_bf16 v[120:123], v[44:47], a[252:255], v[120:123]\n"
      "v_mfma_f32_16x16x32_bf16 v[124:127], v[48:51], a[240:243], v[204:207]\n"
      "v_mfma_f32_16x16x32_bf16 v[124:127], v[52:55], a[244:247], v[124:127]\n"
      "v_mfma_f32_16x16x32_bf16 v[124:127], v[56:59], a[248:251], v[124:127]\n"
      "v_mfma_f32_16x16x32_bf16 v[124:127], v[60:63], a[252:255], v[124:127]\n"
      "v_mfma_f32_16x16x32_bf16 v[128:131], v[32:35], v[216:219], v[208:211]\n"
      "v_mfma_f32_16x16x32_bf16 v[128:131], v[36:39], v[220:223], v[128:131]\n"
      "v_mfma_f32_16x16x32_bf16 v[128:131], v[40:43], v[224:227], v[128:131]\n"
      "v_mfma_f32_16x16x32_bf16 v[128:131], v[44:47], v[228:231], v[128:131]\n"
      "v_mfma_f32_16x16x32_bf16 v[132:135], v[48:51], v[216:219], v[208:211]\n"
      "v_mfma_f32_16x16x32_bf16 v[132:135], v[52:55], v[220:223], v[132:135]\n"
      "v_mfma_f32_16x16x32_bf16 v[132:135], v[56:59], v[224:227], v[132:135]\n"
      "v_mfma_f32_16x16x32_bf16 v[132:135], v[60:63], v[228:231], v[132:135]\n"
      "v_mfma_f32_16x16x32_bf16 v[176:179], v[32:35], v[232:235], v[212:215]\n"
      "v_mfma_f32_16x16x32_bf16 v[176:179], v[36:39], v[236:239], v[176:179]\n"
      "v_mfma_f32_16x16x32_bf16 v[176:179], v[40:43], v[240:243], v[176:179]\n"
      "v_mfma_f32_16x16x32_bf16 v[176:179], v[44:47], v[244:247], v[176:179]\n"
      "v_mfma_f32_16x16x32_bf16 v[168:171], v[48:51], v[232:235], v[212:215]\n"
      "v_mfma_f32_16x16x32_bf16 v[168:171], v[52:55], v[236:239], v[168:171]\n"
      "v_mfma_f32_16x16x32_bf16 v[168:171], v[56:59], v[240:243], v[168:171]\n"
      "v_mfma_f32_16x16x32_bf16 v[168:171], v[60:63], v[244:247], v[168:171]\n"
      "s_nop 7\n"
      "s_branch L_restart_w0\n"
      "L_path_1:\n"
      "s_mov_b32 s58, 0x14000\n"
      "s_mov_b32 m0, 0xf000\n"
      "s_nop 0\n"
      "buffer_load_dwordx4 v7, s[36:39], s58 offen offset:1024 nt lds\n"
      "s_add_u32 s58, s62, 0\n"
      "s_add_u32 s59, s63, 0\n"
      "s_mov_b32 m0, s59\n"
      "s_nop 0\n"
      "buffer_load_dwordx4 v6, s[36:39], s58 offen offset:0 nt lds\n"
      "buffer_load_dwordx4 v7, s[36:39], s58 offen offset:1024 nt lds\n"
      "buffer_load_dwordx4 v8, s[36:39], s58 offen offset:2048 nt lds\n"
      "buffer_load_dwordx4 v9, s[36:39], s58 offen offset:3072 nt lds\n"
      "s_add_u32 s58, s62, 4096\n"
      "s_add_u32 s59, s63, 4096\n"
      "s_mov_b32 m0, s59\n"
      "s_nop 0\n"
      "buffer_load_dwordx4 v6, s[36:39], s58 offen offset:0 nt lds\n"
      "buffer_load_dwordx4 v7, s[36:39], s58 offen offset:1024 nt lds\n"
      "buffer_load_dwordx4 v8, s[36:39], s58 offen offset:2048 nt lds\n"
      "buffer_load_dwordx4 v9, s[36:39], s58 offen offset:3072 nt lds\n"
      "s_add_u32 s58, s62, 8192\n"
      "s_add_u32 s59, s63, 8192\n"
      "s_mov_b32 m0, s59\n"
      "s_nop 0\n"
      "buffer_load_dwordx4 v6, s[36:39], s58 offen offset:0 nt lds\n"
      "buffer_load_dwordx4 v7, s[36:39], s58 offen offset:1024 nt lds\n"
      "buffer_load_dwordx4 v8, s[36:39], s58 offen offset:2048 nt lds\n"
      "buffer_load_dwordx4 v9, s[36:39], s58 offen offset:3072 nt lds\n"
      "s_mov_b64 s[58:59], %2\n"
      "s_mul_i32 s60, s53, 0x80000\n"
      "s_add_u32 s44, s58, s60\n"
      "s_addc_u32 s59, s59, 0\n"
      "s_and_b32 s45, s59, 0xffff\n"
      "s_mov_b32 s46, 0x80000\n"
      "s_mov_b32 s47, 0x20000\n"
      "s_mov_b64 s[58:59], %3\n"
      "s_mul_i32 s60, s53, 0x15000\n"
      "s_add_u32 s48, s58, s60\n"
      "s_addc_u32 s59, s59, 0\n"
      "s_and_b32 s49, s59, 0xffff\n"
      "s_mov_b32 s50, 0x15000\n"
      "s_mov_b32 s51, 0x20000\n"
      "s_mov_b32 s54, 0x3e0293ee\n"
      "s_mov_b32 s65, 0\n"
      "s_mov_b32 s96, 0x57800000\n"
      "s_mov_b32 s97, 0xffff0000\n"
      "v_lshl_or_b32 v0, v20, 8, v22\n"
      "v_xor_b32 v1, 64, v0\n"
      "v_xor_b32 v2, 128, v0\n"
      "v_xor_b32 v3, 192, v0\n"
      "v_bfe_u32 v23, v13, 2, 2\n"
      "v_and_b32 v24, 3, v13\n"
      "v_lshlrev_b32 v25, 4, v24\n"
      "v_lshl_or_b32 v25, v21, 6, v25\n"
      "v_lshl_or_b32 v10, v23, 8, v25\n"
      "v_xor_b32 v11, 32, v10\n"
      "v_lshlrev_b32 v25, 3, v24\n"
      "v_lshl_or_b32 v25, v23, 6, v25\n"
      "v_lshl_or_b32 v25, v21, 10, v25\n"
      "v_and_b32 v23, 1, v21\n"
      "v_lshl_or_b32 v25, v23, 5, v25\n"
      "v_or_b32 v4, 0x10000, v25\n"
      "v_xor_b32 v5, 32, v4\n"
      "v_lshlrev_b32 v12, 4, v13\n"
      "v_add_u32 v248, s63, v0\n"
      "v_add_u32 v249, s63, v1\n"
      "v_add_u32 v250, s63, v2\n"
      "v_add_u32 v251, s63, v3\n"
      "v_mov_b32 v64, 0\n"
      "v_mov_b32 v65, 0\n"
      "v_mov_b32 v66, 0\n"
      "v_mov_b32 v67, 0\n"
      "v_mov_b32 v14, 0\n"
      "v_mov_b32 v15, 0\n"
      "v_mov_b32 v16, 0\n"
      "v_mov_b32 v17, 0\n"
      "v_mov_b32 v18, 0\n"
      "v_mov_b32 v19, 0\n"
      "v_mfma_f32_32x32x16_bf16 a[0:15], v[64:67], v[64:67], 0\n"
      "v_mfma_f32_32x32x16_bf16 a[16:31], v[64:67], v[64:67], 0\n"
      "v_mfma_f32_32x32x16_bf16 a[32:47], v[64:67], v[64:67], 0\n"
      "v_mfma_f32_32x32x16_bf16 a[48:63], v[64:67], v[64:67], 0\n"
      "v_mfma_f32_32x32x16_bf16 a[64:79], v[64:67], v[64:67], 0\n"
      "v_mfma_f32_32x32x16_bf16 a[80:95], v[64:67], v[64:67], 0\n"
      "v_mfma_f32_32x32x16_bf16 a[96:111], v[64:67], v[64:67], 0\n"
      "v_mfma_f32_32x32x16_bf16 a[112:127], v[64:67], v[64:67], 0\n"
      "v_mfma_f32_32x32x16_bf16 a[128:143], v[64:67], v[64:67], 0\n"
      "v_mfma_f32_32x32x16_bf16 a[144:159], v[64:67], v[64:67], 0\n"
      "v_mfma_f32_32x32x16_bf16 a[160:175], v[64:67], v[64:67], 0\n"
      "v_mfma_f32_32x32x16_bf16 a[176:191], v[64:67], v[64:67], 0\n"
      "s_waitcnt vmcnt(8)\n"
      "ds_read_b128 v[136:139], v248 offset:0\n"
      "ds_read_b128 v[140:143], v249 offset:0\n"
      "ds_read_b128 v[144:147], v250 offset:0\n"
      "ds_read_b128 v[148:151], v251 offset:0\n"
      "s_add_u32 s58, s62, 12288\n"
      "s_add_u32 s59, s63, 12288\n"
      "s_mov_b32 m0, s59\n"
      "s_nop 0\n"
      "buffer_load_dwordx4 v6, s[36:39], s58 offen offset:0 nt lds\n"
      "buffer_load_dwordx4 v7, s[36:39], s58 offen offset:1024 nt lds\n"
      "buffer_load_dwordx4 v8, s[36:39], s58 offen offset:2048 nt lds\n"
      "buffer_load_dwordx4 v9, s[36:39], s58 offen offset:3072 nt lds\n"
      "s_waitcnt lgkmcnt(0)\n"
      "v_lshlrev_b32 v20, 16, v136\n"
      "v_and_b32 v21, s97, v136\n"
      "v_lshlrev_b32 v22, 16, v137\n"
      "v_and_b32 v23, s97, v137\n"
      "v_lshlrev_b32 v24, 16, v138\n"
      "v_and_b32 v25, s97, v138\n"
      "v_lshlrev_b32 v26, 16, v139\n"
      "v_and_b32 v27, s97, v139\n"
      "v_pk_mul_f32 v[20:21], v[20:21], s[54:55] op_sel_hi:[1,0]\n"
      "v_pk_mul_f32 v[22:23], v[22:23], s[54:55] op_sel_hi:[1,0]\n"
      "v_pk_mul_f32 v[24:25], v[24:25], s[54:55] op_sel_hi:[1,0]\n"
      "v_pk_mul_f32 v[26:27], v[26:27], s[54:55] op_sel_hi:[1,0]\n"
      "v_cvt_pk_bf16_f32 v136, v20, v21\n"
      "v_cvt_pk_bf16_f32 v137, v22, v23\n"
      "v_cvt_pk_bf16_f32 v138, v24, v25\n"
      "v_cvt_pk_bf16_f32 v139, v26, v27\n"
      "v_accvgpr_write_b32 a192, v136\n"
      "v_accvgpr_write_b32 a193, v137\n"
      "v_accvgpr_write_b32 a194, v138\n"
      "v_accvgpr_write_b32 a195, v139\n"
      "v_lshlrev_b32 v20, 16, v140\n"
      "v_and_b32 v21, s97, v140\n"
      "v_lshlrev_b32 v22, 16, v141\n"
      "v_and_b32 v23, s97, v141\n"
      "v_lshlrev_b32 v24, 16, v142\n"
      "v_and_b32 v25, s97, v142\n"
      "v_lshlrev_b32 v26, 16, v143\n"
      "v_and_b32 v27, s97, v143\n"
      "v_pk_mul_f32 v[20:21], v[20:21], s[54:55] op_sel_hi:[1,0]\n"
      "v_pk_mul_f32 v[22:23], v[22:23], s[54:55] op_sel_hi:[1,0]\n"
      "v_pk_mul_f32 v[24:25], v[24:25], s[54:55] op_sel_hi:[1,0]\n"
      "v_pk_mul_f32 v[26:27], v[26:27], s[54:55] op_sel_hi:[1,0]\n"
      "v_cvt_pk_bf16_f32 v140, v20, v21\n"
      "v_cvt_pk_bf16_f32 v141, v22, v23\n"
      "v_cvt_pk_bf16_f32 v142, v24, v25\n"
      "v_cvt_pk_bf16_f32 v143, v26, v27\n"
      "v_accvgpr_write_b32 a196, v140\n"
      "v_accvgpr_write_b32 a197, v141\n"
      "v_accvgpr_write_b32 a198, v142\n"
      "v_accvgpr_write_b32 a199, v143\n"
      "v_lshlrev_b32 v20, 16, v144\n"
      "v_and_b32 v21, s97, v144\n"
      "v_lshlrev_b32 v22, 16, v145\n"
      "v_and_b32 v23, s97, v145\n"
      "v_lshlrev_b32 v24, 16, v146\n"
      "v_and_b32 v25, s97, v146\n"
      "v_lshlrev_b32 v26, 16, v147\n"
      "v_and_b32 v27, s97, v147\n"
      "v_pk_mul_f32 v[20:21], v[20:21], s[54:55] op_sel_hi:[1,0]\n"
      "v_pk_mul_f32 v[22:23], v[22:23], s[54:55] op_sel_hi:[1,0]\n"
      "v_pk_mul_f32 v[24:25], v[24:25], s[54:55] op_sel_hi:[1,0]\n"
      "v_pk_mul_f32 v[26:27], v[26:27], s[54:55] op_sel_hi:[1,0]\n"
      "v_cvt_pk_bf16_f32 v144, v20, v21\n"
      "v_cvt_pk_bf16_f32 v145, v22, v23\n"
      "v_cvt_pk_bf16_f32 v146, v24, v25\n"
      "v_cvt_pk_bf16_f32 v147, v26, v27\n"
      "v_accvgpr_write_b32 a200, v144\n"
      "v_accvgpr_write_b32 a201, v145\n"
      "v_accvgpr_write_b32 a202, v146\n"
      "v_accvgpr_write_b32 a203, v147\n"
      "v_lshlrev_b32 v20, 16, v148\n"
      "v_and_b32 v21, s97, v148\n"
      "v_lshlrev_b32 v22, 16, v149\n"
      "v_and_b32 v23, s97, v149\n"
      "v_lshlrev_b32 v24, 16, v150\n"
      "v_and_b32 v25, s97, v150\n"
      "v_lshlrev_b32 v26, 16, v151\n"
      "v_and_b32 v27, s97, v151\n"
      "v_pk_mul_f32 v[20:21], v[20:21], s[54:55] op_sel_hi:[1,0]\n"
      "v_pk_mul_f32 v[22:23], v[22:23], s[54:55] op_sel_hi:[1,0]\n"
      "v_pk_mul_f32 v[24:25], v[24:25], s[54:55] op_sel_hi:[1,0]\n"
      "v_pk_mul_f32 v[26:27], v[26:27], s[54:55] op_sel_hi:[1,0]\n"
      "v_cvt_pk_bf16_f32 v148, v20, v21\n"
      "v_cvt_pk_bf16_f32 v149, v22, v23\n"
      "v_cvt_pk_bf16_f32 v150, v24, v25\n"
      "v_cvt_pk_bf16_f32 v151, v26, v27\n"
      "v_accvgpr_write_b32 a204, v148\n"
      "v_accvgpr_write_b32 a205, v149\n"
      "v_accvgpr_write_b32 a206, v150\n"
      "v_accvgpr_write_b32 a207, v151\n"
      "s_waitcnt vmcnt(8)\n"
      "ds_read_b128 v[152:155], v248 offset:4096\n"
      "ds_read_b128 v[156:159], v249 offset:4096\n"
      "ds_read_b128 v[160:163], v250 offset:4096\n"
      "ds_read_b128 v[164:167], v251 offset:4096\n"
      "s_add_u32 s58, s62, 16384\n"
      "s_add_u32 s59, s63, 16384\n"
      "s_mov_b32 m0, s59\n"
      "s_nop 0\n"
      "buffer_load_dwordx4 v6, s[36:39], s58 offen offset:0 nt lds\n"
      "buffer_load_dwordx4 v7, s[36:39], s58 offen offset:1024 nt lds\n"
      "buffer_load_dwordx4 v8, s[36:39], s58 offen offset:2048 nt lds\n"
      "buffer_load_dwordx4 v9, s[36:39], s58 offen offset:3072 nt lds\n"
      "s_waitcnt lgkmcnt(0)\n"
      "v_lshlrev_b32 v20, 16, v152\n"
      "v_and_b32 v21, s97, v152\n"
      "v_lshlrev_b32 v22, 16, v153\n"
      "v_and_b32 v23, s97, v153\n"
      "v_lshlrev_b32 v24, 16, v154\n"
      "v_and_b32 v25, s97, v154\n"
      "v_lshlrev_b32 v26, 16, v155\n"
      "v_and_b32 v27, s97, v155\n"
      "v_pk_mul_f32 v[20:21], v[20:21], s[54:55] op_sel_hi:[1,0]\n"
      "v_pk_mul_f32 v[22:23], v[22:23], s[54:55] op_sel_hi:[1,0]\n"
      "v_pk_mul_f32 v[24:25], v[24:25], s[54:55] op_sel_hi:[1,0]\n"
      "v_pk_mul_f32 v[26:27], v[26:27], s[54:55] op_sel_hi:[1,0]\n"
      "v_cvt_pk_bf16_f32 v152, v20, v21\n"
      "v_cvt_pk_bf16_f32 v153, v22, v23\n"
      "v_cvt_pk_bf16_f32 v154, v24, v25\n"
      "v_cvt_pk_bf16_f32 v155, v26, v27\n"
      "v_accvgpr_write_b32 a208, v152\n"
      "v_accvgpr_write_b32 a209, v153\n"
      "v_accvgpr_write_b32 a210, v154\n"
      "v_accvgpr_write_b32 a211, v155\n"
      "v_lshlrev_b32 v20, 16, v156\n"
      "v_and_b32 v21, s97, v156\n"
      "v_lshlrev_b32 v22, 16, v157\n"
      "v_and_b32 v23, s97, v157\n"
      "v_lshlrev_b32 v24, 16, v158\n"
      "v_and_b32 v25, s97, v158\n"
      "v_lshlrev_b32 v26, 16, v159\n"
      "v_and_b32 v27, s97, v159\n"
      "v_pk_mul_f32 v[20:21], v[20:21], s[54:55] op_sel_hi:[1,0]\n"
      "v_pk_mul_f32 v[22:23], v[22:23], s[54:55] op_sel_hi:[1,0]\n"
      "v_pk_mul_f32 v[24:25], v[24:25], s[54:55] op_sel_hi:[1,0]\n"
      "v_pk_mul_f32 v[26:27], v[26:27], s[54:55] op_sel_hi:[1,0]\n"
      "v_cvt_pk_bf16_f32 v156, v20, v21\n"
      "v_cvt_pk_bf16_f32 v157, v22, v23\n"
      "v_cvt_pk_bf16_f32 v158, v24, v25\n"
      "v_cvt_pk_bf16_f32 v159, v26, v27\n"
      "v_accvgpr_write_b32 a212, v156\n"
      "v_accvgpr_write_b32 a213, v157\n"
      "v_accvgpr_write_b32 a214, v158\n"
      "v_accvgpr_write_b32 a215, v159\n"
      "v_lshlrev_b32 v20, 16, v160\n"
      "v_and_b32 v21, s97, v160\n"
      "v_lshlrev_b32 v22, 16, v161\n"
      "v_and_b32 v23, s97, v161\n"
      "v_lshlrev_b32 v24, 16, v162\n"
      "v_and_b32 v25, s97, v162\n"
      "v_lshlrev_b32 v26, 16, v163\n"
      "v_and_b32 v27, s97, v163\n"
      "v_pk_mul_f32 v[20:21], v[20:21], s[54:55] op_sel_hi:[1,0]\n"
      "v_pk_mul_f32 v[22:23], v[22:23], s[54:55] op_sel_hi:[1,0]\n"
      "v_pk_mul_f32 v[24:25], v[24:25], s[54:55] op_sel_hi:[1,0]\n"
      "v_pk_mul_f32 v[26:27], v[26:27], s[54:55] op_sel_hi:[1,0]\n"
      "v_cvt_pk_bf16_f32 v160, v20, v21\n"
      "v_cvt_pk_bf16_f32 v161, v22, v23\n"
      "v_cvt_pk_bf16_f32 v162, v24, v25\n"
      "v_cvt_pk_bf16_f32 v163, v26, v27\n"
      "v_accvgpr_write_b32 a216, v160\n"
      "v_accvgpr_write_b32 a217, v161\n"
      "v_accvgpr_write_b32 a218, v162\n"
      "v_accvgpr_write_b32 a219, v163\n"
      "v_lshlrev_b32 v20, 16, v164\n"
      "v_and_b32 v21, s97, v164\n"
      "v_lshlrev_b32 v22, 16, v165\n"
      "v_and_b32 v23, s97, v165\n"
      "v_lshlrev_b32 v24, 16, v166\n"
      "v_and_b32 v25, s97, v166\n"
      "v_lshlrev_b32 v26, 16, v167\n"
      "v_and_b32 v27, s97, v167\n"
      "v_pk_mul_f32 v[20:21], v[20:21], s[54:55] op_sel_hi:[1,0]\n"
      "v_pk_mul_f32 v[22:23], v[22:23], s[54:55] op_sel_hi:[1,0]\n"
      "v_pk_mul_f32 v[24:25], v[24:25], s[54:55] op_sel_hi:[1,0]\n"
      "v_pk_mul_f32 v[26:27], v[26:27], s[54:55] op_sel_hi:[1,0]\n"
      "v_cvt_pk_bf16_f32 v164, v20, v21\n"
      "v_cvt_pk_bf16_f32 v165, v22, v23\n"
      "v_cvt_pk_bf16_f32 v166, v24, v25\n"
      "v_cvt_pk_bf16_f32 v167, v26, v27\n"
      "v_accvgpr_write_b32 a220, v164\n"
      "v_accvgpr_write_b32 a221, v165\n"
      "v_accvgpr_write_b32 a222, v166\n"
      "v_accvgpr_write_b32 a223, v167\n"
      "s_waitcnt vmcnt(8)\n"
      "ds_read_b128 v[136:139], v248 offset:8192\n"
      "ds_read_b128 v[140:143], v249 offset:8192\n"
      "ds_read_b128 v[144:147], v250 offset:8192\n"
      "ds_read_b128 v[148:151], v251 offset:8192\n"
      "s_mov_b32 s58, 0x1000\n"
      "s_mov_b32 m0, 0x11000\n"
      "s_nop 0\n"
      "buffer_load_dwordx4 v10, s[44:47], s58 offen offset:0 nt lds\n"
      "buffer_load_dwordx4 v11, s[44:47], s58 offen offset:1024 nt lds\n"
      "buffer_load_dwordx4 v10, s[44:47], s58 offen offset:2048 nt lds\n"
      "buffer_load_dwordx4 v11, s[44:47], s58 offen offset:3072 nt lds\n"
      "s_waitcnt lgkmcnt(0)\n"
      "v_lshlrev_b32 v20, 16, v136\n"
      "v_and_b32 v21, s97, v136\n"
      "v_lshlrev_b32 v22, 16, v137\n"
      "v_and_b32 v23, s97, v137\n"
      "v_lshlrev_b32 v24, 16, v138\n"
      "v_and_b32 v25, s97, v138\n"
      "v_lshlrev_b32 v26, 16, v139\n"
      "v_and_b32 v27, s97, v139\n"
      "v_pk_mul_f32 v[20:21], v[20:21], s[54:55] op_sel_hi:[1,0]\n"
      "v_pk_mul_f32 v[22:23], v[22:23], s[54:55] op_sel_hi:[1,0]\n"
      "v_pk_mul_f32 v[24:25], v[24:25], s[54:55] op_sel_hi:[1,0]\n"
      "v_pk_mul_f32 v[26:27], v[26:27], s[54:55] op_sel_hi:[1,0]\n"
      "v_cvt_pk_bf16_f32 v136, v20, v21\n"
      "v_cvt_pk_bf16_f32 v137, v22, v23\n"
      "v_cvt_pk_bf16_f32 v138, v24, v25\n"
      "v_cvt_pk_bf16_f32 v139, v26, v27\n"
      "v_accvgpr_write_b32 a224, v136\n"
      "v_accvgpr_write_b32 a225, v137\n"
      "v_accvgpr_write_b32 a226, v138\n"
      "v_accvgpr_write_b32 a227, v139\n"
      "v_lshlrev_b32 v20, 16, v140\n"
      "v_and_b32 v21, s97, v140\n"
      "v_lshlrev_b32 v22, 16, v141\n"
      "v_and_b32 v23, s97, v141\n"
      "v_lshlrev_b32 v24, 16, v142\n"
      "v_and_b32 v25, s97, v142\n"
      "v_lshlrev_b32 v26, 16, v143\n"
      "v_and_b32 v27, s97, v143\n"
      "v_pk_mul_f32 v[20:21], v[20:21], s[54:55] op_sel_hi:[1,0]\n"
      "v_pk_mul_f32 v[22:23], v[22:23], s[54:55] op_sel_hi:[1,0]\n"
      "v_pk_mul_f32 v[24:25], v[24:25], s[54:55] op_sel_hi:[1,0]\n"
      "v_pk_mul_f32 v[26:27], v[26:27], s[54:55] op_sel_hi:[1,0]\n"
      "v_cvt_pk_bf16_f32 v140, v20, v21\n"
      "v_cvt_pk_bf16_f32 v141, v22, v23\n"
      "v_cvt_pk_bf16_f32 v142, v24, v25\n"
      "v_cvt_pk_bf16_f32 v143, v26, v27\n"
      "v_accvgpr_write_b32 a228, v140\n"
      "v_accvgpr_write_b32 a229, v141\n"
      "v_accvgpr_write_b32 a230, v142\n"
      "v_accvgpr_write_b32 a231, v143\n"
      "v_lshlrev_b32 v20, 16, v144\n"
      "v_and_b32 v21, s97, v144\n"
      "v_lshlrev_b32 v22, 16, v145\n"
      "v_and_b32 v23, s97, v145\n"
      "v_lshlrev_b32 v24, 16, v146\n"
      "v_and_b32 v25, s97, v146\n"
      "v_lshlrev_b32 v26, 16, v147\n"
      "v_and_b32 v27, s97, v147\n"
      "v_pk_mul_f32 v[20:21], v[20:21], s[54:55] op_sel_hi:[1,0]\n"
      "v_pk_mul_f32 v[22:23], v[22:23], s[54:55] op_sel_hi:[1,0]\n"
      "v_pk_mul_f32 v[24:25], v[24:25], s[54:55] op_sel_hi:[1,0]\n"
      "v_pk_mul_f32 v[26:27], v[26:27], s[54:55] op_sel_hi:[1,0]\n"
      "v_cvt_pk_bf16_f32 v144, v20, v21\n"
      "v_cvt_pk_bf16_f32 v145, v22, v23\n"
      "v_cvt_pk_bf16_f32 v146, v24, v25\n"
      "v_cvt_pk_bf16_f32 v147, v26, v27\n"
      "v_accvgpr_write_b32 a232, v144\n"
      "v_accvgpr_write_b32 a233, v145\n"
      "v_accvgpr_write_b32 a234, v146\n"
      "v_accvgpr_write_b32 a235, v147\n"
      "v_lshlrev_b32 v20, 16, v148\n"
      "v_and_b32 v21, s97, v148\n"
      "v_lshlrev_b32 v22, 16, v149\n"
      "v_and_b32 v23, s97, v149\n"
      "v_lshlrev_b32 v24, 16, v150\n"
      "v_and_b32 v25, s97, v150\n"
      "v_lshlrev_b32 v26, 16, v151\n"
      "v_and_b32 v27, s97, v151\n"
      "v_pk_mul_f32 v[20:21], v[20:21], s[54:55] op_sel_hi:[1,0]\n"
      "v_pk_mul_f32 v[22:23], v[22:23], s[54:55] op_sel_hi:[1,0]\n"
      "v_pk_mul_f32 v[24:25], v[24:25], s[54:55] op_sel_hi:[1,0]\n"
      "v_pk_mul_f32 v[26:27], v[26:27], s[54:55] op_sel_hi:[1,0]\n"
      "v_cvt_pk_bf16_f32 v148, v20, v21\n"
      "v_cvt_pk_bf16_f32 v149, v22, v23\n"
      "v_cvt_pk_bf16_f32 v150, v24, v25\n"
      "v_cvt_pk_bf16_f32 v151, v26, v27\n"
      "v_accvgpr_write_b32 a236, v148\n"
      "v_accvgpr_write_b32 a237, v149\n"
      "v_accvgpr_write_b32 a238, v150\n"
      "v_accvgpr_write_b32 a239, v151\n"
      "s_waitcnt vmcnt(24)\n"
      "s_barrier\n"
      "ds_read_b128 v[152:155], v0 offset:61440\n"
      "ds_read_b128 v[156:159], v1 offset:61440\n"
      "ds_read_b128 v[160:163], v2 offset:61440\n"
      "ds_read_b128 v[164:167], v3 offset:61440\n"
      "ds_read_b128 v[32:35], v0 offset:0\n"
      "ds_read_b128 v[36:39], v1 offset:0\n"
      "ds_read_b128 v[40:43], v2 offset:0\n"
      "ds_read_b128 v[44:47], v3 offset:0\n"
      "ds_read_b128 v[48:51], v0 offset:4096\n"
      "ds_read_b128 v[52:55], v1 offset:4096\n"
      "ds_read_b128 v[56:59], v2 offset:4096\n"
      "ds_read_b128 v[60:63], v3 offset:4096\n"
      "s_mov_b32 s58, 0x3000\n"
      "s_mov_b32 m0, 0x13000\n"
      "s_nop 0\n"
      "buffer_load_dwordx4 v10, s[44:47], s58 offen offset:0 nt lds\n"
      "buffer_load_dwordx4 v11, s[44:47], s58 offen offset:1024 nt lds\n"
      "buffer_load_dwordx4 v10, s[44:47], s58 offen offset:2048 nt lds\n"
      "buffer_load_dwordx4 v11, s[44:47], s58 offen offset:3072 nt lds\n"
      "s_waitcnt lgkmcnt(0)\n"
      "v_mfma_f32_16x16x32_bf16 v[96:99], v[32:35], a[192:195], 0\n"
      "v_lshlrev_b32 v20, 16, v152\n"
      "v_and_b32 v21, s97, v152\n"
      "v_lshlrev_b32 v22, 16, v153\n"
      "v_mfma_f32_16x16x32_bf16 v[96:99], v[36:39], a[196:199], v[96:99]\n"
      "v_and_b32 v23, s97, v153\n"
      "v_lshlrev_b32 v24, 16, v154\n"
      "v_and_b32 v25, s97, v154\n"
      "v_mfma_f32_16x16x32_bf16 v[96:99], v[40:43], a[200:203], v[96:99]\n"
      "v_lshlrev_b32 v26, 16, v155\n"
      "v_and_b32 v27, s97, v155\n"
      "v_pk_mul_f32 v[20:21], v[20:21], s[54:55] op_sel_hi:[1,0]\n"
      "v_mfma_f32_16x16x32_bf16 v[96:99], v[44:47], a[204:207], v[96:99]\n"
      "v_pk_mul_f32 v[22:23], v[22:23], s[54:55] op_sel_hi:[1,0]\n"
      "v_pk_mul_f32 v[24:25], v[24:25], s[54:55] op_sel_hi:[1,0]\n"
      "v_pk_mul_f32 v[26:27], v[26:27], s[54:55] op_sel_hi:[1,0]\n"
      "v_mfma_f32_16x16x32_bf16 v[100:103], v[48:51], a[192:195], 0\n"
      "v_cvt_pk_bf16_f32 v232, v20, v21\n"
      "v_cvt_pk_bf16_f32 v233, v22, v23\n"
      "v_cvt_pk_bf16_f32 v234, v24, v25\n"
      "v_mfma_f32_16x16x32_bf16 v[100:103], v[52:55], a[196:199], v[100:103]\n"
      "v_cvt_pk_bf16_f32 v235, v26, v27\n"
      "v_lshlrev_b32 v20, 16, v156\n"
      "v_and_b32 v21, s97, v156\n"
      "v_mfma_f32_16x16x32_bf16 v[100:103], v[56:59], a[200:203], v[100:103]\n"
      "v_lshlrev_b32 v22, 16, v157\n"
      "v_and_b32 v23, s97, v157\n"
      "v_lshlrev_b32 v24, 16, v158\n"
      "v_mfma_f32_16x16x32_bf16 v[100:103], v[60:63], a[204:207], v[100:103]\n"
      "v_and_b32 v25, s97, v158\n"
      "v_lshlrev_b32 v26, 16, v159\n"
      "v_and_b32 v27, s97, v159\n"
      "v_mfma_f32_16x16x32_bf16 v[104:107], v[32:35], a[208:211], 0\n"
      "v_pk_mul_f32 v[20:21], v[20:21], s[54:55] op_sel_hi:[1,0]\n"
      "v_pk_mul_f32 v[22:23], v[22:23], s[54:55] op_sel_hi:[1,0]\n"
      "v_pk_mul_f32 v[24:25], v[24:25], s[54:55] op_sel_hi:[1,0]\n"
      "v_mfma_f32_16x16x32_bf16 v[104:107], v[36:39], a[212:215], v[104:107]\n"
      "v_pk_mul_f32 v[26:27], v[26:27], s[54:55] op_sel_hi:[1,0]\n"
      "v_cvt_pk_bf16_f32 v236, v20, v21\n"
      "v_cvt_pk_bf16_f32 v237, v22, v23\n"
      "v_mfma_f32_16x16x32_bf16 v[104:107], v[40:43], a[216:219], v[104:107]\n"
      "v_cvt_pk_bf16_f32 v238, v24, v25\n"
      "v_cvt_pk_bf16_f32 v239, v26, v27\n"
      "v_lshlrev_b32 v20, 16, v160\n"
      "v_mfma_f32_16x16x32_bf16 v[104:107], v[44:47], a[220:223], v[104:107]\n"
      "v_and_b32 v21, s97, v160\n"
      "v_lshlrev_b32 v22, 16, v161\n"
      "v_and_b32 v23, s97, v161\n"
      "v_mfma_f32_16x16x32_bf16 v[108:111], v[48:51], a[208:211], 0\n"
      "v_lshlrev_b32 v24, 16, v162\n"
      "v_and_b32 v25, s97, v162\n"
      "v_lshlrev_b32 v26, 16, v163\n"
      "v_mfma_f32_16x16x32_bf16 v[108:111], v[52:55], a[212:215], v[108:111]\n"
      "v_and_b32 v27, s97, v163\n"
      "v_pk_mul_f32 v[20:21], v[20:21], s[54:55] op_sel_hi:[1,0]\n"
      "v_pk_mul_f32 v[22:23], v[22:23], s[54:55] op_sel_hi:[1,0]\n"
      "v_mfma_f32_16x16x32_bf16 v[108:111], v[56:59], a[216:219], v[108:111]\n"
      "v_pk_mul_f32 v[24:25], v[24:25], s[54:55] op_sel_hi:[1,0]\n"
      "v_pk_mul_f32 v[26:27], v[26:27], s[54:55] op_sel_hi:[1,0]\n"
      "v_cvt_pk_bf16_f32 v240, v20, v21\n"
      "v_mfma_f32_16x16x32_bf16 v[108:111], v[60:63], a[220:223], v[108:111]\n"
      "v_cvt_pk_bf16_f32 v241, v22, v23\n"
      "v_cvt_pk_bf16_f32 v242, v24, v25\n"
      "v_cvt_pk_bf16_f32 v243, v26, v27\n"
      "v_mfma_f32_16x16x32_bf16 v[112:115], v[32:35], a[224:227], 0\n"
      "v_lshlrev_b32 v20, 16, v164\n"
      "v_and_b32 v21, s97, v164\n"
      "v_lshlrev_b32 v22, 16, v165\n"
      "v_mfma_f32_16x16x32_bf16 v[112:115], v[36:39], a[228:231], v[112:115]\n"
      "v_and_b32 v23, s97, v165\n"
      "v_lshlrev_b32 v24, 16, v166\n"
      "v_and_b32 v25, s97, v166\n"
      "v_mfma_f32_16x16x32_bf16 v[112:115], v[40:43], a[232:235], v[112:115]\n"
      "v_lshlrev_b32 v26, 16, v167\n"
      "v_and_b32 v27, s97, v167\n"
      "v_pk_mul_f32 v[20:21], v[20:21], s[54:55] op_sel_hi:[1,0]\n"
      "v_mfma_f32_16x16x32_bf16 v[112:115], v[44:47], a[236:239], v[112:115]\n"
      "v_pk_mul_f32 v[22:23], v[22:23], s[54:55] op_sel_hi:[1,0]\n"
      "v_pk_mul_f32 v[24:25], v[24:25], s[54:55] op_sel_hi:[1,0]\n"
      "v_pk_mul_f32 v[26:27], v[26:27], s[54:55] op_sel_hi:[1,0]\n"
      "v_mfma_f32_16x16x32_bf16 v[116:119], v[48:51], a[224:227], 0\n"
      "v_cvt_pk_bf16_f32 v244, v20, v21\n"
      "v_cvt_pk_bf16_f32 v245, v22, v23\n"
      "v_cvt_pk_bf16_f32 v246, v24, v25\n"
      "v_mfma_f32_16x16x32_bf16 v[116:119], v[52:55], a[228:231], v[116:119]\n"
      "v_cvt_pk_bf16_f32 v247, v26, v27\n"
      "v_mfma_f32_16x16x32_bf16 v[116:119], v[56:59], a[232:235], v[116:119]\n"
      "v_mfma_f32_16x16x32_bf16 v[116:119], v[60:63], a[236:239], v[116:119]\n"
      "s_nop 1\n"
      "v_mfma_f32_16x16x32_bf16 v[168:171], v[32:35], v[232:235], 0\n"
      "v_mfma_f32_16x16x32_bf16 v[168:171], v[36:39], v[236:239], v[168:171]\n"
      "v_mfma_f32_16x16x32_bf16 v[168:171], v[40:43], v[240:243], v[168:171]\n"
      "v_mfma_f32_16x16x32_bf16 v[168:171], v[44:47], v[244:247], v[168:171]\n"
      "v_mfma_f32_16x16x32_bf16 v[176:179], v[48:51], v[232:235], 0\n"
      "v_mfma_f32_16x16x32_bf16 v[176:179], v[52:55], v[236:239], v[176:179]\n"
      "v_mfma_f32_16x16x32_bf16 v[176:179], v[56:59], v[240:243], v[176:179]\n"
      "v_mfma_f32_16x16x32_bf16 v[176:179], v[60:63], v[244:247], v[176:179]\n"
      "v_max3_f32 v28, v96, v97, v98\n"
      "v_max3_f32 v29, v99, v100, v101\n"
      "v_max3_f32 v28, v28, v102, v103\n"
      "v_max_f32 v28, v28, v29\n"
      "v_mov_b32 v29, v28\n"
      "s_nop 1\n"
      "v_permlane16_swap_b32 v28, v29\n"
      "s_nop 0\n"
      "v_max_f32 v28, v28, v29\n"
      "v_mov_b32 v29, v28\n"
      "s_nop 1\n"
      "v_permlane32_swap_b32 v28, v29\n"
      "s_nop 0\n"
      "v_max_f32 v28, v28, v29\n"
      "v_sub_f32 v192, 0, v28\n"
      "v_mov_b32 v193, v192\n"
      "v_mov_b32 v194, v192\n"
      "v_mov_b32 v195, v192\n"
      "v_add_f32 v96, v96, v192\n"
      "v_add_f32 v97, v97, v192\n"
      "v_add_f32 v98, v98, v192\n"
      "v_add_f32 v99, v99, v192\n"
      "v_add_f32 v100, v100, v192\n"
      "v_add_f32 v101, v101, v192\n"
      "v_add_f32 v102, v102, v192\n"
      "v_add_f32 v103, v103, v192\n"
      "v_max3_f32 v28, v104, v105, v106\n"
      "v_max3_f32 v29, v107, v108, v109\n"
      "v_max3_f32 v28, v28, v110, v111\n"
      "v_max_f32 v28, v28, v29\n"
      "v_mov_b32 v29, v28\n"
      "s_nop 1\n"
      "v_permlane16_swap_b32 v28, v29\n"
      "s_nop 0\n"
      "v_max_f32 v28, v28, v29\n"
      "v_mov_b32 v29, v28\n"
      "s_nop 1\n"
      "v_permlane32_swap_b32 v28, v29\n"
      "s_nop 0\n"
      "v_max_f32 v28, v28, v29\n"
      "v_sub_f32 v196, 0, v28\n"
      "v_mov_b32 v197, v196\n"
      "v_mov_b32 v198, v196\n"
      "v_mov_b32 v199, v196\n"
      "v_add_f32 v104, v104, v196\n"
      "v_add_f32 v105, v105, v196\n"
      "v_add_f32 v106, v106, v196\n"
      "v_add_f32 v107, v107, v196\n"
      "v_add_f32 v108, v108, v196\n"
      "v_add_f32 v109, v109, v196\n"
      "v_add_f32 v110, v110, v196\n"
      "v_add_f32 v111, v111, v196\n"
      "v_max3_f32 v28, v112, v113, v114\n"
      "v_max3_f32 v29, v115, v116, v117\n"
      "v_max3_f32 v28, v28, v118, v119\n"
      "v_max_f32 v28, v28, v29\n"
      "v_mov_b32 v29, v28\n"
      "s_nop 1\n"
      "v_permlane16_swap_b32 v28, v29\n"
      "s_nop 0\n"
      "v_max_f32 v28, v28, v29\n"
      "v_mov_b32 v29, v28\n"
      "s_nop 1\n"
      "v_permlane32_swap_b32 v28, v29\n"
      "s_nop 0\n"
      "v_max_f32 v28, v28, v29\n"
      "v_sub_f32 v200, 0, v28\n"
      "v_mov_b32 v201, v200\n"
      "v_mov_b32 v202, v200\n"
      "v_mov_b32 v203, v200\n"
      "v_add_f32 v112, v112, v200\n"
      "v_add_f32 v113, v113, v200\n"
      "v_add_f32 v114, v114, v200\n"
      "v_add_f32 v115, v115, v200\n"
      "v_add_f32 v116, v116, v200\n"
      "v_add_f32 v117, v117, v200\n"
      "s_waitcnt vmcnt(12)\n"
      "ds_read_b128 v[136:139], v248 offset:12288\n"
      "ds_read_b128 v[140:143], v249 offset:12288\n"
      "ds_read_b128 v[144:147], v250 offset:12288\n"
      "ds_read_b128 v[148:151], v251 offset:12288\n"
      "s_mov_b32 s58, 0x5000\n"
      "s_mov_b32 m0, 0x15000\n"
      "s_nop 0\n"
      "buffer_load_dwordx4 v10, s[44:47], s58 offen offset:0 nt lds\n"
      "buffer_load_dwordx4 v11, s[44:47], s58 offen offset:1024 nt lds\n"
      "buffer_load_dwordx4 v10, s[44:47], s58 offen offset:2048 nt lds\n"
      "buffer_load_dwordx4 v11, s[44:47], s58 offen offset:3072 nt lds\n"
      "v_add_f32 v118, v118, v200\n"
      "v_add_f32 v119, v119, v200\n"
      "v_max3_f32 v28, v168, v169, v170\n"
      "v_max3_f32 v29, v171, v176, v177\n"
      "v_max3_f32 v28, v28, v178, v179\n"
      "v_max_f32 v28, v28, v29\n"
      "v_mov_b32 v29, v28\n"
      "s_nop 1\n"
      "s_waitcnt lgkmcnt(0)\n"
      "v_permlane16_swap_b32 v28, v29\n"
      "v_lshlrev_b32 v20, 16, v136\n"
      "v_max_f32 v28, v28, v29\n"
      "v_mov_b32 v29, v28\n"
      "v_and_b32 v21, s97, v136\n"
      "v_lshlrev_b32 v22, 16, v137\n"
      "v_permlane32_swap_b32 v28, v29\n"
      "v_and_b32 v23, s97, v137\n"
      "v_max_f32 v28, v28, v29\n"
      "v_sub_f32 v212, 0, v28\n"
      "v_mov_b32 v213, v212\n"
      "v_mov_b32 v214, v212\n"
      "v_mov_b32 v215, v212\n"
      "v_add_f32 v176, v176, v212\n"
      "v_add_f32 v177, v177, v212\n"
      "v_add_f32 v178, v178, v212\n"
      "v_add_f32 v179, v179, v212\n"
      "v_lshlrev_b32 v24, 16, v138\n"
      "v_and_b32 v25, s97, v138\n"
      "v_lshlrev_b32 v26, 16, v139\n"
      "v_and_b32 v27, s97, v139\n"
      "v_pk_mul_f32 v[20:21], v[20:21], s[54:55] op_sel_hi:[1,0]\n"
      "v_pk_mul_f32 v[22:23], v[22:23], s[54:55] op_sel_hi:[1,0]\n"
      "v_pk_mul_f32 v[24:25], v[24:25], s[54:55] op_sel_hi:[1,0]\n"
      "v_pk_mul_f32 v[26:27], v[26:27], s[54:55] op_sel_hi:[1,0]\n"
      "v_cvt_pk_bf16_f32 v136, v20, v21\n"
      "v_cvt_pk_bf16_f32 v137, v22, v23\n"
      "v_cvt_pk_bf16_f32 v138, v24, v25\n"
      "v_cvt_pk_bf16_f32 v139, v26, v27\n"
      "v_accvgpr_write_b32 a240, v136\n"
      "v_accvgpr_write_b32 a241, v137\n"
      "v_accvgpr_write_b32 a242, v138\n"
      "v_accvgpr_write_b32 a243, v139\n"
      "v_lshlrev_b32 v20, 16, v140\n"
      "v_and_b32 v21, s97, v140\n"
      "v_lshlrev_b32 v22, 16, v141\n"
      "v_and_b32 v23, s97, v141\n"
      "v_lshlrev_b32 v24, 16, v142\n"
      "v_and_b32 v25, s97, v142\n"
      "v_lshlrev_b32 v26, 16, v143\n"
      "v_and_b32 v27, s97, v143\n"
      "v_pk_mul_f32 v[20:21], v[20:21], s[54:55] op_sel_hi:[1,0]\n"
      "v_pk_mul_f32 v[22:23], v[22:23], s[54:55] op_sel_hi:[1,0]\n"
      "v_pk_mul_f32 v[24:25], v[24:25], s[54:55] op_sel_hi:[1,0]\n"
      "v_pk_mul_f32 v[26:27], v[26:27], s[54:55] op_sel_hi:[1,0]\n"
      "v_cvt_pk_bf16_f32 v140, v20, v21\n"
      "v_cvt_pk_bf16_f32 v141, v22, v23\n"
      "v_cvt_pk_bf16_f32 v142, v24, v25\n"
      "v_cvt_pk_bf16_f32 v143, v26, v27\n"
      "v_accvgpr_write_b32 a244, v140\n"
      "v_accvgpr_write_b32 a245, v141\n"
      "v_accvgpr_write_b32 a246, v142\n"
      "v_accvgpr_write_b32 a247, v143\n"
      "v_lshlrev_b32 v20, 16, v144\n"
      "v_and_b32 v21, s97, v144\n"
      "v_lshlrev_b32 v22, 16, v145\n"
      "v_and_b32 v23, s97, v145\n"
      "v_lshlrev_b32 v24, 16, v146\n"
      "v_and_b32 v25, s97, v146\n"
      "v_lshlrev_b32 v26, 16, v147\n"
      "v_and_b32 v27, s97, v147\n"
      "v_pk_mul_f32 v[20:21], v[20:21], s[54:55] op_sel_hi:[1,0]\n"
      "v_pk_mul_f32 v[22:23], v[22:23], s[54:55] op_sel_hi:[1,0]\n"
      "v_pk_mul_f32 v[24:25], v[24:25], s[54:55] op_sel_hi:[1,0]\n"
      "v_pk_mul_f32 v[26:27], v[26:27], s[54:55] op_sel_hi:[1,0]\n"
      "v_cvt_pk_bf16_f32 v144, v20, v21\n"
      "v_cvt_pk_bf16_f32 v145, v22, v23\n"
      "v_cvt_pk_bf16_f32 v146, v24, v25\n"
      "v_cvt_pk_bf16_f32 v147, v26, v27\n"
      "v_accvgpr_write_b32 a248, v144\n"
      "v_accvgpr_write_b32 a249, v145\n"
      "v_accvgpr_write_b32 a250, v146\n"
      "v_accvgpr_write_b32 a251, v147\n"
      "v_lshlrev_b32 v20, 16, v148\n"
      "v_and_b32 v21, s97, v148\n"
      "v_lshlrev_b32 v22, 16, v149\n"
      "v_and_b32 v23, s97, v149\n"
      "v_lshlrev_b32 v24, 16, v150\n"
      "v_and_b32 v25, s97, v150\n"
      "v_lshlrev_b32 v26, 16, v151\n"
      "v_and_b32 v27, s97, v151\n"
      "v_pk_mul_f32 v[20:21], v[20:21], s[54:55] op_sel_hi:[1,0]\n"
      "v_pk_mul_f32 v[22:23], v[22:23], s[54:55] op_sel_hi:[1,0]\n"
      "v_pk_mul_f32 v[24:25], v[24:25], s[54:55] op_sel_hi:[1,0]\n"
      "v_pk_mul_f32 v[26:27], v[26:27], s[54:55] op_sel_hi:[1,0]\n"
      "v_cvt_pk_bf16_f32 v148, v20, v21\n"
      "v_cvt_pk_bf16_f32 v149, v22, v23\n"
      "v_cvt_pk_bf16_f32 v150, v24, v25\n"
      "v_cvt_pk_bf16_f32 v151, v26, v27\n"
      "v_accvgpr_write_b32 a252, v148\n"
      "v_accvgpr_write_b32 a253, v149\n"
      "v_accvgpr_write_b32 a254, v150\n"
      "v_accvgpr_write_b32 a255, v151\n"
      "s_nop 1\n"
      "v_mfma_f32_16x16x32_bf16 v[120:123], v[32:35], a[240:243], 0\n"
      "v_mfma_f32_16x16x32_bf16 v[120:123], v[36:39], a[244:247], v[120:123]\n"
      "v_mfma_f32_16x16x32_bf16 v[120:123], v[40:43], a[248:251], v[120:123]\n"
      "v_mfma_f32_16x16x32_bf16 v[120:123], v[44:47], a[252:255], v[120:123]\n"
      "v_mfma_f32_16x16x32_bf16 v[124:127], v[48:51], a[240:243], 0\n"
      "v_mfma_f32_16x16x32_bf16 v[124:127], v[52:55], a[244:247], v[124:127]\n"
      "v_mfma_f32_16x16x32_bf16 v[124:127], v[56:59], a[248:251], v[124:127]\n"
      "v_mfma_f32_16x16x32_bf16 v[124:127], v[60:63], a[252:255], v[124:127]\n"
      "s_nop 3\n"
      "s_waitcnt vmcnt(12)\n"
      "ds_read_b128 v[152:155], v248 offset:16384\n"
      "ds_read_b128 v[156:159], v249 offset:16384\n"
      "ds_read_b128 v[160:163], v250 offset:16384\n"
      "ds_read_b128 v[164:167], v251 offset:16384\n"
      "s_mov_b32 s58, 0x7000\n"
      "s_mov_b32 m0, 0x17000\n"
      "s_nop 0\n"
      "buffer_load_dwordx4 v10, s[44:47], s58 offen offset:0 nt lds\n"
      "buffer_load_dwordx4 v11, s[44:47], s58 offen offset:1024 nt lds\n"
      "buffer_load_dwordx4 v10, s[44:47], s58 offen offset:2048 nt lds\n"
      "buffer_load_dwordx4 v11, s[44:47], s58 offen offset:3072 nt lds\n"
      "v_max3_f32 v28, v120, v121, v122\n"
      "v_max3_f32 v29, v123, v124, v125\n"
      "v_max3_f32 v28, v28, v126, v127\n"
      "v_max_f32 v28, v28, v29\n"
      "v_mov_b32 v29, v28\n"
      "s_nop 1\n"
      "v_permlane16_swap_b32 v28, v29\n"
      "s_nop 0\n"
      "s_waitcnt lgkmcnt(0)\n"
      "v_max_f32 v28, v28, v29\n"
      "v_mov_b32 v29, v28\n"
      "v_lshlrev_b32 v20, 16, v152\n"
      "v_and_b32 v21, s97, v152\n"
      "v_permlane32_swap_b32 v28, v29\n"
      "v_lshlrev_b32 v22, 16, v153\n"
      "v_max_f32 v28, v28, v29\n"
      "v_sub_f32 v204, 0, v28\n"
      "v_mov_b32 v205, v204\n"
      "v_mov_b32 v206, v204\n"
      "v_mov_b32 v207, v204\n"
      "v_add_f32 v120, v120, v204\n"
      "v_add_f32 v121, v121, v204\n"
      "v_add_f32 v122, v122, v204\n"
      "v_add_f32 v123, v123, v204\n"
      "v_add_f32 v124, v124, v204\n"
      "v_add_f32 v125, v125, v204\n"
      "v_add_f32 v126, v126, v204\n"
      "v_add_f32 v127, v127, v204\n"
      "v_and_b32 v23, s97, v153\n"
      "v_lshlrev_b32 v24, 16, v154\n"
      "v_and_b32 v25, s97, v154\n"
      "v_lshlrev_b32 v26, 16, v155\n"
      "v_and_b32 v27, s97, v155\n"
      "v_pk_mul_f32 v[20:21], v[20:21], s[54:55] op_sel_hi:[1,0]\n"
      "v_pk_mul_f32 v[22:23], v[22:23], s[54:55] op_sel_hi:[1,0]\n"
      "v_pk_mul_f32 v[24:25], v[24:25], s[54:55] op_sel_hi:[1,0]\n"
      "v_pk_mul_f32 v[26:27], v[26:27], s[54:55] op_sel_hi:[1,0]\n"
      "v_cvt_pk_bf16_f32 v216, v20, v21\n"
      "v_cvt_pk_bf16_f32 v217, v22, v23\n"
      "v_cvt_pk_bf16_f32 v218, v24, v25\n"
      "v_cvt_pk_bf16_f32 v219, v26, v27\n"
      "v_lshlrev_b32 v20, 16, v156\n"
      "v_and_b32 v21, s97, v156\n"
      "v_lshlrev_b32 v22, 16, v157\n"
      "v_and_b32 v23, s97, v157\n"
      "v_lshlrev_b32 v24, 16, v158\n"
      "v_and_b32 v25, s97, v158\n"
      "v_lshlrev_b32 v26, 16, v159\n"
      "v_and_b32 v27, s97, v159\n"
      "v_pk_mul_f32 v[20:21], v[20:21], s[54:55] op_sel_hi:[1,0]\n"
      "v_pk_mul_f32 v[22:23], v[22:23], s[54:55] op_sel_hi:[1,0]\n"
      "v_pk_mul_f32 v[24:25], v[24:25], s[54:55] op_sel_hi:[1,0]\n"
      "v_pk_mul_f32 v[26:27], v[26:27], s[54:55] op_sel_hi:[1,0]\n"
      "v_cvt_pk_bf16_f32 v220, v20, v21\n"
      "v_cvt_pk_bf16_f32 v221, v22, v23\n"
      "v_cvt_pk_bf16_f32 v222, v24, v25\n"
      "v_cvt_pk_bf16_f32 v223, v26, v27\n"
      "v_lshlrev_b32 v20, 16, v160\n"
      "v_and_b32 v21, s97, v160\n"
      "v_lshlrev_b32 v22, 16, v161\n"
      "v_and_b32 v23, s97, v161\n"
      "v_lshlrev_b32 v24, 16, v162\n"
      "v_and_b32 v25, s97, v162\n"
      "v_lshlrev_b32 v26, 16, v163\n"
      "v_and_b32 v27, s97, v163\n"
      "v_pk_mul_f32 v[20:21], v[20:21], s[54:55] op_sel_hi:[1,0]\n"
      "v_pk_mul_f32 v[22:23], v[22:23], s[54:55] op_sel_hi:[1,0]\n"
      "v_pk_mul_f32 v[24:25], v[24:25], s[54:55] op_sel_hi:[1,0]\n"
      "v_pk_mul_f32 v[26:27], v[26:27], s[54:55] op_sel_hi:[1,0]\n"
      "v_cvt_pk_bf16_f32 v224, v20, v21\n"
      "v_cvt_pk_bf16_f32 v225, v22, v23\n"
      "v_cvt_pk_bf16_f32 v226, v24, v25\n"
      "v_cvt_pk_bf16_f32 v227, v26, v27\n"
      "v_lshlrev_b32 v20, 16, v164\n"
      "v_and_b32 v21, s97, v164\n"
      "v_lshlrev_b32 v22, 16, v165\n"
      "v_and_b32 v23, s97, v165\n"
      "v_lshlrev_b32 v24, 16, v166\n"
      "v_and_b32 v25, s97, v166\n"
      "v_lshlrev_b32 v26, 16, v167\n"
      "v_and_b32 v27, s97, v167\n"
      "v_pk_mul_f32 v[20:21], v[20:21], s[54:55] op_sel_hi:[1,0]\n"
      "v_pk_mul_f32 v[22:23], v[22:23], s[54:55] op_sel_hi:[1,0]\n"
      "v_pk_mul_f32 v[24:25], v[24:25], s[54:55] op_sel_hi:[1,0]\n"
      "v_pk_mul_f32 v[26:27], v[26:27], s[54:55] op_sel_hi:[1,0]\n"
      "v_cvt_pk_bf16_f32 v228, v20, v21\n"
      "v_cvt_pk_bf16_f32 v229, v22, v23\n"
      "v_cvt_pk_bf16_f32 v230, v24, v25\n"
      "v_cvt_pk_bf16_f32 v231, v26, v27\n"
      "s_nop 1\n"
      "v_mfma_f32_16x16x32_bf16 v[128:131], v[32:35], v[216:219], 0\n"
      "v_mfma_f32_16x16x32_bf16 v[128:131], v[36:39], v[220:223], v[128:131]\n"
      "v_mfma_f32_16x16x32_bf16 v[128:131], v[40:43], v[224:227], v[128:131]\n"
      "v_mfma_f32_16x16x32_bf16 v[128:131], v[44:47], v[228:231], v[128:131]\n"
      "v_mfma_f32_16x16x32_bf16 v[132:135], v[48:51], v[216:219], 0\n"
      "v_mfma_f32_16x16x32_bf16 v[132:135], v[52:55], v[220:223], v[132:135]\n"
      "v_mfma_f32_16x16x32_bf16 v[132:135], v[56:59], v[224:227], v[132:135]\n"
      "v_mfma_f32_16x16x32_bf16 v[132:135], v[60:63], v[228:231], v[132:135]\n"
      "s_nop 3\n"
      "s_nop 3\n"
      "v_max3_f32 v28, v128, v129, v130\n"
      "v_max3_f32 v29, v131, v132, v133\n"
      "v_max3_f32 v28, v28, v134, v135\n"
      "v_max_f32 v28, v28, v29\n"
      "v_mov_b32 v29, v28\n"
      "s_nop 1\n"
      "v_permlane16_swap_b32 v28, v29\n"
      "s_nop 0\n"
      "v_max_f32 v28, v28, v29\n"
      "v_mov_b32 v29, v28\n"
      "s_nop 1\n"
      "v_permlane32_swap_b32 v28, v29\n"
      "s_nop 0\n"
      "v_max_f32 v28, v28, v29\n"
      "v_sub_f32 v208, 0, v28\n"
      "v_mov_b32 v209, v208\n"
      "v_mov_b32 v210, v208\n"
      "v_mov_b32 v211, v208\n"
      "v_add_f32 v128, v128, v208\n"
      "v_add_f32 v129, v129, v208\n"
      "v_add_f32 v130, v130, v208\n"
      "v_add_f32 v131, v131, v208\n"
      "v_add_f32 v132, v132, v208\n"
      "v_add_f32 v133, v133, v208\n"
      "v_add_f32 v134, v134, v208\n"
      "v_add_f32 v135, v135, v208\n"
      "L_restart_w1:\n"
      "s_mov_b32 s55, 0x9000\n"
      "s_mov_b32 s56, 0\n"
      "s_mov_b32 s57, 16\n"
      "s_waitcnt vmcnt(12)\n"
      "s_barrier\n"
      "s_xor_b32 m0, s56, 0x19000\n"
      "s_nop 0\n"
      "buffer_load_dwordx4 v10, s[44:47], s55 offen offset:0 nt lds\n"
      "buffer_load_dwordx4 v11, s[44:47], s55 offen offset:1024 nt lds\n"
      "v_exp_f32 v96, v96\n"
      "v_exp_f32 v97, v97\n"
      "v_add_f32 v14, v14, v96\n"
      "v_add_f32 v14, v14, v97\n"
      "v_cvt_pk_bf16_f32 v96, v96, v97\n"
      "buffer_load_dwordx4 v10, s[44:47], s55 offen offset:2048 nt lds\n"
      "v_exp_f32 v98, v98\n"
      "v_exp_f32 v99, v99\n"
      "v_add_f32 v14, v14, v98\n"
      "v_add_f32 v14, v14, v99\n"
      "v_cvt_pk_bf16_f32 v97, v98, v99\n"
      "v_exp_f32 v100, v100\n"
      "v_exp_f32 v101, v101\n"
      "v_add_f32 v14, v14, v100\n"
      "v_add_f32 v14, v14, v101\n"
      "v_cvt_pk_bf16_f32 v98, v100, v101\n"
      "buffer_load_dwordx4 v11, s[44:47], s55 offen offset:3072 nt lds\n"
      "v_exp_f32 v102, v102\n"
      "s_add_u32 s55, s55, 0x2000\n"
      "v_add_f32 v14, v14, v102\n"
      "v_exp_f32 v103, v103\n"
      "v_exp_f32 v104, v104\n"
      "v_add_f32 v14, v14, v103\n"
      "v_cvt_pk_bf16_f32 v99, v102, v103\n"
      "v_add_f32 v15, v15, v104\n"
      "v_exp_f32 v105, v105\n"
      "v_exp_f32 v106, v106\n"
      "v_add_f32 v15, v15, v105\n"
      "v_cvt_pk_bf16_f32 v104, v104, v105\n"
      "v_add_f32 v15, v15, v106\n"
      "v_exp_f32 v107, v107\n"
      "v_exp_f32 v108, v108\n"
      "v_add_f32 v15, v15, v107\n"
      "v_cvt_pk_bf16_f32 v105, v106, v107\n"
      "v_add_f32 v15, v15, v108\n"
      "v_exp_f32 v109, v109\n"
      "s_nop 0\n"
      "v_add_f32 v15, v15, v109\n"
      "v_cvt_pk_bf16_f32 v106, v108, v109\n"
      "s_xor_b32 m0, s56, 0x1b000\n"
      "s_nop 0\n"
      "buffer_load_dwordx4 v10, s[44:47], s55 offen offset:0 nt lds\n"
      "v_exp_f32 v110, v110\n"
      "v_exp_f32 v111, v111\n"
      "v_add_f32 v15, v15, v110\n"
      "v_add_f32 v15, v15, v111\n"
      "v_cvt_pk_bf16_f32 v107, v110, v111\n"
      "v_exp_f32 v112, v112\n"
      "ds_read_b128 v[32:35], v0 offset:8192\n"
      "v_add_f32 v16, v16, v112\n"
      "v_exp_f32 v113, v113\n"
      "s_nop 0\n"
      "v_add_f32 v16, v16, v113\n"
      "v_cvt_pk_bf16_f32 v112, v112, v113\n"
      "buffer_load_dwordx4 v11, s[44:47], s55 offen offset:1024 nt lds\n"
      "v_exp_f32 v114, v114\n"
      "ds_read_b128 v[36:39], v1 offset:8192\n"
      "v_add_f32 v16, v16, v114\n"
      "v_exp_f32 v115, v115\n"
      "v_exp_f32 v116, v116\n"
      "v_add_f32 v16, v16, v115\n"
      "v_cvt_pk_bf16_f32 v113, v114, v115\n"
      "ds_read_b128 v[40:43], v2 offset:8192\n"
      "v_add_f32 v16, v16, v116\n"
      "v_exp_f32 v117, v117\n"
      "s_nop 0\n"
      "v_add_f32 v16, v16, v117\n"
      "v_cvt_pk_bf16_f32 v114, v116, v117\n"
      "buffer_load_dwordx4 v10, s[44:47], s55 offen offset:2048 nt lds\n"
      "v_exp_f32 v118, v118\n"
      "ds_read_b128 v[44:47], v3 offset:8192\n"
      "v_add_f32 v16, v16, v118\n"
      "v_exp_f32 v119, v119\n"
      "v_exp_f32 v120, v120\n"
      "v_add_f32 v16, v16, v119\n"
      "v_cvt_pk_bf16_f32 v115, v118, v119\n"
      "ds_read_b128 v[48:51], v0 offset:12288\n"
      "v_add_f32 v17, v17, v120\n"
      "v_exp_f32 v121, v121\n"
      "s_nop 0\n"
      "v_add_f32 v17, v17, v121\n"
      "v_cvt_pk_bf16_f32 v120, v120, v121\n"
      "buffer_load_dwordx4 v11, s[44:47], s55 offen offset:3072 nt lds\n"
      "v_exp_f32 v122, v122\n"
      "s_add_u32 s55, s55, 0x2000\n"
      "ds_read_b128 v[52:55], v1 offset:12288\n"
      "v_add_f32 v17, v17, v122\n"
      "v_exp_f32 v123, v123\n"
      "v_exp_f32 v124, v124\n"
      "v_add_f32 v17, v17, v123\n"
      "v_cvt_pk_bf16_f32 v121, v122, v123\n"
      "ds_read_b128 v[56:59], v2 offset:12288\n"
      "v_add_f32 v17, v17, v124\n"
      "v_exp_f32 v125, v125\n"
      "v_exp_f32 v126, v126\n"
      "v_add_f32 v17, v17, v125\n"
      "v_cvt_pk_bf16_f32 v122, v124, v125\n"
      "ds_read_b128 v[60:63], v3 offset:12288\n"
      "v_add_f32 v17, v17, v126\n"
      "v_exp_f32 v127, v127\n"
      "v_exp_f32 v128, v128\n"
      "v_add_f32 v17, v17, v127\n"
      "v_cvt_pk_bf16_f32 v123, v126, v127\n"
      "v_add_f32 v18, v18, v128\n"
      "v_exp_f32 v129, v129\n"
      "v_exp_f32 v130, v130\n"
      "v_add_f32 v18, v18, v129\n"
      "v_cvt_pk_bf16_f32 v128, v128, v129\n"
      "v_add_f32 v18, v18, v130\n"
      "s_waitcnt lgkmcnt(0)\n"
      "s_waitcnt vmcnt(16)\n"
      "s_barrier\n"
      "s_branch L_entry_w1\n"
      "L_loop_w1:\n"
      "v_mfma_f32_16x16x32_bf16 v[96:99], v[32:35], a[192:195], v[192:195]\n"
      "v_exp_f32 v170, v170\n"
      "v_mfma_f32_16x16x32_bf16 v[96:99], v[36:39], a[196:199], v[96:99]\n"
      "ds_read_b64_tr_b16 v[64:65], v4 offset:24576\n"
      "v_add_f32 v18, v18, v170\n"
      "v_mfma_f32_16x16x32_bf16 v[96:99], v[40:43], a[200:203], v[96:99]\n"
      "v_exp_f32 v171, v171\n"
      "v_mfma_f32_16x16x32_bf16 v[96:99], v[44:47], a[204:207], v[96:99]\n"
      "v_add_f32 v18, v18, v171\n"
      "v_cvt_pk_bf16_f32 v169, v170, v171\n"
      "v_mfma_f32_16x16x32_bf16 v[100:103], v[48:51], a[192:195], v[192:195]\n"
      "v_exp_f32 v172, v172\n"
      "v_mfma_f32_16x16x32_bf16 v[100:103], v[52:55], a[196:199], v[100:103]\n"
      "ds_read_b64_tr_b16 v[66:67], v4 offset:28672\n"
      "v_add_f32 v18, v18, v172\n"
      "v_mfma_f32_16x16x32_bf16 v[100:103], v[56:59], a[200:203], v[100:103]\n"
      "v_exp_f32 v173, v173\n"
      "v_mfma_f32_16x16x32_bf16 v[100:103], v[60:63], a[204:207], v[100:103]\n"
      "v_add_f32 v18, v18, v173\n"
      "v_cvt_pk_bf16_f32 v170, v172, v173\n"
      "v_mfma_f32_16x16x32_bf16 v[104:107], v[32:35], a[208:211], v[196:199]\n"
      "v_exp_f32 v174, v174\n"
      "v_mfma_f32_16x16x32_bf16 v[104:107], v[36:39], a[212:215], v[104:107]\n"
      "ds_read_b64_tr_b16 v[68:69], v5 offset:24576\n"
      "v_add_f32 v18, v18, v174\n"
      "v_mfma_f32_16x16x32_bf16 v[104:107], v[40:43], a[216:219], v[104:107]\n"
      "v_exp_f32 v175, v175\n"
      "v_mfma_f32_16x16x32_bf16 v[104:107], v[44:47], a[220:223], v[104:107]\n"
      "v_add_f32 v18, v18, v175\n"
      "v_cvt_pk_bf16_f32 v171, v174, v175\n"
      "v_mfma_f32_16x16x32_bf16 v[108:111], v[48:51], a[208:211], v[196:199]\n"
      "ds_read_b64_tr_b16 v[70:71], v5 offset:28672\n"
      "ds_read_b64_tr_b16 v[72:73], v4 offset:24832\n"
      "v_mfma_f32_16x16x32_bf16 v[108:111], v[52:55], a[212:215], v[108:111]\n"
      "v_mfma_f32_16x16x32_bf16 v[108:111], v[56:59], a[216:219], v[108:111]\n"
      "v_exp_f32 v96, v96\n"
      "v_mfma_f32_16x16x32_bf16 v[108:111], v[60:63], a[220:223], v[108:111]\n"
      "ds_read_b64_tr_b16 v[74:75], v4 offset:28928\n"
      "v_add_f32 v14, v14, v96\n"
      "v_mfma_f32_16x16x32_bf16 v[112:115], v[32:35], a[224:227], v[200:203]\n"
      "v_exp_f32 v97, v97\n"
      "v_mfma_f32_16x16x32_bf16 v[112:115], v[36:39], a[228:231], v[112:115]\n"
      "v_add_f32 v14, v14, v97\n"
      "v_cvt_pk_bf16_f32 v96, v96, v97\n"
      "v_mfma_f32_16x16x32_bf16 v[112:115], v[40:43], a[232:235], v[112:115]\n"
      "v_exp_f32 v98, v98\n"
      "v_mfma_f32_16x16x32_bf16 v[112:115], v[44:47], a[236:239], v[112:115]\n"
      "ds_read_b64_tr_b16 v[76:77], v5 offset:24832\n"
      "v_add_f32 v14, v14, v98\n"
      "v_mfma_f32_16x16x32_bf16 v[116:119], v[48:51], a[224:227], v[200:203]\n"
      "v_exp_f32 v99, v99\n"
      "v_mfma_f32_16x16x32_bf16 v[116:119], v[52:55], a[228:231], v[116:119]\n"
      "v_add_f32 v14, v14, v99\n"
      "v_cvt_pk_bf16_f32 v97, v98, v99\n"
      "v_mfma_f32_16x16x32_bf16 v[116:119], v[56:59], a[232:235], v[116:119]\n"
      "v_exp_f32 v100, v100\n"
      "v_mfma_f32_16x16x32_bf16 v[116:119], v[60:63], a[236:239], v[116:119]\n"
      "ds_read_b64_tr_b16 v[78:79], v5 offset:28928\n"
      "v_add_f32 v14, v14, v100\n"
      "v_mfma_f32_16x16x32_bf16 v[120:123], v[32:35], a[240:243], v[204:207]\n"
      "v_exp_f32 v101, v101\n"
      "v_mfma_f32_16x16x32_bf16 v[120:123], v[36:39], a[244:247], v[120:123]\n"
      "v_add_f32 v14, v14, v101\n"
      "v_cvt_pk_bf16_f32 v98, v100, v101\n"
      "v_mfma_f32_16x16x32_bf16 v[120:123], v[40:43], a[248:251], v[120:123]\n"
      "v_exp_f32 v102, v102\n"
      "v_mfma_f32_16x16x32_bf16 v[120:123], v[44:47], a[252:255], v[120:123]\n"
      "ds_read_b64_tr_b16 v[80:81], v4 offset:25088\n"
      "ds_read_b64_tr_b16 v[82:83], v4 offset:29184\n"
      "v_add_f32 v14, v14, v102\n"
      "v_mfma_f32_16x16x32_bf16 v[124:127], v[48:51], a[240:243], v[204:207]\n"
      "v_exp_f32 v103, v103\n"
      "v_mfma_f32_16x16x32_bf16 v[124:127], v[52:55], a[244:247], v[124:127]\n"
      "v_add_f32 v14, v14, v103\n"
      "v_cvt_pk_bf16_f32 v99, v102, v103\n"
      "v_mfma_f32_16x16x32_bf16 v[124:127], v[56:59], a[248:251], v[124:127]\n"
      "v_exp_f32 v104, v104\n"
      "v_mfma_f32_16x16x32_bf16 v[124:127], v[60:63], a[252:255], v[124:127]\n"
      "ds_read_b64_tr_b16 v[84:85], v5 offset:25088\n"
      "ds_read_b64_tr_b16 v[86:87], v5 offset:29184\n"
      "v_add_f32 v15, v15, v104\n"
      "v_mfma_f32_16x16x32_bf16 v[128:131], v[32:35], v[216:219], v[208:211]\n"
      "v_exp_f32 v105, v105\n"
      "v_mfma_f32_16x16x32_bf16 v[128:131], v[36:39], v[220:223], v[128:131]\n"
      "v_add_f32 v15, v15, v105\n"
      "v_cvt_pk_bf16_f32 v104, v104, v105\n"
      "v_mfma_f32_16x16x32_bf16 v[128:131], v[40:43], v[224:227], v[128:131]\n"
      "v_exp_f32 v106, v106\n"
      "v_mfma_f32_16x16x32_bf16 v[128:131], v[44:47], v[228:231], v[128:131]\n"
      "ds_read_b64_tr_b16 v[88:89], v4 offset:25344\n"
      "ds_read_b64_tr_b16 v[90:91], v4 offset:29440\n"
      "v_add_f32 v15, v15, v106\n"
      "v_mfma_f32_16x16x32_bf16 v[132:135], v[48:51], v[216:219], v[208:211]\n"
      "v_exp_f32 v107, v107\n"
      "v_mfma_f32_16x16x32_bf16 v[132:135], v[52:55], v[220:223], v[132:135]\n"
      "v_add_f32 v15, v15, v107\n"
      "v_cvt_pk_bf16_f32 v105, v106, v107\n"
      "v_mfma_f32_16x16x32_bf16 v[132:135], v[56:59], v[224:227], v[132:135]\n"
      "v_exp_f32 v108, v108\n"
      "v_mfma_f32_16x16x32_bf16 v[132:135], v[60:63], v[228:231], v[132:135]\n"
      "ds_read_b64_tr_b16 v[92:93], v5 offset:25344\n"
      "ds_read_b64_tr_b16 v[94:95], v5 offset:29440\n"
      "v_add_f32 v15, v15, v108\n"
      "v_xor_b32 v4, 0x8000, v4\n"
      "v_mfma_f32_16x16x32_bf16 v[176:179], v[48:51], v[232:235], v[212:215]\n"
      "v_exp_f32 v109, v109\n"
      "v_xor_b32 v5, 0x8000, v5\n"
      "v_mfma_f32_16x16x32_bf16 v[176:179], v[52:55], v[236:239], v[176:179]\n"
      "v_add_f32 v15, v15, v109\n"
      "v_cvt_pk_bf16_f32 v106, v108, v109\n"
      "v_mfma_f32_16x16x32_bf16 v[176:179], v[56:59], v[240:243], v[176:179]\n"
      "v_exp_f32 v110, v110\n"
      "v_mfma_f32_16x16x32_bf16 v[176:179], v[60:63], v[244:247], v[176:179]\n"
      "v_add_f32 v15, v15, v110\n"
      "s_waitcnt lgkmcnt(0)\n"
      "v_mfma_f32_16x16x32_bf16 a[0:3], v[136:139], v[64:67], a[0:3]\n"
      "v_exp_f32 v111, v111\n"
      "v_mfma_f32_16x16x32_bf16 a[4:7], v[136:139], v[68:71], a[4:7]\n"
      "v_add_f32 v15, v15, v111\n"
      "v_cvt_pk_bf16_f32 v107, v110, v111\n"
      "v_mfma_f32_16x16x32_bf16 a[8:11], v[136:139], v[72:75], a[8:11]\n"
      "v_exp_f32 v112, v112\n"
      "v_mfma_f32_16x16x32_bf16 a[12:15], v[136:139], v[76:79], a[12:15]\n"
      "ds_read_b128 v[32:35], v0 offset:8192\n"
      "v_add_f32 v16, v16, v112\n"
      "s_xor_b32 m0, s56, 0x1b000\n"
      "v_mfma_f32_16x16x32_bf16 a[16:19], v[136:139], v[80:83], a[16:19]\n"
      "buffer_load_dwordx4 v10, s[44:47], s55 offen offset:0 nt lds\n"
      "v_exp_f32 v113, v113\n"
      "v_mfma_f32_16x16x32_bf16 a[20:23], v[136:139], v[84:87], a[20:23]\n"
      "v_add_f32 v16, v16, v113\n"
      "v_cvt_pk_bf16_f32 v112, v112, v113\n"
      "v_mfma_f32_16x16x32_bf16 a[24:27], v[136:139], v[88:91], a[24:27]\n"
      "v_exp_f32 v114, v114\n"
      "v_mfma_f32_16x16x32_bf16 a[28:31], v[136:139], v[92:95], a[28:31]\n"
      "ds_read_b128 v[36:39], v1 offset:8192\n"
      "v_add_f32 v16, v16, v114\n"
      "v_mfma_f32_16x16x32_bf16 a[32:35], v[144:147], v[64:67], a[32:35]\n"
      "v_exp_f32 v115, v115\n"
      "v_mfma_f32_16x16x32_bf16 a[36:39], v[144:147], v[68:71], a[36:39]\n"
      "v_add_f32 v16, v16, v115\n"
      "v_cvt_pk_bf16_f32 v113, v114, v115\n"
      "v_mfma_f32_16x16x32_bf16 a[40:43], v[144:147], v[72:75], a[40:43]\n"
      "v_exp_f32 v116, v116\n"
      "v_mfma_f32_16x16x32_bf16 a[44:47], v[144:147], v[76:79], a[44:47]\n"
      "ds_read_b128 v[40:43], v2 offset:8192\n"
      "v_add_f32 v16, v16, v116\n"
      "v_mfma_f32_16x16x32_bf16 a[48:51], v[144:147], v[80:83], a[48:51]\n"
      "buffer_load_dwordx4 v11, s[44:47], s55 offen offset:1024 nt lds\n"
      "v_exp_f32 v117, v117\n"
      "v_mfma_f32_16x16x32_bf16 a[52:55], v[144:147], v[84:87], a[52:55]\n"
      "v_add_f32 v16, v16, v117\n"
      "v_cvt_pk_bf16_f32 v114, v116, v117\n"
      "v_mfma_f32_16x16x32_bf16 a[56:59], v[144:147], v[88:91], a[56:59]\n"
      "v_exp_f32 v118, v118\n"
      "v_mfma_f32_16x16x32_bf16 a[60:63], v[144:147], v[92:95], a[60:63]\n"
      "ds_read_b128 v[44:47], v3 offset:8192\n"
      "v_add_f32 v16, v16, v118\n"
      "v_mfma_f32_16x16x32_bf16 a[64:67], v[152:155], v[64:67], a[64:67]\n"
      "v_exp_f32 v119, v119\n"
      "v_mfma_f32_16x16x32_bf16 a[68:71], v[152:155], v[68:71], a[68:71]\n"
      "v_add_f32 v16, v16, v119\n"
      "v_cvt_pk_bf16_f32 v115, v118, v119\n"
      "v_mfma_f32_16x16x32_bf16 a[72:75], v[152:155], v[72:75], a[72:75]\n"
      "v_exp_f32 v120, v120\n"
      "v_mfma_f32_16x16x32_bf16 a[76:79], v[152:155], v[76:79], a[76:79]\n"
      "ds_read_b128 v[48:51], v0 offset:12288\n"
      "v_add_f32 v17, v17, v120\n"
      "v_mfma_f32_16x16x32_bf16 a[80:83], v[152:155], v[80:83], a[80:83]\n"
      "buffer_load_dwordx4 v10, s[44:47], s55 offen offset:2048 nt lds\n"
      "v_exp_f32 v121, v121\n"
      "v_mfma_f32_16x16x32_bf16 a[84:87], v[152:155], v[84:87], a[84:87]\n"
      "v_add_f32 v17, v17, v121\n"
      "v_cvt_pk_bf16_f32 v120, v120, v121\n"
      "v_mfma_f32_16x16x32_bf16 a[88:91], v[152:155], v[88:91], a[88:91]\n"
      "v_exp_f32 v122, v122\n"
      "v_mfma_f32_16x16x32_bf16 a[92:95], v[152:155], v[92:95], a[92:95]\n"
      "ds_read_b128 v[52:55], v1 offset:12288\n"
      "v_add_f32 v17, v17, v122\n"
      "v_mfma_f32_16x16x32_bf16 a[96:99], v[160:163], v[64:67], a[96:99]\n"
      "v_exp_f32 v123, v123\n"
      "v_mfma_f32_16x16x32_bf16 a[100:103], v[160:163], v[68:71], a[100:103]\n"
      "v_add_f32 v17, v17, v123\n"
      "v_cvt_pk_bf16_f32 v121, v122, v123\n"
      "v_mfma_f32_16x16x32_bf16 a[104:107], v[160:163], v[72:75], a[104:107]\n"
      "v_exp_f32 v124, v124\n"
      "v_mfma_f32_16x16x32_bf16 a[108:111], v[160:163], v[76:79], a[108:111]\n"
      "ds_read_b128 v[56:59], v2 offset:12288\n"
      "v_add_f32 v17, v17, v124\n"
      "v_mfma_f32_16x16x32_bf16 a[112:115], v[160:163], v[80:83], a[112:115]\n"
      "buffer_load_dwordx4 v11, s[44:47], s55 offen offset:3072 nt lds\n"
      "v_exp_f32 v125, v125\n"
      "s_add_u32 s55, s55, 0x2000\n"
      "v_mfma_f32_16x16x32_bf16 a[116:119], v[160:163], v[84:87], a[116:119]\n"
      "v_add_f32 v17, v17, v125\n"
      "v_cvt_pk_bf16_f32 v122, v124, v125\n"
      "v_mfma_f32_16x16x32_bf16 a[120:123], v[160:163], v[88:91], a[120:123]\n"
      "v_exp_f32 v126, v126\n"
      "v_mfma_f32_16x16x32_bf16 a[124:127], v[160:163], v[92:95], a[124:127]\n"
      "ds_read_b128 v[60:63], v3 offset:12288\n"
      "v_add_f32 v17, v17, v126\n"
      "v_mfma_f32_16x16x32_bf16 a[128:131], v[168:171], v[64:67], a[128:131]\n"
      "v_exp_f32 v127, v127\n"
      "v_mfma_f32_16x16x32_bf16 a[132:135], v[168:171], v[68:71], a[132:135]\n"
      "v_add_f32 v17, v17, v127\n"
      "v_cvt_pk_bf16_f32 v123, v126, v127\n"
      "v_mfma_f32_16x16x32_bf16 a[136:139], v[168:171], v[72:75], a[136:139]\n"
      "v_exp_f32 v128, v128\n"
      "v_mfma_f32_16x16x32_bf16 a[140:143], v[168:171], v[76:79], a[140:143]\n"
      "v_add_f32 v18, v18, v128\n"
      "v_mfma_f32_16x16x32_bf16 a[144:147], v[168:171], v[80:83], a[144:147]\n"
      "v_exp_f32 v129, v129\n"
      "v_mfma_f32_16x16x32_bf16 a[148:151], v[168:171], v[84:87], a[148:151]\n"
      "v_add_f32 v18, v18, v129\n"
      "v_cvt_pk_bf16_f32 v128, v128, v129\n"
      "v_mfma_f32_16x16x32_bf16 a[152:155], v[168:171], v[88:91], a[152:155]\n"
      "v_exp_f32 v130, v130\n"
      "v_mfma_f32_16x16x32_bf16 a[156:159], v[168:171], v[92:95], a[156:159]\n"
      "v_add_f32 v18, v18, v130\n"
      "s_waitcnt lgkmcnt(0)\n"
      "L_entry_w1:\n"
      "v_mfma_f32_16x16x32_bf16 v[136:139], v[32:35], a[192:195], v[192:195]\n"
      "v_exp_f32 v131, v131\n"
      "v_mfma_f32_16x16x32_bf16 v[136:139], v[36:39], a[196:199], v[136:139]\n"
      "v_add_f32 v18, v18, v131\n"
      "v_cvt_pk_bf16_f32 v129, v130, v131\n"
      "v_mfma_f32_16x16x32_bf16 v[136:139], v[40:43], a[200:203], v[136:139]\n"
      "v_exp_f32 v132, v132\n"
      "v_mfma_f32_16x16x32_bf16 v[136:139], v[44:47], a[204:207], v[136:139]\n"
      "ds_read_b64_tr_b16 v[64:65], v4 offset:0\n"
      "v_add_f32 v18, v18, v132\n"
      "v_mfma_f32_16x16x32_bf16 v[140:143], v[48:51], a[192:195], v[192:195]\n"
      "v_exp_f32 v133, v133\n"
      "v_mfma_f32_16x16x32_bf16 v[140:143], v[52:55], a[196:199], v[140:143]\n"
      "v_add_f32 v18, v18, v133\n"
      "v_cvt_pk_bf16_f32 v130, v132, v133\n"
      "v_mfma_f32_16x16x32_bf16 v[140:143], v[56:59], a[200:203], v[140:143]\n"
      "v_exp_f32 v134, v134\n"
      "v_mfma_f32_16x16x32_bf16 v[140:143], v[60:63], a[204:207], v[140:143]\n"
      "ds_read_b64_tr_b16 v[66:67], v4 offset:4096\n"
      "v_add_f32 v18, v18, v134\n"
      "v_mfma_f32_16x16x32_bf16 v[144:147], v[32:35], a[208:211], v[196:199]\n"
      "v_exp_f32 v135, v135\n"
      "v_mfma_f32_16x16x32_bf16 v[144:147], v[36:39], a[212:215], v[144:147]\n"
      "v_add_f32 v18, v18, v135\n"
      "v_cvt_pk_bf16_f32 v131, v134, v135\n"
      "v_mfma_f32_16x16x32_bf16 v[144:147], v[40:43], a[216:219], v[144:147]\n"
      "v_exp_f32 v176, v176\n"
      "v_mfma_f32_16x16x32_bf16 v[144:147], v[44:47], a[220:223], v[144:147]\n"
      "ds_read_b64_tr_b16 v[68:69], v5 offset:0\n"
      "v_add_f32 v19, v19, v176\n"
      "v_mfma_f32_16x16x32_bf16 v[148:151], v[48:51], a[208:211], v[196:199]\n"
      "v_exp_f32 v177, v177\n"
      "v_mfma_f32_16x16x32_bf16 v[148:151], v[52:55], a[212:215], v[148:151]\n"
      "v_add_f32 v19, v19, v177\n"
      "v_cvt_pk_bf16_f32 v176, v176, v177\n"
      "v_mfma_f32_16x16x32_bf16 v[148:151], v[56:59], a[216:219], v[148:151]\n"
      "v_exp_f32 v178, v178\n"
      "v_mfma_f32_16x16x32_bf16 v[148:151], v[60:63], a[220:223], v[148:151]\n"
      "ds_read_b64_tr_b16 v[70:71], v5 offset:4096\n"
      "v_add_f32 v19, v19, v178\n"
      "v_mfma_f32_16x16x32_bf16 v[152:155], v[32:35], a[224:227], v[200:203]\n"
      "v_exp_f32 v179, v179\n"
      "v_mfma_f32_16x16x32_bf16 v[152:155], v[36:39], a[228:231], v[152:155]\n"
      "v_add_f32 v19, v19, v179\n"
      "v_cvt_pk_bf16_f32 v177, v178, v179\n"
      "v_mfma_f32_16x16x32_bf16 v[152:155], v[40:43], a[232:235], v[152:155]\n"
      "ds_read_b64_tr_b16 v[72:73], v4 offset:256\n"
      "ds_read_b64_tr_b16 v[74:75], v4 offset:4352\n"
      "v_mfma_f32_16x16x32_bf16 v[152:155], v[44:47], a[236:239], v[152:155]\n"
      "ds_read_b64_tr_b16 v[76:77], v5 offset:256\n"
      "ds_read_b64_tr_b16 v[78:79], v5 offset:4352\n"
      "v_mfma_f32_16x16x32_bf16 v[156:159], v[48:51], a[224:227], v[200:203]\n"
      "v_exp_f32 v136, v136\n"
      "v_mfma_f32_16x16x32_bf16 v[156:159], v[52:55], a[228:231], v[156:159]\n"
      "ds_read_b64_tr_b16 v[80:81], v4 offset:512\n"
      "v_add_f32 v14, v14, v136\n"
      "v_mfma_f32_16x16x32_bf16 v[156:159], v[56:59], a[232:235], v[156:159]\n"
      "v_exp_f32 v137, v137\n"
      "v_mfma_f32_16x16x32_bf16 v[156:159], v[60:63], a[236:239], v[156:159]\n"
      "v_add_f32 v14, v14, v137\n"
      "v_cvt_pk_bf16_f32 v136, v136, v137\n"
      "v_mfma_f32_16x16x32_bf16 v[160:163], v[32:35], a[240:243], v[204:207]\n"
      "v_exp_f32 v138, v138\n"
      "v_mfma_f32_16x16x32_bf16 v[160:163], v[36:39], a[244:247], v[160:163]\n"
      "ds_read_b64_tr_b16 v[82:83], v4 offset:4608\n"
      "v_add_f32 v14, v14, v138\n"
      "v_mfma_f32_16x16x32_bf16 v[160:163], v[40:43], a[248:251], v[160:163]\n"
      "v_exp_f32 v139, v139\n"
      "v_mfma_f32_16x16x32_bf16 v[160:163], v[44:47], a[252:255], v[160:163]\n"
      "v_add_f32 v14, v14, v139\n"
      "v_cvt_pk_bf16_f32 v137, v138, v139\n"
      "v_mfma_f32_16x16x32_bf16 v[164:167], v[48:51], a[240:243], v[204:207]\n"
      "v_exp_f32 v140, v140\n"
      "v_mfma_f32_16x16x32_bf16 v[164:167], v[52:55], a[244:247], v[164:167]\n"
      "ds_read_b64_tr_b16 v[84:85], v5 offset:512\n"
      "ds_read_b64_tr_b16 v[86:87], v5 offset:4608\n"
      "v_add_f32 v14, v14, v140\n"
      "v_mfma_f32_16x16x32_bf16 v[164:167], v[56:59], a[248:251], v[164:167]\n"
      "v_exp_f32 v141, v141\n"
      "v_mfma_f32_16x16x32_bf16 v[164:167], v[60:63], a[252:255], v[164:167]\n"
      "v_add_f32 v14, v14, v141\n"
      "v_cvt_pk_bf16_f32 v138, v140, v141\n"
      "v_mfma_f32_16x16x32_bf16 v[168:171], v[32:35], v[216:219], v[208:211]\n"
      "v_exp_f32 v142, v142\n"
      "v_mfma_f32_16x16x32_bf16 v[168:171], v[36:39], v[220:223], v[168:171]\n"
      "ds_read_b64_tr_b16 v[88:89], v4 offset:768\n"
      "ds_read_b64_tr_b16 v[90:91], v4 offset:4864\n"
      "v_add_f32 v14, v14, v142\n"
      "v_mfma_f32_16x16x32_bf16 v[168:171], v[40:43], v[224:227], v[168:171]\n"
      "v_exp_f32 v143, v143\n"
      "v_mfma_f32_16x16x32_bf16 v[168:171], v[44:47], v[228:231], v[168:171]\n"
      "v_add_f32 v14, v14, v143\n"
      "v_cvt_pk_bf16_f32 v139, v142, v143\n"
      "v_mfma_f32_16x16x32_bf16 v[172:175], v[48:51], v[216:219], v[208:211]\n"
      "v_exp_f32 v144, v144\n"
      "v_mfma_f32_16x16x32_bf16 v[172:175], v[52:55], v[220:223], v[172:175]\n"
      "ds_read_b64_tr_b16 v[92:93], v5 offset:768\n"
      "ds_read_b64_tr_b16 v[94:95], v5 offset:4864\n"
      "v_add_f32 v15, v15, v144\n"
      "v_mfma_f32_16x16x32_bf16 v[172:175], v[56:59], v[224:227], v[172:175]\n"
      "v_exp_f32 v145, v145\n"
      "v_mfma_f32_16x16x32_bf16 v[172:175], v[60:63], v[228:231], v[172:175]\n"
      "v_add_f32 v15, v15, v145\n"
      "v_cvt_pk_bf16_f32 v144, v144, v145\n"
      "s_waitcnt lgkmcnt(0)\n"
      "v_mfma_f32_16x16x32_bf16 a[0:3], v[96:99], v[64:67], a[0:3]\n"
      "v_exp_f32 v146, v146\n"
      "v_mfma_f32_16x16x32_bf16 a[4:7], v[96:99], v[68:71], a[4:7]\n"
      "ds_read_b128 v[32:35], v0 offset:16384\n"
      "v_add_f32 v15, v15, v146\n"
      "v_mfma_f32_16x16x32_bf16 a[8:11], v[96:99], v[72:75], a[8:11]\n"
      "v_exp_f32 v147, v147\n"
      "v_mfma_f32_16x16x32_bf16 a[12:15], v[96:99], v[76:79], a[12:15]\n"
      "v_add_f32 v15, v15, v147\n"
      "v_cvt_pk_bf16_f32 v145, v146, v147\n"
      "v_mfma_f32_16x16x32_bf16 a[16:19], v[96:99], v[80:83], a[16:19]\n"
      "v_exp_f32 v148, v148\n"
      "v_mfma_f32_16x16x32_bf16 a[20:23], v[96:99], v[84:87], a[20:23]\n"
      "v_add_f32 v15, v15, v148\n"
      "v_mfma_f32_16x16x32_bf16 a[24:27], v[96:99], v[88:91], a[24:27]\n"
      "v_exp_f32 v149, v149\n"
      "v_mfma_f32_16x16x32_bf16 a[28:31], v[96:99], v[92:95], a[28:31]\n"
      "v_add_f32 v15, v15, v149\n"
      "v_cvt_pk_bf16_f32 v146, v148, v149\n"
      "s_xor_b32 m0, s56, 0x1d000\n"
      "v_mfma_f32_16x16x32_bf16 a[32:35], v[104:107], v[64:67], a[32:35]\n"
      "buffer_load_dwordx4 v10, s[44:47], s55 offen offset:0 nt lds\n"
      "v_exp_f32 v150, v150\n"
      "v_mfma_f32_16x16x32_bf16 a[36:39], v[104:107], v[68:71], a[36:39]\n"
      "ds_read_b128 v[36:39], v1 offset:16384\n"
      "v_add_f32 v15, v15, v150\n"
      "v_mfma_f32_16x16x32_bf16 a[40:43], v[104:107], v[72:75], a[40:43]\n"
      "v_exp_f32 v151, v151\n"
      "v_mfma_f32_16x16x32_bf16 a[44:47], v[104:107], v[76:79], a[44:47]\n"
      "v_add_f32 v15, v15, v151\n"
      "v_cvt_pk_bf16_f32 v147, v150, v151\n"
      "v_mfma_f32_16x16x32_bf16 a[48:51], v[104:107], v[80:83], a[48:51]\n"
      "v_exp_f32 v152, v152\n"
      "v_mfma_f32_16x16x32_bf16 a[52:55], v[104:107], v[84:87], a[52:55]\n"
      "ds_read_b128 v[40:43], v2 offset:16384\n"
      "v_add_f32 v16, v16, v152\n"
      "v_mfma_f32_16x16x32_bf16 a[56:59], v[104:107], v[88:91], a[56:59]\n"
      "v_exp_f32 v153, v153\n"
      "v_mfma_f32_16x16x32_bf16 a[60:63], v[104:107], v[92:95], a[60:63]\n"
      "v_add_f32 v16, v16, v153\n"
      "v_cvt_pk_bf16_f32 v152, v152, v153\n"
      "v_mfma_f32_16x16x32_bf16 a[64:67], v[112:115], v[64:67], a[64:67]\n"
      "buffer_load_dwordx4 v11, s[44:47], s55 offen offset:1024 nt lds\n"
      "v_exp_f32 v154, v154\n"
      "v_mfma_f32_16x16x32_bf16 a[68:71], v[112:115], v[68:71], a[68:71]\n"
      "ds_read_b128 v[44:47], v3 offset:16384\n"
      "v_add_f32 v16, v16, v154\n"
      "v_mfma_f32_16x16x32_bf16 a[72:75], v[112:115], v[72:75], a[72:75]\n"
      "v_exp_f32 v155, v155\n"
      "v_mfma_f32_16x16x32_bf16 a[76:79], v[112:115], v[76:79], a[76:79]\n"
      "v_add_f32 v16, v16, v155\n"
      "v_cvt_pk_bf16_f32 v153, v154, v155\n"
      "v_mfma_f32_16x16x32_bf16 a[80:83], v[112:115], v[80:83], a[80:83]\n"
      "v_exp_f32 v156, v156\n"
      "v_mfma_f32_16x16x32_bf16 a[84:87], v[112:115], v[84:87], a[84:87]\n"
      "ds_read_b128 v[48:51], v0 offset:20480\n"
      "v_add_f32 v16, v16, v156\n"
      "v_mfma_f32_16x16x32_bf16 a[88:91], v[112:115], v[88:91], a[88:91]\n"
      "v_exp_f32 v157, v157\n"
      "v_mfma_f32_16x16x32_bf16 a[92:95], v[112:115], v[92:95], a[92:95]\n"
      "v_add_f32 v16, v16, v157\n"
      "v_cvt_pk_bf16_f32 v154, v156, v157\n"
      "v_mfma_f32_16x16x32_bf16 a[96:99], v[120:123], v[64:67], a[96:99]\n"
      "buffer_load_dwordx4 v10, s[44:47], s55 offen offset:2048 nt lds\n"
      "v_exp_f32 v158, v158\n"
      "v_mfma_f32_16x16x32_bf16 a[100:103], v[120:123], v[68:71], a[100:103]\n"
      "v_add_f32 v16, v16, v158\n"
      "v_mfma_f32_16x16x32_bf16 a[104:107], v[120:123], v[72:75], a[104:107]\n"
      "v_exp_f32 v159, v159\n"
      "v_mfma_f32_16x16x32_bf16 a[108:111], v[120:123], v[76:79], a[108:111]\n"
      "v_add_f32 v16, v16, v159\n"
      "v_cvt_pk_bf16_f32 v155, v158, v159\n"
      "v_mfma_f32_16x16x32_bf16 a[112:115], v[120:123], v[80:83], a[112:115]\n"
      "v_exp_f32 v160, v160\n"
      "v_mfma_f32_16x16x32_bf16 a[116:119], v[120:123], v[84:87], a[116:119]\n"
      "ds_read_b128 v[52:55], v1 offset:20480\n"
      "v_add_f32 v17, v17, v160\n"
      "v_mfma_f32_16x16x32_bf16 a[120:123], v[120:123], v[88:91], a[120:123]\n"
      "v_exp_f32 v161, v161\n"
      "v_mfma_f32_16x16x32_bf16 a[124:127], v[120:123], v[92:95], a[124:127]\n"
      "v_add_f32 v17, v17, v161\n"
      "v_cvt_pk_bf16_f32 v160, v160, v161\n"
      "v_mfma_f32_16x16x32_bf16 a[128:131], v[128:131], v[64:67], a[128:131]\n"
      "buffer_load_dwordx4 v11, s[44:47], s55 offen offset:3072 nt lds\n"
      "v_exp_f32 v162, v162\n"
      "s_add_u32 s55, s55, 0x2000\n"
      "v_mfma_f32_16x16x32_bf16 a[132:135], v[128:131], v[68:71], a[132:135]\n"
      "ds_read_b128 v[56:59], v2 offset:20480\n"
      "v_add_f32 v17, v17, v162\n"
      "v_mfma_f32_16x16x32_bf16 a[136:139], v[128:131], v[72:75], a[136:139]\n"
      "v_exp_f32 v163, v163\n"
      "v_mfma_f32_16x16x32_bf16 a[140:143], v[128:131], v[76:79], a[140:143]\n"
      "v_add_f32 v17, v17, v163\n"
      "v_cvt_pk_bf16_f32 v161, v162, v163\n"
      "v_mfma_f32_16x16x32_bf16 a[144:147], v[128:131], v[80:83], a[144:147]\n"
      "v_exp_f32 v164, v164\n"
      "v_mfma_f32_16x16x32_bf16 a[148:151], v[128:131], v[84:87], a[148:151]\n"
      "ds_read_b128 v[60:63], v3 offset:20480\n"
      "v_add_f32 v17, v17, v164\n"
      "v_mfma_f32_16x16x32_bf16 a[152:155], v[128:131], v[88:91], a[152:155]\n"
      "v_exp_f32 v165, v165\n"
      "v_mfma_f32_16x16x32_bf16 a[156:159], v[128:131], v[92:95], a[156:159]\n"
      "v_add_f32 v17, v17, v165\n"
      "v_cvt_pk_bf16_f32 v162, v164, v165\n"
      "v_mfma_f32_16x16x16_bf16 a[160:163], v[176:177], v[66:67], a[160:163]\n"
      "v_exp_f32 v166, v166\n"
      "v_mfma_f32_16x16x16_bf16 a[164:167], v[176:177], v[70:71], a[164:167]\n"
      "v_add_f32 v17, v17, v166\n"
      "v_mfma_f32_16x16x16_bf16 a[168:171], v[176:177], v[74:75], a[168:171]\n"
      "v_exp_f32 v167, v167\n"
      "v_mfma_f32_16x16x16_bf16 a[172:175], v[176:177], v[78:79], a[172:175]\n"
      "v_add_f32 v17, v17, v167\n"
      "v_cvt_pk_bf16_f32 v163, v166, v167\n"
      "v_mfma_f32_16x16x16_bf16 a[176:179], v[176:177], v[82:83], a[176:179]\n"
      "v_exp_f32 v168, v168\n"
      "v_mfma_f32_16x16x16_bf16 a[180:183], v[176:177], v[86:87], a[180:183]\n"
      "v_add_f32 v18, v18, v168\n"
      "v_mfma_f32_16x16x16_bf16 a[184:187], v[176:177], v[90:91], a[184:187]\n"
      "v_exp_f32 v169, v169\n"
      "v_mfma_f32_16x16x16_bf16 a[188:191], v[176:177], v[94:95], a[188:191]\n"
      "v_add_f32 v18, v18, v169\n"
      "v_cvt_pk_bf16_f32 v168, v168, v169\n"
      "s_waitcnt lgkmcnt(0)\n"
      "s_waitcnt vmcnt(12)\n"
      "s_barrier\n"
      "v_mfma_f32_16x16x32_bf16 v[96:99], v[32:35], a[192:195], v[192:195]\n"
      "v_exp_f32 v170, v170\n"
      "v_mfma_f32_16x16x32_bf16 v[96:99], v[36:39], a[196:199], v[96:99]\n"
      "ds_read_b64_tr_b16 v[64:65], v4 offset:8192\n"
      "v_add_f32 v18, v18, v170\n"
      "v_mfma_f32_16x16x32_bf16 v[96:99], v[40:43], a[200:203], v[96:99]\n"
      "v_exp_f32 v171, v171\n"
      "v_mfma_f32_16x16x32_bf16 v[96:99], v[44:47], a[204:207], v[96:99]\n"
      "v_add_f32 v18, v18, v171\n"
      "v_cvt_pk_bf16_f32 v169, v170, v171\n"
      "v_mfma_f32_16x16x32_bf16 v[100:103], v[48:51], a[192:195], v[192:195]\n"
      "v_exp_f32 v172, v172\n"
      "v_mfma_f32_16x16x32_bf16 v[100:103], v[52:55], a[196:199], v[100:103]\n"
      "ds_read_b64_tr_b16 v[66:67], v4 offset:12288\n"
      "v_add_f32 v18, v18, v172\n"
      "v_mfma_f32_16x16x32_bf16 v[100:103], v[56:59], a[200:203], v[100:103]\n"
      "v_exp_f32 v173, v173\n"
      "v_mfma_f32_16x16x32_bf16 v[100:103], v[60:63], a[204:207], v[100:103]\n"
      "v_add_f32 v18, v18, v173\n"
      "v_cvt_pk_bf16_f32 v170, v172, v173\n"
      "v_mfma_f32_16x16x32_bf16 v[104:107], v[32:35], a[208:211], v[196:199]\n"
      "v_exp_f32 v174, v174\n"
      "v_mfma_f32_16x16x32_bf16 v[104:107], v[36:39], a[212:215], v[104:107]\n"
      "ds_read_b64_tr_b16 v[68:69], v5 offset:8192\n"
      "v_add_f32 v18, v18, v174\n"
      "v_mfma_f32_16x16x32_bf16 v[104:107], v[40:43], a[216:219], v[104:107]\n"
      "v_exp_f32 v175, v175\n"
      "v_mfma_f32_16x16x32_bf16 v[104:107], v[44:47], a[220:223], v[104:107]\n"
      "v_add_f32 v18, v18, v175\n"
      "v_cvt_pk_bf16_f32 v171, v174, v175\n"
      "v_mfma_f32_16x16x32_bf16 v[108:111], v[48:51], a[208:211], v[196:199]\n"
      "ds_read_b64_tr_b16 v[70:71], v5 offset:12288\n"
      "ds_read_b64_tr_b16 v[72:73], v4 offset:8448\n"
      "v_mfma_f32_16x16x32_bf16 v[108:111], v[52:55], a[212:215], v[108:111]\n"
      "v_mfma_f32_16x16x32_bf16 v[108:111], v[56:59], a[216:219], v[108:111]\n"
      "v_exp_f32 v96, v96\n"
      "v_mfma_f32_16x16x32_bf16 v[108:111], v[60:63], a[220:223], v[108:111]\n"
      "ds_read_b64_tr_b16 v[74:75], v4 offset:12544\n"
      "v_add_f32 v14, v14, v96\n"
      "v_mfma_f32_16x16x32_bf16 v[112:115], v[32:35], a[224:227], v[200:203]\n"
      "v_exp_f32 v97, v97\n"
      "v_mfma_f32_16x16x32_bf16 v[112:115], v[36:39], a[228:231], v[112:115]\n"
      "v_add_f32 v14, v14, v97\n"
      "v_cvt_pk_bf16_f32 v96, v96, v97\n"
      "v_mfma_f32_16x16x32_bf16 v[112:115], v[40:43], a[232:235], v[112:115]\n"
      "v_exp_f32 v98, v98\n"
      "v_mfma_f32_16x16x32_bf16 v[112:115], v[44:47], a[236:239], v[112:115]\n"
      "ds_read_b64_tr_b16 v[76:77], v5 offset:8448\n"
      "v_add_f32 v14, v14, v98\n"
      "v_mfma_f32_16x16x32_bf16 v[116:119], v[48:51], a[224:227], v[200:203]\n"
      "v_exp_f32 v99, v99\n"
      "v_mfma_f32_16x16x32_bf16 v[116:119], v[52:55], a[228:231], v[116:119]\n"
      "v_add_f32 v14, v14, v99\n"
      "v_cvt_pk_bf16_f32 v97, v98, v99\n"
      "v_mfma_f32_16x16x32_bf16 v[116:119], v[56:59], a[232:235], v[116:119]\n"
      "v_exp_f32 v100, v100\n"
      "v_mfma_f32_16x16x32_bf16 v[116:119], v[60:63], a[236:239], v[116:119]\n"
      "ds_read_b64_tr_b16 v[78:79], v5 offset:12544\n"
      "v_add_f32 v14, v14, v100\n"
      "v_mfma_f32_16x16x32_bf16 v[120:123], v[32:35], a[240:243], v[204:207]\n"
      "v_exp_f32 v101, v101\n"
      "v_mfma_f32_16x16x32_bf16 v[120:123], v[36:39], a[244:247], v[120:123]\n"
      "v_add_f32 v14, v14, v101\n"
      "v_cvt_pk_bf16_f32 v98, v100, v101\n"
      "v_mfma_f32_16x16x32_bf16 v[120:123], v[40:43], a[248:251], v[120:123]\n"
      "v_exp_f32 v102, v102\n"
      "v_mfma_f32_16x16x32_bf16 v[120:123], v[44:47], a[252:255], v[120:123]\n"
      "ds_read_b64_tr_b16 v[80:81], v4 offset:8704\n"
      "ds_read_b64_tr_b16 v[82:83], v4 offset:12800\n"
      "v_add_f32 v14, v14, v102\n"
      "v_mfma_f32_16x16x32_bf16 v[124:127], v[48:51], a[240:243], v[204:207]\n"
      "v_exp_f32 v103, v103\n"
      "v_mfma_f32_16x16x32_bf16 v[124:127], v[52:55], a[244:247], v[124:127]\n"
      "v_add_f32 v14, v14, v103\n"
      "v_cvt_pk_bf16_f32 v99, v102, v103\n"
      "v_mfma_f32_16x16x32_bf16 v[124:127], v[56:59], a[248:251], v[124:127]\n"
      "v_exp_f32 v104, v104\n"
      "v_mfma_f32_16x16x32_bf16 v[124:127], v[60:63], a[252:255], v[124:127]\n"
      "ds_read_b64_tr_b16 v[84:85], v5 offset:8704\n"
      "ds_read_b64_tr_b16 v[86:87], v5 offset:12800\n"
      "v_add_f32 v15, v15, v104\n"
      "v_mfma_f32_16x16x32_bf16 v[128:131], v[32:35], v[216:219], v[208:211]\n"
      "v_exp_f32 v105, v105\n"
      "v_mfma_f32_16x16x32_bf16 v[128:131], v[36:39], v[220:223], v[128:131]\n"
      "v_add_f32 v15, v15, v105\n"
      "v_cvt_pk_bf16_f32 v104, v104, v105\n"
      "v_mfma_f32_16x16x32_bf16 v[128:131], v[40:43], v[224:227], v[128:131]\n"
      "v_exp_f32 v106, v106\n"
      "v_mfma_f32_16x16x32_bf16 v[128:131], v[44:47], v[228:231], v[128:131]\n"
      "ds_read_b64_tr_b16 v[88:89], v4 offset:8960\n"
      "ds_read_b64_tr_b16 v[90:91], v4 offset:13056\n"
      "v_add_f32 v15, v15, v106\n"
      "v_mfma_f32_16x16x32_bf16 v[132:135], v[48:51], v[216:219], v[208:211]\n"
      "v_exp_f32 v107, v107\n"
      "v_mfma_f32_16x16x32_bf16 v[132:135], v[52:55], v[220:223], v[132:135]\n"
      "v_add_f32 v15, v15, v107\n"
      "v_cvt_pk_bf16_f32 v105, v106, v107\n"
      "v_mfma_f32_16x16x32_bf16 v[132:135], v[56:59], v[224:227], v[132:135]\n"
      "v_exp_f32 v108, v108\n"
      "v_mfma_f32_16x16x32_bf16 v[132:135], v[60:63], v[228:231], v[132:135]\n"
      "ds_read_b64_tr_b16 v[92:93], v5 offset:8960\n"
      "ds_read_b64_tr_b16 v[94:95], v5 offset:13056\n"
      "v_add_f32 v15, v15, v108\n"
      "v_mfma_f32_16x16x32_bf16 v[176:179], v[48:51], v[232:235], v[212:215]\n"
      "v_exp_f32 v109, v109\n"
      "v_mfma_f32_16x16x32_bf16 v[176:179], v[52:55], v[236:239], v[176:179]\n"
      "v_add_f32 v15, v15, v109\n"
      "v_cvt_pk_bf16_f32 v106, v108, v109\n"
      "v_mfma_f32_16x16x32_bf16 v[176:179], v[56:59], v[240:243], v[176:179]\n"
      "v_exp_f32 v110, v110\n"
      "v_mfma_f32_16x16x32_bf16 v[176:179], v[60:63], v[244:247], v[176:179]\n"
      "v_add_f32 v15, v15, v110\n"
      "s_waitcnt lgkmcnt(0)\n"
      "v_mfma_f32_16x16x32_bf16 a[0:3], v[136:139], v[64:67], a[0:3]\n"
      "v_exp_f32 v111, v111\n"
      "v_mfma_f32_16x16x32_bf16 a[4:7], v[136:139], v[68:71], a[4:7]\n"
      "v_add_f32 v15, v15, v111\n"
      "v_cvt_pk_bf16_f32 v107, v110, v111\n"
      "v_mfma_f32_16x16x32_bf16 a[8:11], v[136:139], v[72:75], a[8:11]\n"
      "v_exp_f32 v112, v112\n"
      "v_mfma_f32_16x16x32_bf16 a[12:15], v[136:139], v[76:79], a[12:15]\n"
      "ds_read_b128 v[32:35], v0 offset:24576\n"
      "v_add_f32 v16, v16, v112\n"
      "s_xor_b32 m0, s56, 0x1f000\n"
      "v_mfma_f32_16x16x32_bf16 a[16:19], v[136:139], v[80:83], a[16:19]\n"
      "buffer_load_dwordx4 v10, s[44:47], s55 offen offset:0 nt lds\n"
      "v_exp_f32 v113, v113\n"
      "v_mfma_f32_16x16x32_bf16 a[20:23], v[136:139], v[84:87], a[20:23]\n"
      "v_add_f32 v16, v16, v113\n"
      "v_cvt_pk_bf16_f32 v112, v112, v113\n"
      "v_mfma_f32_16x16x32_bf16 a[24:27], v[136:139], v[88:91], a[24:27]\n"
      "v_exp_f32 v114, v114\n"
      "v_mfma_f32_16x16x32_bf16 a[28:31], v[136:139], v[92:95], a[28:31]\n"
      "ds_read_b128 v[36:39], v1 offset:24576\n"
      "v_add_f32 v16, v16, v114\n"
      "v_mfma_f32_16x16x32_bf16 a[32:35], v[144:147], v[64:67], a[32:35]\n"
      "v_exp_f32 v115, v115\n"
      "v_mfma_f32_16x16x32_bf16 a[36:39], v[144:147], v[68:71], a[36:39]\n"
      "v_add_f32 v16, v16, v115\n"
      "v_cvt_pk_bf16_f32 v113, v114, v115\n"
      "v_mfma_f32_16x16x32_bf16 a[40:43], v[144:147], v[72:75], a[40:43]\n"
      "v_exp_f32 v116, v116\n"
      "v_mfma_f32_16x16x32_bf16 a[44:47], v[144:147], v[76:79], a[44:47]\n"
      "ds_read_b128 v[40:43], v2 offset:24576\n"
      "v_add_f32 v16, v16, v116\n"
      "v_mfma_f32_16x16x32_bf16 a[48:51], v[144:147], v[80:83], a[48:51]\n"
      "buffer_load_dwordx4 v11, s[44:47], s55 offen offset:1024 nt lds\n"
      "v_exp_f32 v117, v117\n"
      "v_mfma_f32_16x16x32_bf16 a[52:55], v[144:147], v[84:87], a[52:55]\n"
      "v_add_f32 v16, v16, v117\n"
      "v_cvt_pk_bf16_f32 v114, v116, v117\n"
      "v_mfma_f32_16x16x32_bf16 a[56:59], v[144:147], v[88:91], a[56:59]\n"
      "v_exp_f32 v118, v118\n"
      "v_mfma_f32_16x16x32_bf16 a[60:63], v[144:147], v[92:95], a[60:63]\n"
      "ds_read_b128 v[44:47], v3 offset:24576\n"
      "v_add_f32 v16, v16, v118\n"
      "v_mfma_f32_16x16x32_bf16 a[64:67], v[152:155], v[64:67], a[64:67]\n"
      "v_exp_f32 v119, v119\n"
      "v_mfma_f32_16x16x32_bf16 a[68:71], v[152:155], v[68:71], a[68:71]\n"
      "v_add_f32 v16, v16, v119\n"
      "v_cvt_pk_bf16_f32 v115, v118, v119\n"
      "v_mfma_f32_16x16x32_bf16 a[72:75], v[152:155], v[72:75], a[72:75]\n"
      "v_exp_f32 v120, v120\n"
      "v_mfma_f32_16x16x32_bf16 a[76:79], v[152:155], v[76:79], a[76:79]\n"
      "ds_read_b128 v[48:51], v0 offset:28672\n"
      "v_add_f32 v17, v17, v120\n"
      "v_mfma_f32_16x16x32_bf16 a[80:83], v[152:155], v[80:83], a[80:83]\n"
      "buffer_load_dwordx4 v10, s[44:47], s55 offen offset:2048 nt lds\n"
      "v_exp_f32 v121, v121\n"
      "v_mfma_f32_16x16x32_bf16 a[84:87], v[152:155], v[84:87], a[84:87]\n"
      "v_add_f32 v17, v17, v121\n"
      "v_cvt_pk_bf16_f32 v120, v120, v121\n"
      "v_mfma_f32_16x16x32_bf16 a[88:91], v[152:155], v[88:91], a[88:91]\n"
      "v_exp_f32 v122, v122\n"
      "v_mfma_f32_16x16x32_bf16 a[92:95], v[152:155], v[92:95], a[92:95]\n"
      "ds_read_b128 v[52:55], v1 offset:28672\n"
      "v_add_f32 v17, v17, v122\n"
      "v_mfma_f32_16x16x32_bf16 a[96:99], v[160:163], v[64:67], a[96:99]\n"
      "v_exp_f32 v123, v123\n"
      "v_mfma_f32_16x16x32_bf16 a[100:103], v[160:163], v[68:71], a[100:103]\n"
      "v_add_f32 v17, v17, v123\n"
      "v_cvt_pk_bf16_f32 v121, v122, v123\n"
      "v_mfma_f32_16x16x32_bf16 a[104:107], v[160:163], v[72:75], a[104:107]\n"
      "v_exp_f32 v124, v124\n"
      "v_mfma_f32_16x16x32_bf16 a[108:111], v[160:163], v[76:79], a[108:111]\n"
      "ds_read_b128 v[56:59], v2 offset:28672\n"
      "v_add_f32 v17, v17, v124\n"
      "v_mfma_f32_16x16x32_bf16 a[112:115], v[160:163], v[80:83], a[112:115]\n"
      "buffer_load_dwordx4 v11, s[44:47], s55 offen offset:3072 nt lds\n"
      "v_exp_f32 v125, v125\n"
      "s_add_u32 s55, s55, 0x2000\n"
      "v_mfma_f32_16x16x32_bf16 a[116:119], v[160:163], v[84:87], a[116:119]\n"
      "v_add_f32 v17, v17, v125\n"
      "v_cvt_pk_bf16_f32 v122, v124, v125\n"
      "v_mfma_f32_16x16x32_bf16 a[120:123], v[160:163], v[88:91], a[120:123]\n"
      "v_exp_f32 v126, v126\n"
      "v_mfma_f32_16x16x32_bf16 a[124:127], v[160:163], v[92:95], a[124:127]\n"
      "ds_read_b128 v[60:63], v3 offset:28672\n"
      "v_add_f32 v17, v17, v126\n"
      "v_mfma_f32_16x16x32_bf16 a[128:131], v[168:171], v[64:67], a[128:131]\n"
      "v_exp_f32 v127, v127\n"
      "v_mfma_f32_16x16x32_bf16 a[132:135], v[168:171], v[68:71], a[132:135]\n"
      "v_add_f32 v17, v17, v127\n"
      "v_cvt_pk_bf16_f32 v123, v126, v127\n"
      "v_mfma_f32_16x16x32_bf16 a[136:139], v[168:171], v[72:75], a[136:139]\n"
      "v_exp_f32 v128, v128\n"
      "v_mfma_f32_16x16x32_bf16 a[140:143], v[168:171], v[76:79], a[140:143]\n"
      "v_add_f32 v18, v18, v128\n"
      "v_mfma_f32_16x16x32_bf16 a[144:147], v[168:171], v[80:83], a[144:147]\n"
      "v_exp_f32 v129, v129\n"
      "v_mfma_f32_16x16x32_bf16 a[148:151], v[168:171], v[84:87], a[148:151]\n"
      "v_add_f32 v18, v18, v129\n"
      "v_cvt_pk_bf16_f32 v128, v128, v129\n"
      "v_mfma_f32_16x16x32_bf16 a[152:155], v[168:171], v[88:91], a[152:155]\n"
      "v_exp_f32 v130, v130\n"
      "v_mfma_f32_16x16x32_bf16 a[156:159], v[168:171], v[92:95], a[156:159]\n"
      "v_add_f32 v18, v18, v130\n"
      "s_waitcnt lgkmcnt(0)\n"
      "v_mfma_f32_16x16x32_bf16 v[136:139], v[32:35], a[192:195], v[192:195]\n"
      "v_exp_f32 v131, v131\n"
      "v_mfma_f32_16x16x32_bf16 v[136:139], v[36:39], a[196:199], v[136:139]\n"
      "v_add_f32 v18, v18, v131\n"
      "v_cvt_pk_bf16_f32 v129, v130, v131\n"
      "v_mfma_f32_16x16x32_bf16 v[136:139], v[40:43], a[200:203], v[136:139]\n"
      "v_exp_f32 v132, v132\n"
      "v_mfma_f32_16x16x32_bf16 v[136:139], v[44:47], a[204:207], v[136:139]\n"
      "ds_read_b64_tr_b16 v[64:65], v4 offset:16384\n"
      "v_add_f32 v18, v18, v132\n"
      "v_mfma_f32_16x16x32_bf16 v[140:143], v[48:51], a[192:195], v[192:195]\n"
      "v_exp_f32 v133, v133\n"
      "v_mfma_f32_16x16x32_bf16 v[140:143], v[52:55], a[196:199], v[140:143]\n"
      "v_add_f32 v18, v18, v133\n"
      "v_cvt_pk_bf16_f32 v130, v132, v133\n"
      "v_mfma_f32_16x16x32_bf16 v[140:143], v[56:59], a[200:203], v[140:143]\n"
      "v_exp_f32 v134, v134\n"
      "v_mfma_f32_16x16x32_bf16 v[140:143], v[60:63], a[204:207], v[140:143]\n"
      "ds_read_b64_tr_b16 v[66:67], v4 offset:20480\n"
      "v_add_f32 v18, v18, v134\n"
      "v_mfma_f32_16x16x32_bf16 v[144:147], v[32:35], a[208:211], v[196:199]\n"
      "v_exp_f32 v135, v135\n"
      "v_mfma_f32_16x16x32_bf16 v[144:147], v[36:39], a[212:215], v[144:147]\n"
      "v_add_f32 v18, v18, v135\n"
      "v_cvt_pk_bf16_f32 v131, v134, v135\n"
      "v_mfma_f32_16x16x32_bf16 v[144:147], v[40:43], a[216:219], v[144:147]\n"
      "v_exp_f32 v176, v176\n"
      "v_mfma_f32_16x16x32_bf16 v[144:147], v[44:47], a[220:223], v[144:147]\n"
      "ds_read_b64_tr_b16 v[68:69], v5 offset:16384\n"
      "v_add_f32 v19, v19, v176\n"
      "v_mfma_f32_16x16x32_bf16 v[148:151], v[48:51], a[208:211], v[196:199]\n"
      "v_exp_f32 v177, v177\n"
      "v_mfma_f32_16x16x32_bf16 v[148:151], v[52:55], a[212:215], v[148:151]\n"
      "v_add_f32 v19, v19, v177\n"
      "v_cvt_pk_bf16_f32 v176, v176, v177\n"
      "v_mfma_f32_16x16x32_bf16 v[148:151], v[56:59], a[216:219], v[148:151]\n"
      "v_exp_f32 v178, v178\n"
      "v_mfma_f32_16x16x32_bf16 v[148:151], v[60:63], a[220:223], v[148:151]\n"
      "ds_read_b64_tr_b16 v[70:71], v5 offset:20480\n"
      "v_add_f32 v19, v19, v178\n"
      "v_mfma_f32_16x16x32_bf16 v[152:155], v[32:35], a[224:227], v[200:203]\n"
      "v_exp_f32 v179, v179\n"
      "v_mfma_f32_16x16x32_bf16 v[152:155], v[36:39], a[228:231], v[152:155]\n"
      "v_add_f32 v19, v19, v179\n"
      "v_cvt_pk_bf16_f32 v177, v178, v179\n"
      "v_mfma_f32_16x16x32_bf16 v[152:155], v[40:43], a[232:235], v[152:155]\n"
      "ds_read_b64_tr_b16 v[72:73], v4 offset:16640\n"
      "ds_read_b64_tr_b16 v[74:75], v4 offset:20736\n"
      "v_mfma_f32_16x16x32_bf16 v[152:155], v[44:47], a[236:239], v[152:155]\n"
      "ds_read_b64_tr_b16 v[76:77], v5 offset:16640\n"
      "ds_read_b64_tr_b16 v[78:79], v5 offset:20736\n"
      "v_mfma_f32_16x16x32_bf16 v[156:159], v[48:51], a[224:227], v[200:203]\n"
      "v_exp_f32 v136, v136\n"
      "v_mfma_f32_16x16x32_bf16 v[156:159], v[52:55], a[228:231], v[156:159]\n"
      "ds_read_b64_tr_b16 v[80:81], v4 offset:16896\n"
      "v_add_f32 v14, v14, v136\n"
      "v_mfma_f32_16x16x32_bf16 v[156:159], v[56:59], a[232:235], v[156:159]\n"
      "v_exp_f32 v137, v137\n"
      "v_mfma_f32_16x16x32_bf16 v[156:159], v[60:63], a[236:239], v[156:159]\n"
      "v_add_f32 v14, v14, v137\n"
      "v_cvt_pk_bf16_f32 v136, v136, v137\n"
      "v_mfma_f32_16x16x32_bf16 v[160:163], v[32:35], a[240:243], v[204:207]\n"
      "v_exp_f32 v138, v138\n"
      "v_mfma_f32_16x16x32_bf16 v[160:163], v[36:39], a[244:247], v[160:163]\n"
      "ds_read_b64_tr_b16 v[82:83], v4 offset:20992\n"
      "v_add_f32 v14, v14, v138\n"
      "v_mfma_f32_16x16x32_bf16 v[160:163], v[40:43], a[248:251], v[160:163]\n"
      "v_exp_f32 v139, v139\n"
      "v_mfma_f32_16x16x32_bf16 v[160:163], v[44:47], a[252:255], v[160:163]\n"
      "v_add_f32 v14, v14, v139\n"
      "v_cvt_pk_bf16_f32 v137, v138, v139\n"
      "v_mfma_f32_16x16x32_bf16 v[164:167], v[48:51], a[240:243], v[204:207]\n"
      "v_exp_f32 v140, v140\n"
      "v_mfma_f32_16x16x32_bf16 v[164:167], v[52:55], a[244:247], v[164:167]\n"
      "ds_read_b64_tr_b16 v[84:85], v5 offset:16896\n"
      "ds_read_b64_tr_b16 v[86:87], v5 offset:20992\n"
      "v_add_f32 v14, v14, v140\n"
      "v_mfma_f32_16x16x32_bf16 v[164:167], v[56:59], a[248:251], v[164:167]\n"
      "v_exp_f32 v141, v141\n"
      "v_mfma_f32_16x16x32_bf16 v[164:167], v[60:63], a[252:255], v[164:167]\n"
      "v_add_f32 v14, v14, v141\n"
      "v_cvt_pk_bf16_f32 v138, v140, v141\n"
      "v_mfma_f32_16x16x32_bf16 v[168:171], v[32:35], v[216:219], v[208:211]\n"
      "v_exp_f32 v142, v142\n"
      "v_mfma_f32_16x16x32_bf16 v[168:171], v[36:39], v[220:223], v[168:171]\n"
      "ds_read_b64_tr_b16 v[88:89], v4 offset:17152\n"
      "ds_read_b64_tr_b16 v[90:91], v4 offset:21248\n"
      "v_add_f32 v14, v14, v142\n"
      "v_mfma_f32_16x16x32_bf16 v[168:171], v[40:43], v[224:227], v[168:171]\n"
      "v_exp_f32 v143, v143\n"
      "v_xor_b32 v3, 0x8000, v3\n"
      "v_mfma_f32_16x16x32_bf16 v[168:171], v[44:47], v[228:231], v[168:171]\n"
      "v_add_f32 v14, v14, v143\n"
      "v_cvt_pk_bf16_f32 v139, v142, v143\n"
      "v_mfma_f32_16x16x32_bf16 v[172:175], v[48:51], v[216:219], v[208:211]\n"
      "v_exp_f32 v144, v144\n"
      "v_xor_b32 v2, 0x8000, v2\n"
      "v_mfma_f32_16x16x32_bf16 v[172:175], v[52:55], v[220:223], v[172:175]\n"
      "ds_read_b64_tr_b16 v[92:93], v5 offset:17152\n"
      "ds_read_b64_tr_b16 v[94:95], v5 offset:21248\n"
      "v_add_f32 v15, v15, v144\n"
      "v_mfma_f32_16x16x32_bf16 v[172:175], v[56:59], v[224:227], v[172:175]\n"
      "v_exp_f32 v145, v145\n"
      "v_xor_b32 v1, 0x8000, v1\n"
      "v_mfma_f32_16x16x32_bf16 v[172:175], v[60:63], v[228:231], v[172:175]\n"
      "v_add_f32 v15, v15, v145\n"
      "v_cvt_pk_bf16_f32 v144, v144, v145\n"
      "s_waitcnt lgkmcnt(0)\n"
      "v_mfma_f32_16x16x32_bf16 a[0:3], v[96:99], v[64:67], a[0:3]\n"
      "v_exp_f32 v146, v146\n"
      "v_xor_b32 v0, 0x8000, v0\n"
      "v_mfma_f32_16x16x32_bf16 a[4:7], v[96:99], v[68:71], a[4:7]\n"
      "ds_read_b128 v[32:35], v0 offset:0\n"
      "v_add_f32 v15, v15, v146\n"
      "v_mfma_f32_16x16x32_bf16 a[8:11], v[96:99], v[72:75], a[8:11]\n"
      "v_exp_f32 v147, v147\n"
      "v_mfma_f32_16x16x32_bf16 a[12:15], v[96:99], v[76:79], a[12:15]\n"
      "v_add_f32 v15, v15, v147\n"
      "v_cvt_pk_bf16_f32 v145, v146, v147\n"
      "v_mfma_f32_16x16x32_bf16 a[16:19], v[96:99], v[80:83], a[16:19]\n"
      "v_exp_f32 v148, v148\n"
      "v_mfma_f32_16x16x32_bf16 a[20:23], v[96:99], v[84:87], a[20:23]\n"
      "v_add_f32 v15, v15, v148\n"
      "v_mfma_f32_16x16x32_bf16 a[24:27], v[96:99], v[88:91], a[24:27]\n"
      "v_exp_f32 v149, v149\n"
      "v_mfma_f32_16x16x32_bf16 a[28:31], v[96:99], v[92:95], a[28:31]\n"
      "v_add_f32 v15, v15, v149\n"
      "v_cvt_pk_bf16_f32 v146, v148, v149\n"
      "s_xor_b32 m0, s56, 0x11000\n"
      "v_mfma_f32_16x16x32_bf16 a[32:35], v[104:107], v[64:67], a[32:35]\n"
      "buffer_load_dwordx4 v10, s[44:47], s55 offen offset:0 nt lds\n"
      "v_exp_f32 v150, v150\n"
      "v_mfma_f32_16x16x32_bf16 a[36:39], v[104:107], v[68:71], a[36:39]\n"
      "ds_read_b128 v[36:39], v1 offset:0\n"
      "v_add_f32 v15, v15, v150\n"
      "v_mfma_f32_16x16x32_bf16 a[40:43], v[104:107], v[72:75], a[40:43]\n"
      "v_exp_f32 v151, v151\n"
      "v_mfma_f32_16x16x32_bf16 a[44:47], v[104:107], v[76:79], a[44:47]\n"
      "v_add_f32 v15, v15, v151\n"
      "v_cvt_pk_bf16_f32 v147, v150, v151\n"
      "v_mfma_f32_16x16x32_bf16 a[48:51], v[104:107], v[80:83], a[48:51]\n"
      "v_exp_f32 v152, v152\n"
      "v_mfma_f32_16x16x32_bf16 a[52:55], v[104:107], v[84:87], a[52:55]\n"
      "ds_read_b128 v[40:43], v2 offset:0\n"
      "v_add_f32 v16, v16, v152\n"
      "v_mfma_f32_16x16x32_bf16 a[56:59], v[104:107], v[88:91], a[56:59]\n"
      "v_exp_f32 v153, v153\n"
      "v_mfma_f32_16x16x32_bf16 a[60:63], v[104:107], v[92:95], a[60:63]\n"
      "v_add_f32 v16, v16, v153\n"
      "v_cvt_pk_bf16_f32 v152, v152, v153\n"
      "v_mfma_f32_16x16x32_bf16 a[64:67], v[112:115], v[64:67], a[64:67]\n"
      "buffer_load_dwordx4 v11, s[44:47], s55 offen offset:1024 nt lds\n"
      "v_exp_f32 v154, v154\n"
      "v_mfma_f32_16x16x32_bf16 a[68:71], v[112:115], v[68:71], a[68:71]\n"
      "ds_read_b128 v[44:47], v3 offset:0\n"
      "v_add_f32 v16, v16, v154\n"
      "v_mfma_f32_16x16x32_bf16 a[72:75], v[112:115], v[72:75], a[72:75]\n"
      "v_exp_f32 v155, v155\n"
      "v_mfma_f32_16x16x32_bf16 a[76:79], v[112:115], v[76:79], a[76:79]\n"
      "v_add_f32 v16, v16, v155\n"
      "v_cvt_pk_bf16_f32 v153, v154, v155\n"
      "v_mfma_f32_16x16x32_bf16 a[80:83], v[112:115], v[80:83], a[80:83]\n"
      "v_exp_f32 v156, v156\n"
      "v_mfma_f32_16x16x32_bf16 a[84:87], v[112:115], v[84:87], a[84:87]\n"
      "ds_read_b128 v[48:51], v0 offset:4096\n"
      "v_add_f32 v16, v16, v156\n"
      "v_mfma_f32_16x16x32_bf16 a[88:91], v[112:115], v[88:91], a[88:91]\n"
      "v_exp_f32 v157, v157\n"
      "v_mfma_f32_16x16x32_bf16 a[92:95], v[112:115], v[92:95], a[92:95]\n"
      "v_add_f32 v16, v16, v157\n"
      "v_cvt_pk_bf16_f32 v154, v156, v157\n"
      "v_mfma_f32_16x16x32_bf16 a[96:99], v[120:123], v[64:67], a[96:99]\n"
      "buffer_load_dwordx4 v10, s[44:47], s55 offen offset:2048 nt lds\n"
      "v_exp_f32 v158, v158\n"
      "v_mfma_f32_16x16x32_bf16 a[100:103], v[120:123], v[68:71], a[100:103]\n"
      "v_add_f32 v16, v16, v158\n"
      "v_mfma_f32_16x16x32_bf16 a[104:107], v[120:123], v[72:75], a[104:107]\n"
      "v_exp_f32 v159, v159\n"
      "v_mfma_f32_16x16x32_bf16 a[108:111], v[120:123], v[76:79], a[108:111]\n"
      "v_add_f32 v16, v16, v159\n"
      "v_cvt_pk_bf16_f32 v155, v158, v159\n"
      "v_mfma_f32_16x16x32_bf16 a[112:115], v[120:123], v[80:83], a[112:115]\n"
      "v_exp_f32 v160, v160\n"
      "v_mfma_f32_16x16x32_bf16 a[116:119], v[120:123], v[84:87], a[116:119]\n"
      "ds_read_b128 v[52:55], v1 offset:4096\n"
      "v_add_f32 v17, v17, v160\n"
      "v_mfma_f32_16x16x32_bf16 a[120:123], v[120:123], v[88:91], a[120:123]\n"
      "v_exp_f32 v161, v161\n"
      "v_mfma_f32_16x16x32_bf16 a[124:127], v[120:123], v[92:95], a[124:127]\n"
      "v_add_f32 v17, v17, v161\n"
      "v_cvt_pk_bf16_f32 v160, v160, v161\n"
      "v_mfma_f32_16x16x32_bf16 a[128:131], v[128:131], v[64:67], a[128:131]\n"
      "buffer_load_dwordx4 v11, s[44:47], s55 offen offset:3072 nt lds\n"
      "v_exp_f32 v162, v162\n"
      "s_add_u32 s55, s55, 0x2000\n"
      "s_xor_b32 s56, s56, 0x8000\n"
      "v_mfma_f32_16x16x32_bf16 a[132:135], v[128:131], v[68:71], a[132:135]\n"
      "ds_read_b128 v[56:59], v2 offset:4096\n"
      "v_add_f32 v17, v17, v162\n"
      "v_mfma_f32_16x16x32_bf16 a[136:139], v[128:131], v[72:75], a[136:139]\n"
      "v_exp_f32 v163, v163\n"
      "v_mfma_f32_16x16x32_bf16 a[140:143], v[128:131], v[76:79], a[140:143]\n"
      "v_add_f32 v17, v17, v163\n"
      "v_cvt_pk_bf16_f32 v161, v162, v163\n"
      "v_mfma_f32_16x16x32_bf16 a[144:147], v[128:131], v[80:83], a[144:147]\n"
      "v_exp_f32 v164, v164\n"
      "v_mfma_f32_16x16x32_bf16 a[148:151], v[128:131], v[84:87], a[148:151]\n"
      "ds_read_b128 v[60:63], v3 offset:4096\n"
      "v_add_f32 v17, v17, v164\n"
      "v_mfma_f32_16x16x32_bf16 a[152:155], v[128:131], v[88:91], a[152:155]\n"
      "v_exp_f32 v165, v165\n"
      "v_mfma_f32_16x16x32_bf16 a[156:159], v[128:131], v[92:95], a[156:159]\n"
      "v_add_f32 v17, v17, v165\n"
      "v_cvt_pk_bf16_f32 v162, v164, v165\n"
      "v_mfma_f32_16x16x16_bf16 a[160:163], v[176:177], v[66:67], a[160:163]\n"
      "v_exp_f32 v166, v166\n"
      "v_mfma_f32_16x16x16_bf16 a[164:167], v[176:177], v[70:71], a[164:167]\n"
      "v_add_f32 v17, v17, v166\n"
      "v_mfma_f32_16x16x16_bf16 a[168:171], v[176:177], v[74:75], a[168:171]\n"
      "v_exp_f32 v167, v167\n"
      "v_mfma_f32_16x16x16_bf16 a[172:175], v[176:177], v[78:79], a[172:175]\n"
      "v_add_f32 v17, v17, v167\n"
      "v_cvt_pk_bf16_f32 v163, v166, v167\n"
      "v_mfma_f32_16x16x16_bf16 a[176:179], v[176:177], v[82:83], a[176:179]\n"
      "v_exp_f32 v168, v168\n"
      "v_mfma_f32_16x16x16_bf16 a[180:183], v[176:177], v[86:87], a[180:183]\n"
      "v_add_f32 v18, v18, v168\n"
      "v_mfma_f32_16x16x16_bf16 a[184:187], v[176:177], v[90:91], a[184:187]\n"
      "v_exp_f32 v169, v169\n"
      "v_mfma_f32_16x16x16_bf16 a[188:191], v[176:177], v[94:95], a[188:191]\n"
      "v_add_f32 v18, v18, v169\n"
      "v_cvt_pk_bf16_f32 v168, v168, v169\n"
      "s_waitcnt lgkmcnt(0)\n"
      "s_waitcnt vmcnt(12)\n"
      "s_barrier\n"
      "s_sub_u32 s57, s57, 1\n"
      "s_cmp_lg_u32 s57, 0\n"
      "s_cbranch_scc1 L_loop_w1\n"
      "v_exp_f32 v170, v170\n"
      "ds_read_b64_tr_b16 v[64:65], v4 offset:24576\n"
      "v_add_f32 v18, v18, v170\n"
      "v_exp_f32 v171, v171\n"
      "v_exp_f32 v172, v172\n"
      "v_add_f32 v18, v18, v171\n"
      "v_cvt_pk_bf16_f32 v169, v170, v171\n"
      "ds_read_b64_tr_b16 v[66:67], v4 offset:28672\n"
      "v_add_f32 v18, v18, v172\n"
      "v_exp_f32 v173, v173\n"
      "v_exp_f32 v174, v174\n"
      "v_add_f32 v18, v18, v173\n"
      "v_cvt_pk_bf16_f32 v170, v172, v173\n"
      "ds_read_b64_tr_b16 v[68:69], v5 offset:24576\n"
      "v_add_f32 v18, v18, v174\n"
      "v_exp_f32 v175, v175\n"
      "s_nop 0\n"
      "v_add_f32 v18, v18, v175\n"
      "v_cvt_pk_bf16_f32 v171, v174, v175\n"
      "ds_read_b64_tr_b16 v[70:71], v5 offset:28672\n"
      "ds_read_b64_tr_b16 v[72:73], v4 offset:24832\n"
      "ds_read_b64_tr_b16 v[74:75], v4 offset:28928\n"
      "ds_read_b64_tr_b16 v[76:77], v5 offset:24832\n"
      "ds_read_b64_tr_b16 v[78:79], v5 offset:28928\n"
      "ds_read_b64_tr_b16 v[80:81], v4 offset:25088\n"
      "ds_read_b64_tr_b16 v[82:83], v4 offset:29184\n"
      "ds_read_b64_tr_b16 v[84:85], v5 offset:25088\n"
      "ds_read_b64_tr_b16 v[86:87], v5 offset:29184\n"
      "ds_read_b64_tr_b16 v[88:89], v4 offset:25344\n"
      "ds_read_b64_tr_b16 v[90:91], v4 offset:29440\n"
      "ds_read_b64_tr_b16 v[92:93], v5 offset:25344\n"
      "ds_read_b64_tr_b16 v[94:95], v5 offset:29440\n"
      "v_and_b32 v22, 15, v13\n"
      "v_lshrrev_b32 v23, 4, v13\n"
      "v_lshrrev_b32 v29, 2, v22\n"
      "v_xor_b32 v29, v29, v23\n"
      "v_lshlrev_b32 v29, 3, v29\n"
      "v_lshl_or_b32 v29, v22, 5, v29\n"
      "v_add_u32 v24, s64, v29\n"
      "v_bfe_u32 v29, v13, 2, 2\n"
      "v_lshl_or_b32 v29, v23, 3, v29\n"
      "v_lshlrev_b32 v29, 5, v29\n"
      "v_add_u32 v29, s64, v29\n"
      "v_lshlrev_b32 v30, 1, v23\n"
      "v_xor_b32 v30, v30, v13\n"
      "v_and_b32 v30, 3, v30\n"
      "v_lshl_add_u32 v25, v30, 3, v29\n"
      "v_xor_b32 v30, 1, v30\n"
      "v_lshl_add_u32 v26, v30, 3, v29\n"
      "v_add_u32 v26, 128, v26\n"
      "v_lshlrev_b32 v29, 4, v23\n"
      "v_lshl_or_b32 v253, v22, 8, v29\n"
      "v_add_u32 v27, 0x20e00, v29\n"
      "v_add_u32 v250, 0x20000, v29\n"
      "v_lshlrev_b32 v29, 2, v22\n"
      "v_add_u32 v251, 0x20e00, v29\n"
      "v_add_u32 v248, 0x16000, v12\n"
      "v_add_u32 v249, 0x14800, v12\n"
      "v_lshlrev_b32 v21, 2, v13\n"
      "v_add_u32 v21, 0x20000, v21\n"
      "v_add_u32 v0, s64, v0\n"
      "v_add_u32 v6, s64, v6\n"
      "v_add_u32 v1, s64, v1\n"
      "v_add_u32 v7, s64, v7\n"
      "v_add_u32 v2, s64, v2\n"
      "v_add_u32 v8, s64, v8\n"
      "v_add_u32 v3, s64, v3\n"
      "v_add_u32 v9, s64, v9\n"
      "s_waitcnt vmcnt(0)\n"
      "s_barrier\n"
      "s_waitcnt lgkmcnt(0)\n"
      "v_mfma_f32_16x16x32_bf16 a[0:3], v[136:139], v[64:67], a[0:3]\n"
      "v_mov_b32 v50, v14\n"
      "v_mov_b32 v51, v15\n"
      "v_mfma_f32_16x16x32_bf16 a[4:7], v[136:139], v[68:71], a[4:7]\n"
      "v_mov_b32 v52, v16\n"
      "v_mov_b32 v53, v17\n"
      "v_mfma_f32_16x16x32_bf16 a[8:11], v[136:139], v[72:75], a[8:11]\n"
      "v_mov_b32 v54, v18\n"
      "v_mov_b32 v55, v19\n"
      "v_mfma_f32_16x16x32_bf16 a[12:15], v[136:139], v[76:79], a[12:15]\n"
      "s_nop 1\n"
      "v_mfma_f32_16x16x32_bf16 a[16:19], v[136:139], v[80:83], a[16:19]\n"
      "v_permlane16_swap_b32 v14, v50\n"
      "v_permlane16_swap_b32 v15, v51\n"
      "v_mfma_f32_16x16x32_bf16 a[20:23], v[136:139], v[84:87], a[20:23]\n"
      "v_permlane16_swap_b32 v16, v52\n"
      "v_permlane16_swap_b32 v17, v53\n"
      "v_mfma_f32_16x16x32_bf16 a[24:27], v[136:139], v[88:91], a[24:27]\n"
      "v_permlane16_swap_b32 v18, v54\n"
      "v_permlane16_swap_b32 v19, v55\n"
      "v_mfma_f32_16x16x32_bf16 a[28:31], v[136:139], v[92:95], a[28:31]\n"
      "s_nop 1\n"
      "v_mfma_f32_16x16x32_bf16 a[32:35], v[144:147], v[64:67], a[32:35]\n"
      "v_add_f32 v14, v14, v50\n"
      "v_add_f32 v15, v15, v51\n"
      "v_mfma_f32_16x16x32_bf16 a[36:39], v[144:147], v[68:71], a[36:39]\n"
      "v_add_f32 v16, v16, v52\n"
      "v_add_f32 v17, v17, v53\n"
      "v_mfma_f32_16x16x32_bf16 a[40:43], v[144:147], v[72:75], a[40:43]\n"
      "v_add_f32 v18, v18, v54\n"
      "v_add_f32 v19, v19, v55\n"
      "v_mfma_f32_16x16x32_bf16 a[44:47], v[144:147], v[76:79], a[44:47]\n"
      "v_mov_b32 v50, v14\n"
      "v_mov_b32 v51, v15\n"
      "v_mfma_f32_16x16x32_bf16 a[48:51], v[144:147], v[80:83], a[48:51]\n"
      "v_mov_b32 v52, v16\n"
      "v_mov_b32 v53, v17\n"
      "v_mfma_f32_16x16x32_bf16 a[52:55], v[144:147], v[84:87], a[52:55]\n"
      "v_mov_b32 v54, v18\n"
      "v_mov_b32 v55, v19\n"
      "v_mfma_f32_16x16x32_bf16 a[56:59], v[144:147], v[88:91], a[56:59]\n"
      "s_nop 1\n"
      "v_mfma_f32_16x16x32_bf16 a[60:63], v[144:147], v[92:95], a[60:63]\n"
      "v_permlane32_swap_b32 v14, v50\n"
      "v_permlane32_swap_b32 v15, v51\n"
      "v_mfma_f32_16x16x32_bf16 a[64:67], v[152:155], v[64:67], a[64:67]\n"
      "v_permlane32_swap_b32 v16, v52\n"
      "v_permlane32_swap_b32 v17, v53\n"
      "v_mfma_f32_16x16x32_bf16 a[68:71], v[152:155], v[68:71], a[68:71]\n"
      "v_permlane32_swap_b32 v18, v54\n"
      "v_permlane32_swap_b32 v19, v55\n"
      "v_mfma_f32_16x16x32_bf16 a[72:75], v[152:155], v[72:75], a[72:75]\n"
      "s_nop 1\n"
      "v_mfma_f32_16x16x32_bf16 a[76:79], v[152:155], v[76:79], a[76:79]\n"
      "v_add_f32 v14, v14, v50\n"
      "v_add_f32 v15, v15, v51\n"
      "v_mfma_f32_16x16x32_bf16 a[80:83], v[152:155], v[80:83], a[80:83]\n"
      "v_add_f32 v16, v16, v52\n"
      "v_add_f32 v17, v17, v53\n"
      "v_mfma_f32_16x16x32_bf16 a[84:87], v[152:155], v[84:87], a[84:87]\n"
      "v_add_f32 v18, v18, v54\n"
      "v_add_f32 v19, v19, v55\n"
      "v_mfma_f32_16x16x32_bf16 a[88:91], v[152:155], v[88:91], a[88:91]\n"
      "s_mov_b64 s[98:99], 0\n"
      "v_cmp_nge_f32 vcc, s96, v14\n"
      "v_mfma_f32_16x16x32_bf16 a[92:95], v[152:155], v[92:95], a[92:95]\n"
      "v_rcp_f32 v40, v14\n"
      "s_nop 1\n"
      "v_mfma_f32_16x16x32_bf16 a[96:99], v[160:163], v[64:67], a[96:99]\n"
      "s_or_b64 s[98:99], s[98:99], vcc\n"
      "v_cmp_nge_f32 vcc, s96, v15\n"
      "v_mfma_f32_16x16x32_bf16 a[100:103], v[160:163], v[68:71], a[100:103]\n"
      "v_rcp_f32 v41, v15\n"
      "s_nop 1\n"
      "v_mfma_f32_16x16x32_bf16 a[104:107], v[160:163], v[72:75], a[104:107]\n"
      "s_or_b64 s[98:99], s[98:99], vcc\n"
      "v_cmp_nge_f32 vcc, s96, v16\n"
      "v_mfma_f32_16x16x32_bf16 a[108:111], v[160:163], v[76:79], a[108:111]\n"
      "v_rcp_f32 v42, v16\n"
      "s_nop 1\n"
      "v_mfma_f32_16x16x32_bf16 a[112:115], v[160:163], v[80:83], a[112:115]\n"
      "s_or_b64 s[98:99], s[98:99], vcc\n"
      "v_cmp_nge_f32 vcc, s96, v17\n"
      "v_mfma_f32_16x16x32_bf16 a[116:119], v[160:163], v[84:87], a[116:119]\n"
      "v_rcp_f32 v43, v17\n"
      "s_nop 1\n"
      "v_mfma_f32_16x16x32_bf16 a[120:123], v[160:163], v[88:91], a[120:123]\n"
      "s_or_b64 s[98:99], s[98:99], vcc\n"
      "v_cmp_nge_f32 vcc, s96, v18\n"
      "v_mfma_f32_16x16x32_bf16 a[124:127], v[160:163], v[92:95], a[124:127]\n"
      "v_rcp_f32 v44, v18\n"
      "s_nop 1\n"
      "v_mfma_f32_16x16x32_bf16 a[128:131], v[168:171], v[64:67], a[128:131]\n"
      "s_or_b64 s[98:99], s[98:99], vcc\n"
      "v_cmp_nge_f32 vcc, s96, v19\n"
      "v_mfma_f32_16x16x32_bf16 a[132:135], v[168:171], v[68:71], a[132:135]\n"
      "s_nop 1\n"
      "v_mfma_f32_16x16x32_bf16 a[136:139], v[168:171], v[72:75], a[136:139]\n"
      "s_or_b64 s[98:99], s[98:99], vcc\n"
      "ds_write_b32 v251, v40 offset:0\n"
      "v_mfma_f32_16x16x32_bf16 a[140:143], v[168:171], v[76:79], a[140:143]\n"
      "ds_write_b32 v251, v41 offset:64\n"
      "ds_write_b32 v251, v42 offset:128\n"
      "v_mfma_f32_16x16x32_bf16 a[144:147], v[168:171], v[80:83], a[144:147]\n"
      "ds_write_b32 v251, v43 offset:192\n"
      "ds_write_b32 v251, v44 offset:256\n"
      "v_mfma_f32_16x16x32_bf16 a[148:151], v[168:171], v[84:87], a[148:151]\n"
      "ds_read_b128 v[196:199], v27 offset:0\n"
      "ds_read_b128 v[200:203], v27 offset:64\n"
      "v_mfma_f32_16x16x32_bf16 a[152:155], v[168:171], v[88:91], a[152:155]\n"
      "ds_read_b128 v[204:207], v27 offset:128\n"
      "ds_read_b128 v[208:211], v27 offset:192\n"
      "v_mfma_f32_16x16x32_bf16 a[156:159], v[168:171], v[92:95], a[156:159]\n"
      "ds_read_b128 v[212:215], v27 offset:256\n"
      "s_cmp_lg_u64 s[98:99], 0\n"
      "s_cselect_b32 s59, 1, 0\n"
      "v_mov_b32 v28, s59\n"
      "v_mov_b32 v20, 0x20804\n"
      "ds_write_b128 v248, a[160:163] offset:0\n"
      "ds_write_b128 v248, a[164:167] offset:1024\n"
      "ds_write_b128 v248, a[168:171] offset:2048\n"
      "ds_write_b128 v248, a[172:175] offset:3072\n"
      "ds_write_b128 v248, a[176:179] offset:4096\n"
      "ds_write_b128 v248, a[180:183] offset:5120\n"
      "ds_write_b128 v248, a[184:187] offset:6144\n"
      "ds_write_b128 v248, a[188:191] offset:7168\n"
      "ds_write_b32 v21, v19 offset:256\n"
      "ds_write_b32 v20, v28 offset:0\n"
      "s_waitcnt lgkmcnt(10)\n"
      "v_accvgpr_read_b32 v96, a0\n"
      "v_accvgpr_read_b32 v97, a1\n"
      "v_accvgpr_read_b32 v98, a2\n"
      "v_accvgpr_read_b32 v99, a3\n"
      "v_mul_f32 v96, v196, v96\n"
      "v_mul_f32 v97, v197, v97\n"
      "v_mul_f32 v98, v198, v98\n"
      "v_mul_f32 v99, v199, v99\n"
      "v_cvt_pk_bf16_f32 v96, v96, v97\n"
      "v_cvt_pk_bf16_f32 v97, v98, v99\n"
      "ds_write_b64 v24, v[96:97] offset:0\n"
      "v_accvgpr_read_b32 v100, a4\n"
      "v_accvgpr_read_b32 v101, a5\n"
      "v_accvgpr_read_b32 v102, a6\n"
      "v_accvgpr_read_b32 v103, a7\n"
      "v_mul_f32 v100, v196, v100\n"
      "v_mul_f32 v101, v197, v101\n"
      "v_mul_f32 v102, v198, v102\n"
      "v_mul_f32 v103, v199, v103\n"
      "v_cvt_pk_bf16_f32 v100, v100, v101\n"
      "v_cvt_pk_bf16_f32 v101, v102, v103\n"
      "ds_write_b64 v24, v[100:101] offset:512\n"
      "v_accvgpr_read_b32 v104, a8\n"
      "v_accvgpr_read_b32 v105, a9\n"
      "v_accvgpr_read_b32 v106, a10\n"
      "v_accvgpr_read_b32 v107, a11\n"
      "v_mul_f32 v104, v196, v104\n"
      "v_mul_f32 v105, v197, v105\n"
      "v_mul_f32 v106, v198, v106\n"
      "v_mul_f32 v107, v199, v107\n"
      "v_cvt_pk_bf16_f32 v104, v104, v105\n"
      "v_cvt_pk_bf16_f32 v105, v106, v107\n"
      "ds_write_b64 v24, v[104:105] offset:1024\n"
      "v_accvgpr_read_b32 v108, a12\n"
      "v_accvgpr_read_b32 v109, a13\n"
      "v_accvgpr_read_b32 v110, a14\n"
      "v_accvgpr_read_b32 v111, a15\n"
      "v_mul_f32 v108, v196, v108\n"
      "v_mul_f32 v109, v197, v109\n"
      "v_mul_f32 v110, v198, v110\n"
      "v_mul_f32 v111, v199, v111\n"
      "v_cvt_pk_bf16_f32 v108, v108, v109\n"
      "v_cvt_pk_bf16_f32 v109, v110, v111\n"
      "ds_write_b64 v24, v[108:109] offset:1536\n"
      "v_accvgpr_read_b32 v112, a16\n"
      "v_accvgpr_read_b32 v113, a17\n"
      "v_accvgpr_read_b32 v114, a18\n"
      "v_accvgpr_read_b32 v115, a19\n"
      "v_mul_f32 v112, v196, v112\n"
      "v_mul_f32 v113, v197, v113\n"
      "v_mul_f32 v114, v198, v114\n"
      "v_mul_f32 v115, v199, v115\n"
      "v_cvt_pk_bf16_f32 v112, v112, v113\n"
      "v_cvt_pk_bf16_f32 v113, v114, v115\n"
      "ds_write_b64 v24, v[112:113] offset:2048\n"
      "v_accvgpr_read_b32 v116, a20\n"
      "v_accvgpr_read_b32 v117, a21\n"
      "v_accvgpr_read_b32 v118, a22\n"
      "v_accvgpr_read_b32 v119, a23\n"
      "v_mul_f32 v116, v196, v116\n"
      "v_mul_f32 v117, v197, v117\n"
      "v_mul_f32 v118, v198, v118\n"
      "v_mul_f32 v119, v199, v119\n"
      "v_cvt_pk_bf16_f32 v116, v116, v117\n"
      "v_cvt_pk_bf16_f32 v117, v118, v119\n"
      "ds_write_b64 v24, v[116:117] offset:2560\n"
      "v_accvgpr_read_b32 v120, a24\n"
      "v_accvgpr_read_b32 v121, a25\n"
      "v_accvgpr_read_b32 v122, a26\n"
      "v_accvgpr_read_b32 v123, a27\n"
      "v_mul_f32 v120, v196, v120\n"
      "v_mul_f32 v121, v197, v121\n"
      "v_mul_f32 v122, v198, v122\n"
      "v_mul_f32 v123, v199, v123\n"
      "v_cvt_pk_bf16_f32 v120, v120, v121\n"
      "v_cvt_pk_bf16_f32 v121, v122, v123\n"
      "ds_write_b64 v24, v[120:121] offset:3072\n"
      "v_accvgpr_read_b32 v124, a28\n"
      "v_accvgpr_read_b32 v125, a29\n"
      "v_accvgpr_read_b32 v126, a30\n"
      "v_accvgpr_read_b32 v127, a31\n"
      "v_mul_f32 v124, v196, v124\n"
      "v_mul_f32 v125, v197, v125\n"
      "v_mul_f32 v126, v198, v126\n"
      "v_mul_f32 v127, v199, v127\n"
      "v_cvt_pk_bf16_f32 v124, v124, v125\n"
      "v_cvt_pk_bf16_f32 v125, v126, v127\n"
      "ds_write_b64 v24, v[124:125] offset:3584\n"
      "ds_read_b64_tr_b16 v[64:65], v25 offset:0\n"
      "ds_read_b64_tr_b16 v[66:67], v26 offset:0\n"
      "ds_read_b64_tr_b16 v[68:69], v25 offset:1024\n"
      "ds_read_b64_tr_b16 v[70:71], v26 offset:1024\n"
      "ds_read_b64_tr_b16 v[72:73], v25 offset:2048\n"
      "ds_read_b64_tr_b16 v[74:75], v26 offset:2048\n"
      "ds_read_b64_tr_b16 v[76:77], v25 offset:3072\n"
      "ds_read_b64_tr_b16 v[78:79], v26 offset:3072\n"
      "v_accvgpr_read_b32 v128, a32\n"
      "v_accvgpr_read_b32 v129, a33\n"
      "v_accvgpr_read_b32 v130, a34\n"
      "v_accvgpr_read_b32 v131, a35\n"
      "v_pk_mul_f32 v[128:129], v[128:129], v[200:201]\n"
      "v_pk_mul_f32 v[130:131], v[130:131], v[202:203]\n"
      "v_cvt_pk_bf16_f32 v128, v128, v129\n"
      "v_cvt_pk_bf16_f32 v129, v130, v131\n"
      "ds_write_b64 v24, v[128:129] offset:4096\n"
      "v_accvgpr_read_b32 v132, a36\n"
      "v_accvgpr_read_b32 v133, a37\n"
      "v_accvgpr_read_b32 v134, a38\n"
      "v_accvgpr_read_b32 v135, a39\n"
      "v_pk_mul_f32 v[132:133], v[132:133], v[200:201]\n"
      "v_pk_mul_f32 v[134:135], v[134:135], v[202:203]\n"
      "v_cvt_pk_bf16_f32 v132, v132, v133\n"
      "v_cvt_pk_bf16_f32 v133, v134, v135\n"
      "ds_write_b64 v24, v[132:133] offset:4608\n"
      "v_accvgpr_read_b32 v136, a40\n"
      "v_accvgpr_read_b32 v137, a41\n"
      "v_accvgpr_read_b32 v138, a42\n"
      "v_accvgpr_read_b32 v139, a43\n"
      "v_pk_mul_f32 v[136:137], v[136:137], v[200:201]\n"
      "v_pk_mul_f32 v[138:139], v[138:139], v[202:203]\n"
      "v_cvt_pk_bf16_f32 v136, v136, v137\n"
      "v_cvt_pk_bf16_f32 v137, v138, v139\n"
      "ds_write_b64 v24, v[136:137] offset:5120\n"
      "v_accvgpr_read_b32 v140, a44\n"
      "v_accvgpr_read_b32 v141, a45\n"
      "v_accvgpr_read_b32 v142, a46\n"
      "v_accvgpr_read_b32 v143, a47\n"
      "v_pk_mul_f32 v[140:141], v[140:141], v[200:201]\n"
      "v_pk_mul_f32 v[142:143], v[142:143], v[202:203]\n"
      "v_cvt_pk_bf16_f32 v140, v140, v141\n"
      "v_cvt_pk_bf16_f32 v141, v142, v143\n"
      "ds_write_b64 v24, v[140:141] offset:5632\n"
      "s_waitcnt lgkmcnt(4)\n"
      "ds_write_b128 v0, v[64:67] offset:0\n"
      "ds_write_b128 v1, v[68:71] offset:0\n"
      "ds_write_b128 v2, v[72:75] offset:0\n"
      "ds_write_b128 v3, v[76:79] offset:0\n"
      "ds_read_b128 v[64:67], v6 offset:0\n"
      "ds_read_b128 v[68:71], v7 offset:1024\n"
      "ds_read_b128 v[72:75], v8 offset:2048\n"
      "ds_read_b128 v[76:79], v9 offset:3072\n"
      "v_accvgpr_read_b32 v144, a48\n"
      "v_accvgpr_read_b32 v145, a49\n"
      "v_accvgpr_read_b32 v146, a50\n"
      "v_accvgpr_read_b32 v147, a51\n"
      "v_pk_mul_f32 v[144:145], v[144:145], v[200:201]\n"
      "v_pk_mul_f32 v[146:147], v[146:147], v[202:203]\n"
      "v_cvt_pk_bf16_f32 v144, v144, v145\n"
      "v_cvt_pk_bf16_f32 v145, v146, v147\n"
      "ds_write_b64 v24, v[144:145] offset:6144\n"
      "v_accvgpr_read_b32 v148, a52\n"
      "v_accvgpr_read_b32 v149, a53\n"
      "v_accvgpr_read_b32 v150, a54\n"
      "v_accvgpr_read_b32 v151, a55\n"
      "v_pk_mul_f32 v[148:149], v[148:149], v[200:201]\n"
      "v_pk_mul_f32 v[150:151], v[150:151], v[202:203]\n"
      "v_cvt_pk_bf16_f32 v148, v148, v149\n"
      "v_cvt_pk_bf16_f32 v149, v150, v151\n"
      "ds_write_b64 v24, v[148:149] offset:6656\n"
      "v_accvgpr_read_b32 v152, a56\n"
      "v_accvgpr_read_b32 v153, a57\n"
      "v_accvgpr_read_b32 v154, a58\n"
      "v_accvgpr_read_b32 v155, a59\n"
      "v_pk_mul_f32 v[152:153], v[152:153], v[200:201]\n"
      "v_pk_mul_f32 v[154:155], v[154:155], v[202:203]\n"
      "v_cvt_pk_bf16_f32 v152, v152, v153\n"
      "v_cvt_pk_bf16_f32 v153, v154, v155\n"
      "ds_write_b64 v24, v[152:153] offset:7168\n"
      "v_accvgpr_read_b32 v156, a60\n"
      "v_accvgpr_read_b32 v157, a61\n"
      "v_accvgpr_read_b32 v158, a62\n"
      "v_accvgpr_read_b32 v159, a63\n"
      "v_pk_mul_f32 v[156:157], v[156:157], v[200:201]\n"
      "v_pk_mul_f32 v[158:159], v[158:159], v[202:203]\n"
      "v_cvt_pk_bf16_f32 v156, v156, v157\n"
      "v_cvt_pk_bf16_f32 v157, v158, v159\n"
      "ds_write_b64 v24, v[156:157] offset:7680\n"
      "s_waitcnt lgkmcnt(4)\n"
      "s_add_u32 s58, s62, 0\n"
      "buffer_store_dwordx4 v[64:67], v12, s[48:51], s58 offen offset:0 nt\n"
      "buffer_store_dwordx4 v[68:71], v12, s[48:51], s58 offen offset:1024 nt\n"
      "buffer_store_dwordx4 v[72:75], v12, s[48:51], s58 offen offset:2048 nt\n"
      "buffer_store_dwordx4 v[76:79], v12, s[48:51], s58 offen offset:3072 nt\n"
      "v_mov_b32 v20, 0x20800\n"
      "ds_read_b64_tr_b16 v[80:81], v25 offset:4096\n"
      "ds_read_b64_tr_b16 v[82:83], v26 offset:4096\n"
      "ds_read_b64_tr_b16 v[84:85], v25 offset:5120\n"
      "ds_read_b64_tr_b16 v[86:87], v26 offset:5120\n"
      "ds_read_b64_tr_b16 v[88:89], v25 offset:6144\n"
      "ds_read_b64_tr_b16 v[90:91], v26 offset:6144\n"
      "ds_read_b64_tr_b16 v[92:93], v25 offset:7168\n"
      "ds_read_b64_tr_b16 v[94:95], v26 offset:7168\n"
      "v_accvgpr_read_b32 v96, a64\n"
      "v_accvgpr_read_b32 v97, a65\n"
      "v_accvgpr_read_b32 v98, a66\n"
      "v_accvgpr_read_b32 v99, a67\n"
      "v_pk_mul_f32 v[96:97], v[96:97], v[204:205]\n"
      "v_pk_mul_f32 v[98:99], v[98:99], v[206:207]\n"
      "v_cvt_pk_bf16_f32 v96, v96, v97\n"
      "v_cvt_pk_bf16_f32 v97, v98, v99\n"
      "ds_write_b64 v24, v[96:97] offset:8192\n"
      "v_accvgpr_read_b32 v100, a68\n"
      "v_accvgpr_read_b32 v101, a69\n"
      "v_accvgpr_read_b32 v102, a70\n"
      "v_accvgpr_read_b32 v103, a71\n"
      "v_pk_mul_f32 v[100:101], v[100:101], v[204:205]\n"
      "v_pk_mul_f32 v[102:103], v[102:103], v[206:207]\n"
      "v_cvt_pk_bf16_f32 v100, v100, v101\n"
      "v_cvt_pk_bf16_f32 v101, v102, v103\n"
      "ds_write_b64 v24, v[100:101] offset:8704\n"
      "v_accvgpr_read_b32 v104, a72\n"
      "v_accvgpr_read_b32 v105, a73\n"
      "v_accvgpr_read_b32 v106, a74\n"
      "v_accvgpr_read_b32 v107, a75\n"
      "v_pk_mul_f32 v[104:105], v[104:105], v[204:205]\n"
      "v_pk_mul_f32 v[106:107], v[106:107], v[206:207]\n"
      "v_cvt_pk_bf16_f32 v104, v104, v105\n"
      "v_cvt_pk_bf16_f32 v105, v106, v107\n"
      "ds_write_b64 v24, v[104:105] offset:9216\n"
      "v_accvgpr_read_b32 v108, a76\n"
      "v_accvgpr_read_b32 v109, a77\n"
      "v_accvgpr_read_b32 v110, a78\n"
      "v_accvgpr_read_b32 v111, a79\n"
      "v_pk_mul_f32 v[108:109], v[108:109], v[204:205]\n"
      "v_pk_mul_f32 v[110:111], v[110:111], v[206:207]\n"
      "v_cvt_pk_bf16_f32 v108, v108, v109\n"
      "v_cvt_pk_bf16_f32 v109, v110, v111\n"
      "ds_write_b64 v24, v[108:109] offset:9728\n"
      "s_waitcnt lgkmcnt(4)\n"
      "ds_write_b128 v0, v[80:83] offset:4096\n"
      "ds_write_b128 v1, v[84:87] offset:4096\n"
      "ds_write_b128 v2, v[88:91] offset:4096\n"
      "ds_write_b128 v3, v[92:95] offset:4096\n"
      "ds_read_b128 v[80:83], v6 offset:4096\n"
      "ds_read_b128 v[84:87], v7 offset:5120\n"
      "ds_read_b128 v[88:91], v8 offset:6144\n"
      "ds_read_b128 v[92:95], v9 offset:7168\n"
      "v_accvgpr_read_b32 v112, a80\n"
      "v_accvgpr_read_b32 v113, a81\n"
      "v_accvgpr_read_b32 v114, a82\n"
      "v_accvgpr_read_b32 v115, a83\n"
      "v_pk_mul_f32 v[112:113], v[112:113], v[204:205]\n"
      "v_pk_mul_f32 v[114:115], v[114:115], v[206:207]\n"
      "v_cvt_pk_bf16_f32 v112, v112, v113\n"
      "v_cvt_pk_bf16_f32 v113, v114, v115\n"
      "ds_write_b64 v24, v[112:113] offset:10240\n"
      "v_accvgpr_read_b32 v116, a84\n"
      "v_accvgpr_read_b32 v117, a85\n"
      "v_accvgpr_read_b32 v118, a86\n"
      "v_accvgpr_read_b32 v119, a87\n"
      "v_pk_mul_f32 v[116:117], v[116:117], v[204:205]\n"
      "v_pk_mul_f32 v[118:119], v[118:119], v[206:207]\n"
      "v_cvt_pk_bf16_f32 v116, v116, v117\n"
      "v_cvt_pk_bf16_f32 v117, v118, v119\n"
      "ds_write_b64 v24, v[116:117] offset:10752\n"
      "v_accvgpr_read_b32 v120, a88\n"
      "v_accvgpr_read_b32 v121, a89\n"
      "v_accvgpr_read_b32 v122, a90\n"
      "v_accvgpr_read_b32 v123, a91\n"
      "v_pk_mul_f32 v[120:121], v[120:121], v[204:205]\n"
      "v_pk_mul_f32 v[122:123], v[122:123], v[206:207]\n"
      "v_cvt_pk_bf16_f32 v120, v120, v121\n"
      "v_cvt_pk_bf16_f32 v121, v122, v123\n"
      "ds_write_b64 v24, v[120:121] offset:11264\n"
      "v_accvgpr_read_b32 v124, a92\n"
      "v_accvgpr_read_b32 v125, a93\n"
      "v_accvgpr_read_b32 v126, a94\n"
      "v_accvgpr_read_b32 v127, a95\n"
      "v_pk_mul_f32 v[124:125], v[124:125], v[204:205]\n"
      "v_pk_mul_f32 v[126:127], v[126:127], v[206:207]\n"
      "v_cvt_pk_bf16_f32 v124, v124, v125\n"
      "v_cvt_pk_bf16_f32 v125, v126, v127\n"
      "ds_write_b64 v24, v[124:125] offset:11776\n"
      "s_waitcnt lgkmcnt(4)\n"
      "s_add_u32 s58, s62, 4096\n"
      "buffer_store_dwordx4 v[80:83], v12, s[48:51], s58 offen offset:0 nt\n"
      "buffer_store_dwordx4 v[84:87], v12, s[48:51], s58 offen offset:1024 nt\n"
      "buffer_store_dwordx4 v[88:91], v12, s[48:51], s58 offen offset:2048 nt\n"
      "buffer_store_dwordx4 v[92:95], v12, s[48:51], s58 offen offset:3072 nt\n"
      "s_barrier\n"
      "ds_read_b64_tr_b16 v[64:65], v25 offset:8192\n"
      "ds_read_b64_tr_b16 v[66:67], v26 offset:8192\n"
      "ds_read_b64_tr_b16 v[68:69], v25 offset:9216\n"
      "ds_read_b64_tr_b16 v[70:71], v26 offset:9216\n"
      "ds_read_b64_tr_b16 v[72:73], v25 offset:10240\n"
      "ds_read_b64_tr_b16 v[74:75], v26 offset:10240\n"
      "ds_read_b64_tr_b16 v[76:77], v25 offset:11264\n"
      "ds_read_b64_tr_b16 v[78:79], v26 offset:11264\n"
      "ds_read_b128 v[160:163], v249 offset:0\n"
      "ds_read_b128 v[164:167], v249 offset:1024\n"
      "ds_read_b128 v[168:171], v249 offset:8192\n"
      "ds_read_b128 v[172:175], v249 offset:9216\n"
      "ds_read_b128 v[176:179], v249 offset:16384\n"
      "ds_read_b128 v[180:183], v249 offset:17408\n"
      "ds_read_b128 v[184:187], v249 offset:24576\n"
      "ds_read_b128 v[188:191], v249 offset:25600\n"
      "ds_read_b128 v[32:35], v250 offset:0\n"
      "ds_read_b128 v[36:39], v250 offset:256\n"
      "ds_read_b128 v[40:43], v250 offset:512\n"
      "ds_read_b128 v[44:47], v250 offset:768\n"
      "ds_read_b128 v[48:51], v20 offset:0\n"
      "v_accvgpr_read_b32 v128, a96\n"
      "v_accvgpr_read_b32 v129, a97\n"
      "v_accvgpr_read_b32 v130, a98\n"
      "v_accvgpr_read_b32 v131, a99\n"
      "v_pk_mul_f32 v[128:129], v[128:129], v[208:209]\n"
      "v_pk_mul_f32 v[130:131], v[130:131], v[210:211]\n"
      "v_cvt_pk_bf16_f32 v128, v128, v129\n"
      "v_cvt_pk_bf16_f32 v129, v130, v131\n"
      "ds_write_b64 v24, v[128:129] offset:12288\n"
      "v_accvgpr_read_b32 v132, a100\n"
      "v_accvgpr_read_b32 v133, a101\n"
      "v_accvgpr_read_b32 v134, a102\n"
      "v_accvgpr_read_b32 v135, a103\n"
      "v_pk_mul_f32 v[132:133], v[132:133], v[208:209]\n"
      "v_pk_mul_f32 v[134:135], v[134:135], v[210:211]\n"
      "v_cvt_pk_bf16_f32 v132, v132, v133\n"
      "v_cvt_pk_bf16_f32 v133, v134, v135\n"
      "ds_write_b64 v24, v[132:133] offset:12800\n"
      "v_accvgpr_read_b32 v136, a104\n"
      "v_accvgpr_read_b32 v137, a105\n"
      "v_accvgpr_read_b32 v138, a106\n"
      "v_accvgpr_read_b32 v139, a107\n"
      "v_pk_mul_f32 v[136:137], v[136:137], v[208:209]\n"
      "v_pk_mul_f32 v[138:139], v[138:139], v[210:211]\n"
      "v_cvt_pk_bf16_f32 v136, v136, v137\n"
      "v_cvt_pk_bf16_f32 v137, v138, v139\n"
      "ds_write_b64 v24, v[136:137] offset:13312\n"
      "v_accvgpr_read_b32 v140, a108\n"
      "v_accvgpr_read_b32 v141, a109\n"
      "v_accvgpr_read_b32 v142, a110\n"
      "v_accvgpr_read_b32 v143, a111\n"
      "v_pk_mul_f32 v[140:141], v[140:141], v[208:209]\n"
      "v_pk_mul_f32 v[142:143], v[142:143], v[210:211]\n"
      "v_cvt_pk_bf16_f32 v140, v140, v141\n"
      "v_cvt_pk_bf16_f32 v141, v142, v143\n"
      "ds_write_b64 v24, v[140:141] offset:13824\n"
      "s_waitcnt lgkmcnt(15)\n"
      "ds_write_b128 v0, v[64:67] offset:8192\n"
      "ds_write_b128 v1, v[68:71] offset:8192\n"
      "ds_write_b128 v2, v[72:75] offset:8192\n"
      "ds_write_b128 v3, v[76:79] offset:8192\n"
      "ds_read_b128 v[64:67], v6 offset:8192\n"
      "ds_read_b128 v[68:71], v7 offset:9216\n"
      "ds_read_b128 v[72:75], v8 offset:10240\n"
      "ds_read_b128 v[76:79], v9 offset:11264\n"
      "v_accvgpr_read_b32 v144, a112\n"
      "v_accvgpr_read_b32 v145, a113\n"
      "v_accvgpr_read_b32 v146, a114\n"
      "v_accvgpr_read_b32 v147, a115\n"
      "v_pk_mul_f32 v[144:145], v[144:145], v[208:209]\n"
      "v_pk_mul_f32 v[146:147], v[146:147], v[210:211]\n"
      "v_cvt_pk_bf16_f32 v144, v144, v145\n"
      "v_cvt_pk_bf16_f32 v145, v146, v147\n"
      "ds_write_b64 v24, v[144:145] offset:14336\n"
      "v_accvgpr_read_b32 v148, a116\n"
      "v_accvgpr_read_b32 v149, a117\n"
      "v_accvgpr_read_b32 v150, a118\n"
      "v_accvgpr_read_b32 v151, a119\n"
      "v_pk_mul_f32 v[148:149], v[148:149], v[208:209]\n"
      "v_pk_mul_f32 v[150:151], v[150:151], v[210:211]\n"
      "v_cvt_pk_bf16_f32 v148, v148, v149\n"
      "v_cvt_pk_bf16_f32 v149, v150, v151\n"
      "ds_write_b64 v24, v[148:149] offset:14848\n"
      "v_accvgpr_read_b32 v152, a120\n"
      "v_accvgpr_read_b32 v153, a121\n"
      "v_accvgpr_read_b32 v154, a122\n"
      "v_accvgpr_read_b32 v155, a123\n"
      "v_pk_mul_f32 v[152:153], v[152:153], v[208:209]\n"
      "v_pk_mul_f32 v[154:155], v[154:155], v[210:211]\n"
      "v_cvt_pk_bf16_f32 v152, v152, v153\n"
      "v_cvt_pk_bf16_f32 v153, v154, v155\n"
      "ds_write_b64 v24, v[152:153] offset:15360\n"
      "v_accvgpr_read_b32 v156, a124\n"
      "v_accvgpr_read_b32 v157, a125\n"
      "v_accvgpr_read_b32 v158, a126\n"
      "v_accvgpr_read_b32 v159, a127\n"
      "v_pk_mul_f32 v[156:157], v[156:157], v[208:209]\n"
      "v_pk_mul_f32 v[158:159], v[158:159], v[210:211]\n"
      "v_cvt_pk_bf16_f32 v156, v156, v157\n"
      "v_cvt_pk_bf16_f32 v157, v158, v159\n"
      "ds_write_b64 v24, v[156:157] offset:15872\n"
      "s_waitcnt lgkmcnt(4)\n"
      "s_add_u32 s58, s62, 8192\n"
      "buffer_store_dwordx4 v[64:67], v12, s[48:51], s58 offen offset:0 nt\n"
      "buffer_store_dwordx4 v[68:71], v12, s[48:51], s58 offen offset:1024 nt\n"
      "buffer_store_dwordx4 v[72:75], v12, s[48:51], s58 offen offset:2048 nt\n"
      "buffer_store_dwordx4 v[76:79], v12, s[48:51], s58 offen offset:3072 nt\n"
      "v_add_f32 v32, v32, v36\n"
      "v_add_f32 v40, v40, v44\n"
      "v_add_f32 v33, v33, v37\n"
      "v_add_f32 v41, v41, v45\n"
      "v_add_f32 v34, v34, v38\n"
      "v_add_f32 v42, v42, v46\n"
      "v_add_f32 v35, v35, v39\n"
      "v_add_f32 v43, v43, v47\n"
      "v_add_f32 v160, v160, v168\n"
      "v_add_f32 v176, v176, v184\n"
      "v_add_f32 v161, v161, v169\n"
      "v_add_f32 v177, v177, v185\n"
      "v_add_f32 v162, v162, v170\n"
      "v_add_f32 v178, v178, v186\n"
      "v_add_f32 v163, v163, v171\n"
      "v_add_f32 v179, v179, v187\n"
      "v_add_f32 v164, v164, v172\n"
      "v_add_f32 v180, v180, v188\n"
      "v_add_f32 v165, v165, v173\n"
      "v_add_f32 v181, v181, v189\n"
      "v_add_f32 v166, v166, v174\n"
      "v_add_f32 v182, v182, v190\n"
      "v_add_f32 v167, v167, v175\n"
      "v_add_f32 v183, v183, v191\n"
      "v_add_f32 v32, v32, v40\n"
      "v_add_f32 v33, v33, v41\n"
      "v_add_f32 v34, v34, v42\n"
      "v_add_f32 v35, v35, v43\n"
      "v_add_f32 v160, v160, v176\n"
      "v_add_f32 v161, v161, v177\n"
      "v_add_f32 v162, v162, v178\n"
      "v_add_f32 v163, v163, v179\n"
      "v_add_f32 v164, v164, v180\n"
      "v_add_f32 v165, v165, v181\n"
      "v_add_f32 v166, v166, v182\n"
      "v_add_f32 v167, v167, v183\n"
      "v_rcp_f32 v52, v32\n"
      "v_rcp_f32 v53, v33\n"
      "v_rcp_f32 v54, v34\n"
      "v_rcp_f32 v55, v35\n"
      "v_or3_b32 v48, v48, v49, v50\n"
      "v_or_b32 v48, v48, v51\n"
      "v_mul_f32 v160, v52, v160\n"
      "v_mul_f32 v161, v53, v161\n"
      "v_mul_f32 v162, v54, v162\n"
      "v_mul_f32 v163, v55, v163\n"
      "v_mul_f32 v164, v52, v164\n"
      "v_mul_f32 v165, v53, v165\n"
      "v_mul_f32 v166, v54, v166\n"
      "v_mul_f32 v167, v55, v167\n"
      "v_cvt_pk_bf16_f32 v56, v160, v161\n"
      "v_cvt_pk_bf16_f32 v57, v162, v163\n"
      "v_cvt_pk_bf16_f32 v58, v164, v165\n"
      "v_cvt_pk_bf16_f32 v59, v166, v167\n"
      "ds_write_b64 v24, v[56:57] offset:0\n"
      "ds_write_b64 v24, v[58:59] offset:512\n"
      "ds_read_b64_tr_b16 v[60:61], v25 offset:0\n"
      "ds_read_b64_tr_b16 v[62:63], v26 offset:0\n"
      "v_readfirstlane_b32 s60, v48\n"
      "s_mov_b32 s59, 0x14040\n"
      "s_waitcnt lgkmcnt(0)\n"
      "buffer_store_dwordx4 v[60:63], v253, s[48:51], s59 offen offset:0 nt\n"
      "ds_read_b64_tr_b16 v[80:81], v25 offset:12288\n"
      "ds_read_b64_tr_b16 v[82:83], v26 offset:12288\n"
      "ds_read_b64_tr_b16 v[84:85], v25 offset:13312\n"
      "ds_read_b64_tr_b16 v[86:87], v26 offset:13312\n"
      "ds_read_b64_tr_b16 v[88:89], v25 offset:14336\n"
      "ds_read_b64_tr_b16 v[90:91], v26 offset:14336\n"
      "ds_read_b64_tr_b16 v[92:93], v25 offset:15360\n"
      "ds_read_b64_tr_b16 v[94:95], v26 offset:15360\n"
      "v_accvgpr_read_b32 v96, a128\n"
      "v_accvgpr_read_b32 v97, a129\n"
      "v_accvgpr_read_b32 v98, a130\n"
      "v_accvgpr_read_b32 v99, a131\n"
      "v_pk_mul_f32 v[96:97], v[96:97], v[212:213]\n"
      "v_pk_mul_f32 v[98:99], v[98:99], v[214:215]\n"
      "v_cvt_pk_bf16_f32 v96, v96, v97\n"
      "v_cvt_pk_bf16_f32 v97, v98, v99\n"
      "ds_write_b64 v24, v[96:97] offset:16384\n"
      "v_accvgpr_read_b32 v100, a132\n"
      "v_accvgpr_read_b32 v101, a133\n"
      "v_accvgpr_read_b32 v102, a134\n"
      "v_accvgpr_read_b32 v103, a135\n"
      "v_pk_mul_f32 v[100:101], v[100:101], v[212:213]\n"
      "v_pk_mul_f32 v[102:103], v[102:103], v[214:215]\n"
      "v_cvt_pk_bf16_f32 v100, v100, v101\n"
      "v_cvt_pk_bf16_f32 v101, v102, v103\n"
      "ds_write_b64 v24, v[100:101] offset:16896\n"
      "v_accvgpr_read_b32 v104, a136\n"
      "v_accvgpr_read_b32 v105, a137\n"
      "v_accvgpr_read_b32 v106, a138\n"
      "v_accvgpr_read_b32 v107, a139\n"
      "v_pk_mul_f32 v[104:105], v[104:105], v[212:213]\n"
      "v_pk_mul_f32 v[106:107], v[106:107], v[214:215]\n"
      "v_cvt_pk_bf16_f32 v104, v104, v105\n"
      "v_cvt_pk_bf16_f32 v105, v106, v107\n"
      "ds_write_b64 v24, v[104:105] offset:17408\n"
      "v_accvgpr_read_b32 v108, a140\n"
      "v_accvgpr_read_b32 v109, a141\n"
      "v_accvgpr_read_b32 v110, a142\n"
      "v_accvgpr_read_b32 v111, a143\n"
      "v_pk_mul_f32 v[108:109], v[108:109], v[212:213]\n"
      "v_pk_mul_f32 v[110:111], v[110:111], v[214:215]\n"
      "v_cvt_pk_bf16_f32 v108, v108, v109\n"
      "v_cvt_pk_bf16_f32 v109, v110, v111\n"
      "ds_write_b64 v24, v[108:109] offset:17920\n"
      "s_waitcnt lgkmcnt(4)\n"
      "ds_write_b128 v0, v[80:83] offset:12288\n"
      "ds_write_b128 v1, v[84:87] offset:12288\n"
      "ds_write_b128 v2, v[88:91] offset:12288\n"
      "ds_write_b128 v3, v[92:95] offset:12288\n"
      "ds_read_b128 v[80:83], v6 offset:12288\n"
      "ds_read_b128 v[84:87], v7 offset:13312\n"
      "ds_read_b128 v[88:91], v8 offset:14336\n"
      "ds_read_b128 v[92:95], v9 offset:15360\n"
      "v_accvgpr_read_b32 v112, a144\n"
      "v_accvgpr_read_b32 v113, a145\n"
      "v_accvgpr_read_b32 v114, a146\n"
      "v_accvgpr_read_b32 v115, a147\n"
      "v_pk_mul_f32 v[112:113], v[112:113], v[212:213]\n"
      "v_pk_mul_f32 v[114:115], v[114:115], v[214:215]\n"
      "v_cvt_pk_bf16_f32 v112, v112, v113\n"
      "v_cvt_pk_bf16_f32 v113, v114, v115\n"
      "ds_write_b64 v24, v[112:113] offset:18432\n"
      "v_accvgpr_read_b32 v116, a148\n"
      "v_accvgpr_read_b32 v117, a149\n"
      "v_accvgpr_read_b32 v118, a150\n"
      "v_accvgpr_read_b32 v119, a151\n"
      "v_pk_mul_f32 v[116:117], v[116:117], v[212:213]\n"
      "v_pk_mul_f32 v[118:119], v[118:119], v[214:215]\n"
      "v_cvt_pk_bf16_f32 v116, v116, v117\n"
      "v_cvt_pk_bf16_f32 v117, v118, v119\n"
      "ds_write_b64 v24, v[116:117] offset:18944\n"
      "v_accvgpr_read_b32 v120, a152\n"
      "v_accvgpr_read_b32 v121, a153\n"
      "v_accvgpr_read_b32 v122, a154\n"
      "v_accvgpr_read_b32 v123, a155\n"
      "v_pk_mul_f32 v[120:121], v[120:121], v[212:213]\n"
      "v_pk_mul_f32 v[122:123], v[122:123], v[214:215]\n"
      "v_cvt_pk_bf16_f32 v120, v120, v121\n"
      "v_cvt_pk_bf16_f32 v121, v122, v123\n"
      "ds_write_b64 v24, v[120:121] offset:19456\n"
      "v_accvgpr_read_b32 v124, a156\n"
      "v_accvgpr_read_b32 v125, a157\n"
      "v_accvgpr_read_b32 v126, a158\n"
      "v_accvgpr_read_b32 v127, a159\n"
      "v_pk_mul_f32 v[124:125], v[124:125], v[212:213]\n"
      "v_pk_mul_f32 v[126:127], v[126:127], v[214:215]\n"
      "v_cvt_pk_bf16_f32 v124, v124, v125\n"
      "v_cvt_pk_bf16_f32 v125, v126, v127\n"
      "ds_write_b64 v24, v[124:125] offset:19968\n"
      "s_waitcnt lgkmcnt(4)\n"
      "s_add_u32 s58, s62, 12288\n"
      "buffer_store_dwordx4 v[80:83], v12, s[48:51], s58 offen offset:0 nt\n"
      "buffer_store_dwordx4 v[84:87], v12, s[48:51], s58 offen offset:1024 nt\n"
      "buffer_store_dwordx4 v[88:91], v12, s[48:51], s58 offen offset:2048 nt\n"
      "buffer_store_dwordx4 v[92:95], v12, s[48:51], s58 offen offset:3072 nt\n"
      "ds_read_b64_tr_b16 v[64:65], v25 offset:16384\n"
      "ds_read_b64_tr_b16 v[66:67], v26 offset:16384\n"
      "ds_read_b64_tr_b16 v[68:69], v25 offset:17408\n"
      "ds_read_b64_tr_b16 v[70:71], v26 offset:17408\n"
      "ds_read_b64_tr_b16 v[72:73], v25 offset:18432\n"
      "ds_read_b64_tr_b16 v[74:75], v26 offset:18432\n"
      "ds_read_b64_tr_b16 v[76:77], v25 offset:19456\n"
      "ds_read_b64_tr_b16 v[78:79], v26 offset:19456\n"
      "s_waitcnt lgkmcnt(0)\n"
      "s_add_u32 s58, s62, 16384\n"
      "ds_write_b128 v0, v[64:67] offset:16384\n"
      "ds_write_b128 v1, v[68:71] offset:16384\n"
      "ds_write_b128 v2, v[72:75] offset:16384\n"
      "ds_write_b128 v3, v[76:79] offset:16384\n"
      "ds_read_b128 v[64:67], v6 offset:16384\n"
      "ds_read_b128 v[68:71], v7 offset:17408\n"
      "ds_read_b128 v[72:75], v8 offset:18432\n"
      "ds_read_b128 v[76:79], v9 offset:19456\n"
      "s_waitcnt lgkmcnt(0)\n"
      "buffer_store_dwordx4 v[64:67], v12, s[48:51], s58 offen offset:0 nt\n"
      "buffer_store_dwordx4 v[68:71], v12, s[48:51], s58 offen offset:1024 nt\n"
      "buffer_store_dwordx4 v[72:75], v12, s[48:51], s58 offen offset:2048 nt\n"
      "buffer_store_dwordx4 v[76:79], v12, s[48:51], s58 offen offset:3072 nt\n"
      "s_andn2_b32 s60, s60, s65\n"
      "s_cmp_lg_u32 s60, 0\n"
      "s_cbranch_scc1 L_fallback_w1\n"
      "s_branch L_end\n"
      "L_fallback_w1:\n"
      "s_mov_b32 s65, 1\n"
      "s_waitcnt vmcnt(0) lgkmcnt(0)\n"
      "s_barrier\n"
      "v_subrev_u32 v0, s64, v0\n"
      "v_subrev_u32 v6, s64, v6\n"
      "v_subrev_u32 v1, s64, v1\n"
      "v_subrev_u32 v7, s64, v7\n"
      "v_subrev_u32 v2, s64, v2\n"
      "v_subrev_u32 v8, s64, v8\n"
      "v_subrev_u32 v3, s64, v3\n"
      "v_subrev_u32 v9, s64, v9\n"
      "v_and_b32 v20, 15, v13\n"
      "v_lshrrev_b32 v21, 4, v13\n"
      "v_lshlrev_b32 v22, 4, v21\n"
      "v_lshl_or_b32 v22, v20, 8, v22\n"
      "v_mov_b32 v64, 0xff800000\n"
      "v_mov_b32 v65, 0xff800000\n"
      "v_mov_b32 v66, 0xff800000\n"
      "v_mov_b32 v67, 0xff800000\n"
      "v_mov_b32 v68, 0xff800000\n"
      "v_mov_b32 v69, 0xff800000\n"
      "s_mov_b32 s60, 0\n"
      "L_fb_loop_w1:\n"
      "s_add_u32 s61, s60, 4096\n"
      "buffer_load_dwordx4 v[32:35], v22, s[40:43], s60 offen offset:0\n"
      "buffer_load_dwordx4 v[36:39], v22, s[40:43], s60 offen offset:64\n"
      "buffer_load_dwordx4 v[40:43], v22, s[40:43], s60 offen offset:128\n"
      "buffer_load_dwordx4 v[44:47], v22, s[40:43], s60 offen offset:192\n"
      "buffer_load_dwordx4 v[48:51], v22, s[40:43], s61 offen offset:0\n"
      "buffer_load_dwordx4 v[52:55], v22, s[40:43], s61 offen offset:64\n"
      "buffer_load_dwordx4 v[56:59], v22, s[40:43], s61 offen offset:128\n"
      "buffer_load_dwordx4 v[60:63], v22, s[40:43], s61 offen offset:192\n"
      "s_waitcnt vmcnt(0)\n"
      "v_mfma_f32_16x16x32_bf16 v[96:99], v[32:35], a[192:195], 0\n"
      "v_mfma_f32_16x16x32_bf16 v[96:99], v[36:39], a[196:199], v[96:99]\n"
      "v_mfma_f32_16x16x32_bf16 v[96:99], v[40:43], a[200:203], v[96:99]\n"
      "v_mfma_f32_16x16x32_bf16 v[96:99], v[44:47], a[204:207], v[96:99]\n"
      "v_mfma_f32_16x16x32_bf16 v[100:103], v[48:51], a[192:195], 0\n"
      "v_mfma_f32_16x16x32_bf16 v[100:103], v[52:55], a[196:199], v[100:103]\n"
      "v_mfma_f32_16x16x32_bf16 v[100:103], v[56:59], a[200:203], v[100:103]\n"
      "v_mfma_f32_16x16x32_bf16 v[100:103], v[60:63], a[204:207], v[100:103]\n"
      "s_nop 7\n"
      "v_max3_f32 v64, v64, v96, v97\n"
      "v_max3_f32 v64, v64, v98, v99\n"
      "v_max3_f32 v64, v64, v100, v101\n"
      "v_max3_f32 v64, v64, v102, v103\n"
      "v_mfma_f32_16x16x32_bf16 v[96:99], v[32:35], a[208:211], 0\n"
      "v_mfma_f32_16x16x32_bf16 v[96:99], v[36:39], a[212:215], v[96:99]\n"
      "v_mfma_f32_16x16x32_bf16 v[96:99], v[40:43], a[216:219], v[96:99]\n"
      "v_mfma_f32_16x16x32_bf16 v[96:99], v[44:47], a[220:223], v[96:99]\n"
      "v_mfma_f32_16x16x32_bf16 v[100:103], v[48:51], a[208:211], 0\n"
      "v_mfma_f32_16x16x32_bf16 v[100:103], v[52:55], a[212:215], v[100:103]\n"
      "v_mfma_f32_16x16x32_bf16 v[100:103], v[56:59], a[216:219], v[100:103]\n"
      "v_mfma_f32_16x16x32_bf16 v[100:103], v[60:63], a[220:223], v[100:103]\n"
      "s_nop 7\n"
      "v_max3_f32 v65, v65, v96, v97\n"
      "v_max3_f32 v65, v65, v98, v99\n"
      "v_max3_f32 v65, v65, v100, v101\n"
      "v_max3_f32 v65, v65, v102, v103\n"
      "v_mfma_f32_16x16x32_bf16 v[96:99], v[32:35], a[224:227], 0\n"
      "v_mfma_f32_16x16x32_bf16 v[96:99], v[36:39], a[228:231], v[96:99]\n"
      "v_mfma_f32_16x16x32_bf16 v[96:99], v[40:43], a[232:235], v[96:99]\n"
      "v_mfma_f32_16x16x32_bf16 v[96:99], v[44:47], a[236:239], v[96:99]\n"
      "v_mfma_f32_16x16x32_bf16 v[100:103], v[48:51], a[224:227], 0\n"
      "v_mfma_f32_16x16x32_bf16 v[100:103], v[52:55], a[228:231], v[100:103]\n"
      "v_mfma_f32_16x16x32_bf16 v[100:103], v[56:59], a[232:235], v[100:103]\n"
      "v_mfma_f32_16x16x32_bf16 v[100:103], v[60:63], a[236:239], v[100:103]\n"
      "s_nop 7\n"
      "v_max3_f32 v66, v66, v96, v97\n"
      "v_max3_f32 v66, v66, v98, v99\n"
      "v_max3_f32 v66, v66, v100, v101\n"
      "v_max3_f32 v66, v66, v102, v103\n"
      "v_mfma_f32_16x16x32_bf16 v[96:99], v[32:35], a[240:243], 0\n"
      "v_mfma_f32_16x16x32_bf16 v[96:99], v[36:39], a[244:247], v[96:99]\n"
      "v_mfma_f32_16x16x32_bf16 v[96:99], v[40:43], a[248:251], v[96:99]\n"
      "v_mfma_f32_16x16x32_bf16 v[96:99], v[44:47], a[252:255], v[96:99]\n"
      "v_mfma_f32_16x16x32_bf16 v[100:103], v[48:51], a[240:243], 0\n"
      "v_mfma_f32_16x16x32_bf16 v[100:103], v[52:55], a[244:247], v[100:103]\n"
      "v_mfma_f32_16x16x32_bf16 v[100:103], v[56:59], a[248:251], v[100:103]\n"
      "v_mfma_f32_16x16x32_bf16 v[100:103], v[60:63], a[252:255], v[100:103]\n"
      "s_nop 7\n"
      "v_max3_f32 v67, v67, v96, v97\n"
      "v_max3_f32 v67, v67, v98, v99\n"
      "v_max3_f32 v67, v67, v100, v101\n"
      "v_max3_f32 v67, v67, v102, v103\n"
      "v_mfma_f32_16x16x32_bf16 v[96:99], v[32:35], v[216:219], 0\n"
      "v_mfma_f32_16x16x32_bf16 v[96:99], v[36:39], v[220:223], v[96:99]\n"
      "v_mfma_f32_16x16x32_bf16 v[96:99], v[40:43], v[224:227], v[96:99]\n"
      "v_mfma_f32_16x16x32_bf16 v[96:99], v[44:47], v[228:231], v[96:99]\n"
      "v_mfma_f32_16x16x32_bf16 v[100:103], v[48:51], v[216:219], 0\n"
      "v_mfma_f32_16x16x32_bf16 v[100:103], v[52:55], v[220:223], v[100:103]\n"
      "v_mfma_f32_16x16x32_bf16 v[100:103], v[56:59], v[224:227], v[100:103]\n"
      "v_mfma_f32_16x16x32_bf16 v[100:103], v[60:63], v[228:231], v[100:103]\n"
      "s_nop 7\n"
      "v_max3_f32 v68, v68, v96, v97\n"
      "v_max3_f32 v68, v68, v98, v99\n"
      "v_max3_f32 v68, v68, v100, v101\n"
      "v_max3_f32 v68, v68, v102, v103\n"
      "v_mfma_f32_16x16x32_bf16 v[96:99], v[32:35], v[232:235], 0\n"
      "v_mfma_f32_16x16x32_bf16 v[96:99], v[36:39], v[236:239], v[96:99]\n"
      "v_mfma_f32_16x16x32_bf16 v[96:99], v[40:43], v[240:243], v[96:99]\n"
      "v_mfma_f32_16x16x32_bf16 v[96:99], v[44:47], v[244:247], v[96:99]\n"
      "v_mfma_f32_16x16x32_bf16 v[100:103], v[48:51], v[232:235], 0\n"
      "v_mfma_f32_16x16x32_bf16 v[100:103], v[52:55], v[236:239], v[100:103]\n"
      "v_mfma_f32_16x16x32_bf16 v[100:103], v[56:59], v[240:243], v[100:103]\n"
      "v_mfma_f32_16x16x32_bf16 v[100:103], v[60:63], v[244:247], v[100:103]\n"
      "s_nop 7\n"
      "v_max3_f32 v69, v69, v96, v97\n"
      "v_max3_f32 v69, v69, v98, v99\n"
      "v_max3_f32 v69, v69, v100, v101\n"
      "v_max3_f32 v69, v69, v102, v103\n"
      "s_add_u32 s60, s60, 8192\n"
      "s_cmp_lt_u32 s60, 0x80000\n"
      "s_cbranch_scc1 L_fb_loop_w1\n"
      "v_mov_b32 v29, v64\n"
      "s_nop 1\n"
      "v_permlane16_swap_b32 v64, v29\n"
      "s_nop 0\n"
      "v_max_f32 v64, v64, v29\n"
      "v_mov_b32 v29, v64\n"
      "s_nop 1\n"
      "v_permlane32_swap_b32 v64, v29\n"
      "s_nop 0\n"
      "v_max_f32 v64, v64, v29\n"
      "v_sub_f32 v192, 0, v64\n"
      "v_mov_b32 v193, v192\n"
      "v_mov_b32 v194, v192\n"
      "v_mov_b32 v195, v192\n"
      "v_mov_b32 v29, v65\n"
      "s_nop 1\n"
      "v_permlane16_swap_b32 v65, v29\n"
      "s_nop 0\n"
      "v_max_f32 v65, v65, v29\n"
      "v_mov_b32 v29, v65\n"
      "s_nop 1\n"
      "v_permlane32_swap_b32 v65, v29\n"
      "s_nop 0\n"
      "v_max_f32 v65, v65, v29\n"
      "v_sub_f32 v196, 0, v65\n"
      "v_mov_b32 v197, v196\n"
      "v_mov_b32 v198, v196\n"
      "v_mov_b32 v199, v196\n"
      "v_mov_b32 v29, v66\n"
      "s_nop 1\n"
      "v_permlane16_swap_b32 v66, v29\n"
      "s_nop 0\n"
      "v_max_f32 v66, v66, v29\n"
      "v_mov_b32 v29, v66\n"
      "s_nop 1\n"
      "v_permlane32_swap_b32 v66, v29\n"
      "s_nop 0\n"
      "v_max_f32 v66, v66, v29\n"
      "v_sub_f32 v200, 0, v66\n"
      "v_mov_b32 v201, v200\n"
      "v_mov_b32 v202, v200\n"
      "v_mov_b32 v203, v200\n"
      "v_mov_b32 v29, v67\n"
      "s_nop 1\n"
      "v_permlane16_swap_b32 v67, v29\n"
      "s_nop 0\n"
      "v_max_f32 v67, v67, v29\n"
      "v_mov_b32 v29, v67\n"
      "s_nop 1\n"
      "v_permlane32_swap_b32 v67, v29\n"
      "s_nop 0\n"
      "v_max_f32 v67, v67, v29\n"
      "v_sub_f32 v204, 0, v67\n"
      "v_mov_b32 v205, v204\n"
      "v_mov_b32 v206, v204\n"
      "v_mov_b32 v207, v204\n"
      "v_mov_b32 v29, v68\n"
      "s_nop 1\n"
      "v_permlane16_swap_b32 v68, v29\n"
      "s_nop 0\n"
      "v_max_f32 v68, v68, v29\n"
      "v_mov_b32 v29, v68\n"
      "s_nop 1\n"
      "v_permlane32_swap_b32 v68, v29\n"
      "s_nop 0\n"
      "v_max_f32 v68, v68, v29\n"
      "v_sub_f32 v208, 0, v68\n"
      "v_mov_b32 v209, v208\n"
      "v_mov_b32 v210, v208\n"
      "v_mov_b32 v211, v208\n"
      "v_mov_b32 v29, v69\n"
      "s_nop 1\n"
      "v_permlane16_swap_b32 v69, v29\n"
      "s_nop 0\n"
      "v_max_f32 v69, v69, v29\n"
      "v_mov_b32 v29, v69\n"
      "s_nop 1\n"
      "v_permlane32_swap_b32 v69, v29\n"
      "s_nop 0\n"
      "v_max_f32 v69, v69, v29\n"
      "v_sub_f32 v212, 0, v69\n"
      "v_mov_b32 v213, v212\n"
      "v_mov_b32 v214, v212\n"
      "v_mov_b32 v215, v212\n"
      "v_mov_b32 v64, 0\n"
      "v_mov_b32 v65, 0\n"
      "v_mov_b32 v66, 0\n"
      "v_mov_b32 v67, 0\n"
      "v_mov_b32 v14, 0\n"
      "v_mov_b32 v15, 0\n"
      "v_mov_b32 v16, 0\n"
      "v_mov_b32 v17, 0\n"
      "v_mov_b32 v18, 0\n"
      "v_mov_b32 v19, 0\n"
      "v_mfma_f32_32x32x16_bf16 a[0:15], v[64:67], v[64:67], 0\n"
      "v_mfma_f32_32x32x16_bf16 a[16:31], v[64:67], v[64:67], 0\n"
      "v_mfma_f32_32x32x16_bf16 a[32:47], v[64:67], v[64:67], 0\n"
      "v_mfma_f32_32x32x16_bf16 a[48:63], v[64:67], v[64:67], 0\n"
      "v_mfma_f32_32x32x16_bf16 a[64:79], v[64:67], v[64:67], 0\n"
      "v_mfma_f32_32x32x16_bf16 a[80:95], v[64:67], v[64:67], 0\n"
      "v_mfma_f32_32x32x16_bf16 a[96:111], v[64:67], v[64:67], 0\n"
      "v_mfma_f32_32x32x16_bf16 a[112:127], v[64:67], v[64:67], 0\n"
      "v_mfma_f32_32x32x16_bf16 a[128:143], v[64:67], v[64:67], 0\n"
      "v_mfma_f32_32x32x16_bf16 a[144:159], v[64:67], v[64:67], 0\n"
      "v_mfma_f32_32x32x16_bf16 a[160:175], v[64:67], v[64:67], 0\n"
      "v_mfma_f32_32x32x16_bf16 a[176:191], v[64:67], v[64:67], 0\n"
      "v_xor_b32 v4, 0x8000, v4\n"
      "v_xor_b32 v5, 0x8000, v5\n"
      "s_mov_b32 s58, 0x1000\n"
      "s_mov_b32 m0, 0x11000\n"
      "s_nop 0\n"
      "buffer_load_dwordx4 v10, s[44:47], s58 offen offset:0 nt lds\n"
      "buffer_load_dwordx4 v11, s[44:47], s58 offen offset:1024 nt lds\n"
      "buffer_load_dwordx4 v10, s[44:47], s58 offen offset:2048 nt lds\n"
      "buffer_load_dwordx4 v11, s[44:47], s58 offen offset:3072 nt lds\n"
      "s_mov_b32 s58, 0x3000\n"
      "s_mov_b32 m0, 0x13000\n"
      "s_nop 0\n"
      "buffer_load_dwordx4 v10, s[44:47], s58 offen offset:0 nt lds\n"
      "buffer_load_dwordx4 v11, s[44:47], s58 offen offset:1024 nt lds\n"
      "buffer_load_dwordx4 v10, s[44:47], s58 offen offset:2048 nt lds\n"
      "buffer_load_dwordx4 v11, s[44:47], s58 offen offset:3072 nt lds\n"
      "s_mov_b32 s58, 0x5000\n"
      "s_mov_b32 m0, 0x15000\n"
      "s_nop 0\n"
      "buffer_load_dwordx4 v10, s[44:47], s58 offen offset:0 nt lds\n"
      "buffer_load_dwordx4 v11, s[44:47], s58 offen offset:1024 nt lds\n"
      "buffer_load_dwordx4 v10, s[44:47], s58 offen offset:2048 nt lds\n"
      "buffer_load_dwordx4 v11, s[44:47], s58 offen offset:3072 nt lds\n"
      "s_mov_b32 s58, 0x7000\n"
      "s_mov_b32 m0, 0x17000\n"
      "s_nop 0\n"
      "buffer_load_dwordx4 v10, s[44:47], s58 offen offset:0 nt lds\n"
      "buffer_load_dwordx4 v11, s[44:47], s58 offen offset:1024 nt lds\n"
      "buffer_load_dwordx4 v10, s[44:47], s58 offen offset:2048 nt lds\n"
      "buffer_load_dwordx4 v11, s[44:47], s58 offen offset:3072 nt lds\n"
      "s_waitcnt vmcnt(8)\n"
      "s_barrier\n"
      "ds_read_b128 v[32:35], v0 offset:0\n"
      "ds_read_b128 v[36:39], v1 offset:0\n"
      "ds_read_b128 v[40:43], v2 offset:0\n"
      "ds_read_b128 v[44:47], v3 offset:0\n"
      "ds_read_b128 v[48:51], v0 offset:4096\n"
      "ds_read_b128 v[52:55], v1 offset:4096\n"
      "ds_read_b128 v[56:59], v2 offset:4096\n"
      "ds_read_b128 v[60:63], v3 offset:4096\n"
      "s_waitcnt lgkmcnt(0)\n"
      "v_mfma_f32_16x16x32_bf16 v[96:99], v[32:35], a[192:195], v[192:195]\n"
      "v_mfma_f32_16x16x32_bf16 v[96:99], v[36:39], a[196:199], v[96:99]\n"
      "v_mfma_f32_16x16x32_bf16 v[96:99], v[40:43], a[200:203], v[96:99]\n"
      "v_mfma_f32_16x16x32_bf16 v[96:99], v[44:47], a[204:207], v[96:99]\n"
      "v_mfma_f32_16x16x32_bf16 v[100:103], v[48:51], a[192:195], v[192:195]\n"
      "v_mfma_f32_16x16x32_bf16 v[100:103], v[52:55], a[196:199], v[100:103]\n"
      "v_mfma_f32_16x16x32_bf16 v[100:103], v[56:59], a[200:203], v[100:103]\n"
      "v_mfma_f32_16x16x32_bf16 v[100:103], v[60:63], a[204:207], v[100:103]\n"
      "v_mfma_f32_16x16x32_bf16 v[104:107], v[32:35], a[208:211], v[196:199]\n"
      "v_mfma_f32_16x16x32_bf16 v[104:107], v[36:39], a[212:215], v[104:107]\n"
      "v_mfma_f32_16x16x32_bf16 v[104:107], v[40:43], a[216:219], v[104:107]\n"
      "v_mfma_f32_16x16x32_bf16 v[104:107], v[44:47], a[220:223], v[104:107]\n"
      "v_mfma_f32_16x16x32_bf16 v[108:111], v[48:51], a[208:211], v[196:199]\n"
      "v_mfma_f32_16x16x32_bf16 v[108:111], v[52:55], a[212:215], v[108:111]\n"
      "v_mfma_f32_16x16x32_bf16 v[108:111], v[56:59], a[216:219], v[108:111]\n"
      "v_mfma_f32_16x16x32_bf16 v[108:111], v[60:63], a[220:223], v[108:111]\n"
      "v_mfma_f32_16x16x32_bf16 v[112:115], v[32:35], a[224:227], v[200:203]\n"
      "v_mfma_f32_16x16x32_bf16 v[112:115], v[36:39], a[228:231], v[112:115]\n"
      "v_mfma_f32_16x16x32_bf16 v[112:115], v[40:43], a[232:235], v[112:115]\n"
      "v_mfma_f32_16x16x32_bf16 v[112:115], v[44:47], a[236:239], v[112:115]\n"
      "v_mfma_f32_16x16x32_bf16 v[116:119], v[48:51], a[224:227], v[200:203]\n"
      "v_mfma_f32_16x16x32_bf16 v[116:119], v[52:55], a[228:231], v[116:119]\n"
      "v_mfma_f32_16x16x32_bf16 v[116:119], v[56:59], a[232:235], v[116:119]\n"
      "v_mfma_f32_16x16x32_bf16 v[116:119], v[60:63], a[236:239], v[116:119]\n"
      "v_mfma_f32_16x16x32_bf16 v[120:123], v[32:35], a[240:243], v[204:207]\n"
      "v_mfma_f32_16x16x32_bf16 v[120:123], v[36:39], a[244:247], v[120:123]\n"
      "v_mfma_f32_16x16x32_bf16 v[120:123], v[40:43], a[248:251], v[120:123]\n"
      "v_mfma_f32_16x16x32_bf16 v[120:123], v[44:47], a[252:255], v[120:123]\n"
      "v_mfma_f32_16x16x32_bf16 v[124:127], v[48:51], a[240:243], v[204:207]\n"
      "v_mfma_f32_16x16x32_bf16 v[124:127], v[52:55], a[244:247], v[124:127]\n"
      "v_mfma_f32_16x16x32_bf16 v[124:127], v[56:59], a[248:251], v[124:127]\n"
      "v_mfma_f32_16x16x32_bf16 v[124:127], v[60:63], a[252:255], v[124:127]\n"
      "v_mfma_f32_16x16x32_bf16 v[128:131], v[32:35], v[216:219], v[208:211]\n"
      "v_mfma_f32_16x16x32_bf16 v[128:131], v[36:39], v[220:223], v[128:131]\n"
      "v_mfma_f32_16x16x32_bf16 v[128:131], v[40:43], v[224:227], v[128:131]\n"
      "v_mfma_f32_16x16x32_bf16 v[128:131], v[44:47], v[228:231], v[128:131]\n"
      "v_mfma_f32_16x16x32_bf16 v[132:135], v[48:51], v[216:219], v[208:211]\n"
      "v_mfma_f32_16x16x32_bf16 v[132:135], v[52:55], v[220:223], v[132:135]\n"
      "v_mfma_f32_16x16x32_bf16 v[132:135], v[56:59], v[224:227], v[132:135]\n"
      "v_mfma_f32_16x16x32_bf16 v[132:135], v[60:63], v[228:231], v[132:135]\n"
      "v_mfma_f32_16x16x32_bf16 v[168:171], v[32:35], v[232:235], v[212:215]\n"
      "v_mfma_f32_16x16x32_bf16 v[168:171], v[36:39], v[236:239], v[168:171]\n"
      "v_mfma_f32_16x16x32_bf16 v[168:171], v[40:43], v[240:243], v[168:171]\n"
      "v_mfma_f32_16x16x32_bf16 v[168:171], v[44:47], v[244:247], v[168:171]\n"
      "v_mfma_f32_16x16x32_bf16 v[176:179], v[48:51], v[232:235], v[212:215]\n"
      "v_mfma_f32_16x16x32_bf16 v[176:179], v[52:55], v[236:239], v[176:179]\n"
      "v_mfma_f32_16x16x32_bf16 v[176:179], v[56:59], v[240:243], v[176:179]\n"
      "v_mfma_f32_16x16x32_bf16 v[176:179], v[60:63], v[244:247], v[176:179]\n"
      "s_nop 7\n"
      "s_branch L_restart_w1\n"
      "L_path_2:\n"
      "s_mov_b32 s58, 0x0\n"
      "s_mov_b32 m0, 0x0\n"
      "s_nop 0\n"
      "buffer_load_dwordx4 v6, s[40:43], s58 offen offset:0 lds\n"
      "buffer_load_dwordx4 v7, s[40:43], s58 offen offset:1024 lds\n"
      "buffer_load_dwordx4 v8, s[40:43], s58 offen offset:2048 lds\n"
      "buffer_load_dwordx4 v9, s[40:43], s58 offen offset:3072 lds\n"
      "s_mov_b32 s58, 0x14000\n"
      "s_mov_b32 m0, 0xf000\n"
      "s_nop 0\n"
      "buffer_load_dwordx4 v8, s[36:39], s58 offen offset:2048 nt lds\n"
      "s_add_u32 s58, s62, 0\n"
      "s_add_u32 s59, s63, 0\n"
      "s_mov_b32 m0, s59\n"
      "s_nop 0\n"
      "buffer_load_dwordx4 v6, s[36:39], s58 offen offset:0 nt lds\n"
      "buffer_load_dwordx4 v7, s[36:39], s58 offen offset:1024 nt lds\n"
      "buffer_load_dwordx4 v8, s[36:39], s58 offen offset:2048 nt lds\n"
      "buffer_load_dwordx4 v9, s[36:39], s58 offen offset:3072 nt lds\n"
      "s_add_u32 s58, s62, 4096\n"
      "s_add_u32 s59, s63, 4096\n"
      "s_mov_b32 m0, s59\n"
      "s_nop 0\n"
      "buffer_load_dwordx4 v6, s[36:39], s58 offen offset:0 nt lds\n"
      "buffer_load_dwordx4 v7, s[36:39], s58 offen offset:1024 nt lds\n"
      "buffer_load_dwordx4 v8, s[36:39], s58 offen offset:2048 nt lds\n"
      "buffer_load_dwordx4 v9, s[36:39], s58 offen offset:3072 nt lds\n"
      "s_add_u32 s58, s62, 8192\n"
      "s_add_u32 s59, s63, 8192\n"
      "s_mov_b32 m0, s59\n"
      "s_nop 0\n"
      "buffer_load_dwordx4 v6, s[36:39], s58 offen offset:0 nt lds\n"
      "buffer_load_dwordx4 v7, s[36:39], s58 offen offset:1024 nt lds\n"
      "buffer_load_dwordx4 v8, s[36:39], s58 offen offset:2048 nt lds\n"
      "buffer_load_dwordx4 v9, s[36:39], s58 offen offset:3072 nt lds\n"
      "s_mov_b64 s[58:59], %2\n"
      "s_mul_i32 s60, s53, 0x80000\n"
      "s_add_u32 s44, s58, s60\n"
      "s_addc_u32 s59, s59, 0\n"
      "s_and_b32 s45, s59, 0xffff\n"
      "s_mov_b32 s46, 0x80000\n"
      "s_mov_b32 s47, 0x20000\n"
      "s_mov_b64 s[58:59], %3\n"
      "s_mul_i32 s60, s53, 0x15000\n"
      "s_add_u32 s48, s58, s60\n"
      "s_addc_u32 s59, s59, 0\n"
      "s_and_b32 s49, s59, 0xffff\n"
      "s_mov_b32 s50, 0x15000\n"
      "s_mov_b32 s51, 0x20000\n"
      "s_mov_b32 s54, 0x3e0293ee\n"
      "s_mov_b32 s65, 0\n"
      "s_mov_b32 s96, 0x57800000\n"
      "s_mov_b32 s97, 0xffff0000\n"
      "v_lshl_or_b32 v0, v20, 8, v22\n"
      "v_xor_b32 v1, 64, v0\n"
      "v_xor_b32 v2, 128, v0\n"
      "v_xor_b32 v3, 192, v0\n"
      "v_bfe_u32 v23, v13, 2, 2\n"
      "v_and_b32 v24, 3, v13\n"
      "v_lshlrev_b32 v25, 4, v24\n"
      "v_lshl_or_b32 v25, v21, 6, v25\n"
      "v_lshl_or_b32 v10, v23, 8, v25\n"
      "v_xor_b32 v11, 32, v10\n"
      "v_lshlrev_b32 v25, 3, v24\n"
      "v_lshl_or_b32 v25, v23, 6, v25\n"
      "v_lshl_or_b32 v25, v21, 10, v25\n"
      "v_and_b32 v23, 1, v21\n"
      "v_lshl_or_b32 v25, v23, 5, v25\n"
      "v_or_b32 v4, 0x10000, v25\n"
      "v_xor_b32 v5, 32, v4\n"
      "v_lshlrev_b32 v12, 4, v13\n"
      "v_add_u32 v248, s63, v0\n"
      "v_add_u32 v249, s63, v1\n"
      "v_add_u32 v250, s63, v2\n"
      "v_add_u32 v251, s63, v3\n"
      "v_mov_b32 v64, 0\n"
      "v_mov_b32 v65, 0\n"
      "v_mov_b32 v66, 0\n"
      "v_mov_b32 v67, 0\n"
      "v_mov_b32 v14, 0\n"
      "v_mov_b32 v15, 0\n"
      "v_mov_b32 v16, 0\n"
      "v_mov_b32 v17, 0\n"
      "v_mov_b32 v18, 0\n"
      "v_mov_b32 v19, 0\n"
      "v_mfma_f32_32x32x16_bf16 a[0:15], v[64:67], v[64:67], 0\n"
      "v_mfma_f32_32x32x16_bf16 a[16:31], v[64:67], v[64:67], 0\n"
      "v_mfma_f32_32x32x16_bf16 a[32:47], v[64:67], v[64:67], 0\n"
      "v_mfma_f32_32x32x16_bf16 a[48:63], v[64:67], v[64:67], 0\n"
      "v_mfma_f32_32x32x16_bf16 a[64:79], v[64:67], v[64:67], 0\n"
      "v_mfma_f32_32x32x16_bf16 a[80:95], v[64:67], v[64:67], 0\n"
      "v_mfma_f32_32x32x16_bf16 a[96:111], v[64:67], v[64:67], 0\n"
      "v_mfma_f32_32x32x16_bf16 a[112:127], v[64:67], v[64:67], 0\n"
      "v_mfma_f32_32x32x16_bf16 a[128:143], v[64:67], v[64:67], 0\n"
      "v_mfma_f32_32x32x16_bf16 a[144:159], v[64:67], v[64:67], 0\n"
      "v_mfma_f32_32x32x16_bf16 a[160:175], v[64:67], v[64:67], 0\n"
      "v_mfma_f32_32x32x16_bf16 a[176:191], v[64:67], v[64:67], 0\n"
      "s_waitcnt vmcnt(8)\n"
      "ds_read_b128 v[136:139], v248 offset:0\n"
      "ds_read_b128 v[140:143], v249 offset:0\n"
      "ds_read_b128 v[144:147], v250 offset:0\n"
      "ds_read_b128 v[148:151], v251 offset:0\n"
      "s_add_u32 s58, s62, 12288\n"
      "s_add_u32 s59, s63, 12288\n"
      "s_mov_b32 m0, s59\n"
      "s_nop 0\n"
      "buffer_load_dwordx4 v6, s[36:39], s58 offen offset:0 nt lds\n"
      "buffer_load_dwordx4 v7, s[36:39], s58 offen offset:1024 nt lds\n"
      "buffer_load_dwordx4 v8, s[36:39], s58 offen offset:2048 nt lds\n"
      "buffer_load_dwordx4 v9, s[36:39], s58 offen offset:3072 nt lds\n"
      "s_waitcnt lgkmcnt(0)\n"
      "v_lshlrev_b32 v20, 16, v136\n"
      "v_and_b32 v21, s97, v136\n"
      "v_lshlrev_b32 v22, 16, v137\n"
      "v_and_b32 v23, s97, v137\n"
      "v_lshlrev_b32 v24, 16, v138\n"
      "v_and_b32 v25, s97, v138\n"
      "v_lshlrev_b32 v26, 16, v139\n"
      "v_and_b32 v27, s97, v139\n"
      "v_pk_mul_f32 v[20:21], v[20:21], s[54:55] op_sel_hi:[1,0]\n"
      "v_pk_mul_f32 v[22:23], v[22:23], s[54:55] op_sel_hi:[1,0]\n"
      "v_pk_mul_f32 v[24:25], v[24:25], s[54:55] op_sel_hi:[1,0]\n"
      "v_pk_mul_f32 v[26:27], v[26:27], s[54:55] op_sel_hi:[1,0]\n"
      "v_cvt_pk_bf16_f32 v136, v20, v21\n"
      "v_cvt_pk_bf16_f32 v137, v22, v23\n"
      "v_cvt_pk_bf16_f32 v138, v24, v25\n"
      "v_cvt_pk_bf16_f32 v139, v26, v27\n"
      "v_accvgpr_write_b32 a192, v136\n"
      "v_accvgpr_write_b32 a193, v137\n"
      "v_accvgpr_write_b32 a194, v138\n"
      "v_accvgpr_write_b32 a195, v139\n"
      "v_lshlrev_b32 v20, 16, v140\n"
      "v_and_b32 v21, s97, v140\n"
      "v_lshlrev_b32 v22, 16, v141\n"
      "v_and_b32 v23, s97, v141\n"
      "v_lshlrev_b32 v24, 16, v142\n"
      "v_and_b32 v25, s97, v142\n"
      "v_lshlrev_b32 v26, 16, v143\n"
      "v_and_b32 v27, s97, v143\n"
      "v_pk_mul_f32 v[20:21], v[20:21], s[54:55] op_sel_hi:[1,0]\n"
      "v_pk_mul_f32 v[22:23], v[22:23], s[54:55] op_sel_hi:[1,0]\n"
      "v_pk_mul_f32 v[24:25], v[24:25], s[54:55] op_sel_hi:[1,0]\n"
      "v_pk_mul_f32 v[26:27], v[26:27], s[54:55] op_sel_hi:[1,0]\n"
      "v_cvt_pk_bf16_f32 v140, v20, v21\n"
      "v_cvt_pk_bf16_f32 v141, v22, v23\n"
      "v_cvt_pk_bf16_f32 v142, v24, v25\n"
      "v_cvt_pk_bf16_f32 v143, v26, v27\n"
      "v_accvgpr_write_b32 a196, v140\n"
      "v_accvgpr_write_b32 a197, v141\n"
      "v_accvgpr_write_b32 a198, v142\n"
      "v_accvgpr_write_b32 a199, v143\n"
      "v_lshlrev_b32 v20, 16, v144\n"
      "v_and_b32 v21, s97, v144\n"
      "v_lshlrev_b32 v22, 16, v145\n"
      "v_and_b32 v23, s97, v145\n"
      "v_lshlrev_b32 v24, 16, v146\n"
      "v_and_b32 v25, s97, v146\n"
      "v_lshlrev_b32 v26, 16, v147\n"
      "v_and_b32 v27, s97, v147\n"
      "v_pk_mul_f32 v[20:21], v[20:21], s[54:55] op_sel_hi:[1,0]\n"
      "v_pk_mul_f32 v[22:23], v[22:23], s[54:55] op_sel_hi:[1,0]\n"
      "v_pk_mul_f32 v[24:25], v[24:25], s[54:55] op_sel_hi:[1,0]\n"
      "v_pk_mul_f32 v[26:27], v[26:27], s[54:55] op_sel_hi:[1,0]\n"
      "v_cvt_pk_bf16_f32 v144, v20, v21\n"
      "v_cvt_pk_bf16_f32 v145, v22, v23\n"
      "v_cvt_pk_bf16_f32 v146, v24, v25\n"
      "v_cvt_pk_bf16_f32 v147, v26, v27\n"
      "v_accvgpr_write_b32 a200, v144\n"
      "v_accvgpr_write_b32 a201, v145\n"
      "v_accvgpr_write_b32 a202, v146\n"
      "v_accvgpr_write_b32 a203, v147\n"
      "v_lshlrev_b32 v20, 16, v148\n"
      "v_and_b32 v21, s97, v148\n"
      "v_lshlrev_b32 v22, 16, v149\n"
      "v_and_b32 v23, s97, v149\n"
      "v_lshlrev_b32 v24, 16, v150\n"
      "v_and_b32 v25, s97, v150\n"
      "v_lshlrev_b32 v26, 16, v151\n"
      "v_and_b32 v27, s97, v151\n"
      "v_pk_mul_f32 v[20:21], v[20:21], s[54:55] op_sel_hi:[1,0]\n"
      "v_pk_mul_f32 v[22:23], v[22:23], s[54:55] op_sel_hi:[1,0]\n"
      "v_pk_mul_f32 v[24:25], v[24:25], s[54:55] op_sel_hi:[1,0]\n"
      "v_pk_mul_f32 v[26:27], v[26:27], s[54:55] op_sel_hi:[1,0]\n"
      "v_cvt_pk_bf16_f32 v148, v20, v21\n"
      "v_cvt_pk_bf16_f32 v149, v22, v23\n"
      "v_cvt_pk_bf16_f32 v150, v24, v25\n"
      "v_cvt_pk_bf16_f32 v151, v26, v27\n"
      "v_accvgpr_write_b32 a204, v148\n"
      "v_accvgpr_write_b32 a205, v149\n"
      "v_accvgpr_write_b32 a206, v150\n"
      "v_accvgpr_write_b32 a207, v151\n"
      "s_waitcnt vmcnt(8)\n"
      "ds_read_b128 v[152:155], v248 offset:4096\n"
      "ds_read_b128 v[156:159], v249 offset:4096\n"
      "ds_read_b128 v[160:163], v250 offset:4096\n"
      "ds_read_b128 v[164:167], v251 offset:4096\n"
      "s_add_u32 s58, s62, 16384\n"
      "s_add_u32 s59, s63, 16384\n"
      "s_mov_b32 m0, s59\n"
      "s_nop 0\n"
      "buffer_load_dwordx4 v6, s[36:39], s58 offen offset:0 nt lds\n"
      "buffer_load_dwordx4 v7, s[36:39], s58 offen offset:1024 nt lds\n"
      "buffer_load_dwordx4 v8, s[36:39], s58 offen offset:2048 nt lds\n"
      "buffer_load_dwordx4 v9, s[36:39], s58 offen offset:3072 nt lds\n"
      "s_waitcnt lgkmcnt(0)\n"
      "v_lshlrev_b32 v20, 16, v152\n"
      "v_and_b32 v21, s97, v152\n"
      "v_lshlrev_b32 v22, 16, v153\n"
      "v_and_b32 v23, s97, v153\n"
      "v_lshlrev_b32 v24, 16, v154\n"
      "v_and_b32 v25, s97, v154\n"
      "v_lshlrev_b32 v26, 16, v155\n"
      "v_and_b32 v27, s97, v155\n"
      "v_pk_mul_f32 v[20:21], v[20:21], s[54:55] op_sel_hi:[1,0]\n"
      "v_pk_mul_f32 v[22:23], v[22:23], s[54:55] op_sel_hi:[1,0]\n"
      "v_pk_mul_f32 v[24:25], v[24:25], s[54:55] op_sel_hi:[1,0]\n"
      "v_pk_mul_f32 v[26:27], v[26:27], s[54:55] op_sel_hi:[1,0]\n"
      "v_cvt_pk_bf16_f32 v152, v20, v21\n"
      "v_cvt_pk_bf16_f32 v153, v22, v23\n"
      "v_cvt_pk_bf16_f32 v154, v24, v25\n"
      "v_cvt_pk_bf16_f32 v155, v26, v27\n"
      "v_accvgpr_write_b32 a208, v152\n"
      "v_accvgpr_write_b32 a209, v153\n"
      "v_accvgpr_write_b32 a210, v154\n"
      "v_accvgpr_write_b32 a211, v155\n"
      "v_lshlrev_b32 v20, 16, v156\n"
      "v_and_b32 v21, s97, v156\n"
      "v_lshlrev_b32 v22, 16, v157\n"
      "v_and_b32 v23, s97, v157\n"
      "v_lshlrev_b32 v24, 16, v158\n"
      "v_and_b32 v25, s97, v158\n"
      "v_lshlrev_b32 v26, 16, v159\n"
      "v_and_b32 v27, s97, v159\n"
      "v_pk_mul_f32 v[20:21], v[20:21], s[54:55] op_sel_hi:[1,0]\n"
      "v_pk_mul_f32 v[22:23], v[22:23], s[54:55] op_sel_hi:[1,0]\n"
      "v_pk_mul_f32 v[24:25], v[24:25], s[54:55] op_sel_hi:[1,0]\n"
      "v_pk_mul_f32 v[26:27], v[26:27], s[54:55] op_sel_hi:[1,0]\n"
      "v_cvt_pk_bf16_f32 v156, v20, v21\n"
      "v_cvt_pk_bf16_f32 v157, v22, v23\n"
      "v_cvt_pk_bf16_f32 v158, v24, v25\n"
      "v_cvt_pk_bf16_f32 v159, v26, v27\n"
      "v_accvgpr_write_b32 a212, v156\n"
      "v_accvgpr_write_b32 a213, v157\n"
      "v_accvgpr_write_b32 a214, v158\n"
      "v_accvgpr_write_b32 a215, v159\n"
      "v_lshlrev_b32 v20, 16, v160\n"
      "v_and_b32 v21, s97, v160\n"
      "v_lshlrev_b32 v22, 16, v161\n"
      "v_and_b32 v23, s97, v161\n"
      "v_lshlrev_b32 v24, 16, v162\n"
      "v_and_b32 v25, s97, v162\n"
      "v_lshlrev_b32 v26, 16, v163\n"
      "v_and_b32 v27, s97, v163\n"
      "v_pk_mul_f32 v[20:21], v[20:21], s[54:55] op_sel_hi:[1,0]\n"
      "v_pk_mul_f32 v[22:23], v[22:23], s[54:55] op_sel_hi:[1,0]\n"
      "v_pk_mul_f32 v[24:25], v[24:25], s[54:55] op_sel_hi:[1,0]\n"
      "v_pk_mul_f32 v[26:27], v[26:27], s[54:55] op_sel_hi:[1,0]\n"
      "v_cvt_pk_bf16_f32 v160, v20, v21\n"
      "v_cvt_pk_bf16_f32 v161, v22, v23\n"
      "v_cvt_pk_bf16_f32 v162, v24, v25\n"
      "v_cvt_pk_bf16_f32 v163, v26, v27\n"
      "v_accvgpr_write_b32 a216, v160\n"
      "v_accvgpr_write_b32 a217, v161\n"
      "v_accvgpr_write_b32 a218, v162\n"
      "v_accvgpr_write_b32 a219, v163\n"
      "v_lshlrev_b32 v20, 16, v164\n"
      "v_and_b32 v21, s97, v164\n"
      "v_lshlrev_b32 v22, 16, v165\n"
      "v_and_b32 v23, s97, v165\n"
      "v_lshlrev_b32 v24, 16, v166\n"
      "v_and_b32 v25, s97, v166\n"
      "v_lshlrev_b32 v26, 16, v167\n"
      "v_and_b32 v27, s97, v167\n"
      "v_pk_mul_f32 v[20:21], v[20:21], s[54:55] op_sel_hi:[1,0]\n"
      "v_pk_mul_f32 v[22:23], v[22:23], s[54:55] op_sel_hi:[1,0]\n"
      "v_pk_mul_f32 v[24:25], v[24:25], s[54:55] op_sel_hi:[1,0]\n"
      "v_pk_mul_f32 v[26:27], v[26:27], s[54:55] op_sel_hi:[1,0]\n"
      "v_cvt_pk_bf16_f32 v164, v20, v21\n"
      "v_cvt_pk_bf16_f32 v165, v22, v23\n"
      "v_cvt_pk_bf16_f32 v166, v24, v25\n"
      "v_cvt_pk_bf16_f32 v167, v26, v27\n"
      "v_accvgpr_write_b32 a220, v164\n"
      "v_accvgpr_write_b32 a221, v165\n"
      "v_accvgpr_write_b32 a222, v166\n"
      "v_accvgpr_write_b32 a223, v167\n"
      "s_waitcnt vmcnt(8)\n"
      "ds_read_b128 v[136:139], v248 offset:8192\n"
      "ds_read_b128 v[140:143], v249 offset:8192\n"
      "ds_read_b128 v[144:147], v250 offset:8192\n"
      "ds_read_b128 v[148:151], v251 offset:8192\n"
      "s_mov_b32 s58, 0x2000\n"
      "s_mov_b32 m0, 0x2000\n"
      "s_nop 0\n"
      "buffer_load_dwordx4 v6, s[40:43], s58 offen offset:0 lds\n"
      "buffer_load_dwordx4 v7, s[40:43], s58 offen offset:1024 lds\n"
      "buffer_load_dwordx4 v8, s[40:43], s58 offen offset:2048 lds\n"
      "buffer_load_dwordx4 v9, s[40:43], s58 offen offset:3072 lds\n"
      "s_waitcnt lgkmcnt(0)\n"
      "v_lshlrev_b32 v20, 16, v136\n"
      "v_and_b32 v21, s97, v136\n"
      "v_lshlrev_b32 v22, 16, v137\n"
      "v_and_b32 v23, s97, v137\n"
      "v_lshlrev_b32 v24, 16, v138\n"
      "v_and_b32 v25, s97, v138\n"
      "v_lshlrev_b32 v26, 16, v139\n"
      "v_and_b32 v27, s97, v139\n"
      "v_pk_mul_f32 v[20:21], v[20:21], s[54:55] op_sel_hi:[1,0]\n"
      "v_pk_mul_f32 v[22:23], v[22:23], s[54:55] op_sel_hi:[1,0]\n"
      "v_pk_mul_f32 v[24:25], v[24:25], s[54:55] op_sel_hi:[1,0]\n"
      "v_pk_mul_f32 v[26:27], v[26:27], s[54:55] op_sel_hi:[1,0]\n"
      "v_cvt_pk_bf16_f32 v136, v20, v21\n"
      "v_cvt_pk_bf16_f32 v137, v22, v23\n"
      "v_cvt_pk_bf16_f32 v138, v24, v25\n"
      "v_cvt_pk_bf16_f32 v139, v26, v27\n"
      "v_accvgpr_write_b32 a224, v136\n"
      "v_accvgpr_write_b32 a225, v137\n"
      "v_accvgpr_write_b32 a226, v138\n"
      "v_accvgpr_write_b32 a227, v139\n"
      "v_lshlrev_b32 v20, 16, v140\n"
      "v_and_b32 v21, s97, v140\n"
      "v_lshlrev_b32 v22, 16, v141\n"
      "v_and_b32 v23, s97, v141\n"
      "v_lshlrev_b32 v24, 16, v142\n"
      "v_and_b32 v25, s97, v142\n"
      "v_lshlrev_b32 v26, 16, v143\n"
      "v_and_b32 v27, s97, v143\n"
      "v_pk_mul_f32 v[20:21], v[20:21], s[54:55] op_sel_hi:[1,0]\n"
      "v_pk_mul_f32 v[22:23], v[22:23], s[54:55] op_sel_hi:[1,0]\n"
      "v_pk_mul_f32 v[24:25], v[24:25], s[54:55] op_sel_hi:[1,0]\n"
      "v_pk_mul_f32 v[26:27], v[26:27], s[54:55] op_sel_hi:[1,0]\n"
      "v_cvt_pk_bf16_f32 v140, v20, v21\n"
      "v_cvt_pk_bf16_f32 v141, v22, v23\n"
      "v_cvt_pk_bf16_f32 v142, v24, v25\n"
      "v_cvt_pk_bf16_f32 v143, v26, v27\n"
      "v_accvgpr_write_b32 a228, v140\n"
      "v_accvgpr_write_b32 a229, v141\n"
      "v_accvgpr_write_b32 a230, v142\n"
      "v_accvgpr_write_b32 a231, v143\n"
      "v_lshlrev_b32 v20, 16, v144\n"
      "v_and_b32 v21, s97, v144\n"
      "v_lshlrev_b32 v22, 16, v145\n"
      "v_and_b32 v23, s97, v145\n"
      "v_lshlrev_b32 v24, 16, v146\n"
      "v_and_b32 v25, s97, v146\n"
      "v_lshlrev_b32 v26, 16, v147\n"
      "v_and_b32 v27, s97, v147\n"
      "v_pk_mul_f32 v[20:21], v[20:21], s[54:55] op_sel_hi:[1,0]\n"
      "v_pk_mul_f32 v[22:23], v[22:23], s[54:55] op_sel_hi:[1,0]\n"
      "v_pk_mul_f32 v[24:25], v[24:25], s[54:55] op_sel_hi:[1,0]\n"
      "v_pk_mul_f32 v[26:27], v[26:27], s[54:55] op_sel_hi:[1,0]\n"
      "v_cvt_pk_bf16_f32 v144, v20, v21\n"
      "v_cvt_pk_bf16_f32 v145, v22, v23\n"
      "v_cvt_pk_bf16_f32 v146, v24, v25\n"
      "v_cvt_pk_bf16_f32 v147, v26, v27\n"
      "v_accvgpr_write_b32 a232, v144\n"
      "v_accvgpr_write_b32 a233, v145\n"
      "v_accvgpr_write_b32 a234, v146\n"
      "v_accvgpr_write_b32 a235, v147\n"
      "v_lshlrev_b32 v20, 16, v148\n"
      "v_and_b32 v21, s97, v148\n"
      "v_lshlrev_b32 v22, 16, v149\n"
      "v_and_b32 v23, s97, v149\n"
      "v_lshlrev_b32 v24, 16, v150\n"
      "v_and_b32 v25, s97, v150\n"
      "v_lshlrev_b32 v26, 16, v151\n"
      "v_and_b32 v27, s97, v151\n"
      "v_pk_mul_f32 v[20:21], v[20:21], s[54:55] op_sel_hi:[1,0]\n"
      "v_pk_mul_f32 v[22:23], v[22:23], s[54:55] op_sel_hi:[1,0]\n"
      "v_pk_mul_f32 v[24:25], v[24:25], s[54:55] op_sel_hi:[1,0]\n"
      "v_pk_mul_f32 v[26:27], v[26:27], s[54:55] op_sel_hi:[1,0]\n"
      "v_cvt_pk_bf16_f32 v148, v20, v21\n"
      "v_cvt_pk_bf16_f32 v149, v22, v23\n"
      "v_cvt_pk_bf16_f32 v150, v24, v25\n"
      "v_cvt_pk_bf16_f32 v151, v26, v27\n"
      "v_accvgpr_write_b32 a236, v148\n"
      "v_accvgpr_write_b32 a237, v149\n"
      "v_accvgpr_write_b32 a238, v150\n"
      "v_accvgpr_write_b32 a239, v151\n"
      "s_waitcnt vmcnt(24)\n"
      "s_barrier\n"
      "ds_read_b128 v[152:155], v0 offset:61440\n"
      "ds_read_b128 v[156:159], v1 offset:61440\n"
      "ds_read_b128 v[160:163], v2 offset:61440\n"
      "ds_read_b128 v[164:167], v3 offset:61440\n"
      "ds_read_b128 v[32:35], v0 offset:0\n"
      "ds_read_b128 v[36:39], v1 offset:0\n"
      "ds_read_b128 v[40:43], v2 offset:0\n"
      "ds_read_b128 v[44:47], v3 offset:0\n"
      "ds_read_b128 v[48:51], v0 offset:4096\n"
      "ds_read_b128 v[52:55], v1 offset:4096\n"
      "ds_read_b128 v[56:59], v2 offset:4096\n"
      "ds_read_b128 v[60:63], v3 offset:4096\n"
      "s_mov_b32 s58, 0x4000\n"
      "s_mov_b32 m0, 0x4000\n"
      "s_nop 0\n"
      "buffer_load_dwordx4 v6, s[40:43], s58 offen offset:0 lds\n"
      "buffer_load_dwordx4 v7, s[40:43], s58 offen offset:1024 lds\n"
      "buffer_load_dwordx4 v8, s[40:43], s58 offen offset:2048 lds\n"
      "buffer_load_dwordx4 v9, s[40:43], s58 offen offset:3072 lds\n"
      "s_waitcnt lgkmcnt(0)\n"
      "v_mfma_f32_16x16x32_bf16 v[96:99], v[32:35], a[192:195], 0\n"
      "v_lshlrev_b32 v20, 16, v152\n"
      "v_and_b32 v21, s97, v152\n"
      "v_lshlrev_b32 v22, 16, v153\n"
      "v_mfma_f32_16x16x32_bf16 v[96:99], v[36:39], a[196:199], v[96:99]\n"
      "v_and_b32 v23, s97, v153\n"
      "v_lshlrev_b32 v24, 16, v154\n"
      "v_and_b32 v25, s97, v154\n"
      "v_mfma_f32_16x16x32_bf16 v[96:99], v[40:43], a[200:203], v[96:99]\n"
      "v_lshlrev_b32 v26, 16, v155\n"
      "v_and_b32 v27, s97, v155\n"
      "v_pk_mul_f32 v[20:21], v[20:21], s[54:55] op_sel_hi:[1,0]\n"
      "v_mfma_f32_16x16x32_bf16 v[96:99], v[44:47], a[204:207], v[96:99]\n"
      "v_pk_mul_f32 v[22:23], v[22:23], s[54:55] op_sel_hi:[1,0]\n"
      "v_pk_mul_f32 v[24:25], v[24:25], s[54:55] op_sel_hi:[1,0]\n"
      "v_pk_mul_f32 v[26:27], v[26:27], s[54:55] op_sel_hi:[1,0]\n"
      "v_mfma_f32_16x16x32_bf16 v[100:103], v[48:51], a[192:195], 0\n"
      "v_cvt_pk_bf16_f32 v232, v20, v21\n"
      "v_cvt_pk_bf16_f32 v233, v22, v23\n"
      "v_cvt_pk_bf16_f32 v234, v24, v25\n"
      "v_mfma_f32_16x16x32_bf16 v[100:103], v[52:55], a[196:199], v[100:103]\n"
      "v_cvt_pk_bf16_f32 v235, v26, v27\n"
      "v_lshlrev_b32 v20, 16, v156\n"
      "v_and_b32 v21, s97, v156\n"
      "v_mfma_f32_16x16x32_bf16 v[100:103], v[56:59], a[200:203], v[100:103]\n"
      "v_lshlrev_b32 v22, 16, v157\n"
      "v_and_b32 v23, s97, v157\n"
      "v_lshlrev_b32 v24, 16, v158\n"
      "v_mfma_f32_16x16x32_bf16 v[100:103], v[60:63], a[204:207], v[100:103]\n"
      "v_and_b32 v25, s97, v158\n"
      "v_lshlrev_b32 v26, 16, v159\n"
      "v_and_b32 v27, s97, v159\n"
      "v_mfma_f32_16x16x32_bf16 v[104:107], v[32:35], a[208:211], 0\n"
      "v_pk_mul_f32 v[20:21], v[20:21], s[54:55] op_sel_hi:[1,0]\n"
      "v_pk_mul_f32 v[22:23], v[22:23], s[54:55] op_sel_hi:[1,0]\n"
      "v_pk_mul_f32 v[24:25], v[24:25], s[54:55] op_sel_hi:[1,0]\n"
      "v_mfma_f32_16x16x32_bf16 v[104:107], v[36:39], a[212:215], v[104:107]\n"
      "v_pk_mul_f32 v[26:27], v[26:27], s[54:55] op_sel_hi:[1,0]\n"
      "v_cvt_pk_bf16_f32 v236, v20, v21\n"
      "v_cvt_pk_bf16_f32 v237, v22, v23\n"
      "v_mfma_f32_16x16x32_bf16 v[104:107], v[40:43], a[216:219], v[104:107]\n"
      "v_cvt_pk_bf16_f32 v238, v24, v25\n"
      "v_cvt_pk_bf16_f32 v239, v26, v27\n"
      "v_lshlrev_b32 v20, 16, v160\n"
      "v_mfma_f32_16x16x32_bf16 v[104:107], v[44:47], a[220:223], v[104:107]\n"
      "v_and_b32 v21, s97, v160\n"
      "v_lshlrev_b32 v22, 16, v161\n"
      "v_and_b32 v23, s97, v161\n"
      "v_mfma_f32_16x16x32_bf16 v[108:111], v[48:51], a[208:211], 0\n"
      "v_lshlrev_b32 v24, 16, v162\n"
      "v_and_b32 v25, s97, v162\n"
      "v_lshlrev_b32 v26, 16, v163\n"
      "v_mfma_f32_16x16x32_bf16 v[108:111], v[52:55], a[212:215], v[108:111]\n"
      "v_and_b32 v27, s97, v163\n"
      "v_pk_mul_f32 v[20:21], v[20:21], s[54:55] op_sel_hi:[1,0]\n"
      "v_pk_mul_f32 v[22:23], v[22:23], s[54:55] op_sel_hi:[1,0]\n"
      "v_mfma_f32_16x16x32_bf16 v[108:111], v[56:59], a[216:219], v[108:111]\n"
      "v_pk_mul_f32 v[24:25], v[24:25], s[54:55] op_sel_hi:[1,0]\n"
      "v_pk_mul_f32 v[26:27], v[26:27], s[54:55] op_sel_hi:[1,0]\n"
      "v_cvt_pk_bf16_f32 v240, v20, v21\n"
      "v_mfma_f32_16x16x32_bf16 v[108:111], v[60:63], a[220:223], v[108:111]\n"
      "v_cvt_pk_bf16_f32 v241, v22, v23\n"
      "v_cvt_pk_bf16_f32 v242, v24, v25\n"
      "v_cvt_pk_bf16_f32 v243, v26, v27\n"
      "v_mfma_f32_16x16x32_bf16 v[112:115], v[32:35], a[224:227], 0\n"
      "v_lshlrev_b32 v20, 16, v164\n"
      "v_and_b32 v21, s97, v164\n"
      "v_lshlrev_b32 v22, 16, v165\n"
      "v_mfma_f32_16x16x32_bf16 v[112:115], v[36:39], a[228:231], v[112:115]\n"
      "v_and_b32 v23, s97, v165\n"
      "v_lshlrev_b32 v24, 16, v166\n"
      "v_and_b32 v25, s97, v166\n"
      "v_mfma_f32_16x16x32_bf16 v[112:115], v[40:43], a[232:235], v[112:115]\n"
      "v_lshlrev_b32 v26, 16, v167\n"
      "v_and_b32 v27, s97, v167\n"
      "v_pk_mul_f32 v[20:21], v[20:21], s[54:55] op_sel_hi:[1,0]\n"
      "v_mfma_f32_16x16x32_bf16 v[112:115], v[44:47], a[236:239], v[112:115]\n"
      "v_pk_mul_f32 v[22:23], v[22:23], s[54:55] op_sel_hi:[1,0]\n"
      "v_pk_mul_f32 v[24:25], v[24:25], s[54:55] op_sel_hi:[1,0]\n"
      "v_pk_mul_f32 v[26:27], v[26:27], s[54:55] op_sel_hi:[1,0]\n"
      "v_mfma_f32_16x16x32_bf16 v[116:119], v[48:51], a[224:227], 0\n"
      "v_cvt_pk_bf16_f32 v244, v20, v21\n"
      "v_cvt_pk_bf16_f32 v245, v22, v23\n"
      "v_cvt_pk_bf16_f32 v246, v24, v25\n"
      "v_mfma_f32_16x16x32_bf16 v[116:119], v[52:55], a[228:231], v[116:119]\n"
      "v_cvt_pk_bf16_f32 v247, v26, v27\n"
      "v_mfma_f32_16x16x32_bf16 v[116:119], v[56:59], a[232:235], v[116:119]\n"
      "v_mfma_f32_16x16x32_bf16 v[116:119], v[60:63], a[236:239], v[116:119]\n"
      "s_nop 1\n"
      "v_mfma_f32_16x16x32_bf16 v[176:179], v[32:35], v[232:235], 0\n"
      "v_mfma_f32_16x16x32_bf16 v[176:179], v[36:39], v[236:239], v[176:179]\n"
      "v_mfma_f32_16x16x32_bf16 v[176:179], v[40:43], v[240:243], v[176:179]\n"
      "v_mfma_f32_16x16x32_bf16 v[176:179], v[44:47], v[244:247], v[176:179]\n"
      "v_mfma_f32_16x16x32_bf16 v[168:171], v[48:51], v[232:235], 0\n"
      "v_mfma_f32_16x16x32_bf16 v[168:171], v[52:55], v[236:239], v[168:171]\n"
      "v_mfma_f32_16x16x32_bf16 v[168:171], v[56:59], v[240:243], v[168:171]\n"
      "v_mfma_f32_16x16x32_bf16 v[168:171], v[60:63], v[244:247], v[168:171]\n"
      "v_max3_f32 v28, v96, v97, v98\n"
      "v_max3_f32 v29, v99, v100, v101\n"
      "v_max3_f32 v28, v28, v102, v103\n"
      "v_max_f32 v28, v28, v29\n"
      "v_mov_b32 v29, v28\n"
      "s_nop 1\n"
      "v_permlane16_swap_b32 v28, v29\n"
      "s_nop 0\n"
      "v_max_f32 v28, v28, v29\n"
      "v_mov_b32 v29, v28\n"
      "s_nop 1\n"
      "v_permlane32_swap_b32 v28, v29\n"
      "s_nop 0\n"
      "v_max_f32 v28, v28, v29\n"
      "v_sub_f32 v192, 0, v28\n"
      "v_mov_b32 v193, v192\n"
      "v_mov_b32 v194, v192\n"
      "v_mov_b32 v195, v192\n"
      "v_add_f32 v96, v96, v192\n"
      "v_add_f32 v97, v97, v192\n"
      "v_add_f32 v98, v98, v192\n"
      "v_add_f32 v99, v99, v192\n"
      "v_add_f32 v100, v100, v192\n"
      "v_add_f32 v101, v101, v192\n"
      "v_add_f32 v102, v102, v192\n"
      "v_add_f32 v103, v103, v192\n"
      "v_max3_f32 v28, v104, v105, v106\n"
      "v_max3_f32 v29, v107, v108, v109\n"
      "v_max3_f32 v28, v28, v110, v111\n"
      "v_max_f32 v28, v28, v29\n"
      "v_mov_b32 v29, v28\n"
      "s_nop 1\n"
      "v_permlane16_swap_b32 v28, v29\n"
      "s_nop 0\n"
      "v_max_f32 v28, v28, v29\n"
      "v_mov_b32 v29, v28\n"
      "s_nop 1\n"
      "v_permlane32_swap_b32 v28, v29\n"
      "s_nop 0\n"
      "v_max_f32 v28, v28, v29\n"
      "v_sub_f32 v196, 0, v28\n"
      "v_mov_b32 v197, v196\n"
      "v_mov_b32 v198, v196\n"
      "v_mov_b32 v199, v196\n"
      "v_add_f32 v104, v104, v196\n"
      "v_add_f32 v105, v105, v196\n"
      "v_add_f32 v106, v106, v196\n"
      "v_add_f32 v107, v107, v196\n"
      "v_add_f32 v108, v108, v196\n"
      "v_add_f32 v109, v109, v196\n"
      "v_add_f32 v110, v110, v196\n"
      "v_add_f32 v111, v111, v196\n"
      "v_max3_f32 v28, v112, v113, v114\n"
      "v_max3_f32 v29, v115, v116, v117\n"
      "v_max3_f32 v28, v28, v118, v119\n"
      "v_max_f32 v28, v28, v29\n"
      "v_mov_b32 v29, v28\n"
      "s_nop 1\n"
      "v_permlane16_swap_b32 v28, v29\n"
      "s_nop 0\n"
      "v_max_f32 v28, v28, v29\n"
      "v_mov_b32 v29, v28\n"
      "s_nop 1\n"
      "v_permlane32_swap_b32 v28, v29\n"
      "s_nop 0\n"
      "v_max_f32 v28, v28, v29\n"
      "v_sub_f32 v200, 0, v28\n"
      "v_mov_b32 v201, v200\n"
      "v_mov_b32 v202, v200\n"
      "v_mov_b32 v203, v200\n"
      "v_add_f32 v112, v112, v200\n"
      "v_add_f32 v113, v113, v200\n"
      "v_add_f32 v114, v114, v200\n"
      "v_add_f32 v115, v115, v200\n"
      "v_add_f32 v116, v116, v200\n"
      "v_add_f32 v117, v117, v200\n"
      "s_waitcnt vmcnt(12)\n"
      "ds_read_b128 v[136:139], v248 offset:12288\n"
      "ds_read_b128 v[140:143], v249 offset:12288\n"
      "ds_read_b128 v[144:147], v250 offset:12288\n"
      "ds_read_b128 v[148:151], v251 offset:12288\n"
      "s_mov_b32 s58, 0x6000\n"
      "s_mov_b32 m0, 0x6000\n"
      "s_nop 0\n"
      "buffer_load_dwordx4 v6, s[40:43], s58 offen offset:0 lds\n"
      "buffer_load_dwordx4 v7, s[40:43], s58 offen offset:1024 lds\n"
      "buffer_load_dwordx4 v8, s[40:43], s58 offen offset:2048 lds\n"
      "buffer_load_dwordx4 v9, s[40:43], s58 offen offset:3072 lds\n"
      "v_add_f32 v118, v118, v200\n"
      "v_add_f32 v119, v119, v200\n"
      "v_max3_f32 v28, v176, v177, v178\n"
      "v_max3_f32 v29, v179, v168, v169\n"
      "v_max3_f32 v28, v28, v170, v171\n"
      "v_max_f32 v28, v28, v29\n"
      "v_mov_b32 v29, v28\n"
      "s_nop 1\n"
      "s_waitcnt lgkmcnt(0)\n"
      "v_permlane16_swap_b32 v28, v29\n"
      "v_lshlrev_b32 v20, 16, v136\n"
      "v_max_f32 v28, v28, v29\n"
      "v_mov_b32 v29, v28\n"
      "v_and_b32 v21, s97, v136\n"
      "v_lshlrev_b32 v22, 16, v137\n"
      "v_permlane32_swap_b32 v28, v29\n"
      "v_and_b32 v23, s97, v137\n"
      "v_max_f32 v28, v28, v29\n"
      "v_sub_f32 v212, 0, v28\n"
      "v_mov_b32 v213, v212\n"
      "v_mov_b32 v214, v212\n"
      "v_mov_b32 v215, v212\n"
      "v_add_f32 v176, v176, v212\n"
      "v_add_f32 v177, v177, v212\n"
      "v_add_f32 v178, v178, v212\n"
      "v_add_f32 v179, v179, v212\n"
      "v_lshlrev_b32 v24, 16, v138\n"
      "v_and_b32 v25, s97, v138\n"
      "v_lshlrev_b32 v26, 16, v139\n"
      "v_and_b32 v27, s97, v139\n"
      "v_pk_mul_f32 v[20:21], v[20:21], s[54:55] op_sel_hi:[1,0]\n"
      "v_pk_mul_f32 v[22:23], v[22:23], s[54:55] op_sel_hi:[1,0]\n"
      "v_pk_mul_f32 v[24:25], v[24:25], s[54:55] op_sel_hi:[1,0]\n"
      "v_pk_mul_f32 v[26:27], v[26:27], s[54:55] op_sel_hi:[1,0]\n"
      "v_cvt_pk_bf16_f32 v136, v20, v21\n"
      "v_cvt_pk_bf16_f32 v137, v22, v23\n"
      "v_cvt_pk_bf16_f32 v138, v24, v25\n"
      "v_cvt_pk_bf16_f32 v139, v26, v27\n"
      "v_accvgpr_write_b32 a240, v136\n"
      "v_accvgpr_write_b32 a241, v137\n"
      "v_accvgpr_write_b32 a242, v138\n"
      "v_accvgpr_write_b32 a243, v139\n"
      "v_lshlrev_b32 v20, 16, v140\n"
      "v_and_b32 v21, s97, v140\n"
      "v_lshlrev_b32 v22, 16, v141\n"
      "v_and_b32 v23, s97, v141\n"
      "v_lshlrev_b32 v24, 16, v142\n"
      "v_and_b32 v25, s97, v142\n"
      "v_lshlrev_b32 v26, 16, v143\n"
      "v_and_b32 v27, s97, v143\n"
      "v_pk_mul_f32 v[20:21], v[20:21], s[54:55] op_sel_hi:[1,0]\n"
      "v_pk_mul_f32 v[22:23], v[22:23], s[54:55] op_sel_hi:[1,0]\n"
      "v_pk_mul_f32 v[24:25], v[24:25], s[54:55] op_sel_hi:[1,0]\n"
      "v_pk_mul_f32 v[26:27], v[26:27], s[54:55] op_sel_hi:[1,0]\n"
      "v_cvt_pk_bf16_f32 v140, v20, v21\n"
      "v_cvt_pk_bf16_f32 v141, v22, v23\n"
      "v_cvt_pk_bf16_f32 v142, v24, v25\n"
      "v_cvt_pk_bf16_f32 v143, v26, v27\n"
      "v_accvgpr_write_b32 a244, v140\n"
      "v_accvgpr_write_b32 a245, v141\n"
      "v_accvgpr_write_b32 a246, v142\n"
      "v_accvgpr_write_b32 a247, v143\n"
      "v_lshlrev_b32 v20, 16, v144\n"
      "v_and_b32 v21, s97, v144\n"
      "v_lshlrev_b32 v22, 16, v145\n"
      "v_and_b32 v23, s97, v145\n"
      "v_lshlrev_b32 v24, 16, v146\n"
      "v_and_b32 v25, s97, v146\n"
      "v_lshlrev_b32 v26, 16, v147\n"
      "v_and_b32 v27, s97, v147\n"
      "v_pk_mul_f32 v[20:21], v[20:21], s[54:55] op_sel_hi:[1,0]\n"
      "v_pk_mul_f32 v[22:23], v[22:23], s[54:55] op_sel_hi:[1,0]\n"
      "v_pk_mul_f32 v[24:25], v[24:25], s[54:55] op_sel_hi:[1,0]\n"
      "v_pk_mul_f32 v[26:27], v[26:27], s[54:55] op_sel_hi:[1,0]\n"
      "v_cvt_pk_bf16_f32 v144, v20, v21\n"
      "v_cvt_pk_bf16_f32 v145, v22, v23\n"
      "v_cvt_pk_bf16_f32 v146, v24, v25\n"
      "v_cvt_pk_bf16_f32 v147, v26, v27\n"
      "v_accvgpr_write_b32 a248, v144\n"
      "v_accvgpr_write_b32 a249, v145\n"
      "v_accvgpr_write_b32 a250, v146\n"
      "v_accvgpr_write_b32 a251, v147\n"
      "v_lshlrev_b32 v20, 16, v148\n"
      "v_and_b32 v21, s97, v148\n"
      "v_lshlrev_b32 v22, 16, v149\n"
      "v_and_b32 v23, s97, v149\n"
      "v_lshlrev_b32 v24, 16, v150\n"
      "v_and_b32 v25, s97, v150\n"
      "v_lshlrev_b32 v26, 16, v151\n"
      "v_and_b32 v27, s97, v151\n"
      "v_pk_mul_f32 v[20:21], v[20:21], s[54:55] op_sel_hi:[1,0]\n"
      "v_pk_mul_f32 v[22:23], v[22:23], s[54:55] op_sel_hi:[1,0]\n"
      "v_pk_mul_f32 v[24:25], v[24:25], s[54:55] op_sel_hi:[1,0]\n"
      "v_pk_mul_f32 v[26:27], v[26:27], s[54:55] op_sel_hi:[1,0]\n"
      "v_cvt_pk_bf16_f32 v148, v20, v21\n"
      "v_cvt_pk_bf16_f32 v149, v22, v23\n"
      "v_cvt_pk_bf16_f32 v150, v24, v25\n"
      "v_cvt_pk_bf16_f32 v151, v26, v27\n"
      "v_accvgpr_write_b32 a252, v148\n"
      "v_accvgpr_write_b32 a253, v149\n"
      "v_accvgpr_write_b32 a254, v150\n"
      "v_accvgpr_write_b32 a255, v151\n"
      "s_nop 1\n"
      "v_mfma_f32_16x16x32_bf16 v[120:123], v[32:35], a[240:243], 0\n"
      "v_mfma_f32_16x16x32_bf16 v[120:123], v[36:39], a[244:247], v[120:123]\n"
      "v_mfma_f32_16x16x32_bf16 v[120:123], v[40:43], a[248:251], v[120:123]\n"
      "v_mfma_f32_16x16x32_bf16 v[120:123], v[44:47], a[252:255], v[120:123]\n"
      "v_mfma_f32_16x16x32_bf16 v[124:127], v[48:51], a[240:243], 0\n"
      "v_mfma_f32_16x16x32_bf16 v[124:127], v[52:55], a[244:247], v[124:127]\n"
      "v_mfma_f32_16x16x32_bf16 v[124:127], v[56:59], a[248:251], v[124:127]\n"
      "v_mfma_f32_16x16x32_bf16 v[124:127], v[60:63], a[252:255], v[124:127]\n"
      "s_nop 3\n"
      "s_waitcnt vmcnt(12)\n"
      "ds_read_b128 v[152:155], v248 offset:16384\n"
      "ds_read_b128 v[156:159], v249 offset:16384\n"
      "ds_read_b128 v[160:163], v250 offset:16384\n"
      "ds_read_b128 v[164:167], v251 offset:16384\n"
      "s_mov_b32 s58, 0x8000\n"
      "s_mov_b32 m0, 0x8000\n"
      "s_nop 0\n"
      "buffer_load_dwordx4 v6, s[40:43], s58 offen offset:0 lds\n"
      "buffer_load_dwordx4 v7, s[40:43], s58 offen offset:1024 lds\n"
      "buffer_load_dwordx4 v8, s[40:43], s58 offen offset:2048 lds\n"
      "buffer_load_dwordx4 v9, s[40:43], s58 offen offset:3072 lds\n"
      "v_max3_f32 v28, v120, v121, v122\n"
      "v_max3_f32 v29, v123, v124, v125\n"
      "v_max3_f32 v28, v28, v126, v127\n"
      "v_max_f32 v28, v28, v29\n"
      "v_mov_b32 v29, v28\n"
      "s_nop 1\n"
      "v_permlane16_swap_b32 v28, v29\n"
      "s_nop 0\n"
      "s_waitcnt lgkmcnt(0)\n"
      "v_max_f32 v28, v28, v29\n"
      "v_mov_b32 v29, v28\n"
      "v_lshlrev_b32 v20, 16, v152\n"
      "v_and_b32 v21, s97, v152\n"
      "v_permlane32_swap_b32 v28, v29\n"
      "v_lshlrev_b32 v22, 16, v153\n"
      "v_max_f32 v28, v28, v29\n"
      "v_sub_f32 v204, 0, v28\n"
      "v_mov_b32 v205, v204\n"
      "v_mov_b32 v206, v204\n"
      "v_mov_b32 v207, v204\n"
      "v_add_f32 v120, v120, v204\n"
      "v_add_f32 v121, v121, v204\n"
      "v_add_f32 v122, v122, v204\n"
      "v_add_f32 v123, v123, v204\n"
      "v_add_f32 v124, v124, v204\n"
      "v_add_f32 v125, v125, v204\n"
      "v_add_f32 v126, v126, v204\n"
      "v_add_f32 v127, v127, v204\n"
      "v_and_b32 v23, s97, v153\n"
      "v_lshlrev_b32 v24, 16, v154\n"
      "v_and_b32 v25, s97, v154\n"
      "v_lshlrev_b32 v26, 16, v155\n"
      "v_and_b32 v27, s97, v155\n"
      "v_pk_mul_f32 v[20:21], v[20:21], s[54:55] op_sel_hi:[1,0]\n"
      "v_pk_mul_f32 v[22:23], v[22:23], s[54:55] op_sel_hi:[1,0]\n"
      "v_pk_mul_f32 v[24:25], v[24:25], s[54:55] op_sel_hi:[1,0]\n"
      "v_pk_mul_f32 v[26:27], v[26:27], s[54:55] op_sel_hi:[1,0]\n"
      "v_cvt_pk_bf16_f32 v216, v20, v21\n"
      "v_cvt_pk_bf16_f32 v217, v22, v23\n"
      "v_cvt_pk_bf16_f32 v218, v24, v25\n"
      "v_cvt_pk_bf16_f32 v219, v26, v27\n"
      "v_lshlrev_b32 v20, 16, v156\n"
      "v_and_b32 v21, s97, v156\n"
      "v_lshlrev_b32 v22, 16, v157\n"
      "v_and_b32 v23, s97, v157\n"
      "v_lshlrev_b32 v24, 16, v158\n"
      "v_and_b32 v25, s97, v158\n"
      "v_lshlrev_b32 v26, 16, v159\n"
      "v_and_b32 v27, s97, v159\n"
      "v_pk_mul_f32 v[20:21], v[20:21], s[54:55] op_sel_hi:[1,0]\n"
      "v_pk_mul_f32 v[22:23], v[22:23], s[54:55] op_sel_hi:[1,0]\n"
      "v_pk_mul_f32 v[24:25], v[24:25], s[54:55] op_sel_hi:[1,0]\n"
      "v_pk_mul_f32 v[26:27], v[26:27], s[54:55] op_sel_hi:[1,0]\n"
      "v_cvt_pk_bf16_f32 v220, v20, v21\n"
      "v_cvt_pk_bf16_f32 v221, v22, v23\n"
      "v_cvt_pk_bf16_f32 v222, v24, v25\n"
      "v_cvt_pk_bf16_f32 v223, v26, v27\n"
      "v_lshlrev_b32 v20, 16, v160\n"
      "v_and_b32 v21, s97, v160\n"
      "v_lshlrev_b32 v22, 16, v161\n"
      "v_and_b32 v23, s97, v161\n"
      "v_lshlrev_b32 v24, 16, v162\n"
      "v_and_b32 v25, s97, v162\n"
      "v_lshlrev_b32 v26, 16, v163\n"
      "v_and_b32 v27, s97, v163\n"
      "v_pk_mul_f32 v[20:21], v[20:21], s[54:55] op_sel_hi:[1,0]\n"
      "v_pk_mul_f32 v[22:23], v[22:23], s[54:55] op_sel_hi:[1,0]\n"
      "v_pk_mul_f32 v[24:25], v[24:25], s[54:55] op_sel_hi:[1,0]\n"
      "v_pk_mul_f32 v[26:27], v[26:27], s[54:55] op_sel_hi:[1,0]\n"
      "v_cvt_pk_bf16_f32 v224, v20, v21\n"
      "v_cvt_pk_bf16_f32 v225, v22, v23\n"
      "v_cvt_pk_bf16_f32 v226, v24, v25\n"
      "v_cvt_pk_bf16_f32 v227, v26, v27\n"
      "v_lshlrev_b32 v20, 16, v164\n"
      "v_and_b32 v21, s97, v164\n"
      "v_lshlrev_b32 v22, 16, v165\n"
      "v_and_b32 v23, s97, v165\n"
      "v_lshlrev_b32 v24, 16, v166\n"
      "v_and_b32 v25, s97, v166\n"
      "v_lshlrev_b32 v26, 16, v167\n"
      "v_and_b32 v27, s97, v167\n"
      "v_pk_mul_f32 v[20:21], v[20:21], s[54:55] op_sel_hi:[1,0]\n"
      "v_pk_mul_f32 v[22:23], v[22:23], s[54:55] op_sel_hi:[1,0]\n"
      "v_pk_mul_f32 v[24:25], v[24:25], s[54:55] op_sel_hi:[1,0]\n"
      "v_pk_mul_f32 v[26:27], v[26:27], s[54:55] op_sel_hi:[1,0]\n"
      "v_cvt_pk_bf16_f32 v228, v20, v21\n"
      "v_cvt_pk_bf16_f32 v229, v22, v23\n"
      "v_cvt_pk_bf16_f32 v230, v24, v25\n"
      "v_cvt_pk_bf16_f32 v231, v26, v27\n"
      "s_nop 1\n"
      "v_mfma_f32_16x16x32_bf16 v[128:131], v[32:35], v[216:219], 0\n"
      "v_mfma_f32_16x16x32_bf16 v[128:131], v[36:39], v[220:223], v[128:131]\n"
      "v_mfma_f32_16x16x32_bf16 v[128:131], v[40:43], v[224:227], v[128:131]\n"
      "v_mfma_f32_16x16x32_bf16 v[128:131], v[44:47], v[228:231], v[128:131]\n"
      "v_mfma_f32_16x16x32_bf16 v[132:135], v[48:51], v[216:219], 0\n"
      "v_mfma_f32_16x16x32_bf16 v[132:135], v[52:55], v[220:223], v[132:135]\n"
      "v_mfma_f32_16x16x32_bf16 v[132:135], v[56:59], v[224:227], v[132:135]\n"
      "v_mfma_f32_16x16x32_bf16 v[132:135], v[60:63], v[228:231], v[132:135]\n"
      "s_nop 3\n"
      "s_nop 3\n"
      "v_max3_f32 v28, v128, v129, v130\n"
      "v_max3_f32 v29, v131, v132, v133\n"
      "v_max3_f32 v28, v28, v134, v135\n"
      "v_max_f32 v28, v28, v29\n"
      "v_mov_b32 v29, v28\n"
      "s_nop 1\n"
      "v_permlane16_swap_b32 v28, v29\n"
      "s_nop 0\n"
      "v_max_f32 v28, v28, v29\n"
      "v_mov_b32 v29, v28\n"
      "s_nop 1\n"
      "v_permlane32_swap_b32 v28, v29\n"
      "s_nop 0\n"
      "v_max_f32 v28, v28, v29\n"
      "v_sub_f32 v208, 0, v28\n"
      "v_mov_b32 v209, v208\n"
      "v_mov_b32 v210, v208\n"
      "v_mov_b32 v211, v208\n"
      "v_add_f32 v128, v128, v208\n"
      "v_add_f32 v129, v129, v208\n"
      "v_add_f32 v130, v130, v208\n"
      "v_add_f32 v131, v131, v208\n"
      "v_add_f32 v132, v132, v208\n"
      "v_add_f32 v133, v133, v208\n"
      "v_add_f32 v134, v134, v208\n"
      "v_add_f32 v135, v135, v208\n"
      "L_restart_w2:\n"
      "s_mov_b32 s55, 0xa000\n"
      "s_mov_b32 s56, 0\n"
      "s_mov_b32 s57, 16\n"
      "s_waitcnt vmcnt(12)\n"
      "s_barrier\n"
      "s_xor_b32 m0, s56, 0xa000\n"
      "s_nop 0\n"
      "buffer_load_dwordx4 v6, s[40:43], s55 offen offset:0 lds\n"
      "buffer_load_dwordx4 v7, s[40:43], s55 offen offset:1024 lds\n"
      "buffer_load_dwordx4 v8, s[40:43], s55 offen offset:2048 lds\n"
      "v_exp_f32 v96, v96\n"
      "v_exp_f32 v97, v97\n"
      "v_add_f32 v14, v14, v96\n"
      "v_add_f32 v14, v14, v97\n"
      "v_cvt_pk_bf16_f32 v96, v96, v97\n"
      "v_exp_f32 v98, v98\n"
      "s_nop 0\n"
      "v_add_f32 v14, v14, v98\n"
      "buffer_load_dwordx4 v9, s[40:43], s55 offen offset:3072 lds\n"
      "v_exp_f32 v99, v99\n"
      "s_add_u32 s55, s55, 0x2000\n"
      "v_add_f32 v14, v14, v99\n"
      "v_cvt_pk_bf16_f32 v97, v98, v99\n"
      "v_exp_f32 v100, v100\n"
      "v_exp_f32 v101, v101\n"
      "v_add_f32 v14, v14, v100\n"
      "v_add_f32 v14, v14, v101\n"
      "v_cvt_pk_bf16_f32 v98, v100, v101\n"
      "v_exp_f32 v102, v102\n"
      "v_exp_f32 v103, v103\n"
      "v_add_f32 v14, v14, v102\n"
      "v_add_f32 v14, v14, v103\n"
      "v_cvt_pk_bf16_f32 v99, v102, v103\n"
      "v_exp_f32 v104, v104\n"
      "v_exp_f32 v105, v105\n"
      "v_add_f32 v15, v15, v104\n"
      "v_add_f32 v15, v15, v105\n"
      "v_cvt_pk_bf16_f32 v104, v104, v105\n"
      "v_exp_f32 v106, v106\n"
      "ds_read_b128 v[32:35], v0 offset:8192\n"
      "v_add_f32 v15, v15, v106\n"
      "s_xor_b32 m0, s56, 0xc000\n"
      "s_nop 0\n"
      "buffer_load_dwordx4 v6, s[40:43], s55 offen offset:0 lds\n"
      "v_exp_f32 v107, v107\n"
      "v_exp_f32 v108, v108\n"
      "v_add_f32 v15, v15, v107\n"
      "v_cvt_pk_bf16_f32 v105, v106, v107\n"
      "v_add_f32 v15, v15, v108\n"
      "v_exp_f32 v109, v109\n"
      "v_exp_f32 v110, v110\n"
      "v_add_f32 v15, v15, v109\n"
      "v_cvt_pk_bf16_f32 v106, v108, v109\n"
      "ds_read_b128 v[36:39], v1 offset:8192\n"
      "v_add_f32 v15, v15, v110\n"
      "buffer_load_dwordx4 v7, s[40:43], s55 offen offset:1024 lds\n"
      "v_exp_f32 v111, v111\n"
      "v_exp_f32 v112, v112\n"
      "v_add_f32 v15, v15, v111\n"
      "v_cvt_pk_bf16_f32 v107, v110, v111\n"
      "ds_read_b128 v[40:43], v2 offset:8192\n"
      "v_add_f32 v16, v16, v112\n"
      "v_exp_f32 v113, v113\n"
      "v_exp_f32 v114, v114\n"
      "v_add_f32 v16, v16, v113\n"
      "v_cvt_pk_bf16_f32 v112, v112, v113\n"
      "ds_read_b128 v[44:47], v3 offset:8192\n"
      "v_add_f32 v16, v16, v114\n"
      "buffer_load_dwordx4 v8, s[40:43], s55 offen offset:2048 lds\n"
      "v_exp_f32 v115, v115\n"
      "v_exp_f32 v116, v116\n"
      "v_add_f32 v16, v16, v115\n"
      "v_cvt_pk_bf16_f32 v113, v114, v115\n"
      "ds_read_b128 v[48:51], v0 offset:12288\n"
      "v_add_f32 v16, v16, v116\n"
      "v_exp_f32 v117, v117\n"
      "v_exp_f32 v118, v118\n"
      "v_add_f32 v16, v16, v117\n"
      "v_cvt_pk_bf16_f32 v114, v116, v117\n"
      "v_add_f32 v16, v16, v118\n"
      "buffer_load_dwordx4 v9, s[40:43], s55 offen offset:3072 lds\n"
      "v_exp_f32 v119, v119\n"
      "s_add_u32 s55, s55, 0x2000\n"
      "v_add_f32 v16, v16, v119\n"
      "v_cvt_pk_bf16_f32 v115, v118, v119\n"
      "v_exp_f32 v120, v120\n"
      "ds_read_b128 v[52:55], v1 offset:12288\n"
      "v_add_f32 v17, v17, v120\n"
      "v_exp_f32 v121, v121\n"
      "v_exp_f32 v122, v122\n"
      "v_add_f32 v17, v17, v121\n"
      "v_cvt_pk_bf16_f32 v120, v120, v121\n"
      "ds_read_b128 v[56:59], v2 offset:12288\n"
      "v_add_f32 v17, v17, v122\n"
      "v_exp_f32 v123, v123\n"
      "v_exp_f32 v124, v124\n"
      "v_add_f32 v17, v17, v123\n"
      "v_cvt_pk_bf16_f32 v121, v122, v123\n"
      "ds_read_b128 v[60:63], v3 offset:12288\n"
      "v_add_f32 v17, v17, v124\n"
      "v_exp_f32 v125, v125\n"
      "v_exp_f32 v126, v126\n"
      "v_add_f32 v17, v17, v125\n"
      "v_cvt_pk_bf16_f32 v122, v124, v125\n"
      "v_add_f32 v17, v17, v126\n"
      "v_exp_f32 v127, v127\n"
      "v_exp_f32 v128, v128\n"
      "v_add_f32 v17, v17, v127\n"
      "v_cvt_pk_bf16_f32 v123, v126, v127\n"
      "v_add_f32 v18, v18, v128\n"
      "v_exp_f32 v129, v129\n"
      "s_nop 0\n"
      "v_add_f32 v18, v18, v129\n"
      "v_cvt_pk_bf16_f32 v128, v128, v129\n"
      "s_waitcnt lgkmcnt(0)\n"
      "s_waitcnt vmcnt(16)\n"
      "s_barrier\n"
      "s_branch L_entry_w2\n"
      "L_loop_w2:\n"
      "v_mfma_f32_16x16x32_bf16 v[96:99], v[32:35], a[192:195], v[192:195]\n"
      "v_exp_f32 v171, v171\n"
      "v_mfma_f32_16x16x32_bf16 v[96:99], v[36:39], a[196:199], v[96:99]\n"
      "v_add_f32 v18, v18, v171\n"
      "v_cvt_pk_bf16_f32 v169, v170, v171\n"
      "v_mfma_f32_16x16x32_bf16 v[96:99], v[40:43], a[200:203], v[96:99]\n"
      "v_exp_f32 v172, v172\n"
      "v_mfma_f32_16x16x32_bf16 v[96:99], v[44:47], a[204:207], v[96:99]\n"
      "ds_read_b64_tr_b16 v[64:65], v4 offset:24576\n"
      "v_add_f32 v18, v18, v172\n"
      "v_mfma_f32_16x16x32_bf16 v[100:103], v[48:51], a[192:195], v[192:195]\n"
      "v_exp_f32 v173, v173\n"
      "v_mfma_f32_16x16x32_bf16 v[100:103], v[52:55], a[196:199], v[100:103]\n"
      "v_add_f32 v18, v18, v173\n"
      "v_cvt_pk_bf16_f32 v170, v172, v173\n"
      "v_mfma_f32_16x16x32_bf16 v[100:103], v[56:59], a[200:203], v[100:103]\n"
      "v_exp_f32 v174, v174\n"
      "v_mfma_f32_16x16x32_bf16 v[100:103], v[60:63], a[204:207], v[100:103]\n"
      "ds_read_b64_tr_b16 v[66:67], v4 offset:28672\n"
      "v_add_f32 v18, v18, v174\n"
      "v_mfma_f32_16x16x32_bf16 v[104:107], v[32:35], a[208:211], v[196:199]\n"
      "v_exp_f32 v175, v175\n"
      "v_mfma_f32_16x16x32_bf16 v[104:107], v[36:39], a[212:215], v[104:107]\n"
      "v_add_f32 v18, v18, v175\n"
      "v_cvt_pk_bf16_f32 v171, v174, v175\n"
      "v_mfma_f32_16x16x32_bf16 v[104:107], v[40:43], a[216:219], v[104:107]\n"
      "v_exp_f32 v176, v176\n"
      "v_mfma_f32_16x16x32_bf16 v[104:107], v[44:47], a[220:223], v[104:107]\n"
      "ds_read_b64_tr_b16 v[68:69], v5 offset:24576\n"
      "v_add_f32 v19, v19, v176\n"
      "v_mfma_f32_16x16x32_bf16 v[108:111], v[48:51], a[208:211], v[196:199]\n"
      "v_exp_f32 v177, v177\n"
      "v_mfma_f32_16x16x32_bf16 v[108:111], v[52:55], a[212:215], v[108:111]\n"
      "v_add_f32 v19, v19, v177\n"
      "v_cvt_pk_bf16_f32 v176, v176, v177\n"
      "v_mfma_f32_16x16x32_bf16 v[108:111], v[56:59], a[216:219], v[108:111]\n"
      "v_exp_f32 v178, v178\n"
      "v_mfma_f32_16x16x32_bf16 v[108:111], v[60:63], a[220:223], v[108:111]\n"
      "ds_read_b64_tr_b16 v[70:71], v5 offset:28672\n"
      "v_add_f32 v19, v19, v178\n"
      "v_mfma_f32_16x16x32_bf16 v[112:115], v[32:35], a[224:227], v[200:203]\n"
      "v_exp_f32 v179, v179\n"
      "v_mfma_f32_16x16x32_bf16 v[112:115], v[36:39], a[228:231], v[112:115]\n"
      "v_add_f32 v19, v19, v179\n"
      "v_cvt_pk_bf16_f32 v177, v178, v179\n"
      "v_mfma_f32_16x16x32_bf16 v[112:115], v[40:43], a[232:235], v[112:115]\n"
      "ds_read_b64_tr_b16 v[72:73], v4 offset:24832\n"
      "ds_read_b64_tr_b16 v[74:75], v4 offset:28928\n"
      "v_mfma_f32_16x16x32_bf16 v[112:115], v[44:47], a[236:239], v[112:115]\n"
      "ds_read_b64_tr_b16 v[76:77], v5 offset:24832\n"
      "ds_read_b64_tr_b16 v[78:79], v5 offset:28928\n"
      "v_mfma_f32_16x16x32_bf16 v[116:119], v[48:51], a[224:227], v[200:203]\n"
      "v_exp_f32 v96, v96\n"
      "v_mfma_f32_16x16x32_bf16 v[116:119], v[52:55], a[228:231], v[116:119]\n"
      "ds_read_b64_tr_b16 v[80:81], v4 offset:25088\n"
      "v_add_f32 v14, v14, v96\n"
      "v_mfma_f32_16x16x32_bf16 v[116:119], v[56:59], a[232:235], v[116:119]\n"
      "v_exp_f32 v97, v97\n"
      "v_mfma_f32_16x16x32_bf16 v[116:119], v[60:63], a[236:239], v[116:119]\n"
      "v_add_f32 v14, v14, v97\n"
      "v_cvt_pk_bf16_f32 v96, v96, v97\n"
      "v_mfma_f32_16x16x32_bf16 v[120:123], v[32:35], a[240:243], v[204:207]\n"
      "v_exp_f32 v98, v98\n"
      "v_mfma_f32_16x16x32_bf16 v[120:123], v[36:39], a[244:247], v[120:123]\n"
      "ds_read_b64_tr_b16 v[82:83], v4 offset:29184\n"
      "v_add_f32 v14, v14, v98\n"
      "v_mfma_f32_16x16x32_bf16 v[120:123], v[40:43], a[248:251], v[120:123]\n"
      "v_exp_f32 v99, v99\n"
      "v_mfma_f32_16x16x32_bf16 v[120:123], v[44:47], a[252:255], v[120:123]\n"
      "v_add_f32 v14, v14, v99\n"
      "v_cvt_pk_bf16_f32 v97, v98, v99\n"
      "v_mfma_f32_16x16x32_bf16 v[124:127], v[48:51], a[240:243], v[204:207]\n"
      "v_exp_f32 v100, v100\n"
      "v_mfma_f32_16x16x32_bf16 v[124:127], v[52:55], a[244:247], v[124:127]\n"
      "ds_read_b64_tr_b16 v[84:85], v5 offset:25088\n"
      "ds_read_b64_tr_b16 v[86:87], v5 offset:29184\n"
      "v_add_f32 v14, v14, v100\n"
      "v_mfma_f32_16x16x32_bf16 v[124:127], v[56:59], a[248:251], v[124:127]\n"
      "v_exp_f32 v101, v101\n"
      "v_mfma_f32_16x16x32_bf16 v[124:127], v[60:63], a[252:255], v[124:127]\n"
      "v_add_f32 v14, v14, v101\n"
      "v_cvt_pk_bf16_f32 v98, v100, v101\n"
      "v_mfma_f32_16x16x32_bf16 v[128:131], v[32:35], v[216:219], v[208:211]\n"
      "v_exp_f32 v102, v102\n"
      "v_mfma_f32_16x16x32_bf16 v[128:131], v[36:39], v[220:223], v[128:131]\n"
      "ds_read_b64_tr_b16 v[88:89], v4 offset:25344\n"
      "ds_read_b64_tr_b16 v[90:91], v4 offset:29440\n"
      "v_add_f32 v14, v14, v102\n"
      "v_mfma_f32_16x16x32_bf16 v[128:131], v[40:43], v[224:227], v[128:131]\n"
      "v_exp_f32 v103, v103\n"
      "v_mfma_f32_16x16x32_bf16 v[128:131], v[44:47], v[228:231], v[128:131]\n"
      "v_add_f32 v14, v14, v103\n"
      "v_cvt_pk_bf16_f32 v99, v102, v103\n"
      "v_mfma_f32_16x16x32_bf16 v[132:135], v[48:51], v[216:219], v[208:211]\n"
      "v_exp_f32 v104, v104\n"
      "v_mfma_f32_16x16x32_bf16 v[132:135], v[52:55], v[220:223], v[132:135]\n"
      "ds_read_b64_tr_b16 v[92:93], v5 offset:25344\n"
      "ds_read_b64_tr_b16 v[94:95], v5 offset:29440\n"
      "v_add_f32 v15, v15, v104\n"
      "v_xor_b32 v4, 0x8000, v4\n"
      "v_mfma_f32_16x16x32_bf16 v[132:135], v[56:59], v[224:227], v[132:135]\n"
      "v_exp_f32 v105, v105\n"
      "v_xor_b32 v5, 0x8000, v5\n"
      "v_mfma_f32_16x16x32_bf16 v[132:135], v[60:63], v[228:231], v[132:135]\n"
      "v_add_f32 v15, v15, v105\n"
      "v_cvt_pk_bf16_f32 v104, v104, v105\n"
      "s_waitcnt lgkmcnt(0)\n"
      "v_mfma_f32_16x16x32_bf16 a[0:3], v[136:139], v[64:67], a[0:3]\n"
      "v_exp_f32 v106, v106\n"
      "v_mfma_f32_16x16x32_bf16 a[4:7], v[136:139], v[68:71], a[4:7]\n"
      "ds_read_b128 v[32:35], v0 offset:8192\n"
      "v_add_f32 v15, v15, v106\n"
      "v_mfma_f32_16x16x32_bf16 a[8:11], v[136:139], v[72:75], a[8:11]\n"
      "v_exp_f32 v107, v107\n"
      "v_mfma_f32_16x16x32_bf16 a[12:15], v[136:139], v[76:79], a[12:15]\n"
      "v_add_f32 v15, v15, v107\n"
      "v_cvt_pk_bf16_f32 v105, v106, v107\n"
      "v_mfma_f32_16x16x32_bf16 a[16:19], v[136:139], v[80:83], a[16:19]\n"
      "v_exp_f32 v108, v108\n"
      "v_mfma_f32_16x16x32_bf16 a[20:23], v[136:139], v[84:87], a[20:23]\n"
      "v_add_f32 v15, v15, v108\n"
      "v_mfma_f32_16x16x32_bf16 a[24:27], v[136:139], v[88:91], a[24:27]\n"
      "v_exp_f32 v109, v109\n"
      "v_mfma_f32_16x16x32_bf16 a[28:31], v[136:139], v[92:95], a[28:31]\n"
      "v_add_f32 v15, v15, v109\n"
      "v_cvt_pk_bf16_f32 v106, v108, v109\n"
      "v_mfma_f32_16x16x32_bf16 a[32:35], v[144:147], v[64:67], a[32:35]\n"
      "v_exp_f32 v110, v110\n"
      "v_mfma_f32_16x16x32_bf16 a[36:39], v[144:147], v[68:71], a[36:39]\n"
      "ds_read_b128 v[36:39], v1 offset:8192\n"
      "v_add_f32 v15, v15, v110\n"
      "s_xor_b32 m0, s56, 0xc000\n"
      "v_mfma_f32_16x16x32_bf16 a[40:43], v[144:147], v[72:75], a[40:43]\n"
      "buffer_load_dwordx4 v6, s[40:43], s55 offen offset:0 lds\n"
      "v_exp_f32 v111, v111\n"
      "v_mfma_f32_16x16x32_bf16 a[44:47], v[144:147], v[76:79], a[44:47]\n"
      "v_add_f32 v15, v15, v111\n"
      "v_cvt_pk_bf16_f32 v107, v110, v111\n"
      "v_mfma_f32_16x16x32_bf16 a[48:51], v[144:147], v[80:83], a[48:51]\n"
      "v_exp_f32 v112, v112\n"
      "v_mfma_f32_16x16x32_bf16 a[52:55], v[144:147], v[84:87], a[52:55]\n"
      "ds_read_b128 v[40:43], v2 offset:8192\n"
      "v_add_f32 v16, v16, v112\n"
      "v_mfma_f32_16x16x32_bf16 a[56:59], v[144:147], v[88:91], a[56:59]\n"
      "v_exp_f32 v113, v113\n"
      "v_mfma_f32_16x16x32_bf16 a[60:63], v[144:147], v[92:95], a[60:63]\n"
      "v_add_f32 v16, v16, v113\n"
      "v_cvt_pk_bf16_f32 v112, v112, v113\n"
      "v_mfma_f32_16x16x32_bf16 a[64:67], v[152:155], v[64:67], a[64:67]\n"
      "v_exp_f32 v114, v114\n"
      "v_mfma_f32_16x16x32_bf16 a[68:71], v[152:155], v[68:71], a[68:71]\n"
      "ds_read_b128 v[44:47], v3 offset:8192\n"
      "v_add_f32 v16, v16, v114\n"
      "v_mfma_f32_16x16x32_bf16 a[72:75], v[152:155], v[72:75], a[72:75]\n"
      "buffer_load_dwordx4 v7, s[40:43], s55 offen offset:1024 lds\n"
      "v_exp_f32 v115, v115\n"
      "v_mfma_f32_16x16x32_bf16 a[76:79], v[152:155], v[76:79], a[76:79]\n"
      "v_add_f32 v16, v16, v115\n"
      "v_cvt_pk_bf16_f32 v113, v114, v115\n"
      "v_mfma_f32_16x16x32_bf16 a[80:83], v[152:155], v[80:83], a[80:83]\n"
      "v_exp_f32 v116, v116\n"
      "v_mfma_f32_16x16x32_bf16 a[84:87], v[152:155], v[84:87], a[84:87]\n"
      "ds_read_b128 v[48:51], v0 offset:12288\n"
      "v_add_f32 v16, v16, v116\n"
      "v_mfma_f32_16x16x32_bf16 a[88:91], v[152:155], v[88:91], a[88:91]\n"
      "v_exp_f32 v117, v117\n"
      "v_mfma_f32_16x16x32_bf16 a[92:95], v[152:155], v[92:95], a[92:95]\n"
      "v_add_f32 v16, v16, v117\n"
      "v_cvt_pk_bf16_f32 v114, v116, v117\n"
      "v_mfma_f32_16x16x32_bf16 a[96:99], v[160:163], v[64:67], a[96:99]\n"
      "v_exp_f32 v118, v118\n"
      "v_mfma_f32_16x16x32_bf16 a[100:103], v[160:163], v[68:71], a[100:103]\n"
      "v_add_f32 v16, v16, v118\n"
      "v_mfma_f32_16x16x32_bf16 a[104:107], v[160:163], v[72:75], a[104:107]\n"
      "buffer_load_dwordx4 v8, s[40:43], s55 offen offset:2048 lds\n"
      "v_exp_f32 v119, v119\n"
      "v_mfma_f32_16x16x32_bf16 a[108:111], v[160:163], v[76:79], a[108:111]\n"
      "v_add_f32 v16, v16, v119\n"
      "v_cvt_pk_bf16_f32 v115, v118, v119\n"
      "v_mfma_f32_16x16x32_bf16 a[112:115], v[160:163], v[80:83], a[112:115]\n"
      "v_exp_f32 v120, v120\n"
      "v_mfma_f32_16x16x32_bf16 a[116:119], v[160:163], v[84:87], a[116:119]\n"
      "ds_read_b128 v[52:55], v1 offset:12288\n"
      "v_add_f32 v17, v17, v120\n"
      "v_mfma_f32_16x16x32_bf16 a[120:123], v[160:163], v[88:91], a[120:123]\n"
      "v_exp_f32 v121, v121\n"
      "v_mfma_f32_16x16x32_bf16 a[124:127], v[160:163], v[92:95], a[124:127]\n"
      "v_add_f32 v17, v17, v121\n"
      "v_cvt_pk_bf16_f32 v120, v120, v121\n"
      "v_mfma_f32_16x16x32_bf16 a[128:131], v[168:171], v[64:67], a[128:131]\n"
      "v_exp_f32 v122, v122\n"
      "v_mfma_f32_16x16x32_bf16 a[132:135], v[168:171], v[68:71], a[132:135]\n"
      "ds_read_b128 v[56:59], v2 offset:12288\n"
      "v_add_f32 v17, v17, v122\n"
      "v_mfma_f32_16x16x32_bf16 a[136:139], v[168:171], v[72:75], a[136:139]\n"
      "buffer_load_dwordx4 v9, s[40:43], s55 offen offset:3072 lds\n"
      "v_exp_f32 v123, v123\n"
      "s_add_u32 s55, s55, 0x2000\n"
      "v_mfma_f32_16x16x32_bf16 a[140:143], v[168:171], v[76:79], a[140:143]\n"
      "v_add_f32 v17, v17, v123\n"
      "v_cvt_pk_bf16_f32 v121, v122, v123\n"
      "v_mfma_f32_16x16x32_bf16 a[144:147], v[168:171], v[80:83], a[144:147]\n"
      "v_exp_f32 v124, v124\n"
      "v_mfma_f32_16x16x32_bf16 a[148:151], v[168:171], v[84:87], a[148:151]\n"
      "ds_read_b128 v[60:63], v3 offset:12288\n"
      "v_add_f32 v17, v17, v124\n"
      "v_mfma_f32_16x16x32_bf16 a[152:155], v[168:171], v[88:91], a[152:155]\n"
      "v_exp_f32 v125, v125\n"
      "v_mfma_f32_16x16x32_bf16 a[156:159], v[168:171], v[92:95], a[156:159]\n"
      "v_add_f32 v17, v17, v125\n"
      "v_cvt_pk_bf16_f32 v122, v124, v125\n"
      "v_mfma_f32_16x16x16_bf16 a[160:163], v[176:177], v[64:65], a[160:163]\n"
      "v_exp_f32 v126, v126\n"
      "v_mfma_f32_16x16x16_bf16 a[164:167], v[176:177], v[68:69], a[164:167]\n"
      "v_add_f32 v17, v17, v126\n"
      "v_mfma_f32_16x16x16_bf16 a[168:171], v[176:177], v[72:73], a[168:171]\n"
      "v_exp_f32 v127, v127\n"
      "v_mfma_f32_16x16x16_bf16 a[172:175], v[176:177], v[76:77], a[172:175]\n"
      "v_add_f32 v17, v17, v127\n"
      "v_cvt_pk_bf16_f32 v123, v126, v127\n"
      "v_mfma_f32_16x16x16_bf16 a[176:179], v[176:177], v[80:81], a[176:179]\n"
      "v_exp_f32 v128, v128\n"
      "v_mfma_f32_16x16x16_bf16 a[180:183], v[176:177], v[84:85], a[180:183]\n"
      "v_add_f32 v18, v18, v128\n"
      "v_mfma_f32_16x16x16_bf16 a[184:187], v[176:177], v[88:89], a[184:187]\n"
      "v_exp_f32 v129, v129\n"
      "v_mfma_f32_16x16x16_bf16 a[188:191], v[176:177], v[92:93], a[188:191]\n"
      "v_add_f32 v18, v18, v129\n"
      "v_cvt_pk_bf16_f32 v128, v128, v129\n"
      "s_waitcnt lgkmcnt(0)\n"
      "L_entry_w2:\n"
      "v_mfma_f32_16x16x32_bf16 v[136:139], v[32:35], a[192:195], v[192:195]\n"
      "v_exp_f32 v130, v130\n"
      "v_mfma_f32_16x16x32_bf16 v[136:139], v[36:39], a[196:199], v[136:139]\n"
      "ds_read_b64_tr_b16 v[64:65], v4 offset:0\n"
      "v_add_f32 v18, v18, v130\n"
      "v_mfma_f32_16x16x32_bf16 v[136:139], v[40:43], a[200:203], v[136:139]\n"
      "v_exp_f32 v131, v131\n"
      "v_mfma_f32_16x16x32_bf16 v[136:139], v[44:47], a[204:207], v[136:139]\n"
      "v_add_f32 v18, v18, v131\n"
      "v_cvt_pk_bf16_f32 v129, v130, v131\n"
      "v_mfma_f32_16x16x32_bf16 v[140:143], v[48:51], a[192:195], v[192:195]\n"
      "v_exp_f32 v132, v132\n"
      "v_mfma_f32_16x16x32_bf16 v[140:143], v[52:55], a[196:199], v[140:143]\n"
      "ds_read_b64_tr_b16 v[66:67], v4 offset:4096\n"
      "v_add_f32 v18, v18, v132\n"
      "v_mfma_f32_16x16x32_bf16 v[140:143], v[56:59], a[200:203], v[140:143]\n"
      "v_exp_f32 v133, v133\n"
      "v_mfma_f32_16x16x32_bf16 v[140:143], v[60:63], a[204:207], v[140:143]\n"
      "v_add_f32 v18, v18, v133\n"
      "v_cvt_pk_bf16_f32 v130, v132, v133\n"
      "v_mfma_f32_16x16x32_bf16 v[144:147], v[32:35], a[208:211], v[196:199]\n"
      "v_exp_f32 v134, v134\n"
      "v_mfma_f32_16x16x32_bf16 v[144:147], v[36:39], a[212:215], v[144:147]\n"
      "ds_read_b64_tr_b16 v[68:69], v5 offset:0\n"
      "v_add_f32 v18, v18, v134\n"
      "v_mfma_f32_16x16x32_bf16 v[144:147], v[40:43], a[216:219], v[144:147]\n"
      "v_exp_f32 v135, v135\n"
      "v_mfma_f32_16x16x32_bf16 v[144:147], v[44:47], a[220:223], v[144:147]\n"
      "v_add_f32 v18, v18, v135\n"
      "v_cvt_pk_bf16_f32 v131, v134, v135\n"
      "v_mfma_f32_16x16x32_bf16 v[148:151], v[48:51], a[208:211], v[196:199]\n"
      "ds_read_b64_tr_b16 v[70:71], v5 offset:4096\n"
      "ds_read_b64_tr_b16 v[72:73], v4 offset:256\n"
      "v_mfma_f32_16x16x32_bf16 v[148:151], v[52:55], a[212:215], v[148:151]\n"
      "v_mfma_f32_16x16x32_bf16 v[148:151], v[56:59], a[216:219], v[148:151]\n"
      "v_exp_f32 v136, v136\n"
      "v_mfma_f32_16x16x32_bf16 v[148:151], v[60:63], a[220:223], v[148:151]\n"
      "ds_read_b64_tr_b16 v[74:75], v4 offset:4352\n"
      "v_add_f32 v14, v14, v136\n"
      "v_mfma_f32_16x16x32_bf16 v[152:155], v[32:35], a[224:227], v[200:203]\n"
      "v_exp_f32 v137, v137\n"
      "v_mfma_f32_16x16x32_bf16 v[152:155], v[36:39], a[228:231], v[152:155]\n"
      "v_add_f32 v14, v14, v137\n"
      "v_cvt_pk_bf16_f32 v136, v136, v137\n"
      "v_mfma_f32_16x16x32_bf16 v[152:155], v[40:43], a[232:235], v[152:155]\n"
      "v_exp_f32 v138, v138\n"
      "v_mfma_f32_16x16x32_bf16 v[152:155], v[44:47], a[236:239], v[152:155]\n"
      "ds_read_b64_tr_b16 v[76:77], v5 offset:256\n"
      "v_add_f32 v14, v14, v138\n"
      "v_mfma_f32_16x16x32_bf16 v[156:159], v[48:51], a[224:227], v[200:203]\n"
      "v_exp_f32 v139, v139\n"
      "v_mfma_f32_16x16x32_bf16 v[156:159], v[52:55], a[228:231], v[156:159]\n"
      "v_add_f32 v14, v14, v139\n"
      "v_cvt_pk_bf16_f32 v137, v138, v139\n"
      "v_mfma_f32_16x16x32_bf16 v[156:159], v[56:59], a[232:235], v[156:159]\n"
      "v_exp_f32 v140, v140\n"
      "v_mfma_f32_16x16x32_bf16 v[156:159], v[60:63], a[236:239], v[156:159]\n"
      "ds_read_b64_tr_b16 v[78:79], v5 offset:4352\n"
      "v_add_f32 v14, v14, v140\n"
      "v_mfma_f32_16x16x32_bf16 v[160:163], v[32:35], a[240:243], v[204:207]\n"
      "v_exp_f32 v141, v141\n"
      "v_mfma_f32_16x16x32_bf16 v[160:163], v[36:39], a[244:247], v[160:163]\n"
      "v_add_f32 v14, v14, v141\n"
      "v_cvt_pk_bf16_f32 v138, v140, v141\n"
      "v_mfma_f32_16x16x32_bf16 v[160:163], v[40:43], a[248:251], v[160:163]\n"
      "v_exp_f32 v142, v142\n"
      "v_mfma_f32_16x16x32_bf16 v[160:163], v[44:47], a[252:255], v[160:163]\n"
      "ds_read_b64_tr_b16 v[80:81], v4 offset:512\n"
      "ds_read_b64_tr_b16 v[82:83], v4 offset:4608\n"
      "v_add_f32 v14, v14, v142\n"
      "v_mfma_f32_16x16x32_bf16 v[164:167], v[48:51], a[240:243], v[204:207]\n"
      "v_exp_f32 v143, v143\n"
      "v_mfma_f32_16x16x32_bf16 v[164:167], v[52:55], a[244:247], v[164:167]\n"
      "v_add_f32 v14, v14, v143\n"
      "v_cvt_pk_bf16_f32 v139, v142, v143\n"
      "v_mfma_f32_16x16x32_bf16 v[164:167], v[56:59], a[248:251], v[164:167]\n"
      "v_exp_f32 v144, v144\n"
      "v_mfma_f32_16x16x32_bf16 v[164:167], v[60:63], a[252:255], v[164:167]\n"
      "ds_read_b64_tr_b16 v[84:85], v5 offset:512\n"
      "ds_read_b64_tr_b16 v[86:87], v5 offset:4608\n"
      "v_add_f32 v15, v15, v144\n"
      "v_mfma_f32_16x16x32_bf16 v[168:171], v[32:35], v[216:219], v[208:211]\n"
      "v_exp_f32 v145, v145\n"
      "v_mfma_f32_16x16x32_bf16 v[168:171], v[36:39], v[220:223], v[168:171]\n"
      "v_add_f32 v15, v15, v145\n"
      "v_cvt_pk_bf16_f32 v144, v144, v145\n"
      "v_mfma_f32_16x16x32_bf16 v[168:171], v[40:43], v[224:227], v[168:171]\n"
      "v_exp_f32 v146, v146\n"
      "v_mfma_f32_16x16x32_bf16 v[168:171], v[44:47], v[228:231], v[168:171]\n"
      "ds_read_b64_tr_b16 v[88:89], v4 offset:768\n"
      "ds_read_b64_tr_b16 v[90:91], v4 offset:4864\n"
      "v_add_f32 v15, v15, v146\n"
      "v_mfma_f32_16x16x32_bf16 v[172:175], v[48:51], v[216:219], v[208:211]\n"
      "v_exp_f32 v147, v147\n"
      "v_mfma_f32_16x16x32_bf16 v[172:175], v[52:55], v[220:223], v[172:175]\n"
      "v_add_f32 v15, v15, v147\n"
      "v_cvt_pk_bf16_f32 v145, v146, v147\n"
      "v_mfma_f32_16x16x32_bf16 v[172:175], v[56:59], v[224:227], v[172:175]\n"
      "v_exp_f32 v148, v148\n"
      "v_mfma_f32_16x16x32_bf16 v[172:175], v[60:63], v[228:231], v[172:175]\n"
      "ds_read_b64_tr_b16 v[92:93], v5 offset:768\n"
      "ds_read_b64_tr_b16 v[94:95], v5 offset:4864\n"
      "v_add_f32 v15, v15, v148\n"
      "v_mfma_f32_16x16x32_bf16 v[176:179], v[32:35], v[232:235], v[212:215]\n"
      "v_exp_f32 v149, v149\n"
      "v_mfma_f32_16x16x32_bf16 v[176:179], v[36:39], v[236:239], v[176:179]\n"
      "v_add_f32 v15, v15, v149\n"
      "v_cvt_pk_bf16_f32 v146, v148, v149\n"
      "v_mfma_f32_16x16x32_bf16 v[176:179], v[40:43], v[240:243], v[176:179]\n"
      "v_exp_f32 v150, v150\n"
      "v_mfma_f32_16x16x32_bf16 v[176:179], v[44:47], v[244:247], v[176:179]\n"
      "v_add_f32 v15, v15, v150\n"
      "s_waitcnt lgkmcnt(0)\n"
      "v_mfma_f32_16x16x32_bf16 a[0:3], v[96:99], v[64:67], a[0:3]\n"
      "v_exp_f32 v151, v151\n"
      "v_mfma_f32_16x16x32_bf16 a[4:7], v[96:99], v[68:71], a[4:7]\n"
      "v_add_f32 v15, v15, v151\n"
      "v_cvt_pk_bf16_f32 v147, v150, v151\n"
      "v_mfma_f32_16x16x32_bf16 a[8:11], v[96:99], v[72:75], a[8:11]\n"
      "v_exp_f32 v152, v152\n"
      "v_mfma_f32_16x16x32_bf16 a[12:15], v[96:99], v[76:79], a[12:15]\n"
      "ds_read_b128 v[32:35], v0 offset:16384\n"
      "v_add_f32 v16, v16, v152\n"
      "v_mfma_f32_16x16x32_bf16 a[16:19], v[96:99], v[80:83], a[16:19]\n"
      "v_exp_f32 v153, v153\n"
      "v_mfma_f32_16x16x32_bf16 a[20:23], v[96:99], v[84:87], a[20:23]\n"
      "v_add_f32 v16, v16, v153\n"
      "v_cvt_pk_bf16_f32 v152, v152, v153\n"
      "s_xor_b32 m0, s56, 0xe000\n"
      "v_mfma_f32_16x16x32_bf16 a[24:27], v[96:99], v[88:91], a[24:27]\n"
      "buffer_load_dwordx4 v6, s[40:43], s55 offen offset:0 nt lds\n"
      "v_exp_f32 v154, v154\n"
      "v_mfma_f32_16x16x32_bf16 a[28:31], v[96:99], v[92:95], a[28:31]\n"
      "ds_read_b128 v[36:39], v1 offset:16384\n"
      "v_add_f32 v16, v16, v154\n"
      "v_mfma_f32_16x16x32_bf16 a[32:35], v[104:107], v[64:67], a[32:35]\n"
      "v_exp_f32 v155, v155\n"
      "v_mfma_f32_16x16x32_bf16 a[36:39], v[104:107], v[68:71], a[36:39]\n"
      "v_add_f32 v16, v16, v155\n"
      "v_cvt_pk_bf16_f32 v153, v154, v155\n"
      "v_mfma_f32_16x16x32_bf16 a[40:43], v[104:107], v[72:75], a[40:43]\n"
      "v_exp_f32 v156, v156\n"
      "v_mfma_f32_16x16x32_bf16 a[44:47], v[104:107], v[76:79], a[44:47]\n"
      "ds_read_b128 v[40:43], v2 offset:16384\n"
      "v_add_f32 v16, v16, v156\n"
      "v_mfma_f32_16x16x32_bf16 a[48:51], v[104:107], v[80:83], a[48:51]\n"
      "v_exp_f32 v157, v157\n"
      "v_mfma_f32_16x16x32_bf16 a[52:55], v[104:107], v[84:87], a[52:55]\n"
      "v_add_f32 v16, v16, v157\n"
      "v_cvt_pk_bf16_f32 v154, v156, v157\n"
      "v_mfma_f32_16x16x32_bf16 a[56:59], v[104:107], v[88:91], a[56:59]\n"
      "buffer_load_dwordx4 v7, s[40:43], s55 offen offset:1024 nt lds\n"
      "v_exp_f32 v158, v158\n"
      "v_mfma_f32_16x16x32_bf16 a[60:63], v[104:107], v[92:95], a[60:63]\n"
      "ds_read_b128 v[44:47], v3 offset:16384\n"
      "v_add_f32 v16, v16, v158\n"
      "v_mfma_f32_16x16x32_bf16 a[64:67], v[112:115], v[64:67], a[64:67]\n"
      "v_exp_f32 v159, v159\n"
      "v_mfma_f32_16x16x32_bf16 a[68:71], v[112:115], v[68:71], a[68:71]\n"
      "v_add_f32 v16, v16, v159\n"
      "v_cvt_pk_bf16_f32 v155, v158, v159\n"
      "v_mfma_f32_16x16x32_bf16 a[72:75], v[112:115], v[72:75], a[72:75]\n"
      "v_exp_f32 v160, v160\n"
      "v_mfma_f32_16x16x32_bf16 a[76:79], v[112:115], v[76:79], a[76:79]\n"
      "ds_read_b128 v[48:51], v0 offset:20480\n"
      "v_add_f32 v17, v17, v160\n"
      "v_mfma_f32_16x16x32_bf16 a[80:83], v[112:115], v[80:83], a[80:83]\n"
      "v_exp_f32 v161, v161\n"
      "v_mfma_f32_16x16x32_bf16 a[84:87], v[112:115], v[84:87], a[84:87]\n"
      "v_add_f32 v17, v17, v161\n"
      "v_cvt_pk_bf16_f32 v160, v160, v161\n"
      "v_mfma_f32_16x16x32_bf16 a[88:91], v[112:115], v[88:91], a[88:91]\n"
      "buffer_load_dwordx4 v8, s[40:43], s55 offen offset:2048 nt lds\n"
      "v_exp_f32 v162, v162\n"
      "v_mfma_f32_16x16x32_bf16 a[92:95], v[112:115], v[92:95], a[92:95]\n"
      "ds_read_b128 v[52:55], v1 offset:20480\n"
      "v_add_f32 v17, v17, v162\n"
      "v_mfma_f32_16x16x32_bf16 a[96:99], v[120:123], v[64:67], a[96:99]\n"
      "v_exp_f32 v163, v163\n"
      "v_mfma_f32_16x16x32_bf16 a[100:103], v[120:123], v[68:71], a[100:103]\n"
      "v_add_f32 v17, v17, v163\n"
      "v_cvt_pk_bf16_f32 v161, v162, v163\n"
      "v_mfma_f32_16x16x32_bf16 a[104:107], v[120:123], v[72:75], a[104:107]\n"
      "v_exp_f32 v164, v164\n"
      "v_mfma_f32_16x16x32_bf16 a[108:111], v[120:123], v[76:79], a[108:111]\n"
      "ds_read_b128 v[56:59], v2 offset:20480\n"
      "v_add_f32 v17, v17, v164\n"
      "v_mfma_f32_16x16x32_bf16 a[112:115], v[120:123], v[80:83], a[112:115]\n"
      "v_exp_f32 v165, v165\n"
      "v_mfma_f32_16x16x32_bf16 a[116:119], v[120:123], v[84:87], a[116:119]\n"
      "v_add_f32 v17, v17, v165\n"
      "v_cvt_pk_bf16_f32 v162, v164, v165\n"
      "v_mfma_f32_16x16x32_bf16 a[120:123], v[120:123], v[88:91], a[120:123]\n"
      "buffer_load_dwordx4 v9, s[40:43], s55 offen offset:3072 nt lds\n"
      "v_exp_f32 v166, v166\n"
      "s_add_u32 s55, s55, 0x2000\n"
      "v_mfma_f32_16x16x32_bf16 a[124:127], v[120:123], v[92:95], a[124:127]\n"
      "ds_read_b128 v[60:63], v3 offset:20480\n"
      "v_add_f32 v17, v17, v166\n"
      "v_mfma_f32_16x16x32_bf16 a[128:131], v[128:131], v[64:67], a[128:131]\n"
      "v_exp_f32 v167, v167\n"
      "v_mfma_f32_16x16x32_bf16 a[132:135], v[128:131], v[68:71], a[132:135]\n"
      "v_add_f32 v17, v17, v167\n"
      "v_cvt_pk_bf16_f32 v163, v166, v167\n"
      "v_mfma_f32_16x16x32_bf16 a[136:139], v[128:131], v[72:75], a[136:139]\n"
      "v_exp_f32 v168, v168\n"
      "v_mfma_f32_16x16x32_bf16 a[140:143], v[128:131], v[76:79], a[140:143]\n"
      "v_add_f32 v18, v18, v168\n"
      "v_mfma_f32_16x16x32_bf16 a[144:147], v[128:131], v[80:83], a[144:147]\n"
      "v_exp_f32 v169, v169\n"
      "v_mfma_f32_16x16x32_bf16 a[148:151], v[128:131], v[84:87], a[148:151]\n"
      "v_add_f32 v18, v18, v169\n"
      "v_cvt_pk_bf16_f32 v168, v168, v169\n"
      "v_mfma_f32_16x16x32_bf16 a[152:155], v[128:131], v[88:91], a[152:155]\n"
      "v_exp_f32 v170, v170\n"
      "v_mfma_f32_16x16x32_bf16 a[156:159], v[128:131], v[92:95], a[156:159]\n"
      "v_add_f32 v18, v18, v170\n"
      "s_waitcnt lgkmcnt(0)\n"
      "s_waitcnt vmcnt(12)\n"
      "s_barrier\n"
      "v_mfma_f32_16x16x32_bf16 v[96:99], v[32:35], a[192:195], v[192:195]\n"
      "v_exp_f32 v171, v171\n"
      "v_mfma_f32_16x16x32_bf16 v[96:99], v[36:39], a[196:199], v[96:99]\n"
      "v_add_f32 v18, v18, v171\n"
      "v_cvt_pk_bf16_f32 v169, v170, v171\n"
      "v_mfma_f32_16x16x32_bf16 v[96:99], v[40:43], a[200:203], v[96:99]\n"
      "v_exp_f32 v172, v172\n"
      "v_mfma_f32_16x16x32_bf16 v[96:99], v[44:47], a[204:207], v[96:99]\n"
      "ds_read_b64_tr_b16 v[64:65], v4 offset:8192\n"
      "v_add_f32 v18, v18, v172\n"
      "v_mfma_f32_16x16x32_bf16 v[100:103], v[48:51], a[192:195], v[192:195]\n"
      "v_exp_f32 v173, v173\n"
      "v_mfma_f32_16x16x32_bf16 v[100:103], v[52:55], a[196:199], v[100:103]\n"
      "v_add_f32 v18, v18, v173\n"
      "v_cvt_pk_bf16_f32 v170, v172, v173\n"
      "v_mfma_f32_16x16x32_bf16 v[100:103], v[56:59], a[200:203], v[100:103]\n"
      "v_exp_f32 v174, v174\n"
      "v_mfma_f32_16x16x32_bf16 v[100:103], v[60:63], a[204:207], v[100:103]\n"
      "ds_read_b64_tr_b16 v[66:67], v4 offset:12288\n"
      "v_add_f32 v18, v18, v174\n"
      "v_mfma_f32_16x16x32_bf16 v[104:107], v[32:35], a[208:211], v[196:199]\n"
      "v_exp_f32 v175, v175\n"
      "v_mfma_f32_16x16x32_bf16 v[104:107], v[36:39], a[212:215], v[104:107]\n"
      "v_add_f32 v18, v18, v175\n"
      "v_cvt_pk_bf16_f32 v171, v174, v175\n"
      "v_mfma_f32_16x16x32_bf16 v[104:107], v[40:43], a[216:219], v[104:107]\n"
      "v_exp_f32 v176, v176\n"
      "v_mfma_f32_16x16x32_bf16 v[104:107], v[44:47], a[220:223], v[104:107]\n"
      "ds_read_b64_tr_b16 v[68:69], v5 offset:8192\n"
      "v_add_f32 v19, v19, v176\n"
      "v_mfma_f32_16x16x32_bf16 v[108:111], v[48:51], a[208:211], v[196:199]\n"
      "v_exp_f32 v177, v177\n"
      "v_mfma_f32_16x16x32_bf16 v[108:111], v[52:55], a[212:215], v[108:111]\n"
      "v_add_f32 v19, v19, v177\n"
      "v_cvt_pk_bf16_f32 v176, v176, v177\n"
      "v_mfma_f32_16x16x32_bf16 v[108:111], v[56:59], a[216:219], v[108:111]\n"
      "v_exp_f32 v178, v178\n"
      "v_mfma_f32_16x16x32_bf16 v[108:111], v[60:63], a[220:223], v[108:111]\n"
      "ds_read_b64_tr_b16 v[70:71], v5 offset:12288\n"
      "v_add_f32 v19, v19, v178\n"
      "v_mfma_f32_16x16x32_bf16 v[112:115], v[32:35], a[224:227], v[200:203]\n"
      "v_exp_f32 v179, v179\n"
      "v_mfma_f32_16x16x32_bf16 v[112:115], v[36:39], a[228:231], v[112:115]\n"
      "v_add_f32 v19, v19, v179\n"
      "v_cvt_pk_bf16_f32 v177, v178, v179\n"
      "v_mfma_f32_16x16x32_bf16 v[112:115], v[40:43], a[232:235], v[112:115]\n"
      "ds_read_b64_tr_b16 v[72:73], v4 offset:8448\n"
      "ds_read_b64_tr_b16 v[74:75], v4 offset:12544\n"
      "v_mfma_f32_16x16x32_bf16 v[112:115], v[44:47], a[236:239], v[112:115]\n"
      "ds_read_b64_tr_b16 v[76:77], v5 offset:8448\n"
      "ds_read_b64_tr_b16 v[78:79], v5 offset:12544\n"
      "v_mfma_f32_16x16x32_bf16 v[116:119], v[48:51], a[224:227], v[200:203]\n"
      "v_exp_f32 v96, v96\n"
      "v_mfma_f32_16x16x32_bf16 v[116:119], v[52:55], a[228:231], v[116:119]\n"
      "ds_read_b64_tr_b16 v[80:81], v4 offset:8704\n"
      "v_add_f32 v14, v14, v96\n"
      "v_mfma_f32_16x16x32_bf16 v[116:119], v[56:59], a[232:235], v[116:119]\n"
      "v_exp_f32 v97, v97\n"
      "v_mfma_f32_16x16x32_bf16 v[116:119], v[60:63], a[236:239], v[116:119]\n"
      "v_add_f32 v14, v14, v97\n"
      "v_cvt_pk_bf16_f32 v96, v96, v97\n"
      "v_mfma_f32_16x16x32_bf16 v[120:123], v[32:35], a[240:243], v[204:207]\n"
      "v_exp_f32 v98, v98\n"
      "v_mfma_f32_16x16x32_bf16 v[120:123], v[36:39], a[244:247], v[120:123]\n"
      "ds_read_b64_tr_b16 v[82:83], v4 offset:12800\n"
      "v_add_f32 v14, v14, v98\n"
      "v_mfma_f32_16x16x32_bf16 v[120:123], v[40:43], a[248:251], v[120:123]\n"
      "v_exp_f32 v99, v99\n"
      "v_mfma_f32_16x16x32_bf16 v[120:123], v[44:47], a[252:255], v[120:123]\n"
      "v_add_f32 v14, v14, v99\n"
      "v_cvt_pk_bf16_f32 v97, v98, v99\n"
      "v_mfma_f32_16x16x32_bf16 v[124:127], v[48:51], a[240:243], v[204:207]\n"
      "v_exp_f32 v100, v100\n"
      "v_mfma_f32_16x16x32_bf16 v[124:127], v[52:55], a[244:247], v[124:127]\n"
      "ds_read_b64_tr_b16 v[84:85], v5 offset:8704\n"
      "ds_read_b64_tr_b16 v[86:87], v5 offset:12800\n"
      "v_add_f32 v14, v14, v100\n"
      "v_mfma_f32_16x16x32_bf16 v[124:127], v[56:59], a[248:251], v[124:127]\n"
      "v_exp_f32 v101, v101\n"
      "v_mfma_f32_16x16x32_bf16 v[124:127], v[60:63], a[252:255], v[124:127]\n"
      "v_add_f32 v14, v14, v101\n"
      "v_cvt_pk_bf16_f32 v98, v100, v101\n"
      "v_mfma_f32_16x16x32_bf16 v[128:131], v[32:35], v[216:219], v[208:211]\n"
      "v_exp_f32 v102, v102\n"
      "v_mfma_f32_16x16x32_bf16 v[128:131], v[36:39], v[220:223], v[128:131]\n"
      "ds_read_b64_tr_b16 v[88:89], v4 offset:8960\n"
      "ds_read_b64_tr_b16 v[90:91], v4 offset:13056\n"
      "v_add_f32 v14, v14, v102\n"
      "v_mfma_f32_16x16x32_bf16 v[128:131], v[40:43], v[224:227], v[128:131]\n"
      "v_exp_f32 v103, v103\n"
      "v_mfma_f32_16x16x32_bf16 v[128:131], v[44:47], v[228:231], v[128:131]\n"
      "v_add_f32 v14, v14, v103\n"
      "v_cvt_pk_bf16_f32 v99, v102, v103\n"
      "v_mfma_f32_16x16x32_bf16 v[132:135], v[48:51], v[216:219], v[208:211]\n"
      "v_exp_f32 v104, v104\n"
      "v_mfma_f32_16x16x32_bf16 v[132:135], v[52:55], v[220:223], v[132:135]\n"
      "ds_read_b64_tr_b16 v[92:93], v5 offset:8960\n"
      "ds_read_b64_tr_b16 v[94:95], v5 offset:13056\n"
      "v_add_f32 v15, v15, v104\n"
      "v_mfma_f32_16x16x32_bf16 v[132:135], v[56:59], v[224:227], v[132:135]\n"
      "v_exp_f32 v105, v105\n"
      "v_mfma_f32_16x16x32_bf16 v[132:135], v[60:63], v[228:231], v[132:135]\n"
      "v_add_f32 v15, v15, v105\n"
      "v_cvt_pk_bf16_f32 v104, v104, v105\n"
      "s_waitcnt lgkmcnt(0)\n"
      "v_mfma_f32_16x16x32_bf16 a[0:3], v[136:139], v[64:67], a[0:3]\n"
      "v_exp_f32 v106, v106\n"
      "v_mfma_f32_16x16x32_bf16 a[4:7], v[136:139], v[68:71], a[4:7]\n"
      "ds_read_b128 v[32:35], v0 offset:24576\n"
      "v_add_f32 v15, v15, v106\n"
      "v_mfma_f32_16x16x32_bf16 a[8:11], v[136:139], v[72:75], a[8:11]\n"
      "v_exp_f32 v107, v107\n"
      "v_mfma_f32_16x16x32_bf16 a[12:15], v[136:139], v[76:79], a[12:15]\n"
      "v_add_f32 v15, v15, v107\n"
      "v_cvt_pk_bf16_f32 v105, v106, v107\n"
      "v_mfma_f32_16x16x32_bf16 a[16:19], v[136:139], v[80:83], a[16:19]\n"
      "v_exp_f32 v108, v108\n"
      "v_mfma_f32_16x16x32_bf16 a[20:23], v[136:139], v[84:87], a[20:23]\n"
      "v_add_f32 v15, v15, v108\n"
      "v_mfma_f32_16x16x32_bf16 a[24:27], v[136:139], v[88:91], a[24:27]\n"
      "v_exp_f32 v109, v109\n"
      "v_mfma_f32_16x16x32_bf16 a[28:31], v[136:139], v[92:95], a[28:31]\n"
      "v_add_f32 v15, v15, v109\n"
      "v_cvt_pk_bf16_f32 v106, v108, v109\n"
      "v_mfma_f32_16x16x32_bf16 a[32:35], v[144:147], v[64:67], a[32:35]\n"
      "v_exp_f32 v110, v110\n"
      "v_mfma_f32_16x16x32_bf16 a[36:39], v[144:147], v[68:71], a[36:39]\n"
      "ds_read_b128 v[36:39], v1 offset:24576\n"
      "v_add_f32 v15, v15, v110\n"
      "s_xor_b32 m0, s56, 0x0\n"
      "v_mfma_f32_16x16x32_bf16 a[40:43], v[144:147], v[72:75], a[40:43]\n"
      "buffer_load_dwordx4 v6, s[40:43], s55 offen offset:0 lds\n"
      "v_exp_f32 v111, v111\n"
      "v_mfma_f32_16x16x32_bf16 a[44:47], v[144:147], v[76:79], a[44:47]\n"
      "v_add_f32 v15, v15, v111\n"
      "v_cvt_pk_bf16_f32 v107, v110, v111\n"
      "v_mfma_f32_16x16x32_bf16 a[48:51], v[144:147], v[80:83], a[48:51]\n"
      "v_exp_f32 v112, v112\n"
      "v_mfma_f32_16x16x32_bf16 a[52:55], v[144:147], v[84:87], a[52:55]\n"
      "ds_read_b128 v[40:43], v2 offset:24576\n"
      "v_add_f32 v16, v16, v112\n"
      "v_mfma_f32_16x16x32_bf16 a[56:59], v[144:147], v[88:91], a[56:59]\n"
      "v_exp_f32 v113, v113\n"
      "v_mfma_f32_16x16x32_bf16 a[60:63], v[144:147], v[92:95], a[60:63]\n"
      "v_add_f32 v16, v16, v113\n"
      "v_cvt_pk_bf16_f32 v112, v112, v113\n"
      "v_mfma_f32_16x16x32_bf16 a[64:67], v[152:155], v[64:67], a[64:67]\n"
      "v_exp_f32 v114, v114\n"
      "v_mfma_f32_16x16x32_bf16 a[68:71], v[152:155], v[68:71], a[68:71]\n"
      "ds_read_b128 v[44:47], v3 offset:24576\n"
      "v_add_f32 v16, v16, v114\n"
      "v_mfma_f32_16x16x32_bf16 a[72:75], v[152:155], v[72:75], a[72:75]\n"
      "buffer_load_dwordx4 v7, s[40:43], s55 offen offset:1024 lds\n"
      "v_exp_f32 v115, v115\n"
      "v_mfma_f32_16x16x32_bf16 a[76:79], v[152:155], v[76:79], a[76:79]\n"
      "v_add_f32 v16, v16, v115\n"
      "v_cvt_pk_bf16_f32 v113, v114, v115\n"
      "v_mfma_f32_16x16x32_bf16 a[80:83], v[152:155], v[80:83], a[80:83]\n"
      "v_exp_f32 v116, v116\n"
      "v_mfma_f32_16x16x32_bf16 a[84:87], v[152:155], v[84:87], a[84:87]\n"
      "ds_read_b128 v[48:51], v0 offset:28672\n"
      "v_add_f32 v16, v16, v116\n"
      "v_mfma_f32_16x16x32_bf16 a[88:91], v[152:155], v[88:91], a[88:91]\n"
      "v_exp_f32 v117, v117\n"
      "v_mfma_f32_16x16x32_bf16 a[92:95], v[152:155], v[92:95], a[92:95]\n"
      "v_add_f32 v16, v16, v117\n"
      "v_cvt_pk_bf16_f32 v114, v116, v117\n"
      "v_mfma_f32_16x16x32_bf16 a[96:99], v[160:163], v[64:67], a[96:99]\n"
      "v_exp_f32 v118, v118\n"
      "v_mfma_f32_16x16x32_bf16 a[100:103], v[160:163], v[68:71], a[100:103]\n"
      "v_add_f32 v16, v16, v118\n"
      "v_mfma_f32_16x16x32_bf16 a[104:107], v[160:163], v[72:75], a[104:107]\n"
      "buffer_load_dwordx4 v8, s[40:43], s55 offen offset:2048 lds\n"
      "v_exp_f32 v119, v119\n"
      "v_mfma_f32_16x16x32_bf16 a[108:111], v[160:163], v[76:79], a[108:111]\n"
      "v_add_f32 v16, v16, v119\n"
      "v_cvt_pk_bf16_f32 v115, v118, v119\n"
      "v_mfma_f32_16x16x32_bf16 a[112:115], v[160:163], v[80:83], a[112:115]\n"
      "v_exp_f32 v120, v120\n"
      "v_mfma_f32_16x16x32_bf16 a[116:119], v[160:163], v[84:87], a[116:119]\n"
      "ds_read_b128 v[52:55], v1 offset:28672\n"
      "v_add_f32 v17, v17, v120\n"
      "v_mfma_f32_16x16x32_bf16 a[120:123], v[160:163], v[88:91], a[120:123]\n"
      "v_exp_f32 v121, v121\n"
      "v_mfma_f32_16x16x32_bf16 a[124:127], v[160:163], v[92:95], a[124:127]\n"
      "v_add_f32 v17, v17, v121\n"
      "v_cvt_pk_bf16_f32 v120, v120, v121\n"
      "v_mfma_f32_16x16x32_bf16 a[128:131], v[168:171], v[64:67], a[128:131]\n"
      "v_exp_f32 v122, v122\n"
      "v_mfma_f32_16x16x32_bf16 a[132:135], v[168:171], v[68:71], a[132:135]\n"
      "ds_read_b128 v[56:59], v2 offset:28672\n"
      "v_add_f32 v17, v17, v122\n"
      "v_mfma_f32_16x16x32_bf16 a[136:139], v[168:171], v[72:75], a[136:139]\n"
      "buffer_load_dwordx4 v9, s[40:43], s55 offen offset:3072 lds\n"
      "v_exp_f32 v123, v123\n"
      "s_add_u32 s55, s55, 0x2000\n"
      "v_mfma_f32_16x16x32_bf16 a[140:143], v[168:171], v[76:79], a[140:143]\n"
      "v_add_f32 v17, v17, v123\n"
      "v_cvt_pk_bf16_f32 v121, v122, v123\n"
      "v_mfma_f32_16x16x32_bf16 a[144:147], v[168:171], v[80:83], a[144:147]\n"
      "v_exp_f32 v124, v124\n"
      "v_mfma_f32_16x16x32_bf16 a[148:151], v[168:171], v[84:87], a[148:151]\n"
      "ds_read_b128 v[60:63], v3 offset:28672\n"
      "v_add_f32 v17, v17, v124\n"
      "v_mfma_f32_16x16x32_bf16 a[152:155], v[168:171], v[88:91], a[152:155]\n"
      "v_exp_f32 v125, v125\n"
      "v_mfma_f32_16x16x32_bf16 a[156:159], v[168:171], v[92:95], a[156:159]\n"
      "v_add_f32 v17, v17, v125\n"
      "v_cvt_pk_bf16_f32 v122, v124, v125\n"
      "v_mfma_f32_16x16x16_bf16 a[160:163], v[176:177], v[64:65], a[160:163]\n"
      "v_exp_f32 v126, v126\n"
      "v_mfma_f32_16x16x16_bf16 a[164:167], v[176:177], v[68:69], a[164:167]\n"
      "v_add_f32 v17, v17, v126\n"
      "v_mfma_f32_16x16x16_bf16 a[168:171], v[176:177], v[72:73], a[168:171]\n"
      "v_exp_f32 v127, v127\n"
      "v_mfma_f32_16x16x16_bf16 a[172:175], v[176:177], v[76:77], a[172:175]\n"
      "v_add_f32 v17, v17, v127\n"
      "v_cvt_pk_bf16_f32 v123, v126, v127\n"
      "v_mfma_f32_16x16x16_bf16 a[176:179], v[176:177], v[80:81], a[176:179]\n"
      "v_exp_f32 v128, v128\n"
      "v_mfma_f32_16x16x16_bf16 a[180:183], v[176:177], v[84:85], a[180:183]\n"
      "v_add_f32 v18, v18, v128\n"
      "v_mfma_f32_16x16x16_bf16 a[184:187], v[176:177], v[88:89], a[184:187]\n"
      "v_exp_f32 v129, v129\n"
      "v_mfma_f32_16x16x16_bf16 a[188:191], v[176:177], v[92:93], a[188:191]\n"
      "v_add_f32 v18, v18, v129\n"
      "v_cvt_pk_bf16_f32 v128, v128, v129\n"
      "s_waitcnt lgkmcnt(0)\n"
      "v_mfma_f32_16x16x32_bf16 v[136:139], v[32:35], a[192:195], v[192:195]\n"
      "v_exp_f32 v130, v130\n"
      "v_mfma_f32_16x16x32_bf16 v[136:139], v[36:39], a[196:199], v[136:139]\n"
      "ds_read_b64_tr_b16 v[64:65], v4 offset:16384\n"
      "v_add_f32 v18, v18, v130\n"
      "v_mfma_f32_16x16x32_bf16 v[136:139], v[40:43], a[200:203], v[136:139]\n"
      "v_exp_f32 v131, v131\n"
      "v_mfma_f32_16x16x32_bf16 v[136:139], v[44:47], a[204:207], v[136:139]\n"
      "v_add_f32 v18, v18, v131\n"
      "v_cvt_pk_bf16_f32 v129, v130, v131\n"
      "v_mfma_f32_16x16x32_bf16 v[140:143], v[48:51], a[192:195], v[192:195]\n"
      "v_exp_f32 v132, v132\n"
      "v_mfma_f32_16x16x32_bf16 v[140:143], v[52:55], a[196:199], v[140:143]\n"
      "ds_read_b64_tr_b16 v[66:67], v4 offset:20480\n"
      "v_add_f32 v18, v18, v132\n"
      "v_mfma_f32_16x16x32_bf16 v[140:143], v[56:59], a[200:203], v[140:143]\n"
      "v_exp_f32 v133, v133\n"
      "v_mfma_f32_16x16x32_bf16 v[140:143], v[60:63], a[204:207], v[140:143]\n"
      "v_add_f32 v18, v18, v133\n"
      "v_cvt_pk_bf16_f32 v130, v132, v133\n"
      "v_mfma_f32_16x16x32_bf16 v[144:147], v[32:35], a[208:211], v[196:199]\n"
      "v_exp_f32 v134, v134\n"
      "v_mfma_f32_16x16x32_bf16 v[144:147], v[36:39], a[212:215], v[144:147]\n"
      "ds_read_b64_tr_b16 v[68:69], v5 offset:16384\n"
      "v_add_f32 v18, v18, v134\n"
      "v_mfma_f32_16x16x32_bf16 v[144:147], v[40:43], a[216:219], v[144:147]\n"
      "v_exp_f32 v135, v135\n"
      "v_mfma_f32_16x16x32_bf16 v[144:147], v[44:47], a[220:223], v[144:147]\n"
      "v_add_f32 v18, v18, v135\n"
      "v_cvt_pk_bf16_f32 v131, v134, v135\n"
      "v_mfma_f32_16x16x32_bf16 v[148:151], v[48:51], a[208:211], v[196:199]\n"
      "ds_read_b64_tr_b16 v[70:71], v5 offset:20480\n"
      "ds_read_b64_tr_b16 v[72:73], v4 offset:16640\n"
      "v_mfma_f32_16x16x32_bf16 v[148:151], v[52:55], a[212:215], v[148:151]\n"
      "v_mfma_f32_16x16x32_bf16 v[148:151], v[56:59], a[216:219], v[148:151]\n"
      "v_exp_f32 v136, v136\n"
      "v_mfma_f32_16x16x32_bf16 v[148:151], v[60:63], a[220:223], v[148:151]\n"
      "ds_read_b64_tr_b16 v[74:75], v4 offset:20736\n"
      "v_add_f32 v14, v14, v136\n"
      "v_mfma_f32_16x16x32_bf16 v[152:155], v[32:35], a[224:227], v[200:203]\n"
      "v_exp_f32 v137, v137\n"
      "v_mfma_f32_16x16x32_bf16 v[152:155], v[36:39], a[228:231], v[152:155]\n"
      "v_add_f32 v14, v14, v137\n"
      "v_cvt_pk_bf16_f32 v136, v136, v137\n"
      "v_mfma_f32_16x16x32_bf16 v[152:155], v[40:43], a[232:235], v[152:155]\n"
      "v_exp_f32 v138, v138\n"
      "v_mfma_f32_16x16x32_bf16 v[152:155], v[44:47], a[236:239], v[152:155]\n"
      "ds_read_b64_tr_b16 v[76:77], v5 offset:16640\n"
      "v_add_f32 v14, v14, v138\n"
      "v_mfma_f32_16x16x32_bf16 v[156:159], v[48:51], a[224:227], v[200:203]\n"
      "v_exp_f32 v139, v139\n"
      "v_mfma_f32_16x16x32_bf16 v[156:159], v[52:55], a[228:231], v[156:159]\n"
      "v_add_f32 v14, v14, v139\n"
      "v_cvt_pk_bf16_f32 v137, v138, v139\n"
      "v_mfma_f32_16x16x32_bf16 v[156:159], v[56:59], a[232:235], v[156:159]\n"
      "v_exp_f32 v140, v140\n"
      "v_mfma_f32_16x16x32_bf16 v[156:159], v[60:63], a[236:239], v[156:159]\n"
      "ds_read_b64_tr_b16 v[78:79], v5 offset:20736\n"
      "v_add_f32 v14, v14, v140\n"
      "v_mfma_f32_16x16x32_bf16 v[160:163], v[32:35], a[240:243], v[204:207]\n"
      "v_exp_f32 v141, v141\n"
      "v_mfma_f32_16x16x32_bf16 v[160:163], v[36:39], a[244:247], v[160:163]\n"
      "v_add_f32 v14, v14, v141\n"
      "v_cvt_pk_bf16_f32 v138, v140, v141\n"
      "v_mfma_f32_16x16x32_bf16 v[160:163], v[40:43], a[248:251], v[160:163]\n"
      "v_exp_f32 v142, v142\n"
      "v_mfma_f32_16x16x32_bf16 v[160:163], v[44:47], a[252:255], v[160:163]\n"
      "ds_read_b64_tr_b16 v[80:81], v4 offset:16896\n"
      "ds_read_b64_tr_b16 v[82:83], v4 offset:20992\n"
      "v_add_f32 v14, v14, v142\n"
      "v_mfma_f32_16x16x32_bf16 v[164:167], v[48:51], a[240:243], v[204:207]\n"
      "v_exp_f32 v143, v143\n"
      "v_mfma_f32_16x16x32_bf16 v[164:167], v[52:55], a[244:247], v[164:167]\n"
      "v_add_f32 v14, v14, v143\n"
      "v_cvt_pk_bf16_f32 v139, v142, v143\n"
      "v_mfma_f32_16x16x32_bf16 v[164:167], v[56:59], a[248:251], v[164:167]\n"
      "v_exp_f32 v144, v144\n"
      "v_mfma_f32_16x16x32_bf16 v[164:167], v[60:63], a[252:255], v[164:167]\n"
      "ds_read_b64_tr_b16 v[84:85], v5 offset:16896\n"
      "ds_read_b64_tr_b16 v[86:87], v5 offset:20992\n"
      "v_add_f32 v15, v15, v144\n"
      "v_mfma_f32_16x16x32_bf16 v[168:171], v[32:35], v[216:219], v[208:211]\n"
      "v_exp_f32 v145, v145\n"
      "v_mfma_f32_16x16x32_bf16 v[168:171], v[36:39], v[220:223], v[168:171]\n"
      "v_add_f32 v15, v15, v145\n"
      "v_cvt_pk_bf16_f32 v144, v144, v145\n"
      "v_mfma_f32_16x16x32_bf16 v[168:171], v[40:43], v[224:227], v[168:171]\n"
      "v_exp_f32 v146, v146\n"
      "v_mfma_f32_16x16x32_bf16 v[168:171], v[44:47], v[228:231], v[168:171]\n"
      "ds_read_b64_tr_b16 v[88:89], v4 offset:17152\n"
      "ds_read_b64_tr_b16 v[90:91], v4 offset:21248\n"
      "v_add_f32 v15, v15, v146\n"
      "v_mfma_f32_16x16x32_bf16 v[172:175], v[48:51], v[216:219], v[208:211]\n"
      "v_exp_f32 v147, v147\n"
      "v_mfma_f32_16x16x32_bf16 v[172:175], v[52:55], v[220:223], v[172:175]\n"
      "v_add_f32 v15, v15, v147\n"
      "v_cvt_pk_bf16_f32 v145, v146, v147\n"
      "v_mfma_f32_16x16x32_bf16 v[172:175], v[56:59], v[224:227], v[172:175]\n"
      "v_exp_f32 v148, v148\n"
      "v_mfma_f32_16x16x32_bf16 v[172:175], v[60:63], v[228:231], v[172:175]\n"
      "ds_read_b64_tr_b16 v[92:93], v5 offset:17152\n"
      "ds_read_b64_tr_b16 v[94:95], v5 offset:21248\n"
      "v_add_f32 v15, v15, v148\n"
      "v_mfma_f32_16x16x32_bf16 v[176:179], v[32:35], v[232:235], v[212:215]\n"
      "v_exp_f32 v149, v149\n"
      "v_xor_b32 v3, 0x8000, v3\n"
      "v_mfma_f32_16x16x32_bf16 v[176:179], v[36:39], v[236:239], v[176:179]\n"
      "v_add_f32 v15, v15, v149\n"
      "v_cvt_pk_bf16_f32 v146, v148, v149\n"
      "v_mfma_f32_16x16x32_bf16 v[176:179], v[40:43], v[240:243], v[176:179]\n"
      "v_exp_f32 v150, v150\n"
      "v_xor_b32 v2, 0x8000, v2\n"
      "v_mfma_f32_16x16x32_bf16 v[176:179], v[44:47], v[244:247], v[176:179]\n"
      "v_add_f32 v15, v15, v150\n"
      "s_waitcnt lgkmcnt(0)\n"
      "v_mfma_f32_16x16x32_bf16 a[0:3], v[96:99], v[64:67], a[0:3]\n"
      "v_exp_f32 v151, v151\n"
      "v_xor_b32 v1, 0x8000, v1\n"
      "v_mfma_f32_16x16x32_bf16 a[4:7], v[96:99], v[68:71], a[4:7]\n"
      "v_add_f32 v15, v15, v151\n"
      "v_cvt_pk_bf16_f32 v147, v150, v151\n"
      "v_mfma_f32_16x16x32_bf16 a[8:11], v[96:99], v[72:75], a[8:11]\n"
      "v_exp_f32 v152, v152\n"
      "v_xor_b32 v0, 0x8000, v0\n"
      "v_mfma_f32_16x16x32_bf16 a[12:15], v[96:99], v[76:79], a[12:15]\n"
      "ds_read_b128 v[32:35], v0 offset:0\n"
      "v_add_f32 v16, v16, v152\n"
      "v_mfma_f32_16x16x32_bf16 a[16:19], v[96:99], v[80:83], a[16:19]\n"
      "v_exp_f32 v153, v153\n"
      "v_mfma_f32_16x16x32_bf16 a[20:23], v[96:99], v[84:87], a[20:23]\n"
      "v_add_f32 v16, v16, v153\n"
      "v_cvt_pk_bf16_f32 v152, v152, v153\n"
      "s_xor_b32 m0, s56, 0x2000\n"
      "v_mfma_f32_16x16x32_bf16 a[24:27], v[96:99], v[88:91], a[24:27]\n"
      "buffer_load_dwordx4 v6, s[40:43], s55 offen offset:0 nt lds\n"
      "v_exp_f32 v154, v154\n"
      "v_mfma_f32_16x16x32_bf16 a[28:31], v[96:99], v[92:95], a[28:31]\n"
      "ds_read_b128 v[36:39], v1 offset:0\n"
      "v_add_f32 v16, v16, v154\n"
      "v_mfma_f32_16x16x32_bf16 a[32:35], v[104:107], v[64:67], a[32:35]\n"
      "v_exp_f32 v155, v155\n"
      "v_mfma_f32_16x16x32_bf16 a[36:39], v[104:107], v[68:71], a[36:39]\n"
      "v_add_f32 v16, v16, v155\n"
      "v_cvt_pk_bf16_f32 v153, v154, v155\n"
      "v_mfma_f32_16x16x32_bf16 a[40:43], v[104:107], v[72:75], a[40:43]\n"
      "v_exp_f32 v156, v156\n"
      "v_mfma_f32_16x16x32_bf16 a[44:47], v[104:107], v[76:79], a[44:47]\n"
      "ds_read_b128 v[40:43], v2 offset:0\n"
      "v_add_f32 v16, v16, v156\n"
      "v_mfma_f32_16x16x32_bf16 a[48:51], v[104:107], v[80:83], a[48:51]\n"
      "v_exp_f32 v157, v157\n"
      "v_mfma_f32_16x16x32_bf16 a[52:55], v[104:107], v[84:87], a[52:55]\n"
      "v_add_f32 v16, v16, v157\n"
      "v_cvt_pk_bf16_f32 v154, v156, v157\n"
      "v_mfma_f32_16x16x32_bf16 a[56:59], v[104:107], v[88:91], a[56:59]\n"
      "buffer_load_dwordx4 v7, s[40:43], s55 offen offset:1024 nt lds\n"
      "v_exp_f32 v158, v158\n"
      "v_mfma_f32_16x16x32_bf16 a[60:63], v[104:107], v[92:95], a[60:63]\n"
      "ds_read_b128 v[44:47], v3 offset:0\n"
      "v_add_f32 v16, v16, v158\n"
      "v_mfma_f32_16x16x32_bf16 a[64:67], v[112:115], v[64:67], a[64:67]\n"
      "v_exp_f32 v159, v159\n"
      "v_mfma_f32_16x16x32_bf16 a[68:71], v[112:115], v[68:71], a[68:71]\n"
      "v_add_f32 v16, v16, v159\n"
      "v_cvt_pk_bf16_f32 v155, v158, v159\n"
      "v_mfma_f32_16x16x32_bf16 a[72:75], v[112:115], v[72:75], a[72:75]\n"
      "v_exp_f32 v160, v160\n"
      "v_mfma_f32_16x16x32_bf16 a[76:79], v[112:115], v[76:79], a[76:79]\n"
      "ds_read_b128 v[48:51], v0 offset:4096\n"
      "v_add_f32 v17, v17, v160\n"
      "v_mfma_f32_16x16x32_bf16 a[80:83], v[112:115], v[80:83], a[80:83]\n"
      "v_exp_f32 v161, v161\n"
      "v_mfma_f32_16x16x32_bf16 a[84:87], v[112:115], v[84:87], a[84:87]\n"
      "v_add_f32 v17, v17, v161\n"
      "v_cvt_pk_bf16_f32 v160, v160, v161\n"
      "v_mfma_f32_16x16x32_bf16 a[88:91], v[112:115], v[88:91], a[88:91]\n"
      "buffer_load_dwordx4 v8, s[40:43], s55 offen offset:2048 nt lds\n"
      "v_exp_f32 v162, v162\n"
      "v_mfma_f32_16x16x32_bf16 a[92:95], v[112:115], v[92:95], a[92:95]\n"
      "ds_read_b128 v[52:55], v1 offset:4096\n"
      "v_add_f32 v17, v17, v162\n"
      "v_mfma_f32_16x16x32_bf16 a[96:99], v[120:123], v[64:67], a[96:99]\n"
      "v_exp_f32 v163, v163\n"
      "v_mfma_f32_16x16x32_bf16 a[100:103], v[120:123], v[68:71], a[100:103]\n"
      "v_add_f32 v17, v17, v163\n"
      "v_cvt_pk_bf16_f32 v161, v162, v163\n"
      "v_mfma_f32_16x16x32_bf16 a[104:107], v[120:123], v[72:75], a[104:107]\n"
      "v_exp_f32 v164, v164\n"
      "v_mfma_f32_16x16x32_bf16 a[108:111], v[120:123], v[76:79], a[108:111]\n"
      "ds_read_b128 v[56:59], v2 offset:4096\n"
      "v_add_f32 v17, v17, v164\n"
      "v_mfma_f32_16x16x32_bf16 a[112:115], v[120:123], v[80:83], a[112:115]\n"
      "v_exp_f32 v165, v165\n"
      "v_mfma_f32_16x16x32_bf16 a[116:119], v[120:123], v[84:87], a[116:119]\n"
      "v_add_f32 v17, v17, v165\n"
      "v_cvt_pk_bf16_f32 v162, v164, v165\n"
      "v_mfma_f32_16x16x32_bf16 a[120:123], v[120:123], v[88:91], a[120:123]\n"
      "buffer_load_dwordx4 v9, s[40:43], s55 offen offset:3072 nt lds\n"
      "v_exp_f32 v166, v166\n"
      "s_add_u32 s55, s55, 0x2000\n"
      "s_xor_b32 s56, s56, 0x8000\n"
      "v_mfma_f32_16x16x32_bf16 a[124:127], v[120:123], v[92:95], a[124:127]\n"
      "ds_read_b128 v[60:63], v3 offset:4096\n"
      "v_add_f32 v17, v17, v166\n"
      "v_mfma_f32_16x16x32_bf16 a[128:131], v[128:131], v[64:67], a[128:131]\n"
      "v_exp_f32 v167, v167\n"
      "v_mfma_f32_16x16x32_bf16 a[132:135], v[128:131], v[68:71], a[132:135]\n"
      "v_add_f32 v17, v17, v167\n"
      "v_cvt_pk_bf16_f32 v163, v166, v167\n"
      "v_mfma_f32_16x16x32_bf16 a[136:139], v[128:131], v[72:75], a[136:139]\n"
      "v_exp_f32 v168, v168\n"
      "v_mfma_f32_16x16x32_bf16 a[140:143], v[128:131], v[76:79], a[140:143]\n"
      "v_add_f32 v18, v18, v168\n"
      "v_mfma_f32_16x16x32_bf16 a[144:147], v[128:131], v[80:83], a[144:147]\n"
      "v_exp_f32 v169, v169\n"
      "v_mfma_f32_16x16x32_bf16 a[148:151], v[128:131], v[84:87], a[148:151]\n"
      "v_add_f32 v18, v18, v169\n"
      "v_cvt_pk_bf16_f32 v168, v168, v169\n"
      "v_mfma_f32_16x16x32_bf16 a[152:155], v[128:131], v[88:91], a[152:155]\n"
      "v_exp_f32 v170, v170\n"
      "v_mfma_f32_16x16x32_bf16 a[156:159], v[128:131], v[92:95], a[156:159]\n"
      "v_add_f32 v18, v18, v170\n"
      "s_waitcnt lgkmcnt(0)\n"
      "s_waitcnt vmcnt(12)\n"
      "s_barrier\n"
      "s_sub_u32 s57, s57, 1\n"
      "s_cmp_lg_u32 s57, 0\n"
      "s_cbranch_scc1 L_loop_w2\n"
      "v_exp_f32 v171, v171\n"
      "v_exp_f32 v172, v172\n"
      "v_add_f32 v18, v18, v171\n"
      "v_cvt_pk_bf16_f32 v169, v170, v171\n"
      "ds_read_b64_tr_b16 v[64:65], v4 offset:24576\n"
      "v_add_f32 v18, v18, v172\n"
      "v_exp_f32 v173, v173\n"
      "v_exp_f32 v174, v174\n"
      "v_add_f32 v18, v18, v173\n"
      "v_cvt_pk_bf16_f32 v170, v172, v173\n"
      "ds_read_b64_tr_b16 v[66:67], v4 offset:28672\n"
      "v_add_f32 v18, v18, v174\n"
      "v_exp_f32 v175, v175\n"
      "v_exp_f32 v176, v176\n"
      "v_add_f32 v18, v18, v175\n"
      "v_cvt_pk_bf16_f32 v171, v174, v175\n"
      "ds_read_b64_tr_b16 v[68:69], v5 offset:24576\n"
      "v_add_f32 v19, v19, v176\n"
      "v_exp_f32 v177, v177\n"
      "v_exp_f32 v178, v178\n"
      "v_add_f32 v19, v19, v177\n"
      "v_cvt_pk_bf16_f32 v176, v176, v177\n"
      "ds_read_b64_tr_b16 v[70:71], v5 offset:28672\n"
      "v_add_f32 v19, v19, v178\n"
      "v_exp_f32 v179, v179\n"
      "s_nop 0\n"
      "v_add_f32 v19, v19, v179\n"
      "v_cvt_pk_bf16_f32 v177, v178, v179\n"
      "ds_read_b64_tr_b16 v[72:73], v4 offset:24832\n"
      "ds_read_b64_tr_b16 v[74:75], v4 offset:28928\n"
      "ds_read_b64_tr_b16 v[76:77], v5 offset:24832\n"
      "ds_read_b64_tr_b16 v[78:79], v5 offset:28928\n"
      "ds_read_b64_tr_b16 v[80:81], v4 offset:25088\n"
      "ds_read_b64_tr_b16 v[82:83], v4 offset:29184\n"
      "ds_read_b64_tr_b16 v[84:85], v5 offset:25088\n"
      "ds_read_b64_tr_b16 v[86:87], v5 offset:29184\n"
      "ds_read_b64_tr_b16 v[88:89], v4 offset:25344\n"
      "ds_read_b64_tr_b16 v[90:91], v4 offset:29440\n"
      "ds_read_b64_tr_b16 v[92:93], v5 offset:25344\n"
      "ds_read_b64_tr_b16 v[94:95], v5 offset:29440\n"
      "v_and_b32 v22, 15, v13\n"
      "v_lshrrev_b32 v23, 4, v13\n"
      "v_lshrrev_b32 v29, 2, v22\n"
      "v_xor_b32 v29, v29, v23\n"
      "v_lshlrev_b32 v29, 3, v29\n"
      "v_lshl_or_b32 v29, v22, 5, v29\n"
      "v_add_u32 v24, s64, v29\n"
      "v_bfe_u32 v29, v13, 2, 2\n"
      "v_lshl_or_b32 v29, v23, 3, v29\n"
      "v_lshlrev_b32 v29, 5, v29\n"
      "v_add_u32 v29, s64, v29\n"
      "v_lshlrev_b32 v30, 1, v23\n"
      "v_xor_b32 v30, v30, v13\n"
      "v_and_b32 v30, 3, v30\n"
      "v_lshl_add_u32 v25, v30, 3, v29\n"
      "v_xor_b32 v30, 1, v30\n"
      "v_lshl_add_u32 v26, v30, 3, v29\n"
      "v_add_u32 v26, 128, v26\n"
      "v_lshlrev_b32 v29, 4, v23\n"
      "v_lshl_or_b32 v253, v22, 8, v29\n"
      "v_add_u32 v27, 0x21000, v29\n"
      "v_add_u32 v250, 0x20000, v29\n"
      "v_lshlrev_b32 v29, 2, v22\n"
      "v_add_u32 v251, 0x21000, v29\n"
      "v_add_u32 v248, 0x18000, v12\n"
      "v_add_u32 v249, 0x15000, v12\n"
      "v_lshlrev_b32 v21, 2, v13\n"
      "v_add_u32 v21, 0x20000, v21\n"
      "v_add_u32 v0, s64, v0\n"
      "v_add_u32 v6, s64, v6\n"
      "v_add_u32 v1, s64, v1\n"
      "v_add_u32 v7, s64, v7\n"
      "v_add_u32 v2, s64, v2\n"
      "v_add_u32 v8, s64, v8\n"
      "v_add_u32 v3, s64, v3\n"
      "v_add_u32 v9, s64, v9\n"
      "s_waitcnt vmcnt(0)\n"
      "s_barrier\n"
      "s_waitcnt lgkmcnt(0)\n"
      "v_mfma_f32_16x16x16_bf16 a[160:163], v[176:177], v[64:65], a[160:163]\n"
      "v_mov_b32 v50, v14\n"
      "v_mov_b32 v51, v15\n"
      "v_mfma_f32_16x16x16_bf16 a[164:167], v[176:177], v[68:69], a[164:167]\n"
      "v_mov_b32 v52, v16\n"
      "v_mov_b32 v53, v17\n"
      "v_mfma_f32_16x16x16_bf16 a[168:171], v[176:177], v[72:73], a[168:171]\n"
      "v_mov_b32 v54, v18\n"
      "v_mov_b32 v55, v19\n"
      "v_mfma_f32_16x16x16_bf16 a[172:175], v[176:177], v[76:77], a[172:175]\n"
      "s_nop 1\n"
      "v_mfma_f32_16x16x16_bf16 a[176:179], v[176:177], v[80:81], a[176:179]\n"
      "v_permlane16_swap_b32 v14, v50\n"
      "v_permlane16_swap_b32 v15, v51\n"
      "v_mfma_f32_16x16x16_bf16 a[180:183], v[176:177], v[84:85], a[180:183]\n"
      "v_permlane16_swap_b32 v16, v52\n"
      "v_permlane16_swap_b32 v17, v53\n"
      "v_mfma_f32_16x16x16_bf16 a[184:187], v[176:177], v[88:89], a[184:187]\n"
      "v_permlane16_swap_b32 v18, v54\n"
      "v_permlane16_swap_b32 v19, v55\n"
      "v_mfma_f32_16x16x16_bf16 a[188:191], v[176:177], v[92:93], a[188:191]\n"
      "s_nop 1\n"
      "v_mfma_f32_16x16x32_bf16 a[0:3], v[136:139], v[64:67], a[0:3]\n"
      "v_add_f32 v14, v14, v50\n"
      "v_add_f32 v15, v15, v51\n"
      "v_mfma_f32_16x16x32_bf16 a[4:7], v[136:139], v[68:71], a[4:7]\n"
      "v_add_f32 v16, v16, v52\n"
      "v_add_f32 v17, v17, v53\n"
      "v_mfma_f32_16x16x32_bf16 a[8:11], v[136:139], v[72:75], a[8:11]\n"
      "v_add_f32 v18, v18, v54\n"
      "v_add_f32 v19, v19, v55\n"
      "v_mfma_f32_16x16x32_bf16 a[12:15], v[136:139], v[76:79], a[12:15]\n"
      "v_mov_b32 v50, v14\n"
      "v_mov_b32 v51, v15\n"
      "v_mfma_f32_16x16x32_bf16 a[16:19], v[136:139], v[80:83], a[16:19]\n"
      "v_mov_b32 v52, v16\n"
      "v_mov_b32 v53, v17\n"
      "v_mfma_f32_16x16x32_bf16 a[20:23], v[136:139], v[84:87], a[20:23]\n"
      "v_mov_b32 v54, v18\n"
      "v_mov_b32 v55, v19\n"
      "v_mfma_f32_16x16x32_bf16 a[24:27], v[136:139], v[88:91], a[24:27]\n"
      "s_nop 1\n"
      "v_mfma_f32_16x16x32_bf16 a[28:31], v[136:139], v[92:95], a[28:31]\n"
      "v_permlane32_swap_b32 v14, v50\n"
      "v_permlane32_swap_b32 v15, v51\n"
      "v_mfma_f32_16x16x32_bf16 a[32:35], v[144:147], v[64:67], a[32:35]\n"
      "v_permlane32_swap_b32 v16, v52\n"
      "v_permlane32_swap_b32 v17, v53\n"
      "v_mfma_f32_16x16x32_bf16 a[36:39], v[144:147], v[68:71], a[36:39]\n"
      "v_permlane32_swap_b32 v18, v54\n"
      "v_permlane32_swap_b32 v19, v55\n"
      "v_mfma_f32_16x16x32_bf16 a[40:43], v[144:147], v[72:75], a[40:43]\n"
      "s_nop 1\n"
      "v_mfma_f32_16x16x32_bf16 a[44:47], v[144:147], v[76:79], a[44:47]\n"
      "v_add_f32 v14, v14, v50\n"
      "v_add_f32 v15, v15, v51\n"
      "v_mfma_f32_16x16x32_bf16 a[48:51], v[144:147], v[80:83], a[48:51]\n"
      "v_add_f32 v16, v16, v52\n"
      "v_add_f32 v17, v17, v53\n"
      "v_mfma_f32_16x16x32_bf16 a[52:55], v[144:147], v[84:87], a[52:55]\n"
      "v_add_f32 v18, v18, v54\n"
      "v_add_f32 v19, v19, v55\n"
      "v_mfma_f32_16x16x32_bf16 a[56:59], v[144:147], v[88:91], a[56:59]\n"
      "s_mov_b64 s[98:99], 0\n"
      "v_cmp_nge_f32 vcc, s96, v14\n"
      "v_mfma_f32_16x16x32_bf16 a[60:63], v[144:147], v[92:95], a[60:63]\n"
      "v_rcp_f32 v40, v14\n"
      "s_nop 1\n"
      "v_mfma_f32_16x16x32_bf16 a[64:67], v[152:155], v[64:67], a[64:67]\n"
      "s_or_b64 s[98:99], s[98:99], vcc\n"
      "v_cmp_nge_f32 vcc, s96, v15\n"
      "v_mfma_f32_16x16x32_bf16 a[68:71], v[152:155], v[68:71], a[68:71]\n"
      "v_rcp_f32 v41, v15\n"
      "s_nop 1\n"
      "v_mfma_f32_16x16x32_bf16 a[72:75], v[152:155], v[72:75], a[72:75]\n"
      "s_or_b64 s[98:99], s[98:99], vcc\n"
      "v_cmp_nge_f32 vcc, s96, v16\n"
      "v_mfma_f32_16x16x32_bf16 a[76:79], v[152:155], v[76:79], a[76:79]\n"
      "v_rcp_f32 v42, v16\n"
      "s_nop 1\n"
      "v_mfma_f32_16x16x32_bf16 a[80:83], v[152:155], v[80:83], a[80:83]\n"
      "s_or_b64 s[98:99], s[98:99], vcc\n"
      "v_cmp_nge_f32 vcc, s96, v17\n"
      "v_mfma_f32_16x16x32_bf16 a[84:87], v[152:155], v[84:87], a[84:87]\n"
      "v_rcp_f32 v43, v17\n"
      "s_nop 1\n"
      "v_mfma_f32_16x16x32_bf16 a[88:91], v[152:155], v[88:91], a[88:91]\n"
      "s_or_b64 s[98:99], s[98:99], vcc\n"
      "v_cmp_nge_f32 vcc, s96, v18\n"
      "v_mfma_f32_16x16x32_bf16 a[92:95], v[152:155], v[92:95], a[92:95]\n"
      "v_rcp_f32 v44, v18\n"
      "s_nop 1\n"
      "v_mfma_f32_16x16x32_bf16 a[96:99], v[160:163], v[64:67], a[96:99]\n"
      "s_or_b64 s[98:99], s[98:99], vcc\n"
      "v_cmp_nge_f32 vcc, s96, v19\n"
      "v_mfma_f32_16x16x32_bf16 a[100:103], v[160:163], v[68:71], a[100:103]\n"
      "s_nop 1\n"
      "v_mfma_f32_16x16x32_bf16 a[104:107], v[160:163], v[72:75], a[104:107]\n"
      "s_or_b64 s[98:99], s[98:99], vcc\n"
      "ds_write_b32 v251, v40 offset:0\n"
      "v_mfma_f32_16x16x32_bf16 a[108:111], v[160:163], v[76:79], a[108:111]\n"
      "ds_write_b32 v251, v41 offset:64\n"
      "ds_write_b32 v251, v42 offset:128\n"
      "v_mfma_f32_16x16x32_bf16 a[112:115], v[160:163], v[80:83], a[112:115]\n"
      "ds_write_b32 v251, v43 offset:192\n"
      "ds_write_b32 v251, v44 offset:256\n"
      "v_mfma_f32_16x16x32_bf16 a[116:119], v[160:163], v[84:87], a[116:119]\n"
      "ds_read_b128 v[196:199], v27 offset:0\n"
      "ds_read_b128 v[200:203], v27 offset:64\n"
      "v_mfma_f32_16x16x32_bf16 a[120:123], v[160:163], v[88:91], a[120:123]\n"
      "ds_read_b128 v[204:207], v27 offset:128\n"
      "ds_read_b128 v[208:211], v27 offset:192\n"
      "v_mfma_f32_16x16x32_bf16 a[124:127], v[160:163], v[92:95], a[124:127]\n"
      "ds_read_b128 v[212:215], v27 offset:256\n"
      "s_cmp_lg_u64 s[98:99], 0\n"
      "v_mfma_f32_16x16x32_bf16 a[128:131], v[168:171], v[64:67], a[128:131]\n"
      "s_cselect_b32 s59, 1, 0\n"
      "v_mov_b32 v28, s59\n"
      "v_mfma_f32_16x16x32_bf16 a[132:135], v[168:171], v[68:71], a[132:135]\n"
      "v_mov_b32 v20, 0x20808\n"
      "ds_write_b128 v248, a[160:163] offset:0\n"
      "v_mfma_f32_16x16x32_bf16 a[136:139], v[168:171], v[72:75], a[136:139]\n"
      "ds_write_b128 v248, a[164:167] offset:1024\n"
      "ds_write_b128 v248, a[168:171] offset:2048\n"
      "v_mfma_f32_16x16x32_bf16 a[140:143], v[168:171], v[76:79], a[140:143]\n"
      "ds_write_b128 v248, a[172:175] offset:3072\n"
      "ds_write_b128 v248, a[176:179] offset:4096\n"
      "v_mfma_f32_16x16x32_bf16 a[144:147], v[168:171], v[80:83], a[144:147]\n"
      "ds_write_b128 v248, a[180:183] offset:5120\n"
      "ds_write_b128 v248, a[184:187] offset:6144\n"
      "v_mfma_f32_16x16x32_bf16 a[148:151], v[168:171], v[84:87], a[148:151]\n"
      "ds_write_b128 v248, a[188:191] offset:7168\n"
      "ds_write_b32 v21, v19 offset:512\n"
      "v_mfma_f32_16x16x32_bf16 a[152:155], v[168:171], v[88:91], a[152:155]\n"
      "ds_write_b32 v20, v28 offset:0\n"
      "s_waitcnt lgkmcnt(10)\n"
      "v_mfma_f32_16x16x32_bf16 a[156:159], v[168:171], v[92:95], a[156:159]\n"
      "v_accvgpr_read_b32 v96, a0\n"
      "v_accvgpr_read_b32 v97, a1\n"
      "v_accvgpr_read_b32 v98, a2\n"
      "v_accvgpr_read_b32 v99, a3\n"
      "v_mul_f32 v96, v196, v96\n"
      "v_mul_f32 v97, v197, v97\n"
      "v_mul_f32 v98, v198, v98\n"
      "v_mul_f32 v99, v199, v99\n"
      "v_cvt_pk_bf16_f32 v96, v96, v97\n"
      "v_cvt_pk_bf16_f32 v97, v98, v99\n"
      "ds_write_b64 v24, v[96:97] offset:0\n"
      "v_accvgpr_read_b32 v100, a4\n"
      "v_accvgpr_read_b32 v101, a5\n"
      "v_accvgpr_read_b32 v102, a6\n"
      "v_accvgpr_read_b32 v103, a7\n"
      "v_mul_f32 v100, v196, v100\n"
      "v_mul_f32 v101, v197, v101\n"
      "v_mul_f32 v102, v198, v102\n"
      "v_mul_f32 v103, v199, v103\n"
      "v_cvt_pk_bf16_f32 v100, v100, v101\n"
      "v_cvt_pk_bf16_f32 v101, v102, v103\n"
      "ds_write_b64 v24, v[100:101] offset:512\n"
      "v_accvgpr_read_b32 v104, a8\n"
      "v_accvgpr_read_b32 v105, a9\n"
      "v_accvgpr_read_b32 v106, a10\n"
      "v_accvgpr_read_b32 v107, a11\n"
      "v_mul_f32 v104, v196, v104\n"
      "v_mul_f32 v105, v197, v105\n"
      "v_mul_f32 v106, v198, v106\n"
      "v_mul_f32 v107, v199, v107\n"
      "v_cvt_pk_bf16_f32 v104, v104, v105\n"
      "v_cvt_pk_bf16_f32 v105, v106, v107\n"
      "ds_write_b64 v24, v[104:105] offset:1024\n"
      "v_accvgpr_read_b32 v108, a12\n"
      "v_accvgpr_read_b32 v109, a13\n"
      "v_accvgpr_read_b32 v110, a14\n"
      "v_accvgpr_read_b32 v111, a15\n"
      "v_mul_f32 v108, v196, v108\n"
      "v_mul_f32 v109, v197, v109\n"
      "v_mul_f32 v110, v198, v110\n"
      "v_mul_f32 v111, v199, v111\n"
      "v_cvt_pk_bf16_f32 v108, v108, v109\n"
      "v_cvt_pk_bf16_f32 v109, v110, v111\n"
      "ds_write_b64 v24, v[108:109] offset:1536\n"
      "v_accvgpr_read_b32 v112, a16\n"
      "v_accvgpr_read_b32 v113, a17\n"
      "v_accvgpr_read_b32 v114, a18\n"
      "v_accvgpr_read_b32 v115, a19\n"
      "v_mul_f32 v112, v196, v112\n"
      "v_mul_f32 v113, v197, v113\n"
      "v_mul_f32 v114, v198, v114\n"
      "v_mul_f32 v115, v199, v115\n"
      "v_cvt_pk_bf16_f32 v112, v112, v113\n"
      "v_cvt_pk_bf16_f32 v113, v114, v115\n"
      "ds_write_b64 v24, v[112:113] offset:2048\n"
      "v_accvgpr_read_b32 v116, a20\n"
      "v_accvgpr_read_b32 v117, a21\n"
      "v_accvgpr_read_b32 v118, a22\n"
      "v_accvgpr_read_b32 v119, a23\n"
      "v_mul_f32 v116, v196, v116\n"
      "v_mul_f32 v117, v197, v117\n"
      "v_mul_f32 v118, v198, v118\n"
      "v_mul_f32 v119, v199, v119\n"
      "v_cvt_pk_bf16_f32 v116, v116, v117\n"
      "v_cvt_pk_bf16_f32 v117, v118, v119\n"
      "ds_write_b64 v24, v[116:117] offset:2560\n"
      "v_accvgpr_read_b32 v120, a24\n"
      "v_accvgpr_read_b32 v121, a25\n"
      "v_accvgpr_read_b32 v122, a26\n"
      "v_accvgpr_read_b32 v123, a27\n"
      "v_mul_f32 v120, v196, v120\n"
      "v_mul_f32 v121, v197, v121\n"
      "v_mul_f32 v122, v198, v122\n"
      "v_mul_f32 v123, v199, v123\n"
      "v_cvt_pk_bf16_f32 v120, v120, v121\n"
      "v_cvt_pk_bf16_f32 v121, v122, v123\n"
      "ds_write_b64 v24, v[120:121] offset:3072\n"
      "v_accvgpr_read_b32 v124, a28\n"
      "v_accvgpr_read_b32 v125, a29\n"
      "v_accvgpr_read_b32 v126, a30\n"
      "v_accvgpr_read_b32 v127, a31\n"
      "v_mul_f32 v124, v196, v124\n"
      "v_mul_f32 v125, v197, v125\n"
      "v_mul_f32 v126, v198, v126\n"
      "v_mul_f32 v127, v199, v127\n"
      "v_cvt_pk_bf16_f32 v124, v124, v125\n"
      "v_cvt_pk_bf16_f32 v125, v126, v127\n"
      "ds_write_b64 v24, v[124:125] offset:3584\n"
      "ds_read_b64_tr_b16 v[64:65], v25 offset:0\n"
      "ds_read_b64_tr_b16 v[66:67], v26 offset:0\n"
      "ds_read_b64_tr_b16 v[68:69], v25 offset:1024\n"
      "ds_read_b64_tr_b16 v[70:71], v26 offset:1024\n"
      "ds_read_b64_tr_b16 v[72:73], v25 offset:2048\n"
      "ds_read_b64_tr_b16 v[74:75], v26 offset:2048\n"
      "ds_read_b64_tr_b16 v[76:77], v25 offset:3072\n"
      "ds_read_b64_tr_b16 v[78:79], v26 offset:3072\n"
      "v_accvgpr_read_b32 v128, a32\n"
      "v_accvgpr_read_b32 v129, a33\n"
      "v_accvgpr_read_b32 v130, a34\n"
      "v_accvgpr_read_b32 v131, a35\n"
      "v_pk_mul_f32 v[128:129], v[128:129], v[200:201]\n"
      "v_pk_mul_f32 v[130:131], v[130:131], v[202:203]\n"
      "v_cvt_pk_bf16_f32 v128, v128, v129\n"
      "v_cvt_pk_bf16_f32 v129, v130, v131\n"
      "ds_write_b64 v24, v[128:129] offset:4096\n"
      "v_accvgpr_read_b32 v132, a36\n"
      "v_accvgpr_read_b32 v133, a37\n"
      "v_accvgpr_read_b32 v134, a38\n"
      "v_accvgpr_read_b32 v135, a39\n"
      "v_pk_mul_f32 v[132:133], v[132:133], v[200:201]\n"
      "v_pk_mul_f32 v[134:135], v[134:135], v[202:203]\n"
      "v_cvt_pk_bf16_f32 v132, v132, v133\n"
      "v_cvt_pk_bf16_f32 v133, v134, v135\n"
      "ds_write_b64 v24, v[132:133] offset:4608\n"
      "v_accvgpr_read_b32 v136, a40\n"
      "v_accvgpr_read_b32 v137, a41\n"
      "v_accvgpr_read_b32 v138, a42\n"
      "v_accvgpr_read_b32 v139, a43\n"
      "v_pk_mul_f32 v[136:137], v[136:137], v[200:201]\n"
      "v_pk_mul_f32 v[138:139], v[138:139], v[202:203]\n"
      "v_cvt_pk_bf16_f32 v136, v136, v137\n"
      "v_cvt_pk_bf16_f32 v137, v138, v139\n"
      "ds_write_b64 v24, v[136:137] offset:5120\n"
      "v_accvgpr_read_b32 v140, a44\n"
      "v_accvgpr_read_b32 v141, a45\n"
      "v_accvgpr_read_b32 v142, a46\n"
      "v_accvgpr_read_b32 v143, a47\n"
      "v_pk_mul_f32 v[140:141], v[140:141], v[200:201]\n"
      "v_pk_mul_f32 v[142:143], v[142:143], v[202:203]\n"
      "v_cvt_pk_bf16_f32 v140, v140, v141\n"
      "v_cvt_pk_bf16_f32 v141, v142, v143\n"
      "ds_write_b64 v24, v[140:141] offset:5632\n"
      "s_waitcnt lgkmcnt(4)\n"
      "ds_write_b128 v0, v[64:67] offset:0\n"
      "ds_write_b128 v1, v[68:71] offset:0\n"
      "ds_write_b128 v2, v[72:75] offset:0\n"
      "ds_write_b128 v3, v[76:79] offset:0\n"
      "ds_read_b128 v[64:67], v6 offset:0\n"
      "ds_read_b128 v[68:71], v7 offset:1024\n"
      "ds_read_b128 v[72:75], v8 offset:2048\n"
      "ds_read_b128 v[76:79], v9 offset:3072\n"
      "v_accvgpr_read_b32 v144, a48\n"
      "v_accvgpr_read_b32 v145, a49\n"
      "v_accvgpr_read_b32 v146, a50\n"
      "v_accvgpr_read_b32 v147, a51\n"
      "v_pk_mul_f32 v[144:145], v[144:145], v[200:201]\n"
      "v_pk_mul_f32 v[146:147], v[146:147], v[202:203]\n"
      "v_cvt_pk_bf16_f32 v144, v144, v145\n"
      "v_cvt_pk_bf16_f32 v145, v146, v147\n"
      "ds_write_b64 v24, v[144:145] offset:6144\n"
      "v_accvgpr_read_b32 v148, a52\n"
      "v_accvgpr_read_b32 v149, a53\n"
      "v_accvgpr_read_b32 v150, a54\n"
      "v_accvgpr_read_b32 v151, a55\n"
      "v_pk_mul_f32 v[148:149], v[148:149], v[200:201]\n"
      "v_pk_mul_f32 v[150:151], v[150:151], v[202:203]\n"
      "v_cvt_pk_bf16_f32 v148, v148, v149\n"
      "v_cvt_pk_bf16_f32 v149, v150, v151\n"
      "ds_write_b64 v24, v[148:149] offset:6656\n"
      "v_accvgpr_read_b32 v152, a56\n"
      "v_accvgpr_read_b32 v153, a57\n"
      "v_accvgpr_read_b32 v154, a58\n"
      "v_accvgpr_read_b32 v155, a59\n"
      "v_pk_mul_f32 v[152:153], v[152:153], v[200:201]\n"
      "v_pk_mul_f32 v[154:155], v[154:155], v[202:203]\n"
      "v_cvt_pk_bf16_f32 v152, v152, v153\n"
      "v_cvt_pk_bf16_f32 v153, v154, v155\n"
      "ds_write_b64 v24, v[152:153] offset:7168\n"
      "v_accvgpr_read_b32 v156, a60\n"
      "v_accvgpr_read_b32 v157, a61\n"
      "v_accvgpr_read_b32 v158, a62\n"
      "v_accvgpr_read_b32 v159, a63\n"
      "v_pk_mul_f32 v[156:157], v[156:157], v[200:201]\n"
      "v_pk_mul_f32 v[158:159], v[158:159], v[202:203]\n"
      "v_cvt_pk_bf16_f32 v156, v156, v157\n"
      "v_cvt_pk_bf16_f32 v157, v158, v159\n"
      "ds_write_b64 v24, v[156:157] offset:7680\n"
      "s_waitcnt lgkmcnt(4)\n"
      "s_add_u32 s58, s62, 0\n"
      "buffer_store_dwordx4 v[64:67], v12, s[48:51], s58 offen offset:0 nt\n"
      "buffer_store_dwordx4 v[68:71], v12, s[48:51], s58 offen offset:1024 nt\n"
      "buffer_store_dwordx4 v[72:75], v12, s[48:51], s58 offen offset:2048 nt\n"
      "buffer_store_dwordx4 v[76:79], v12, s[48:51], s58 offen offset:3072 nt\n"
      "v_mov_b32 v20, 0x20800\n"
      "ds_read_b64_tr_b16 v[80:81], v25 offset:4096\n"
      "ds_read_b64_tr_b16 v[82:83], v26 offset:4096\n"
      "ds_read_b64_tr_b16 v[84:85], v25 offset:5120\n"
      "ds_read_b64_tr_b16 v[86:87], v26 offset:5120\n"
      "ds_read_b64_tr_b16 v[88:89], v25 offset:6144\n"
      "ds_read_b64_tr_b16 v[90:91], v26 offset:6144\n"
      "ds_read_b64_tr_b16 v[92:93], v25 offset:7168\n"
      "ds_read_b64_tr_b16 v[94:95], v26 offset:7168\n"
      "v_accvgpr_read_b32 v96, a64\n"
      "v_accvgpr_read_b32 v97, a65\n"
      "v_accvgpr_read_b32 v98, a66\n"
      "v_accvgpr_read_b32 v99, a67\n"
      "v_pk_mul_f32 v[96:97], v[96:97], v[204:205]\n"
      "v_pk_mul_f32 v[98:99], v[98:99], v[206:207]\n"
      "v_cvt_pk_bf16_f32 v96, v96, v97\n"
      "v_cvt_pk_bf16_f32 v97, v98, v99\n"
      "ds_write_b64 v24, v[96:97] offset:8192\n"
      "v_accvgpr_read_b32 v100, a68\n"
      "v_accvgpr_read_b32 v101, a69\n"
      "v_accvgpr_read_b32 v102, a70\n"
      "v_accvgpr_read_b32 v103, a71\n"
      "v_pk_mul_f32 v[100:101], v[100:101], v[204:205]\n"
      "v_pk_mul_f32 v[102:103], v[102:103], v[206:207]\n"
      "v_cvt_pk_bf16_f32 v100, v100, v101\n"
      "v_cvt_pk_bf16_f32 v101, v102, v103\n"
      "ds_write_b64 v24, v[100:101] offset:8704\n"
      "v_accvgpr_read_b32 v104, a72\n"
      "v_accvgpr_read_b32 v105, a73\n"
      "v_accvgpr_read_b32 v106, a74\n"
      "v_accvgpr_read_b32 v107, a75\n"
      "v_pk_mul_f32 v[104:105], v[104:105], v[204:205]\n"
      "v_pk_mul_f32 v[106:107], v[106:107], v[206:207]\n"
      "v_cvt_pk_bf16_f32 v104, v104, v105\n"
      "v_cvt_pk_bf16_f32 v105, v106, v107\n"
      "ds_write_b64 v24, v[104:105] offset:9216\n"
      "v_accvgpr_read_b32 v108, a76\n"
      "v_accvgpr_read_b32 v109, a77\n"
      "v_accvgpr_read_b32 v110, a78\n"
      "v_accvgpr_read_b32 v111, a79\n"
      "v_pk_mul_f32 v[108:109], v[108:109], v[204:205]\n"
      "v_pk_mul_f32 v[110:111], v[110:111], v[206:207]\n"
      "v_cvt_pk_bf16_f32 v108, v108, v109\n"
      "v_cvt_pk_bf16_f32 v109, v110, v111\n"
      "ds_write_b64 v24, v[108:109] offset:9728\n"
      "s_waitcnt lgkmcnt(4)\n"
      "ds_write_b128 v0, v[80:83] offset:4096\n"
      "ds_write_b128 v1, v[84:87] offset:4096\n"
      "ds_write_b128 v2, v[88:91] offset:4096\n"
      "ds_write_b128 v3, v[92:95] offset:4096\n"
      "ds_read_b128 v[80:83], v6 offset:4096\n"
      "ds_read_b128 v[84:87], v7 offset:5120\n"
      "ds_read_b128 v[88:91], v8 offset:6144\n"
      "ds_read_b128 v[92:95], v9 offset:7168\n"
      "v_accvgpr_read_b32 v112, a80\n"
      "v_accvgpr_read_b32 v113, a81\n"
      "v_accvgpr_read_b32 v114, a82\n"
      "v_accvgpr_read_b32 v115, a83\n"
      "v_pk_mul_f32 v[112:113], v[112:113], v[204:205]\n"
      "v_pk_mul_f32 v[114:115], v[114:115], v[206:207]\n"
      "v_cvt_pk_bf16_f32 v112, v112, v113\n"
      "v_cvt_pk_bf16_f32 v113, v114, v115\n"
      "ds_write_b64 v24, v[112:113] offset:10240\n"
      "v_accvgpr_read_b32 v116, a84\n"
      "v_accvgpr_read_b32 v117, a85\n"
      "v_accvgpr_read_b32 v118, a86\n"
      "v_accvgpr_read_b32 v119, a87\n"
      "v_pk_mul_f32 v[116:117], v[116:117], v[204:205]\n"
      "v_pk_mul_f32 v[118:119], v[118:119], v[206:207]\n"
      "v_cvt_pk_bf16_f32 v116, v116, v117\n"
      "v_cvt_pk_bf16_f32 v117, v118, v119\n"
      "ds_write_b64 v24, v[116:117] offset:10752\n"
      "v_accvgpr_read_b32 v120, a88\n"
      "v_accvgpr_read_b32 v121, a89\n"
      "v_accvgpr_read_b32 v122, a90\n"
      "v_accvgpr_read_b32 v123, a91\n"
      "v_pk_mul_f32 v[120:121], v[120:121], v[204:205]\n"
      "v_pk_mul_f32 v[122:123], v[122:123], v[206:207]\n"
      "v_cvt_pk_bf16_f32 v120, v120, v121\n"
      "v_cvt_pk_bf16_f32 v121, v122, v123\n"
      "ds_write_b64 v24, v[120:121] offset:11264\n"
      "v_accvgpr_read_b32 v124, a92\n"
      "v_accvgpr_read_b32 v125, a93\n"
      "v_accvgpr_read_b32 v126, a94\n"
      "v_accvgpr_read_b32 v127, a95\n"
      "v_pk_mul_f32 v[124:125], v[124:125], v[204:205]\n"
      "v_pk_mul_f32 v[126:127], v[126:127], v[206:207]\n"
      "v_cvt_pk_bf16_f32 v124, v124, v125\n"
      "v_cvt_pk_bf16_f32 v125, v126, v127\n"
      "ds_write_b64 v24, v[124:125] offset:11776\n"
      "s_waitcnt lgkmcnt(4)\n"
      "s_add_u32 s58, s62, 4096\n"
      "buffer_store_dwordx4 v[80:83], v12, s[48:51], s58 offen offset:0 nt\n"
      "buffer_store_dwordx4 v[84:87], v12, s[48:51], s58 offen offset:1024 nt\n"
      "buffer_store_dwordx4 v[88:91], v12, s[48:51], s58 offen offset:2048 nt\n"
      "buffer_store_dwordx4 v[92:95], v12, s[48:51], s58 offen offset:3072 nt\n"
      "s_barrier\n"
      "ds_read_b64_tr_b16 v[64:65], v25 offset:8192\n"
      "ds_read_b64_tr_b16 v[66:67], v26 offset:8192\n"
      "ds_read_b64_tr_b16 v[68:69], v25 offset:9216\n"
      "ds_read_b64_tr_b16 v[70:71], v26 offset:9216\n"
      "ds_read_b64_tr_b16 v[72:73], v25 offset:10240\n"
      "ds_read_b64_tr_b16 v[74:75], v26 offset:10240\n"
      "ds_read_b64_tr_b16 v[76:77], v25 offset:11264\n"
      "ds_read_b64_tr_b16 v[78:79], v26 offset:11264\n"
      "ds_read_b128 v[160:163], v249 offset:0\n"
      "ds_read_b128 v[164:167], v249 offset:1024\n"
      "ds_read_b128 v[168:171], v249 offset:8192\n"
      "ds_read_b128 v[172:175], v249 offset:9216\n"
      "ds_read_b128 v[176:179], v249 offset:16384\n"
      "ds_read_b128 v[180:183], v249 offset:17408\n"
      "ds_read_b128 v[184:187], v249 offset:24576\n"
      "ds_read_b128 v[188:191], v249 offset:25600\n"
      "ds_read_b128 v[32:35], v250 offset:0\n"
      "ds_read_b128 v[36:39], v250 offset:256\n"
      "ds_read_b128 v[40:43], v250 offset:512\n"
      "ds_read_b128 v[44:47], v250 offset:768\n"
      "ds_read_b128 v[48:51], v20 offset:0\n"
      "v_accvgpr_read_b32 v128, a96\n"
      "v_accvgpr_read_b32 v129, a97\n"
      "v_accvgpr_read_b32 v130, a98\n"
      "v_accvgpr_read_b32 v131, a99\n"
      "v_pk_mul_f32 v[128:129], v[128:129], v[208:209]\n"
      "v_pk_mul_f32 v[130:131], v[130:131], v[210:211]\n"
      "v_cvt_pk_bf16_f32 v128, v128, v129\n"
      "v_cvt_pk_bf16_f32 v129, v130, v131\n"
      "ds_write_b64 v24, v[128:129] offset:12288\n"
      "v_accvgpr_read_b32 v132, a100\n"
      "v_accvgpr_read_b32 v133, a101\n"
      "v_accvgpr_read_b32 v134, a102\n"
      "v_accvgpr_read_b32 v135, a103\n"
      "v_pk_mul_f32 v[132:133], v[132:133], v[208:209]\n"
      "v_pk_mul_f32 v[134:135], v[134:135], v[210:211]\n"
      "v_cvt_pk_bf16_f32 v132, v132, v133\n"
      "v_cvt_pk_bf16_f32 v133, v134, v135\n"
      "ds_write_b64 v24, v[132:133] offset:12800\n"
      "v_accvgpr_read_b32 v136, a104\n"
      "v_accvgpr_read_b32 v137, a105\n"
      "v_accvgpr_read_b32 v138, a106\n"
      "v_accvgpr_read_b32 v139, a107\n"
      "v_pk_mul_f32 v[136:137], v[136:137], v[208:209]\n"
      "v_pk_mul_f32 v[138:139], v[138:139], v[210:211]\n"
      "v_cvt_pk_bf16_f32 v136, v136, v137\n"
      "v_cvt_pk_bf16_f32 v137, v138, v139\n"
      "ds_write_b64 v24, v[136:137] offset:13312\n"
      "v_accvgpr_read_b32 v140, a108\n"
      "v_accvgpr_read_b32 v141, a109\n"
      "v_accvgpr_read_b32 v142, a110\n"
      "v_accvgpr_read_b32 v143, a111\n"
      "v_pk_mul_f32 v[140:141], v[140:141], v[208:209]\n"
      "v_pk_mul_f32 v[142:143], v[142:143], v[210:211]\n"
      "v_cvt_pk_bf16_f32 v140, v140, v141\n"
      "v_cvt_pk_bf16_f32 v141, v142, v143\n"
      "ds_write_b64 v24, v[140:141] offset:13824\n"
      "s_waitcnt lgkmcnt(15)\n"
      "ds_write_b128 v0, v[64:67] offset:8192\n"
      "ds_write_b128 v1, v[68:71] offset:8192\n"
      "ds_write_b128 v2, v[72:75] offset:8192\n"
      "ds_write_b128 v3, v[76:79] offset:8192\n"
      "ds_read_b128 v[64:67], v6 offset:8192\n"
      "ds_read_b128 v[68:71], v7 offset:9216\n"
      "ds_read_b128 v[72:75], v8 offset:10240\n"
      "ds_read_b128 v[76:79], v9 offset:11264\n"
      "v_accvgpr_read_b32 v144, a112\n"
      "v_accvgpr_read_b32 v145, a113\n"
      "v_accvgpr_read_b32 v146, a114\n"
      "v_accvgpr_read_b32 v147, a115\n"
      "v_pk_mul_f32 v[144:145], v[144:145], v[208:209]\n"
      "v_pk_mul_f32 v[146:147], v[146:147], v[210:211]\n"
      "v_cvt_pk_bf16_f32 v144, v144, v145\n"
      "v_cvt_pk_bf16_f32 v145, v146, v147\n"
      "ds_write_b64 v24, v[144:145] offset:14336\n"
      "v_accvgpr_read_b32 v148, a116\n"
      "v_accvgpr_read_b32 v149, a117\n"
      "v_accvgpr_read_b32 v150, a118\n"
      "v_accvgpr_read_b32 v151, a119\n"
      "v_pk_mul_f32 v[148:149], v[148:149], v[208:209]\n"
      "v_pk_mul_f32 v[150:151], v[150:151], v[210:211]\n"
      "v_cvt_pk_bf16_f32 v148, v148, v149\n"
      "v_cvt_pk_bf16_f32 v149, v150, v151\n"
      "ds_write_b64 v24, v[148:149] offset:14848\n"
      "v_accvgpr_read_b32 v152, a120\n"
      "v_accvgpr_read_b32 v153, a121\n"
      "v_accvgpr_read_b32 v154, a122\n"
      "v_accvgpr_read_b32 v155, a123\n"
      "v_pk_mul_f32 v[152:153], v[152:153], v[208:209]\n"
      "v_pk_mul_f32 v[154:155], v[154:155], v[210:211]\n"
      "v_cvt_pk_bf16_f32 v152, v152, v153\n"
      "v_cvt_pk_bf16_f32 v153, v154, v155\n"
      "ds_write_b64 v24, v[152:153] offset:15360\n"
      "v_accvgpr_read_b32 v156, a124\n"
      "v_accvgpr_read_b32 v157, a125\n"
      "v_accvgpr_read_b32 v158, a126\n"
      "v_accvgpr_read_b32 v159, a127\n"
      "v_pk_mul_f32 v[156:157], v[156:157], v[208:209]\n"
      "v_pk_mul_f32 v[158:159], v[158:159], v[210:211]\n"
      "v_cvt_pk_bf16_f32 v156, v156, v157\n"
      "v_cvt_pk_bf16_f32 v157, v158, v159\n"
      "ds_write_b64 v24, v[156:157] offset:15872\n"
      "s_waitcnt lgkmcnt(4)\n"
      "s_add_u32 s58, s62, 8192\n"
      "buffer_store_dwordx4 v[64:67], v12, s[48:51], s58 offen offset:0 nt\n"
      "buffer_store_dwordx4 v[68:71], v12, s[48:51], s58 offen offset:1024 nt\n"
      "buffer_store_dwordx4 v[72:75], v12, s[48:51], s58 offen offset:2048 nt\n"
      "buffer_store_dwordx4 v[76:79], v12, s[48:51], s58 offen offset:3072 nt\n"
      "v_add_f32 v32, v32, v36\n"
      "v_add_f32 v40, v40, v44\n"
      "v_add_f32 v33, v33, v37\n"
      "v_add_f32 v41, v41, v45\n"
      "v_add_f32 v34, v34, v38\n"
      "v_add_f32 v42, v42, v46\n"
      "v_add_f32 v35, v35, v39\n"
      "v_add_f32 v43, v43, v47\n"
      "v_add_f32 v160, v160, v168\n"
      "v_add_f32 v176, v176, v184\n"
      "v_add_f32 v161, v161, v169\n"
      "v_add_f32 v177, v177, v185\n"
      "v_add_f32 v162, v162, v170\n"
      "v_add_f32 v178, v178, v186\n"
      "v_add_f32 v163, v163, v171\n"
      "v_add_f32 v179, v179, v187\n"
      "v_add_f32 v164, v164, v172\n"
      "v_add_f32 v180, v180, v188\n"
      "v_add_f32 v165, v165, v173\n"
      "v_add_f32 v181, v181, v189\n"
      "v_add_f32 v166, v166, v174\n"
      "v_add_f32 v182, v182, v190\n"
      "v_add_f32 v167, v167, v175\n"
      "v_add_f32 v183, v183, v191\n"
      "v_add_f32 v32, v32, v40\n"
      "v_add_f32 v33, v33, v41\n"
      "v_add_f32 v34, v34, v42\n"
      "v_add_f32 v35, v35, v43\n"
      "v_add_f32 v160, v160, v176\n"
      "v_add_f32 v161, v161, v177\n"
      "v_add_f32 v162, v162, v178\n"
      "v_add_f32 v163, v163, v179\n"
      "v_add_f32 v164, v164, v180\n"
      "v_add_f32 v165, v165, v181\n"
      "v_add_f32 v166, v166, v182\n"
      "v_add_f32 v167, v167, v183\n"
      "v_rcp_f32 v52, v32\n"
      "v_rcp_f32 v53, v33\n"
      "v_rcp_f32 v54, v34\n"
      "v_rcp_f32 v55, v35\n"
      "v_or3_b32 v48, v48, v49, v50\n"
      "v_or_b32 v48, v48, v51\n"
      "v_mul_f32 v160, v52, v160\n"
      "v_mul_f32 v161, v53, v161\n"
      "v_mul_f32 v162, v54, v162\n"
      "v_mul_f32 v163, v55, v163\n"
      "v_mul_f32 v164, v52, v164\n"
      "v_mul_f32 v165, v53, v165\n"
      "v_mul_f32 v166, v54, v166\n"
      "v_mul_f32 v167, v55, v167\n"
      "v_cvt_pk_bf16_f32 v56, v160, v161\n"
      "v_cvt_pk_bf16_f32 v57, v162, v163\n"
      "v_cvt_pk_bf16_f32 v58, v164, v165\n"
      "v_cvt_pk_bf16_f32 v59, v166, v167\n"
      "ds_write_b64 v24, v[56:57] offset:0\n"
      "ds_write_b64 v24, v[58:59] offset:512\n"
      "ds_read_b64_tr_b16 v[60:61], v25 offset:0\n"
      "ds_read_b64_tr_b16 v[62:63], v26 offset:0\n"
      "v_readfirstlane_b32 s60, v48\n"
      "s_mov_b32 s59, 0x14080\n"
      "s_waitcnt lgkmcnt(0)\n"
      "buffer_store_dwordx4 v[60:63], v253, s[48:51], s59 offen offset:0 nt\n"
      "ds_read_b64_tr_b16 v[80:81], v25 offset:12288\n"
      "ds_read_b64_tr_b16 v[82:83], v26 offset:12288\n"
      "ds_read_b64_tr_b16 v[84:85], v25 offset:13312\n"
      "ds_read_b64_tr_b16 v[86:87], v26 offset:13312\n"
      "ds_read_b64_tr_b16 v[88:89], v25 offset:14336\n"
      "ds_read_b64_tr_b16 v[90:91], v26 offset:14336\n"
      "ds_read_b64_tr_b16 v[92:93], v25 offset:15360\n"
      "ds_read_b64_tr_b16 v[94:95], v26 offset:15360\n"
      "v_accvgpr_read_b32 v96, a128\n"
      "v_accvgpr_read_b32 v97, a129\n"
      "v_accvgpr_read_b32 v98, a130\n"
      "v_accvgpr_read_b32 v99, a131\n"
      "v_pk_mul_f32 v[96:97], v[96:97], v[212:213]\n"
      "v_pk_mul_f32 v[98:99], v[98:99], v[214:215]\n"
      "v_cvt_pk_bf16_f32 v96, v96, v97\n"
      "v_cvt_pk_bf16_f32 v97, v98, v99\n"
      "ds_write_b64 v24, v[96:97] offset:16384\n"
      "v_accvgpr_read_b32 v100, a132\n"
      "v_accvgpr_read_b32 v101, a133\n"
      "v_accvgpr_read_b32 v102, a134\n"
      "v_accvgpr_read_b32 v103, a135\n"
      "v_pk_mul_f32 v[100:101], v[100:101], v[212:213]\n"
      "v_pk_mul_f32 v[102:103], v[102:103], v[214:215]\n"
      "v_cvt_pk_bf16_f32 v100, v100, v101\n"
      "v_cvt_pk_bf16_f32 v101, v102, v103\n"
      "ds_write_b64 v24, v[100:101] offset:16896\n"
      "v_accvgpr_read_b32 v104, a136\n"
      "v_accvgpr_read_b32 v105, a137\n"
      "v_accvgpr_read_b32 v106, a138\n"
      "v_accvgpr_read_b32 v107, a139\n"
      "v_pk_mul_f32 v[104:105], v[104:105], v[212:213]\n"
      "v_pk_mul_f32 v[106:107], v[106:107], v[214:215]\n"
      "v_cvt_pk_bf16_f32 v104, v104, v105\n"
      "v_cvt_pk_bf16_f32 v105, v106, v107\n"
      "ds_write_b64 v24, v[104:105] offset:17408\n"
      "v_accvgpr_read_b32 v108, a140\n"
      "v_accvgpr_read_b32 v109, a141\n"
      "v_accvgpr_read_b32 v110, a142\n"
      "v_accvgpr_read_b32 v111, a143\n"
      "v_pk_mul_f32 v[108:109], v[108:109], v[212:213]\n"
      "v_pk_mul_f32 v[110:111], v[110:111], v[214:215]\n"
      "v_cvt_pk_bf16_f32 v108, v108, v109\n"
      "v_cvt_pk_bf16_f32 v109, v110, v111\n"
      "ds_write_b64 v24, v[108:109] offset:17920\n"
      "s_waitcnt lgkmcnt(4)\n"
      "ds_write_b128 v0, v[80:83] offset:12288\n"
      "ds_write_b128 v1, v[84:87] offset:12288\n"
      "ds_write_b128 v2, v[88:91] offset:12288\n"
      "ds_write_b128 v3, v[92:95] offset:12288\n"
      "ds_read_b128 v[80:83], v6 offset:12288\n"
      "ds_read_b128 v[84:87], v7 offset:13312\n"
      "ds_read_b128 v[88:91], v8 offset:14336\n"
      "ds_read_b128 v[92:95], v9 offset:15360\n"
      "v_accvgpr_read_b32 v112, a144\n"
      "v_accvgpr_read_b32 v113, a145\n"
      "v_accvgpr_read_b32 v114, a146\n"
      "v_accvgpr_read_b32 v115, a147\n"
      "v_pk_mul_f32 v[112:113], v[112:113], v[212:213]\n"
      "v_pk_mul_f32 v[114:115], v[114:115], v[214:215]\n"
      "v_cvt_pk_bf16_f32 v112, v112, v113\n"
      "v_cvt_pk_bf16_f32 v113, v114, v115\n"
      "ds_write_b64 v24, v[112:113] offset:18432\n"
      "v_accvgpr_read_b32 v116, a148\n"
      "v_accvgpr_read_b32 v117, a149\n"
      "v_accvgpr_read_b32 v118, a150\n"
      "v_accvgpr_read_b32 v119, a151\n"
      "v_pk_mul_f32 v[116:117], v[116:117], v[212:213]\n"
      "v_pk_mul_f32 v[118:119], v[118:119], v[214:215]\n"
      "v_cvt_pk_bf16_f32 v116, v116, v117\n"
      "v_cvt_pk_bf16_f32 v117, v118, v119\n"
      "ds_write_b64 v24, v[116:117] offset:18944\n"
      "v_accvgpr_read_b32 v120, a152\n"
      "v_accvgpr_read_b32 v121, a153\n"
      "v_accvgpr_read_b32 v122, a154\n"
      "v_accvgpr_read_b32 v123, a155\n"
      "v_pk_mul_f32 v[120:121], v[120:121], v[212:213]\n"
      "v_pk_mul_f32 v[122:123], v[122:123], v[214:215]\n"
      "v_cvt_pk_bf16_f32 v120, v120, v121\n"
      "v_cvt_pk_bf16_f32 v121, v122, v123\n"
      "ds_write_b64 v24, v[120:121] offset:19456\n"
      "v_accvgpr_read_b32 v124, a156\n"
      "v_accvgpr_read_b32 v125, a157\n"
      "v_accvgpr_read_b32 v126, a158\n"
      "v_accvgpr_read_b32 v127, a159\n"
      "v_pk_mul_f32 v[124:125], v[124:125], v[212:213]\n"
      "v_pk_mul_f32 v[126:127], v[126:127], v[214:215]\n"
      "v_cvt_pk_bf16_f32 v124, v124, v125\n"
      "v_cvt_pk_bf16_f32 v125, v126, v127\n"
      "ds_write_b64 v24, v[124:125] offset:19968\n"
      "s_waitcnt lgkmcnt(4)\n"
      "s_add_u32 s58, s62, 12288\n"
      "buffer_store_dwordx4 v[80:83], v12, s[48:51], s58 offen offset:0 nt\n"
      "buffer_store_dwordx4 v[84:87], v12, s[48:51], s58 offen offset:1024 nt\n"
      "buffer_store_dwordx4 v[88:91], v12, s[48:51], s58 offen offset:2048 nt\n"
      "buffer_store_dwordx4 v[92:95], v12, s[48:51], s58 offen offset:3072 nt\n"
      "ds_read_b64_tr_b16 v[64:65], v25 offset:16384\n"
      "ds_read_b64_tr_b16 v[66:67], v26 offset:16384\n"
      "ds_read_b64_tr_b16 v[68:69], v25 offset:17408\n"
      "ds_read_b64_tr_b16 v[70:71], v26 offset:17408\n"
      "ds_read_b64_tr_b16 v[72:73], v25 offset:18432\n"
      "ds_read_b64_tr_b16 v[74:75], v26 offset:18432\n"
      "ds_read_b64_tr_b16 v[76:77], v25 offset:19456\n"
      "ds_read_b64_tr_b16 v[78:79], v26 offset:19456\n"
      "s_waitcnt lgkmcnt(0)\n"
      "s_add_u32 s58, s62, 16384\n"
      "ds_write_b128 v0, v[64:67] offset:16384\n"
      "ds_write_b128 v1, v[68:71] offset:16384\n"
      "ds_write_b128 v2, v[72:75] offset:16384\n"
      "ds_write_b128 v3, v[76:79] offset:16384\n"
      "ds_read_b128 v[64:67], v6 offset:16384\n"
      "ds_read_b128 v[68:71], v7 offset:17408\n"
      "ds_read_b128 v[72:75], v8 offset:18432\n"
      "ds_read_b128 v[76:79], v9 offset:19456\n"
      "s_waitcnt lgkmcnt(0)\n"
      "buffer_store_dwordx4 v[64:67], v12, s[48:51], s58 offen offset:0 nt\n"
      "buffer_store_dwordx4 v[68:71], v12, s[48:51], s58 offen offset:1024 nt\n"
      "buffer_store_dwordx4 v[72:75], v12, s[48:51], s58 offen offset:2048 nt\n"
      "buffer_store_dwordx4 v[76:79], v12, s[48:51], s58 offen offset:3072 nt\n"
      "s_andn2_b32 s60, s60, s65\n"
      "s_cmp_lg_u32 s60, 0\n"
      "s_cbranch_scc1 L_fallback_w2\n"
      "s_branch L_end\n"
      "L_fallback_w2:\n"
      "s_mov_b32 s65, 1\n"
      "s_waitcnt vmcnt(0) lgkmcnt(0)\n"
      "s_barrier\n"
      "v_subrev_u32 v0, s64, v0\n"
      "v_subrev_u32 v6, s64, v6\n"
      "v_subrev_u32 v1, s64, v1\n"
      "v_subrev_u32 v7, s64, v7\n"
      "v_subrev_u32 v2, s64, v2\n"
      "v_subrev_u32 v8, s64, v8\n"
      "v_subrev_u32 v3, s64, v3\n"
      "v_subrev_u32 v9, s64, v9\n"
      "v_and_b32 v20, 15, v13\n"
      "v_lshrrev_b32 v21, 4, v13\n"
      "v_lshlrev_b32 v22, 4, v21\n"
      "v_lshl_or_b32 v22, v20, 8, v22\n"
      "v_mov_b32 v64, 0xff800000\n"
      "v_mov_b32 v65, 0xff800000\n"
      "v_mov_b32 v66, 0xff800000\n"
      "v_mov_b32 v67, 0xff800000\n"
      "v_mov_b32 v68, 0xff800000\n"
      "v_mov_b32 v69, 0xff800000\n"
      "s_mov_b32 s60, 0\n"
      "L_fb_loop_w2:\n"
      "s_add_u32 s61, s60, 4096\n"
      "buffer_load_dwordx4 v[32:35], v22, s[40:43], s60 offen offset:0\n"
      "buffer_load_dwordx4 v[36:39], v22, s[40:43], s60 offen offset:64\n"
      "buffer_load_dwordx4 v[40:43], v22, s[40:43], s60 offen offset:128\n"
      "buffer_load_dwordx4 v[44:47], v22, s[40:43], s60 offen offset:192\n"
      "buffer_load_dwordx4 v[48:51], v22, s[40:43], s61 offen offset:0\n"
      "buffer_load_dwordx4 v[52:55], v22, s[40:43], s61 offen offset:64\n"
      "buffer_load_dwordx4 v[56:59], v22, s[40:43], s61 offen offset:128\n"
      "buffer_load_dwordx4 v[60:63], v22, s[40:43], s61 offen offset:192\n"
      "s_waitcnt vmcnt(0)\n"
      "v_mfma_f32_16x16x32_bf16 v[96:99], v[32:35], a[192:195], 0\n"
      "v_mfma_f32_16x16x32_bf16 v[96:99], v[36:39], a[196:199], v[96:99]\n"
      "v_mfma_f32_16x16x32_bf16 v[96:99], v[40:43], a[200:203], v[96:99]\n"
      "v_mfma_f32_16x16x32_bf16 v[96:99], v[44:47], a[204:207], v[96:99]\n"
      "v_mfma_f32_16x16x32_bf16 v[100:103], v[48:51], a[192:195], 0\n"
      "v_mfma_f32_16x16x32_bf16 v[100:103], v[52:55], a[196:199], v[100:103]\n"
      "v_mfma_f32_16x16x32_bf16 v[100:103], v[56:59], a[200:203], v[100:103]\n"
      "v_mfma_f32_16x16x32_bf16 v[100:103], v[60:63], a[204:207], v[100:103]\n"
      "s_nop 7\n"
      "v_max3_f32 v64, v64, v96, v97\n"
      "v_max3_f32 v64, v64, v98, v99\n"
      "v_max3_f32 v64, v64, v100, v101\n"
      "v_max3_f32 v64, v64, v102, v103\n"
      "v_mfma_f32_16x16x32_bf16 v[96:99], v[32:35], a[208:211], 0\n"
      "v_mfma_f32_16x16x32_bf16 v[96:99], v[36:39], a[212:215], v[96:99]\n"
      "v_mfma_f32_16x16x32_bf16 v[96:99], v[40:43], a[216:219], v[96:99]\n"
      "v_mfma_f32_16x16x32_bf16 v[96:99], v[44:47], a[220:223], v[96:99]\n"
      "v_mfma_f32_16x16x32_bf16 v[100:103], v[48:51], a[208:211], 0\n"
      "v_mfma_f32_16x16x32_bf16 v[100:103], v[52:55], a[212:215], v[100:103]\n"
      "v_mfma_f32_16x16x32_bf16 v[100:103], v[56:59], a[216:219], v[100:103]\n"
      "v_mfma_f32_16x16x32_bf16 v[100:103], v[60:63], a[220:223], v[100:103]\n"
      "s_nop 7\n"
      "v_max3_f32 v65, v65, v96, v97\n"
      "v_max3_f32 v65, v65, v98, v99\n"
      "v_max3_f32 v65, v65, v100, v101\n"
      "v_max3_f32 v65, v65, v102, v103\n"
      "v_mfma_f32_16x16x32_bf16 v[96:99], v[32:35], a[224:227], 0\n"
      "v_mfma_f32_16x16x32_bf16 v[96:99], v[36:39], a[228:231], v[96:99]\n"
      "v_mfma_f32_16x16x32_bf16 v[96:99], v[40:43], a[232:235], v[96:99]\n"
      "v_mfma_f32_16x16x32_bf16 v[96:99], v[44:47], a[236:239], v[96:99]\n"
      "v_mfma_f32_16x16x32_bf16 v[100:103], v[48:51], a[224:227], 0\n"
      "v_mfma_f32_16x16x32_bf16 v[100:103], v[52:55], a[228:231], v[100:103]\n"
      "v_mfma_f32_16x16x32_bf16 v[100:103], v[56:59], a[232:235], v[100:103]\n"
      "v_mfma_f32_16x16x32_bf16 v[100:103], v[60:63], a[236:239], v[100:103]\n"
      "s_nop 7\n"
      "v_max3_f32 v66, v66, v96, v97\n"
      "v_max3_f32 v66, v66, v98, v99\n"
      "v_max3_f32 v66, v66, v100, v101\n"
      "v_max3_f32 v66, v66, v102, v103\n"
      "v_mfma_f32_16x16x32_bf16 v[96:99], v[32:35], a[240:243], 0\n"
      "v_mfma_f32_16x16x32_bf16 v[96:99], v[36:39], a[244:247], v[96:99]\n"
      "v_mfma_f32_16x16x32_bf16 v[96:99], v[40:43], a[248:251], v[96:99]\n"
      "v_mfma_f32_16x16x32_bf16 v[96:99], v[44:47], a[252:255], v[96:99]\n"
      "v_mfma_f32_16x16x32_bf16 v[100:103], v[48:51], a[240:243], 0\n"
      "v_mfma_f32_16x16x32_bf16 v[100:103], v[52:55], a[244:247], v[100:103]\n"
      "v_mfma_f32_16x16x32_bf16 v[100:103], v[56:59], a[248:251], v[100:103]\n"
      "v_mfma_f32_16x16x32_bf16 v[100:103], v[60:63], a[252:255], v[100:103]\n"
      "s_nop 7\n"
      "v_max3_f32 v67, v67, v96, v97\n"
      "v_max3_f32 v67, v67, v98, v99\n"
      "v_max3_f32 v67, v67, v100, v101\n"
      "v_max3_f32 v67, v67, v102, v103\n"
      "v_mfma_f32_16x16x32_bf16 v[96:99], v[32:35], v[216:219], 0\n"
      "v_mfma_f32_16x16x32_bf16 v[96:99], v[36:39], v[220:223], v[96:99]\n"
      "v_mfma_f32_16x16x32_bf16 v[96:99], v[40:43], v[224:227], v[96:99]\n"
      "v_mfma_f32_16x16x32_bf16 v[96:99], v[44:47], v[228:231], v[96:99]\n"
      "v_mfma_f32_16x16x32_bf16 v[100:103], v[48:51], v[216:219], 0\n"
      "v_mfma_f32_16x16x32_bf16 v[100:103], v[52:55], v[220:223], v[100:103]\n"
      "v_mfma_f32_16x16x32_bf16 v[100:103], v[56:59], v[224:227], v[100:103]\n"
      "v_mfma_f32_16x16x32_bf16 v[100:103], v[60:63], v[228:231], v[100:103]\n"
      "s_nop 7\n"
      "v_max3_f32 v68, v68, v96, v97\n"
      "v_max3_f32 v68, v68, v98, v99\n"
      "v_max3_f32 v68, v68, v100, v101\n"
      "v_max3_f32 v68, v68, v102, v103\n"
      "v_mfma_f32_16x16x32_bf16 v[96:99], v[32:35], v[232:235], 0\n"
      "v_mfma_f32_16x16x32_bf16 v[96:99], v[36:39], v[236:239], v[96:99]\n"
      "v_mfma_f32_16x16x32_bf16 v[96:99], v[40:43], v[240:243], v[96:99]\n"
      "v_mfma_f32_16x16x32_bf16 v[96:99], v[44:47], v[244:247], v[96:99]\n"
      "v_mfma_f32_16x16x32_bf16 v[100:103], v[48:51], v[232:235], 0\n"
      "v_mfma_f32_16x16x32_bf16 v[100:103], v[52:55], v[236:239], v[100:103]\n"
      "v_mfma_f32_16x16x32_bf16 v[100:103], v[56:59], v[240:243], v[100:103]\n"
      "v_mfma_f32_16x16x32_bf16 v[100:103], v[60:63], v[244:247], v[100:103]\n"
      "s_nop 7\n"
      "v_max3_f32 v69, v69, v96, v97\n"
      "v_max3_f32 v69, v69, v98, v99\n"
      "v_max3_f32 v69, v69, v100, v101\n"
      "v_max3_f32 v69, v69, v102, v103\n"
      "s_add_u32 s60, s60, 8192\n"
      "s_cmp_lt_u32 s60, 0x80000\n"
      "s_cbranch_scc1 L_fb_loop_w2\n"
      "v_mov_b32 v29, v64\n"
      "s_nop 1\n"
      "v_permlane16_swap_b32 v64, v29\n"
      "s_nop 0\n"
      "v_max_f32 v64, v64, v29\n"
      "v_mov_b32 v29, v64\n"
      "s_nop 1\n"
      "v_permlane32_swap_b32 v64, v29\n"
      "s_nop 0\n"
      "v_max_f32 v64, v64, v29\n"
      "v_sub_f32 v192, 0, v64\n"
      "v_mov_b32 v193, v192\n"
      "v_mov_b32 v194, v192\n"
      "v_mov_b32 v195, v192\n"
      "v_mov_b32 v29, v65\n"
      "s_nop 1\n"
      "v_permlane16_swap_b32 v65, v29\n"
      "s_nop 0\n"
      "v_max_f32 v65, v65, v29\n"
      "v_mov_b32 v29, v65\n"
      "s_nop 1\n"
      "v_permlane32_swap_b32 v65, v29\n"
      "s_nop 0\n"
      "v_max_f32 v65, v65, v29\n"
      "v_sub_f32 v196, 0, v65\n"
      "v_mov_b32 v197, v196\n"
      "v_mov_b32 v198, v196\n"
      "v_mov_b32 v199, v196\n"
      "v_mov_b32 v29, v66\n"
      "s_nop 1\n"
      "v_permlane16_swap_b32 v66, v29\n"
      "s_nop 0\n"
      "v_max_f32 v66, v66, v29\n"
      "v_mov_b32 v29, v66\n"
      "s_nop 1\n"
      "v_permlane32_swap_b32 v66, v29\n"
      "s_nop 0\n"
      "v_max_f32 v66, v66, v29\n"
      "v_sub_f32 v200, 0, v66\n"
      "v_mov_b32 v201, v200\n"
      "v_mov_b32 v202, v200\n"
      "v_mov_b32 v203, v200\n"
      "v_mov_b32 v29, v67\n"
      "s_nop 1\n"
      "v_permlane16_swap_b32 v67, v29\n"
      "s_nop 0\n"
      "v_max_f32 v67, v67, v29\n"
      "v_mov_b32 v29, v67\n"
      "s_nop 1\n"
      "v_permlane32_swap_b32 v67, v29\n"
      "s_nop 0\n"
      "v_max_f32 v67, v67, v29\n"
      "v_sub_f32 v204, 0, v67\n"
      "v_mov_b32 v205, v204\n"
      "v_mov_b32 v206, v204\n"
      "v_mov_b32 v207, v204\n"
      "v_mov_b32 v29, v68\n"
      "s_nop 1\n"
      "v_permlane16_swap_b32 v68, v29\n"
      "s_nop 0\n"
      "v_max_f32 v68, v68, v29\n"
      "v_mov_b32 v29, v68\n"
      "s_nop 1\n"
      "v_permlane32_swap_b32 v68, v29\n"
      "s_nop 0\n"
      "v_max_f32 v68, v68, v29\n"
      "v_sub_f32 v208, 0, v68\n"
      "v_mov_b32 v209, v208\n"
      "v_mov_b32 v210, v208\n"
      "v_mov_b32 v211, v208\n"
      "v_mov_b32 v29, v69\n"
      "s_nop 1\n"
      "v_permlane16_swap_b32 v69, v29\n"
      "s_nop 0\n"
      "v_max_f32 v69, v69, v29\n"
      "v_mov_b32 v29, v69\n"
      "s_nop 1\n"
      "v_permlane32_swap_b32 v69, v29\n"
      "s_nop 0\n"
      "v_max_f32 v69, v69, v29\n"
      "v_sub_f32 v212, 0, v69\n"
      "v_mov_b32 v213, v212\n"
      "v_mov_b32 v214, v212\n"
      "v_mov_b32 v215, v212\n"
      "v_mov_b32 v64, 0\n"
      "v_mov_b32 v65, 0\n"
      "v_mov_b32 v66, 0\n"
      "v_mov_b32 v67, 0\n"
      "v_mov_b32 v14, 0\n"
      "v_mov_b32 v15, 0\n"
      "v_mov_b32 v16, 0\n"
      "v_mov_b32 v17, 0\n"
      "v_mov_b32 v18, 0\n"
      "v_mov_b32 v19, 0\n"
      "v_mfma_f32_32x32x16_bf16 a[0:15], v[64:67], v[64:67], 0\n"
      "v_mfma_f32_32x32x16_bf16 a[16:31], v[64:67], v[64:67], 0\n"
      "v_mfma_f32_32x32x16_bf16 a[32:47], v[64:67], v[64:67], 0\n"
      "v_mfma_f32_32x32x16_bf16 a[48:63], v[64:67], v[64:67], 0\n"
      "v_mfma_f32_32x32x16_bf16 a[64:79], v[64:67], v[64:67], 0\n"
      "v_mfma_f32_32x32x16_bf16 a[80:95], v[64:67], v[64:67], 0\n"
      "v_mfma_f32_32x32x16_bf16 a[96:111], v[64:67], v[64:67], 0\n"
      "v_mfma_f32_32x32x16_bf16 a[112:127], v[64:67], v[64:67], 0\n"
      "v_mfma_f32_32x32x16_bf16 a[128:143], v[64:67], v[64:67], 0\n"
      "v_mfma_f32_32x32x16_bf16 a[144:159], v[64:67], v[64:67], 0\n"
      "v_mfma_f32_32x32x16_bf16 a[160:175], v[64:67], v[64:67], 0\n"
      "v_mfma_f32_32x32x16_bf16 a[176:191], v[64:67], v[64:67], 0\n"
      "v_xor_b32 v4, 0x8000, v4\n"
      "v_xor_b32 v5, 0x8000, v5\n"
      "s_mov_b32 s58, 0x0\n"
      "s_mov_b32 m0, 0x0\n"
      "s_nop 0\n"
      "buffer_load_dwordx4 v6, s[40:43], s58 offen offset:0 lds\n"
      "buffer_load_dwordx4 v7, s[40:43], s58 offen offset:1024 lds\n"
      "buffer_load_dwordx4 v8, s[40:43], s58 offen offset:2048 lds\n"
      "buffer_load_dwordx4 v9, s[40:43], s58 offen offset:3072 lds\n"
      "s_mov_b32 s58, 0x2000\n"
      "s_mov_b32 m0, 0x2000\n"
      "s_nop 0\n"
      "buffer_load_dwordx4 v6, s[40:43], s58 offen offset:0 lds\n"
      "buffer_load_dwordx4 v7, s[40:43], s58 offen offset:1024 lds\n"
      "buffer_load_dwordx4 v8, s[40:43], s58 offen offset:2048 lds\n"
      "buffer_load_dwordx4 v9, s[40:43], s58 offen offset:3072 lds\n"
      "s_mov_b32 s58, 0x4000\n"
      "s_mov_b32 m0, 0x4000\n"
      "s_nop 0\n"
      "buffer_load_dwordx4 v6, s[40:43], s58 offen offset:0 lds\n"
      "buffer_load_dwordx4 v7, s[40:43], s58 offen offset:1024 lds\n"
      "buffer_load_dwordx4 v8, s[40:43], s58 offen offset:2048 lds\n"
      "buffer_load_dwordx4 v9, s[40:43], s58 offen offset:3072 lds\n"
      "s_mov_b32 s58, 0x6000\n"
      "s_mov_b32 m0, 0x6000\n"
      "s_nop 0\n"
      "buffer_load_dwordx4 v6, s[40:43], s58 offen offset:0 lds\n"
      "buffer_load_dwordx4 v7, s[40:43], s58 offen offset:1024 lds\n"
      "buffer_load_dwordx4 v8, s[40:43], s58 offen offset:2048 lds\n"
      "buffer_load_dwordx4 v9, s[40:43], s58 offen offset:3072 lds\n"
      "s_mov_b32 s58, 0x8000\n"
      "s_mov_b32 m0, 0x8000\n"
      "s_nop 0\n"
      "buffer_load_dwordx4 v6, s[40:43], s58 offen offset:0 lds\n"
      "buffer_load_dwordx4 v7, s[40:43], s58 offen offset:1024 lds\n"
      "buffer_load_dwordx4 v8, s[40:43], s58 offen offset:2048 lds\n"
      "buffer_load_dwordx4 v9, s[40:43], s58 offen offset:3072 lds\n"
      "s_waitcnt vmcnt(8)\n"
      "s_barrier\n"
      "ds_read_b128 v[32:35], v0 offset:0\n"
      "ds_read_b128 v[36:39], v1 offset:0\n"
      "ds_read_b128 v[40:43], v2 offset:0\n"
      "ds_read_b128 v[44:47], v3 offset:0\n"
      "ds_read_b128 v[48:51], v0 offset:4096\n"
      "ds_read_b128 v[52:55], v1 offset:4096\n"
      "ds_read_b128 v[56:59], v2 offset:4096\n"
      "ds_read_b128 v[60:63], v3 offset:4096\n"
      "s_waitcnt lgkmcnt(0)\n"
      "v_mfma_f32_16x16x32_bf16 v[96:99], v[32:35], a[192:195], v[192:195]\n"
      "v_mfma_f32_16x16x32_bf16 v[96:99], v[36:39], a[196:199], v[96:99]\n"
      "v_mfma_f32_16x16x32_bf16 v[96:99], v[40:43], a[200:203], v[96:99]\n"
      "v_mfma_f32_16x16x32_bf16 v[96:99], v[44:47], a[204:207], v[96:99]\n"
      "v_mfma_f32_16x16x32_bf16 v[100:103], v[48:51], a[192:195], v[192:195]\n"
      "v_mfma_f32_16x16x32_bf16 v[100:103], v[52:55], a[196:199], v[100:103]\n"
      "v_mfma_f32_16x16x32_bf16 v[100:103], v[56:59], a[200:203], v[100:103]\n"
      "v_mfma_f32_16x16x32_bf16 v[100:103], v[60:63], a[204:207], v[100:103]\n"
      "v_mfma_f32_16x16x32_bf16 v[104:107], v[32:35], a[208:211], v[196:199]\n"
      "v_mfma_f32_16x16x32_bf16 v[104:107], v[36:39], a[212:215], v[104:107]\n"
      "v_mfma_f32_16x16x32_bf16 v[104:107], v[40:43], a[216:219], v[104:107]\n"
      "v_mfma_f32_16x16x32_bf16 v[104:107], v[44:47], a[220:223], v[104:107]\n"
      "v_mfma_f32_16x16x32_bf16 v[108:111], v[48:51], a[208:211], v[196:199]\n"
      "v_mfma_f32_16x16x32_bf16 v[108:111], v[52:55], a[212:215], v[108:111]\n"
      "v_mfma_f32_16x16x32_bf16 v[108:111], v[56:59], a[216:219], v[108:111]\n"
      "v_mfma_f32_16x16x32_bf16 v[108:111], v[60:63], a[220:223], v[108:111]\n"
      "v_mfma_f32_16x16x32_bf16 v[112:115], v[32:35], a[224:227], v[200:203]\n"
      "v_mfma_f32_16x16x32_bf16 v[112:115], v[36:39], a[228:231], v[112:115]\n"
      "v_mfma_f32_16x16x32_bf16 v[112:115], v[40:43], a[232:235], v[112:115]\n"
      "v_mfma_f32_16x16x32_bf16 v[112:115], v[44:47], a[236:239], v[112:115]\n"
      "v_mfma_f32_16x16x32_bf16 v[116:119], v[48:51], a[224:227], v[200:203]\n"
      "v_mfma_f32_16x16x32_bf16 v[116:119], v[52:55], a[228:231], v[116:119]\n"
      "v_mfma_f32_16x16x32_bf16 v[116:119], v[56:59], a[232:235], v[116:119]\n"
      "v_mfma_f32_16x16x32_bf16 v[116:119], v[60:63], a[236:239], v[116:119]\n"
      "v_mfma_f32_16x16x32_bf16 v[120:123], v[32:35], a[240:243], v[204:207]\n"
      "v_mfma_f32_16x16x32_bf16 v[120:123], v[36:39], a[244:247], v[120:123]\n"
      "v_mfma_f32_16x16x32_bf16 v[120:123], v[40:43], a[248:251], v[120:123]\n"
      "v_mfma_f32_16x16x32_bf16 v[120:123], v[44:47], a[252:255], v[120:123]\n"
      "v_mfma_f32_16x16x32_bf16 v[124:127], v[48:51], a[240:243], v[204:207]\n"
      "v_mfma_f32_16x16x32_bf16 v[124:127], v[52:55], a[244:247], v[124:127]\n"
      "v_mfma_f32_16x16x32_bf16 v[124:127], v[56:59], a[248:251], v[124:127]\n"
      "v_mfma_f32_16x16x32_bf16 v[124:127], v[60:63], a[252:255], v[124:127]\n"
      "v_mfma_f32_16x16x32_bf16 v[128:131], v[32:35], v[216:219], v[208:211]\n"
      "v_mfma_f32_16x16x32_bf16 v[128:131], v[36:39], v[220:223], v[128:131]\n"
      "v_mfma_f32_16x16x32_bf16 v[128:131], v[40:43], v[224:227], v[128:131]\n"
      "v_mfma_f32_16x16x32_bf16 v[128:131], v[44:47], v[228:231], v[128:131]\n"
      "v_mfma_f32_16x16x32_bf16 v[132:135], v[48:51], v[216:219], v[208:211]\n"
      "v_mfma_f32_16x16x32_bf16 v[132:135], v[52:55], v[220:223], v[132:135]\n"
      "v_mfma_f32_16x16x32_bf16 v[132:135], v[56:59], v[224:227], v[132:135]\n"
      "v_mfma_f32_16x16x32_bf16 v[132:135], v[60:63], v[228:231], v[132:135]\n"
      "v_mfma_f32_16x16x32_bf16 v[176:179], v[32:35], v[232:235], v[212:215]\n"
      "v_mfma_f32_16x16x32_bf16 v[176:179], v[36:39], v[236:239], v[176:179]\n"
      "v_mfma_f32_16x16x32_bf16 v[176:179], v[40:43], v[240:243], v[176:179]\n"
      "v_mfma_f32_16x16x32_bf16 v[176:179], v[44:47], v[244:247], v[176:179]\n"
      "v_mfma_f32_16x16x32_bf16 v[168:171], v[48:51], v[232:235], v[212:215]\n"
      "v_mfma_f32_16x16x32_bf16 v[168:171], v[52:55], v[236:239], v[168:171]\n"
      "v_mfma_f32_16x16x32_bf16 v[168:171], v[56:59], v[240:243], v[168:171]\n"
      "v_mfma_f32_16x16x32_bf16 v[168:171], v[60:63], v[244:247], v[168:171]\n"
      "s_nop 7\n"
      "s_branch L_restart_w2\n"
      "L_path_3:\n"
      "s_mov_b32 s58, 0x1000\n"
      "s_mov_b32 m0, 0x1000\n"
      "s_nop 0\n"
      "buffer_load_dwordx4 v6, s[40:43], s58 offen offset:0 lds\n"
      "buffer_load_dwordx4 v7, s[40:43], s58 offen offset:1024 lds\n"
      "buffer_load_dwordx4 v8, s[40:43], s58 offen offset:2048 lds\n"
      "buffer_load_dwordx4 v9, s[40:43], s58 offen offset:3072 lds\n"
      "s_mov_b32 s58, 0x14000\n"
      "s_mov_b32 m0, 0xf000\n"
      "s_nop 0\n"
      "buffer_load_dwordx4 v9, s[36:39], s58 offen offset:3072 nt lds\n"
      "s_add_u32 s58, s62, 0\n"
      "s_add_u32 s59, s63, 0\n"
      "s_mov_b32 m0, s59\n"
      "s_nop 0\n"
      "buffer_load_dwordx4 v6, s[36:39], s58 offen offset:0 nt lds\n"
      "buffer_load_dwordx4 v7, s[36:39], s58 offen offset:1024 nt lds\n"
      "buffer_load_dwordx4 v8, s[36:39], s58 offen offset:2048 nt lds\n"
      "buffer_load_dwordx4 v9, s[36:39], s58 offen offset:3072 nt lds\n"
      "s_add_u32 s58, s62, 4096\n"
      "s_add_u32 s59, s63, 4096\n"
      "s_mov_b32 m0, s59\n"
      "s_nop 0\n"
      "buffer_load_dwordx4 v6, s[36:39], s58 offen offset:0 nt lds\n"
      "buffer_load_dwordx4 v7, s[36:39], s58 offen offset:1024 nt lds\n"
      "buffer_load_dwordx4 v8, s[36:39], s58 offen offset:2048 nt lds\n"
      "buffer_load_dwordx4 v9, s[36:39], s58 offen offset:3072 nt lds\n"
      "s_add_u32 s58, s62, 8192\n"
      "s_add_u32 s59, s63, 8192\n"
      "s_mov_b32 m0, s59\n"
      "s_nop 0\n"
      "buffer_load_dwordx4 v6, s[36:39], s58 offen offset:0 nt lds\n"
      "buffer_load_dwordx4 v7, s[36:39], s58 offen offset:1024 nt lds\n"
      "buffer_load_dwordx4 v8, s[36:39], s58 offen offset:2048 nt lds\n"
      "buffer_load_dwordx4 v9, s[36:39], s58 offen offset:3072 nt lds\n"
      "s_mov_b64 s[58:59], %2\n"
      "s_mul_i32 s60, s53, 0x80000\n"
      "s_add_u32 s44, s58, s60\n"
      "s_addc_u32 s59, s59, 0\n"
      "s_and_b32 s45, s59, 0xffff\n"
      "s_mov_b32 s46, 0x80000\n"
      "s_mov_b32 s47, 0x20000\n"
      "s_mov_b64 s[58:59], %3\n"
      "s_mul_i32 s60, s53, 0x15000\n"
      "s_add_u32 s48, s58, s60\n"
      "s_addc_u32 s59, s59, 0\n"
      "s_and_b32 s49, s59, 0xffff\n"
      "s_mov_b32 s50, 0x15000\n"
      "s_mov_b32 s51, 0x20000\n"
      "s_mov_b32 s54, 0x3e0293ee\n"
      "s_mov_b32 s65, 0\n"
      "s_mov_b32 s96, 0x57800000\n"
      "s_mov_b32 s97, 0xffff0000\n"
      "v_lshl_or_b32 v0, v20, 8, v22\n"
      "v_xor_b32 v1, 64, v0\n"
      "v_xor_b32 v2, 128, v0\n"
      "v_xor_b32 v3, 192, v0\n"
      "v_bfe_u32 v23, v13, 2, 2\n"
      "v_and_b32 v24, 3, v13\n"
      "v_lshlrev_b32 v25, 4, v24\n"
      "v_lshl_or_b32 v25, v21, 6, v25\n"
      "v_lshl_or_b32 v10, v23, 8, v25\n"
      "v_xor_b32 v11, 32, v10\n"
      "v_lshlrev_b32 v25, 3, v24\n"
      "v_lshl_or_b32 v25, v23, 6, v25\n"
      "v_lshl_or_b32 v25, v21, 10, v25\n"
      "v_and_b32 v23, 1, v21\n"
      "v_lshl_or_b32 v25, v23, 5, v25\n"
      "v_or_b32 v4, 0x10000, v25\n"
      "v_xor_b32 v5, 32, v4\n"
      "v_lshlrev_b32 v12, 4, v13\n"
      "v_add_u32 v248, s63, v0\n"
      "v_add_u32 v249, s63, v1\n"
      "v_add_u32 v250, s63, v2\n"
      "v_add_u32 v251, s63, v3\n"
      "v_mov_b32 v64, 0\n"
      "v_mov_b32 v65, 0\n"
      "v_mov_b32 v66, 0\n"
      "v_mov_b32 v67, 0\n"
      "v_mov_b32 v14, 0\n"
      "v_mov_b32 v15, 0\n"
      "v_mov_b32 v16, 0\n"
      "v_mov_b32 v17, 0\n"
      "v_mov_b32 v18, 0\n"
      "v_mov_b32 v19, 0\n"
      "v_mfma_f32_32x32x16_bf16 a[0:15], v[64:67], v[64:67], 0\n"
      "v_mfma_f32_32x32x16_bf16 a[16:31], v[64:67], v[64:67], 0\n"
      "v_mfma_f32_32x32x16_bf16 a[32:47], v[64:67], v[64:67], 0\n"
      "v_mfma_f32_32x32x16_bf16 a[48:63], v[64:67], v[64:67], 0\n"
      "v_mfma_f32_32x32x16_bf16 a[64:79], v[64:67], v[64:67], 0\n"
      "v_mfma_f32_32x32x16_bf16 a[80:95], v[64:67], v[64:67], 0\n"
      "v_mfma_f32_32x32x16_bf16 a[96:111], v[64:67], v[64:67], 0\n"
      "v_mfma_f32_32x32x16_bf16 a[112:127], v[64:67], v[64:67], 0\n"
      "v_mfma_f32_32x32x16_bf16 a[128:143], v[64:67], v[64:67], 0\n"
      "v_mfma_f32_32x32x16_bf16 a[144:159], v[64:67], v[64:67], 0\n"
      "v_mfma_f32_32x32x16_bf16 a[160:175], v[64:67], v[64:67], 0\n"
      "v_mfma_f32_32x32x16_bf16 a[176:191], v[64:67], v[64:67], 0\n"
      "s_waitcnt vmcnt(8)\n"
      "ds_read_b128 v[136:139], v248 offset:0\n"
      "ds_read_b128 v[140:143], v249 offset:0\n"
      "ds_read_b128 v[144:147], v250 offset:0\n"
      "ds_read_b128 v[148:151], v251 offset:0\n"
      "s_add_u32 s58, s62, 12288\n"
      "s_add_u32 s59, s63, 12288\n"
      "s_mov_b32 m0, s59\n"
      "s_nop 0\n"
      "buffer_load_dwordx4 v6, s[36:39], s58 offen offset:0 nt lds\n"
      "buffer_load_dwordx4 v7, s[36:39], s58 offen offset:1024 nt lds\n"
      "buffer_load_dwordx4 v8, s[36:39], s58 offen offset:2048 nt lds\n"
      "buffer_load_dwordx4 v9, s[36:39], s58 offen offset:3072 nt lds\n"
      "s_waitcnt lgkmcnt(0)\n"
      "v_lshlrev_b32 v20, 16, v136\n"
      "v_and_b32 v21, s97, v136\n"
      "v_lshlrev_b32 v22, 16, v137\n"
      "v_and_b32 v23, s97, v137\n"
      "v_lshlrev_b32 v24, 16, v138\n"
      "v_and_b32 v25, s97, v138\n"
      "v_lshlrev_b32 v26, 16, v139\n"
      "v_and_b32 v27, s97, v139\n"
      "v_pk_mul_f32 v[20:21], v[20:21], s[54:55] op_sel_hi:[1,0]\n"
      "v_pk_mul_f32 v[22:23], v[22:23], s[54:55] op_sel_hi:[1,0]\n"
      "v_pk_mul_f32 v[24:25], v[24:25], s[54:55] op_sel_hi:[1,0]\n"
      "v_pk_mul_f32 v[26:27], v[26:27], s[54:55] op_sel_hi:[1,0]\n"
      "v_cvt_pk_bf16_f32 v136, v20, v21\n"
      "v_cvt_pk_bf16_f32 v137, v22, v23\n"
      "v_cvt_pk_bf16_f32 v138, v24, v25\n"
      "v_cvt_pk_bf16_f32 v139, v26, v27\n"
      "v_accvgpr_write_b32 a192, v136\n"
      "v_accvgpr_write_b32 a193, v137\n"
      "v_accvgpr_write_b32 a194, v138\n"
      "v_accvgpr_write_b32 a195, v139\n"
      "v_lshlrev_b32 v20, 16, v140\n"
      "v_and_b32 v21, s97, v140\n"
      "v_lshlrev_b32 v22, 16, v141\n"
      "v_and_b32 v23, s97, v141\n"
      "v_lshlrev_b32 v24, 16, v142\n"
      "v_and_b32 v25, s97, v142\n"
      "v_lshlrev_b32 v26, 16, v143\n"
      "v_and_b32 v27, s97, v143\n"
      "v_pk_mul_f32 v[20:21], v[20:21], s[54:55] op_sel_hi:[1,0]\n"
      "v_pk_mul_f32 v[22:23], v[22:23], s[54:55] op_sel_hi:[1,0]\n"
      "v_pk_mul_f32 v[24:25], v[24:25], s[54:55] op_sel_hi:[1,0]\n"
      "v_pk_mul_f32 v[26:27], v[26:27], s[54:55] op_sel_hi:[1,0]\n"
      "v_cvt_pk_bf16_f32 v140, v20, v21\n"
      "v_cvt_pk_bf16_f32 v141, v22, v23\n"
      "v_cvt_pk_bf16_f32 v142, v24, v25\n"
      "v_cvt_pk_bf16_f32 v143, v26, v27\n"
      "v_accvgpr_write_b32 a196, v140\n"
      "v_accvgpr_write_b32 a197, v141\n"
      "v_accvgpr_write_b32 a198, v142\n"
      "v_accvgpr_write_b32 a199, v143\n"
      "v_lshlrev_b32 v20, 16, v144\n"
      "v_and_b32 v21, s97, v144\n"
      "v_lshlrev_b32 v22, 16, v145\n"
      "v_and_b32 v23, s97, v145\n"
      "v_lshlrev_b32 v24, 16, v146\n"
      "v_and_b32 v25, s97, v146\n"
      "v_lshlrev_b32 v26, 16, v147\n"
      "v_and_b32 v27, s97, v147\n"
      "v_pk_mul_f32 v[20:21], v[20:21], s[54:55] op_sel_hi:[1,0]\n"
      "v_pk_mul_f32 v[22:23], v[22:23], s[54:55] op_sel_hi:[1,0]\n"
      "v_pk_mul_f32 v[24:25], v[24:25], s[54:55] op_sel_hi:[1,0]\n"
      "v_pk_mul_f32 v[26:27], v[26:27], s[54:55] op_sel_hi:[1,0]\n"
      "v_cvt_pk_bf16_f32 v144, v20, v21\n"
      "v_cvt_pk_bf16_f32 v145, v22, v23\n"
      "v_cvt_pk_bf16_f32 v146, v24, v25\n"
      "v_cvt_pk_bf16_f32 v147, v26, v27\n"
      "v_accvgpr_write_b32 a200, v144\n"
      "v_accvgpr_write_b32 a201, v145\n"
      "v_accvgpr_write_b32 a202, v146\n"
      "v_accvgpr_write_b32 a203, v147\n"
      "v_lshlrev_b32 v20, 16, v148\n"
      "v_and_b32 v21, s97, v148\n"
      "v_lshlrev_b32 v22, 16, v149\n"
      "v_and_b32 v23, s97, v149\n"
      "v_lshlrev_b32 v24, 16, v150\n"
      "v_and_b32 v25, s97, v150\n"
      "v_lshlrev_b32 v26, 16, v151\n"
      "v_and_b32 v27, s97, v151\n"
      "v_pk_mul_f32 v[20:21], v[20:21], s[54:55] op_sel_hi:[1,0]\n"
      "v_pk_mul_f32 v[22:23], v[22:23], s[54:55] op_sel_hi:[1,0]\n"
      "v_pk_mul_f32 v[24:25], v[24:25], s[54:55] op_sel_hi:[1,0]\n"
      "v_pk_mul_f32 v[26:27], v[26:27], s[54:55] op_sel_hi:[1,0]\n"
      "v_cvt_pk_bf16_f32 v148, v20, v21\n"
      "v_cvt_pk_bf16_f32 v149, v22, v23\n"
      "v_cvt_pk_bf16_f32 v150, v24, v25\n"
      "v_cvt_pk_bf16_f32 v151, v26, v27\n"
      "v_accvgpr_write_b32 a204, v148\n"
      "v_accvgpr_write_b32 a205, v149\n"
      "v_accvgpr_write_b32 a206, v150\n"
      "v_accvgpr_write_b32 a207, v151\n"
      "s_waitcnt vmcnt(8)\n"
      "ds_read_b128 v[152:155], v248 offset:4096\n"
      "ds_read_b128 v[156:159], v249 offset:4096\n"
      "ds_read_b128 v[160:163], v250 offset:4096\n"
      "ds_read_b128 v[164:167], v251 offset:4096\n"
      "s_add_u32 s58, s62, 16384\n"
      "s_add_u32 s59, s63, 16384\n"
      "s_mov_b32 m0, s59\n"
      "s_nop 0\n"
      "buffer_load_dwordx4 v6, s[36:39], s58 offen offset:0 nt lds\n"
      "buffer_load_dwordx4 v7, s[36:39], s58 offen offset:1024 nt lds\n"
      "buffer_load_dwordx4 v8, s[36:39], s58 offen offset:2048 nt lds\n"
      "buffer_load_dwordx4 v9, s[36:39], s58 offen offset:3072 nt lds\n"
      "s_waitcnt lgkmcnt(0)\n"
      "v_lshlrev_b32 v20, 16, v152\n"
      "v_and_b32 v21, s97, v152\n"
      "v_lshlrev_b32 v22, 16, v153\n"
      "v_and_b32 v23, s97, v153\n"
      "v_lshlrev_b32 v24, 16, v154\n"
      "v_and_b32 v25, s97, v154\n"
      "v_lshlrev_b32 v26, 16, v155\n"
      "v_and_b32 v27, s97, v155\n"
      "v_pk_mul_f32 v[20:21], v[20:21], s[54:55] op_sel_hi:[1,0]\n"
      "v_pk_mul_f32 v[22:23], v[22:23], s[54:55] op_sel_hi:[1,0]\n"
      "v_pk_mul_f32 v[24:25], v[24:25], s[54:55] op_sel_hi:[1,0]\n"
      "v_pk_mul_f32 v[26:27], v[26:27], s[54:55] op_sel_hi:[1,0]\n"
      "v_cvt_pk_bf16_f32 v152, v20, v21\n"
      "v_cvt_pk_bf16_f32 v153, v22, v23\n"
      "v_cvt_pk_bf16_f32 v154, v24, v25\n"
      "v_cvt_pk_bf16_f32 v155, v26, v27\n"
      "v_accvgpr_write_b32 a208, v152\n"
      "v_accvgpr_write_b32 a209, v153\n"
      "v_accvgpr_write_b32 a210, v154\n"
      "v_accvgpr_write_b32 a211, v155\n"
      "v_lshlrev_b32 v20, 16, v156\n"
      "v_and_b32 v21, s97, v156\n"
      "v_lshlrev_b32 v22, 16, v157\n"
      "v_and_b32 v23, s97, v157\n"
      "v_lshlrev_b32 v24, 16, v158\n"
      "v_and_b32 v25, s97, v158\n"
      "v_lshlrev_b32 v26, 16, v159\n"
      "v_and_b32 v27, s97, v159\n"
      "v_pk_mul_f32 v[20:21], v[20:21], s[54:55] op_sel_hi:[1,0]\n"
      "v_pk_mul_f32 v[22:23], v[22:23], s[54:55] op_sel_hi:[1,0]\n"
      "v_pk_mul_f32 v[24:25], v[24:25], s[54:55] op_sel_hi:[1,0]\n"
      "v_pk_mul_f32 v[26:27], v[26:27], s[54:55] op_sel_hi:[1,0]\n"
      "v_cvt_pk_bf16_f32 v156, v20, v21\n"
      "v_cvt_pk_bf16_f32 v157, v22, v23\n"
      "v_cvt_pk_bf16_f32 v158, v24, v25\n"
      "v_cvt_pk_bf16_f32 v159, v26, v27\n"
      "v_accvgpr_write_b32 a212, v156\n"
      "v_accvgpr_write_b32 a213, v157\n"
      "v_accvgpr_write_b32 a214, v158\n"
      "v_accvgpr_write_b32 a215, v159\n"
      "v_lshlrev_b32 v20, 16, v160\n"
      "v_and_b32 v21, s97, v160\n"
      "v_lshlrev_b32 v22, 16, v161\n"
      "v_and_b32 v23, s97, v161\n"
      "v_lshlrev_b32 v24, 16, v162\n"
      "v_and_b32 v25, s97, v162\n"
      "v_lshlrev_b32 v26, 16, v163\n"
      "v_and_b32 v27, s97, v163\n"
      "v_pk_mul_f32 v[20:21], v[20:21], s[54:55] op_sel_hi:[1,0]\n"
      "v_pk_mul_f32 v[22:23], v[22:23], s[54:55] op_sel_hi:[1,0]\n"
      "v_pk_mul_f32 v[24:25], v[24:25], s[54:55] op_sel_hi:[1,0]\n"
      "v_pk_mul_f32 v[26:27], v[26:27], s[54:55] op_sel_hi:[1,0]\n"
      "v_cvt_pk_bf16_f32 v160, v20, v21\n"
      "v_cvt_pk_bf16_f32 v161, v22, v23\n"
      "v_cvt_pk_bf16_f32 v162, v24, v25\n"
      "v_cvt_pk_bf16_f32 v163, v26, v27\n"
      "v_accvgpr_write_b32 a216, v160\n"
      "v_accvgpr_write_b32 a217, v161\n"
      "v_accvgpr_write_b32 a218, v162\n"
      "v_accvgpr_write_b32 a219, v163\n"
      "v_lshlrev_b32 v20, 16, v164\n"
      "v_and_b32 v21, s97, v164\n"
      "v_lshlrev_b32 v22, 16, v165\n"
      "v_and_b32 v23, s97, v165\n"
      "v_lshlrev_b32 v24, 16, v166\n"
      "v_and_b32 v25, s97, v166\n"
      "v_lshlrev_b32 v26, 16, v167\n"
      "v_and_b32 v27, s97, v167\n"
      "v_pk_mul_f32 v[20:21], v[20:21], s[54:55] op_sel_hi:[1,0]\n"
      "v_pk_mul_f32 v[22:23], v[22:23], s[54:55] op_sel_hi:[1,0]\n"
      "v_pk_mul_f32 v[24:25], v[24:25], s[54:55] op_sel_hi:[1,0]\n"
      "v_pk_mul_f32 v[26:27], v[26:27], s[54:55] op_sel_hi:[1,0]\n"
      "v_cvt_pk_bf16_f32 v164, v20, v21\n"
      "v_cvt_pk_bf16_f32 v165, v22, v23\n"
      "v_cvt_pk_bf16_f32 v166, v24, v25\n"
      "v_cvt_pk_bf16_f32 v167, v26, v27\n"
      "v_accvgpr_write_b32 a220, v164\n"
      "v_accvgpr_write_b32 a221, v165\n"
      "v_accvgpr_write_b32 a222, v166\n"
      "v_accvgpr_write_b32 a223, v167\n"
      "s_waitcnt vmcnt(8)\n"
      "ds_read_b128 v[136:139], v248 offset:8192\n"
      "ds_read_b128 v[140:143], v249 offset:8192\n"
      "ds_read_b128 v[144:147], v250 offset:8192\n"
      "ds_read_b128 v[148:151], v251 offset:8192\n"
      "s_mov_b32 s58, 0x3000\n"
      "s_mov_b32 m0, 0x3000\n"
      "s_nop 0\n"
      "buffer_load_dwordx4 v6, s[40:43], s58 offen offset:0 lds\n"
      "buffer_load_dwordx4 v7, s[40:43], s58 offen offset:1024 lds\n"
      "buffer_load_dwordx4 v8, s[40:43], s58 offen offset:2048 lds\n"
      "buffer_load_dwordx4 v9, s[40:43], s58 offen offset:3072 lds\n"
      "s_waitcnt lgkmcnt(0)\n"
      "v_lshlrev_b32 v20, 16, v136\n"
      "v_and_b32 v21, s97, v136\n"
      "v_lshlrev_b32 v22, 16, v137\n"
      "v_and_b32 v23, s97, v137\n"
      "v_lshlrev_b32 v24, 16, v138\n"
      "v_and_b32 v25, s97, v138\n"
      "v_lshlrev_b32 v26, 16, v139\n"
      "v_and_b32 v27, s97, v139\n"
      "v_pk_mul_f32 v[20:21], v[20:21], s[54:55] op_sel_hi:[1,0]\n"
      "v_pk_mul_f32 v[22:23], v[22:23], s[54:55] op_sel_hi:[1,0]\n"
      "v_pk_mul_f32 v[24:25], v[24:25], s[54:55] op_sel_hi:[1,0]\n"
      "v_pk_mul_f32 v[26:27], v[26:27], s[54:55] op_sel_hi:[1,0]\n"
      "v_cvt_pk_bf16_f32 v136, v20, v21\n"
      "v_cvt_pk_bf16_f32 v137, v22, v23\n"
      "v_cvt_pk_bf16_f32 v138, v24, v25\n"
      "v_cvt_pk_bf16_f32 v139, v26, v27\n"
      "v_accvgpr_write_b32 a224, v136\n"
      "v_accvgpr_write_b32 a225, v137\n"
      "v_accvgpr_write_b32 a226, v138\n"
      "v_accvgpr_write_b32 a227, v139\n"
      "v_lshlrev_b32 v20, 16, v140\n"
      "v_and_b32 v21, s97, v140\n"
      "v_lshlrev_b32 v22, 16, v141\n"
      "v_and_b32 v23, s97, v141\n"
      "v_lshlrev_b32 v24, 16, v142\n"
      "v_and_b32 v25, s97, v142\n"
      "v_lshlrev_b32 v26, 16, v143\n"
      "v_and_b32 v27, s97, v143\n"
      "v_pk_mul_f32 v[20:21], v[20:21], s[54:55] op_sel_hi:[1,0]\n"
      "v_pk_mul_f32 v[22:23], v[22:23], s[54:55] op_sel_hi:[1,0]\n"
      "v_pk_mul_f32 v[24:25], v[24:25], s[54:55] op_sel_hi:[1,0]\n"
      "v_pk_mul_f32 v[26:27], v[26:27], s[54:55] op_sel_hi:[1,0]\n"
      "v_cvt_pk_bf16_f32 v140, v20, v21\n"
      "v_cvt_pk_bf16_f32 v141, v22, v23\n"
      "v_cvt_pk_bf16_f32 v142, v24, v25\n"
      "v_cvt_pk_bf16_f32 v143, v26, v27\n"
      "v_accvgpr_write_b32 a228, v140\n"
      "v_accvgpr_write_b32 a229, v141\n"
      "v_accvgpr_write_b32 a230, v142\n"
      "v_accvgpr_write_b32 a231, v143\n"
      "v_lshlrev_b32 v20, 16, v144\n"
      "v_and_b32 v21, s97, v144\n"
      "v_lshlrev_b32 v22, 16, v145\n"
      "v_and_b32 v23, s97, v145\n"
      "v_lshlrev_b32 v24, 16, v146\n"
      "v_and_b32 v25, s97, v146\n"
      "v_lshlrev_b32 v26, 16, v147\n"
      "v_and_b32 v27, s97, v147\n"
      "v_pk_mul_f32 v[20:21], v[20:21], s[54:55] op_sel_hi:[1,0]\n"
      "v_pk_mul_f32 v[22:23], v[22:23], s[54:55] op_sel_hi:[1,0]\n"
      "v_pk_mul_f32 v[24:25], v[24:25], s[54:55] op_sel_hi:[1,0]\n"
      "v_pk_mul_f32 v[26:27], v[26:27], s[54:55] op_sel_hi:[1,0]\n"
      "v_cvt_pk_bf16_f32 v144, v20, v21\n"
      "v_cvt_pk_bf16_f32 v145, v22, v23\n"
      "v_cvt_pk_bf16_f32 v146, v24, v25\n"
      "v_cvt_pk_bf16_f32 v147, v26, v27\n"
      "v_accvgpr_write_b32 a232, v144\n"
      "v_accvgpr_write_b32 a233, v145\n"
      "v_accvgpr_write_b32 a234, v146\n"
      "v_accvgpr_write_b32 a235, v147\n"
      "v_lshlrev_b32 v20, 16, v148\n"
      "v_and_b32 v21, s97, v148\n"
      "v_lshlrev_b32 v22, 16, v149\n"
      "v_and_b32 v23, s97, v149\n"
      "v_lshlrev_b32 v24, 16, v150\n"
      "v_and_b32 v25, s97, v150\n"
      "v_lshlrev_b32 v26, 16, v151\n"
      "v_and_b32 v27, s97, v151\n"
      "v_pk_mul_f32 v[20:21], v[20:21], s[54:55] op_sel_hi:[1,0]\n"
      "v_pk_mul_f32 v[22:23], v[22:23], s[54:55] op_sel_hi:[1,0]\n"
      "v_pk_mul_f32 v[24:25], v[24:25], s[54:55] op_sel_hi:[1,0]\n"
      "v_pk_mul_f32 v[26:27], v[26:27], s[54:55] op_sel_hi:[1,0]\n"
      "v_cvt_pk_bf16_f32 v148, v20, v21\n"
      "v_cvt_pk_bf16_f32 v149, v22, v23\n"
      "v_cvt_pk_bf16_f32 v150, v24, v25\n"
      "v_cvt_pk_bf16_f32 v151, v26, v27\n"
      "v_accvgpr_write_b32 a236, v148\n"
      "v_accvgpr_write_b32 a237, v149\n"
      "v_accvgpr_write_b32 a238, v150\n"
      "v_accvgpr_write_b32 a239, v151\n"
      "s_waitcnt vmcnt(24)\n"
      "s_barrier\n"
      "ds_read_b128 v[152:155], v0 offset:61440\n"
      "ds_read_b128 v[156:159], v1 offset:61440\n"
      "ds_read_b128 v[160:163], v2 offset:61440\n"
      "ds_read_b128 v[164:167], v3 offset:61440\n"
      "ds_read_b128 v[32:35], v0 offset:0\n"
      "ds_read_b128 v[36:39], v1 offset:0\n"
      "ds_read_b128 v[40:43], v2 offset:0\n"
      "ds_read_b128 v[44:47], v3 offset:0\n"
      "ds_read_b128 v[48:51], v0 offset:4096\n"
      "ds_read_b128 v[52:55], v1 offset:4096\n"
      "ds_read_b128 v[56:59], v2 offset:4096\n"
      "ds_read_b128 v[60:63], v3 offset:4096\n"
      "s_mov_b32 s58, 0x5000\n"
      "s_mov_b32 m0, 0x5000\n"
      "s_nop 0\n"
      "buffer_load_dwordx4 v6, s[40:43], s58 offen offset:0 lds\n"
      "buffer_load_dwordx4 v7, s[40:43], s58 offen offset:1024 lds\n"
      "buffer_load_dwordx4 v8, s[40:43], s58 offen offset:2048 lds\n"
      "buffer_load_dwordx4 v9, s[40:43], s58 offen offset:3072 lds\n"
      "s_waitcnt lgkmcnt(0)\n"
      "v_mfma_f32_16x16x32_bf16 v[96:99], v[32:35], a[192:195], 0\n"
      "v_lshlrev_b32 v20, 16, v152\n"
      "v_and_b32 v21, s97, v152\n"
      "v_lshlrev_b32 v22, 16, v153\n"
      "v_mfma_f32_16x16x32_bf16 v[96:99], v[36:39], a[196:199], v[96:99]\n"
      "v_and_b32 v23, s97, v153\n"
      "v_lshlrev_b32 v24, 16, v154\n"
      "v_and_b32 v25, s97, v154\n"
      "v_mfma_f32_16x16x32_bf16 v[96:99], v[40:43], a[200:203], v[96:99]\n"
      "v_lshlrev_b32 v26, 16, v155\n"
      "v_and_b32 v27, s97, v155\n"
      "v_pk_mul_f32 v[20:21], v[20:21], s[54:55] op_sel_hi:[1,0]\n"
      "v_mfma_f32_16x16x32_bf16 v[96:99], v[44:47], a[204:207], v[96:99]\n"
      "v_pk_mul_f32 v[22:23], v[22:23], s[54:55] op_sel_hi:[1,0]\n"
      "v_pk_mul_f32 v[24:25], v[24:25], s[54:55] op_sel_hi:[1,0]\n"
      "v_pk_mul_f32 v[26:27], v[26:27], s[54:55] op_sel_hi:[1,0]\n"
      "v_mfma_f32_16x16x32_bf16 v[100:103], v[48:51], a[192:195], 0\n"
      "v_cvt_pk_bf16_f32 v232, v20, v21\n"
      "v_cvt_pk_bf16_f32 v233, v22, v23\n"
      "v_cvt_pk_bf16_f32 v234, v24, v25\n"
      "v_mfma_f32_16x16x32_bf16 v[100:103], v[52:55], a[196:199], v[100:103]\n"
      "v_cvt_pk_bf16_f32 v235, v26, v27\n"
      "v_lshlrev_b32 v20, 16, v156\n"
      "v_and_b32 v21, s97, v156\n"
      "v_mfma_f32_16x16x32_bf16 v[100:103], v[56:59], a[200:203], v[100:103]\n"
      "v_lshlrev_b32 v22, 16, v157\n"
      "v_and_b32 v23, s97, v157\n"
      "v_lshlrev_b32 v24, 16, v158\n"
      "v_mfma_f32_16x16x32_bf16 v[100:103], v[60:63], a[204:207], v[100:103]\n"
      "v_and_b32 v25, s97, v158\n"
      "v_lshlrev_b32 v26, 16, v159\n"
      "v_and_b32 v27, s97, v159\n"
      "v_mfma_f32_16x16x32_bf16 v[104:107], v[32:35], a[208:211], 0\n"
      "v_pk_mul_f32 v[20:21], v[20:21], s[54:55] op_sel_hi:[1,0]\n"
      "v_pk_mul_f32 v[22:23], v[22:23], s[54:55] op_sel_hi:[1,0]\n"
      "v_pk_mul_f32 v[24:25], v[24:25], s[54:55] op_sel_hi:[1,0]\n"
      "v_mfma_f32_16x16x32_bf16 v[104:107], v[36:39], a[212:215], v[104:107]\n"
      "v_pk_mul_f32 v[26:27], v[26:27], s[54:55] op_sel_hi:[1,0]\n"
      "v_cvt_pk_bf16_f32 v236, v20, v21\n"
      "v_cvt_pk_bf16_f32 v237, v22, v23\n"
      "v_mfma_f32_16x16x32_bf16 v[104:107], v[40:43], a[216:219], v[104:107]\n"
      "v_cvt_pk_bf16_f32 v238, v24, v25\n"
      "v_cvt_pk_bf16_f32 v239, v26, v27\n"
      "v_lshlrev_b32 v20, 16, v160\n"
      "v_mfma_f32_16x16x32_bf16 v[104:107], v[44:47], a[220:223], v[104:107]\n"
      "v_and_b32 v21, s97, v160\n"
      "v_lshlrev_b32 v22, 16, v161\n"
      "v_and_b32 v23, s97, v161\n"
      "v_mfma_f32_16x16x32_bf16 v[108:111], v[48:51], a[208:211], 0\n"
      "v_lshlrev_b32 v24, 16, v162\n"
      "v_and_b32 v25, s97, v162\n"
      "v_lshlrev_b32 v26, 16, v163\n"
      "v_mfma_f32_16x16x32_bf16 v[108:111], v[52:55], a[212:215], v[108:111]\n"
      "v_and_b32 v27, s97, v163\n"
      "v_pk_mul_f32 v[20:21], v[20:21], s[54:55] op_sel_hi:[1,0]\n"
      "v_pk_mul_f32 v[22:23], v[22:23], s[54:55] op_sel_hi:[1,0]\n"
      "v_mfma_f32_16x16x32_bf16 v[108:111], v[56:59], a[216:219], v[108:111]\n"
      "v_pk_mul_f32 v[24:25], v[24:25], s[54:55] op_sel_hi:[1,0]\n"
      "v_pk_mul_f32 v[26:27], v[26:27], s[54:55] op_sel_hi:[1,0]\n"
      "v_cvt_pk_bf16_f32 v240, v20, v21\n"
      "v_mfma_f32_16x16x32_bf16 v[108:111], v[60:63], a[220:223], v[108:111]\n"
      "v_cvt_pk_bf16_f32 v241, v22, v23\n"
      "v_cvt_pk_bf16_f32 v242, v24, v25\n"
      "v_cvt_pk_bf16_f32 v243, v26, v27\n"
      "v_mfma_f32_16x16x32_bf16 v[112:115], v[32:35], a[224:227], 0\n"
      "v_lshlrev_b32 v20, 16, v164\n"
      "v_and_b32 v21, s97, v164\n"
      "v_lshlrev_b32 v22, 16, v165\n"
      "v_mfma_f32_16x16x32_bf16 v[112:115], v[36:39], a[228:231], v[112:115]\n"
      "v_and_b32 v23, s97, v165\n"
      "v_lshlrev_b32 v24, 16, v166\n"
      "v_and_b32 v25, s97, v166\n"
      "v_mfma_f32_16x16x32_bf16 v[112:115], v[40:43], a[232:235], v[112:115]\n"
      "v_lshlrev_b32 v26, 16, v167\n"
      "v_and_b32 v27, s97, v167\n"
      "v_pk_mul_f32 v[20:21], v[20:21], s[54:55] op_sel_hi:[1,0]\n"
      "v_mfma_f32_16x16x32_bf16 v[112:115], v[44:47], a[236:239], v[112:115]\n"
      "v_pk_mul_f32 v[22:23], v[22:23], s[54:55] op_sel_hi:[1,0]\n"
      "v_pk_mul_f32 v[24:25], v[24:25], s[54:55] op_sel_hi:[1,0]\n"
      "v_pk_mul_f32 v[26:27], v[26:27], s[54:55] op_sel_hi:[1,0]\n"
      "v_mfma_f32_16x16x32_bf16 v[116:119], v[48:51], a[224:227], 0\n"
      "v_cvt_pk_bf16_f32 v244, v20, v21\n"
      "v_cvt_pk_bf16_f32 v245, v22, v23\n"
      "v_cvt_pk_bf16_f32 v246, v24, v25\n"
      "v_mfma_f32_16x16x32_bf16 v[116:119], v[52:55], a[228:231], v[116:119]\n"
      "v_cvt_pk_bf16_f32 v247, v26, v27\n"
      "v_mfma_f32_16x16x32_bf16 v[116:119], v[56:59], a[232:235], v[116:119]\n"
      "v_mfma_f32_16x16x32_bf16 v[116:119], v[60:63], a[236:239], v[116:119]\n"
      "s_nop 1\n"
      "v_mfma_f32_16x16x32_bf16 v[168:171], v[32:35], v[232:235], 0\n"
      "v_mfma_f32_16x16x32_bf16 v[168:171], v[36:39], v[236:239], v[168:171]\n"
      "v_mfma_f32_16x16x32_bf16 v[168:171], v[40:43], v[240:243], v[168:171]\n"
      "v_mfma_f32_16x16x32_bf16 v[168:171], v[44:47], v[244:247], v[168:171]\n"
      "v_mfma_f32_16x16x32_bf16 v[176:179], v[48:51], v[232:235], 0\n"
      "v_mfma_f32_16x16x32_bf16 v[176:179], v[52:55], v[236:239], v[176:179]\n"
      "v_mfma_f32_16x16x32_bf16 v[176:179], v[56:59], v[240:243], v[176:179]\n"
      "v_mfma_f32_16x16x32_bf16 v[176:179], v[60:63], v[244:247], v[176:179]\n"
      "v_max3_f32 v28, v96, v97, v98\n"
      "v_max3_f32 v29, v99, v100, v101\n"
      "v_max3_f32 v28, v28, v102, v103\n"
      "v_max_f32 v28, v28, v29\n"
      "v_mov_b32 v29, v28\n"
      "s_nop 1\n"
      "v_permlane16_swap_b32 v28, v29\n"
      "s_nop 0\n"
      "v_max_f32 v28, v28, v29\n"
      "v_mov_b32 v29, v28\n"
      "s_nop 1\n"
      "v_permlane32_swap_b32 v28, v29\n"
      "s_nop 0\n"
      "v_max_f32 v28, v28, v29\n"
      "v_sub_f32 v192, 0, v28\n"
      "v_mov_b32 v193, v192\n"
      "v_mov_b32 v194, v192\n"
      "v_mov_b32 v195, v192\n"
      "v_add_f32 v96, v96, v192\n"
      "v_add_f32 v97, v97, v192\n"
      "v_add_f32 v98, v98, v192\n"
      "v_add_f32 v99, v99, v192\n"
      "v_add_f32 v100, v100, v192\n"
      "v_add_f32 v101, v101, v192\n"
      "v_add_f32 v102, v102, v192\n"
      "v_add_f32 v103, v103, v192\n"
      "v_max3_f32 v28, v104, v105, v106\n"
      "v_max3_f32 v29, v107, v108, v109\n"
      "v_max3_f32 v28, v28, v110, v111\n"
      "v_max_f32 v28, v28, v29\n"
      "v_mov_b32 v29, v28\n"
      "s_nop 1\n"
      "v_permlane16_swap_b32 v28, v29\n"
      "s_nop 0\n"
      "v_max_f32 v28, v28, v29\n"
      "v_mov_b32 v29, v28\n"
      "s_nop 1\n"
      "v_permlane32_swap_b32 v28, v29\n"
      "s_nop 0\n"
      "v_max_f32 v28, v28, v29\n"
      "v_sub_f32 v196, 0, v28\n"
      "v_mov_b32 v197, v196\n"
      "v_mov_b32 v198, v196\n"
      "v_mov_b32 v199, v196\n"
      "v_add_f32 v104, v104, v196\n"
      "v_add_f32 v105, v105, v196\n"
      "v_add_f32 v106, v106, v196\n"
      "v_add_f32 v107, v107, v196\n"
      "v_add_f32 v108, v108, v196\n"
      "v_add_f32 v109, v109, v196\n"
      "v_add_f32 v110, v110, v196\n"
      "v_add_f32 v111, v111, v196\n"
      "v_max3_f32 v28, v112, v113, v114\n"
      "v_max3_f32 v29, v115, v116, v117\n"
      "v_max3_f32 v28, v28, v118, v119\n"
      "v_max_f32 v28, v28, v29\n"
      "v_mov_b32 v29, v28\n"
      "s_nop 1\n"
      "v_permlane16_swap_b32 v28, v29\n"
      "s_nop 0\n"
      "v_max_f32 v28, v28, v29\n"
      "v_mov_b32 v29, v28\n"
      "s_nop 1\n"
      "v_permlane32_swap_b32 v28, v29\n"
      "s_nop 0\n"
      "v_max_f32 v28, v28, v29\n"
      "v_sub_f32 v200, 0, v28\n"
      "v_mov_b32 v201, v200\n"
      "v_mov_b32 v202, v200\n"
      "v_mov_b32 v203, v200\n"
      "v_add_f32 v112, v112, v200\n"
      "v_add_f32 v113, v113, v200\n"
      "v_add_f32 v114, v114, v200\n"
      "v_add_f32 v115, v115, v200\n"
      "v_add_f32 v116, v116, v200\n"
      "v_add_f32 v117, v117, v200\n"
      "s_waitcnt vmcnt(12)\n"
      "ds_read_b128 v[136:139], v248 offset:12288\n"
      "ds_read_b128 v[140:143], v249 offset:12288\n"
      "ds_read_b128 v[144:147], v250 offset:12288\n"
      "ds_read_b128 v[148:151], v251 offset:12288\n"
      "s_mov_b32 s58, 0x7000\n"
      "s_mov_b32 m0, 0x7000\n"
      "s_nop 0\n"
      "buffer_load_dwordx4 v6, s[40:43], s58 offen offset:0 lds\n"
      "buffer_load_dwordx4 v7, s[40:43], s58 offen offset:1024 lds\n"
      "buffer_load_dwordx4 v8, s[40:43], s58 offen offset:2048 lds\n"
      "buffer_load_dwordx4 v9, s[40:43], s58 offen offset:3072 lds\n"
      "v_add_f32 v118, v118, v200\n"
      "v_add_f32 v119, v119, v200\n"
      "v_max3_f32 v28, v168, v169, v170\n"
      "v_max3_f32 v29, v171, v176, v177\n"
      "v_max3_f32 v28, v28, v178, v179\n"
      "v_max_f32 v28, v28, v29\n"
      "v_mov_b32 v29, v28\n"
      "s_nop 1\n"
      "s_waitcnt lgkmcnt(0)\n"
      "v_permlane16_swap_b32 v28, v29\n"
      "v_lshlrev_b32 v20, 16, v136\n"
      "v_max_f32 v28, v28, v29\n"
      "v_mov_b32 v29, v28\n"
      "v_and_b32 v21, s97, v136\n"
      "v_lshlrev_b32 v22, 16, v137\n"
      "v_permlane32_swap_b32 v28, v29\n"
      "v_and_b32 v23, s97, v137\n"
      "v_max_f32 v28, v28, v29\n"
      "v_sub_f32 v212, 0, v28\n"
      "v_mov_b32 v213, v212\n"
      "v_mov_b32 v214, v212\n"
      "v_mov_b32 v215, v212\n"
      "v_add_f32 v176, v176, v212\n"
      "v_add_f32 v177, v177, v212\n"
      "v_add_f32 v178, v178, v212\n"
      "v_add_f32 v179, v179, v212\n"
      "v_lshlrev_b32 v24, 16, v138\n"
      "v_and_b32 v25, s97, v138\n"
      "v_lshlrev_b32 v26, 16, v139\n"
      "v_and_b32 v27, s97, v139\n"
      "v_pk_mul_f32 v[20:21], v[20:21], s[54:55] op_sel_hi:[1,0]\n"
      "v_pk_mul_f32 v[22:23], v[22:23], s[54:55] op_sel_hi:[1,0]\n"
      "v_pk_mul_f32 v[24:25], v[24:25], s[54:55] op_sel_hi:[1,0]\n"
      "v_pk_mul_f32 v[26:27], v[26:27], s[54:55] op_sel_hi:[1,0]\n"
      "v_cvt_pk_bf16_f32 v136, v20, v21\n"
      "v_cvt_pk_bf16_f32 v137, v22, v23\n"
      "v_cvt_pk_bf16_f32 v138, v24, v25\n"
      "v_cvt_pk_bf16_f32 v139, v26, v27\n"
      "v_accvgpr_write_b32 a240, v136\n"
      "v_accvgpr_write_b32 a241, v137\n"
      "v_accvgpr_write_b32 a242, v138\n"
      "v_accvgpr_write_b32 a243, v139\n"
      "v_lshlrev_b32 v20, 16, v140\n"
      "v_and_b32 v21, s97, v140\n"
      "v_lshlrev_b32 v22, 16, v141\n"
      "v_and_b32 v23, s97, v141\n"
      "v_lshlrev_b32 v24, 16, v142\n"
      "v_and_b32 v25, s97, v142\n"
      "v_lshlrev_b32 v26, 16, v143\n"
      "v_and_b32 v27, s97, v143\n"
      "v_pk_mul_f32 v[20:21], v[20:21], s[54:55] op_sel_hi:[1,0]\n"
      "v_pk_mul_f32 v[22:23], v[22:23], s[54:55] op_sel_hi:[1,0]\n"
      "v_pk_mul_f32 v[24:25], v[24:25], s[54:55] op_sel_hi:[1,0]\n"
      "v_pk_mul_f32 v[26:27], v[26:27], s[54:55] op_sel_hi:[1,0]\n"
      "v_cvt_pk_bf16_f32 v140, v20, v21\n"
      "v_cvt_pk_bf16_f32 v141, v22, v23\n"
      "v_cvt_pk_bf16_f32 v142, v24, v25\n"
      "v_cvt_pk_bf16_f32 v143, v26, v27\n"
      "v_accvgpr_write_b32 a244, v140\n"
      "v_accvgpr_write_b32 a245, v141\n"
      "v_accvgpr_write_b32 a246, v142\n"
      "v_accvgpr_write_b32 a247, v143\n"
      "v_lshlrev_b32 v20, 16, v144\n"
      "v_and_b32 v21, s97, v144\n"
      "v_lshlrev_b32 v22, 16, v145\n"
      "v_and_b32 v23, s97, v145\n"
      "v_lshlrev_b32 v24, 16, v146\n"
      "v_and_b32 v25, s97, v146\n"
      "v_lshlrev_b32 v26, 16, v147\n"
      "v_and_b32 v27, s97, v147\n"
      "v_pk_mul_f32 v[20:21], v[20:21], s[54:55] op_sel_hi:[1,0]\n"
      "v_pk_mul_f32 v[22:23], v[22:23], s[54:55] op_sel_hi:[1,0]\n"
      "v_pk_mul_f32 v[24:25], v[24:25], s[54:55] op_sel_hi:[1,0]\n"
      "v_pk_mul_f32 v[26:27], v[26:27], s[54:55] op_sel_hi:[1,0]\n"
      "v_cvt_pk_bf16_f32 v144, v20, v21\n"
      "v_cvt_pk_bf16_f32 v145, v22, v23\n"
      "v_cvt_pk_bf16_f32 v146, v24, v25\n"
      "v_cvt_pk_bf16_f32 v147, v26, v27\n"
      "v_accvgpr_write_b32 a248, v144\n"
      "v_accvgpr_write_b32 a249, v145\n"
      "v_accvgpr_write_b32 a250, v146\n"
      "v_accvgpr_write_b32 a251, v147\n"
      "v_lshlrev_b32 v20, 16, v148\n"
      "v_and_b32 v21, s97, v148\n"
      "v_lshlrev_b32 v22, 16, v149\n"
      "v_and_b32 v23, s97, v149\n"
      "v_lshlrev_b32 v24, 16, v150\n"
      "v_and_b32 v25, s97, v150\n"
      "v_lshlrev_b32 v26, 16, v151\n"
      "v_and_b32 v27, s97, v151\n"
      "v_pk_mul_f32 v[20:21], v[20:21], s[54:55] op_sel_hi:[1,0]\n"
      "v_pk_mul_f32 v[22:23], v[22:23], s[54:55] op_sel_hi:[1,0]\n"
      "v_pk_mul_f32 v[24:25], v[24:25], s[54:55] op_sel_hi:[1,0]\n"
      "v_pk_mul_f32 v[26:27], v[26:27], s[54:55] op_sel_hi:[1,0]\n"
      "v_cvt_pk_bf16_f32 v148, v20, v21\n"
      "v_cvt_pk_bf16_f32 v149, v22, v23\n"
      "v_cvt_pk_bf16_f32 v150, v24, v25\n"
      "v_cvt_pk_bf16_f32 v151, v26, v27\n"
      "v_accvgpr_write_b32 a252, v148\n"
      "v_accvgpr_write_b32 a253, v149\n"
      "v_accvgpr_write_b32 a254, v150\n"
      "v_accvgpr_write_b32 a255, v151\n"
      "s_nop 1\n"
      "v_mfma_f32_16x16x32_bf16 v[120:123], v[32:35], a[240:243], 0\n"
      "v_mfma_f32_16x16x32_bf16 v[120:123], v[36:39], a[244:247], v[120:123]\n"
      "v_mfma_f32_16x16x32_bf16 v[120:123], v[40:43], a[248:251], v[120:123]\n"
      "v_mfma_f32_16x16x32_bf16 v[120:123], v[44:47], a[252:255], v[120:123]\n"
      "v_mfma_f32_16x16x32_bf16 v[124:127], v[48:51], a[240:243], 0\n"
      "v_mfma_f32_16x16x32_bf16 v[124:127], v[52:55], a[244:247], v[124:127]\n"
      "v_mfma_f32_16x16x32_bf16 v[124:127], v[56:59], a[248:251], v[124:127]\n"
      "v_mfma_f32_16x16x32_bf16 v[124:127], v[60:63], a[252:255], v[124:127]\n"
      "s_nop 3\n"
      "s_waitcnt vmcnt(12)\n"
      "ds_read_b128 v[152:155], v248 offset:16384\n"
      "ds_read_b128 v[156:159], v249 offset:16384\n"
      "ds_read_b128 v[160:163], v250 offset:16384\n"
      "ds_read_b128 v[164:167], v251 offset:16384\n"
      "s_mov_b32 s58, 0x9000\n"
      "s_mov_b32 m0, 0x9000\n"
      "s_nop 0\n"
      "buffer_load_dwordx4 v6, s[40:43], s58 offen offset:0 lds\n"
      "buffer_load_dwordx4 v7, s[40:43], s58 offen offset:1024 lds\n"
      "buffer_load_dwordx4 v8, s[40:43], s58 offen offset:2048 lds\n"
      "buffer_load_dwordx4 v9, s[40:43], s58 offen offset:3072 lds\n"
      "v_max3_f32 v28, v120, v121, v122\n"
      "v_max3_f32 v29, v123, v124, v125\n"
      "v_max3_f32 v28, v28, v126, v127\n"
      "v_max_f32 v28, v28, v29\n"
      "v_mov_b32 v29, v28\n"
      "s_nop 1\n"
      "v_permlane16_swap_b32 v28, v29\n"
      "s_nop 0\n"
      "s_waitcnt lgkmcnt(0)\n"
      "v_max_f32 v28, v28, v29\n"
      "v_mov_b32 v29, v28\n"
      "v_lshlrev_b32 v20, 16, v152\n"
      "v_and_b32 v21, s97, v152\n"
      "v_permlane32_swap_b32 v28, v29\n"
      "v_lshlrev_b32 v22, 16, v153\n"
      "v_max_f32 v28, v28, v29\n"
      "v_sub_f32 v204, 0, v28\n"
      "v_mov_b32 v205, v204\n"
      "v_mov_b32 v206, v204\n"
      "v_mov_b32 v207, v204\n"
      "v_add_f32 v120, v120, v204\n"
      "v_add_f32 v121, v121, v204\n"
      "v_add_f32 v122, v122, v204\n"
      "v_add_f32 v123, v123, v204\n"
      "v_add_f32 v124, v124, v204\n"
      "v_add_f32 v125, v125, v204\n"
      "v_add_f32 v126, v126, v204\n"
      "v_add_f32 v127, v127, v204\n"
      "v_and_b32 v23, s97, v153\n"
      "v_lshlrev_b32 v24, 16, v154\n"
      "v_and_b32 v25, s97, v154\n"
      "v_lshlrev_b32 v26, 16, v155\n"
      "v_and_b32 v27, s97, v155\n"
      "v_pk_mul_f32 v[20:21], v[20:21], s[54:55] op_sel_hi:[1,0]\n"
      "v_pk_mul_f32 v[22:23], v[22:23], s[54:55] op_sel_hi:[1,0]\n"
      "v_pk_mul_f32 v[24:25], v[24:25], s[54:55] op_sel_hi:[1,0]\n"
      "v_pk_mul_f32 v[26:27], v[26:27], s[54:55] op_sel_hi:[1,0]\n"
      "v_cvt_pk_bf16_f32 v216, v20, v21\n"
      "v_cvt_pk_bf16_f32 v217, v22, v23\n"
      "v_cvt_pk_bf16_f32 v218, v24, v25\n"
      "v_cvt_pk_bf16_f32 v219, v26, v27\n"
      "v_lshlrev_b32 v20, 16, v156\n"
      "v_and_b32 v21, s97, v156\n"
      "v_lshlrev_b32 v22, 16, v157\n"
      "v_and_b32 v23, s97, v157\n"
      "v_lshlrev_b32 v24, 16, v158\n"
      "v_and_b32 v25, s97, v158\n"
      "v_lshlrev_b32 v26, 16, v159\n"
      "v_and_b32 v27, s97, v159\n"
      "v_pk_mul_f32 v[20:21], v[20:21], s[54:55] op_sel_hi:[1,0]\n"
      "v_pk_mul_f32 v[22:23], v[22:23], s[54:55] op_sel_hi:[1,0]\n"
      "v_pk_mul_f32 v[24:25], v[24:25], s[54:55] op_sel_hi:[1,0]\n"
      "v_pk_mul_f32 v[26:27], v[26:27], s[54:55] op_sel_hi:[1,0]\n"
      "v_cvt_pk_bf16_f32 v220, v20, v21\n"
      "v_cvt_pk_bf16_f32 v221, v22, v23\n"
      "v_cvt_pk_bf16_f32 v222, v24, v25\n"
      "v_cvt_pk_bf16_f32 v223, v26, v27\n"
      "v_lshlrev_b32 v20, 16, v160\n"
      "v_and_b32 v21, s97, v160\n"
      "v_lshlrev_b32 v22, 16, v161\n"
      "v_and_b32 v23, s97, v161\n"
      "v_lshlrev_b32 v24, 16, v162\n"
      "v_and_b32 v25, s97, v162\n"
      "v_lshlrev_b32 v26, 16, v163\n"
      "v_and_b32 v27, s97, v163\n"
      "v_pk_mul_f32 v[20:21], v[20:21], s[54:55] op_sel_hi:[1,0]\n"
      "v_pk_mul_f32 v[22:23], v[22:23], s[54:55] op_sel_hi:[1,0]\n"
      "v_pk_mul_f32 v[24:25], v[24:25], s[54:55] op_sel_hi:[1,0]\n"
      "v_pk_mul_f32 v[26:27], v[26:27], s[54:55] op_sel_hi:[1,0]\n"
      "v_cvt_pk_bf16_f32 v224, v20, v21\n"
      "v_cvt_pk_bf16_f32 v225, v22, v23\n"
      "v_cvt_pk_bf16_f32 v226, v24, v25\n"
      "v_cvt_pk_bf16_f32 v227, v26, v27\n"
      "v_lshlrev_b32 v20, 16, v164\n"
      "v_and_b32 v21, s97, v164\n"
      "v_lshlrev_b32 v22, 16, v165\n"
      "v_and_b32 v23, s97, v165\n"
      "v_lshlrev_b32 v24, 16, v166\n"
      "v_and_b32 v25, s97, v166\n"
      "v_lshlrev_b32 v26, 16, v167\n"
      "v_and_b32 v27, s97, v167\n"
      "v_pk_mul_f32 v[20:21], v[20:21], s[54:55] op_sel_hi:[1,0]\n"
      "v_pk_mul_f32 v[22:23], v[22:23], s[54:55] op_sel_hi:[1,0]\n"
      "v_pk_mul_f32 v[24:25], v[24:25], s[54:55] op_sel_hi:[1,0]\n"
      "v_pk_mul_f32 v[26:27], v[26:27], s[54:55] op_sel_hi:[1,0]\n"
      "v_cvt_pk_bf16_f32 v228, v20, v21\n"
      "v_cvt_pk_bf16_f32 v229, v22, v23\n"
      "v_cvt_pk_bf16_f32 v230, v24, v25\n"
      "v_cvt_pk_bf16_f32 v231, v26, v27\n"
      "s_nop 1\n"
      "v_mfma_f32_16x16x32_bf16 v[128:131], v[32:35], v[216:219], 0\n"
      "v_mfma_f32_16x16x32_bf16 v[128:131], v[36:39], v[220:223], v[128:131]\n"
      "v_mfma_f32_16x16x32_bf16 v[128:131], v[40:43], v[224:227], v[128:131]\n"
      "v_mfma_f32_16x16x32_bf16 v[128:131], v[44:47], v[228:231], v[128:131]\n"
      "v_mfma_f32_16x16x32_bf16 v[132:135], v[48:51], v[216:219], 0\n"
      "v_mfma_f32_16x16x32_bf16 v[132:135], v[52:55], v[220:223], v[132:135]\n"
      "v_mfma_f32_16x16x32_bf16 v[132:135], v[56:59], v[224:227], v[132:135]\n"
      "v_mfma_f32_16x16x32_bf16 v[132:135], v[60:63], v[228:231], v[132:135]\n"
      "s_nop 3\n"
      "s_nop 3\n"
      "v_max3_f32 v28, v128, v129, v130\n"
      "v_max3_f32 v29, v131, v132, v133\n"
      "v_max3_f32 v28, v28, v134, v135\n"
      "v_max_f32 v28, v28, v29\n"
      "v_mov_b32 v29, v28\n"
      "s_nop 1\n"
      "v_permlane16_swap_b32 v28, v29\n"
      "s_nop 0\n"
      "v_max_f32 v28, v28, v29\n"
      "v_mov_b32 v29, v28\n"
      "s_nop 1\n"
      "v_permlane32_swap_b32 v28, v29\n"
      "s_nop 0\n"
      "v_max_f32 v28, v28, v29\n"
      "v_sub_f32 v208, 0, v28\n"
      "v_mov_b32 v209, v208\n"
      "v_mov_b32 v210, v208\n"
      "v_mov_b32 v211, v208\n"
      "v_add_f32 v128, v128, v208\n"
      "v_add_f32 v129, v129, v208\n"
      "v_add_f32 v130, v130, v208\n"
      "v_add_f32 v131, v131, v208\n"
      "v_add_f32 v132, v132, v208\n"
      "v_add_f32 v133, v133, v208\n"
      "v_add_f32 v134, v134, v208\n"
      "v_add_f32 v135, v135, v208\n"
      "L_restart_w3:\n"
      "s_mov_b32 s55, 0xb000\n"
      "s_mov_b32 s56, 0\n"
      "s_mov_b32 s57, 16\n"
      "s_waitcnt vmcnt(12)\n"
      "s_barrier\n"
      "s_xor_b32 m0, s56, 0xb000\n"
      "s_nop 0\n"
      "buffer_load_dwordx4 v6, s[40:43], s55 offen offset:0 lds\n"
      "buffer_load_dwordx4 v7, s[40:43], s55 offen offset:1024 lds\n"
      "buffer_load_dwordx4 v8, s[40:43], s55 offen offset:2048 lds\n"
      "v_exp_f32 v96, v96\n"
      "v_exp_f32 v97, v97\n"
      "v_add_f32 v14, v14, v96\n"
      "v_add_f32 v14, v14, v97\n"
      "v_cvt_pk_bf16_f32 v96, v96, v97\n"
      "v_exp_f32 v98, v98\n"
      "s_nop 0\n"
      "v_add_f32 v14, v14, v98\n"
      "buffer_load_dwordx4 v9, s[40:43], s55 offen offset:3072 lds\n"
      "v_exp_f32 v99, v99\n"
      "s_add_u32 s55, s55, 0x2000\n"
      "v_add_f32 v14, v14, v99\n"
      "v_cvt_pk_bf16_f32 v97, v98, v99\n"
      "v_exp_f32 v100, v100\n"
      "v_exp_f32 v101, v101\n"
      "v_add_f32 v14, v14, v100\n"
      "v_add_f32 v14, v14, v101\n"
      "v_cvt_pk_bf16_f32 v98, v100, v101\n"
      "v_exp_f32 v102, v102\n"
      "v_exp_f32 v103, v103\n"
      "v_add_f32 v14, v14, v102\n"
      "v_add_f32 v14, v14, v103\n"
      "v_cvt_pk_bf16_f32 v99, v102, v103\n"
      "v_exp_f32 v104, v104\n"
      "v_exp_f32 v105, v105\n"
      "v_add_f32 v15, v15, v104\n"
      "v_add_f32 v15, v15, v105\n"
      "v_cvt_pk_bf16_f32 v104, v104, v105\n"
      "v_exp_f32 v106, v106\n"
      "ds_read_b128 v[32:35], v0 offset:8192\n"
      "v_add_f32 v15, v15, v106\n"
      "s_xor_b32 m0, s56, 0xd000\n"
      "s_nop 0\n"
      "buffer_load_dwordx4 v6, s[40:43], s55 offen offset:0 lds\n"
      "v_exp_f32 v107, v107\n"
      "v_exp_f32 v108, v108\n"
      "v_add_f32 v15, v15, v107\n"
      "v_cvt_pk_bf16_f32 v105, v106, v107\n"
      "v_add_f32 v15, v15, v108\n"
      "v_exp_f32 v109, v109\n"
      "v_exp_f32 v110, v110\n"
      "v_add_f32 v15, v15, v109\n"
      "v_cvt_pk_bf16_f32 v106, v108, v109\n"
      "ds_read_b128 v[36:39], v1 offset:8192\n"
      "v_add_f32 v15, v15, v110\n"
      "buffer_load_dwordx4 v7, s[40:43], s55 offen offset:1024 lds\n"
      "v_exp_f32 v111, v111\n"
      "v_exp_f32 v112, v112\n"
      "v_add_f32 v15, v15, v111\n"
      "v_cvt_pk_bf16_f32 v107, v110, v111\n"
      "ds_read_b128 v[40:43], v2 offset:8192\n"
      "v_add_f32 v16, v16, v112\n"
      "v_exp_f32 v113, v113\n"
      "v_exp_f32 v114, v114\n"
      "v_add_f32 v16, v16, v113\n"
      "v_cvt_pk_bf16_f32 v112, v112, v113\n"
      "ds_read_b128 v[44:47], v3 offset:8192\n"
      "v_add_f32 v16, v16, v114\n"
      "buffer_load_dwordx4 v8, s[40:43], s55 offen offset:2048 lds\n"
      "v_exp_f32 v115, v115\n"
      "v_exp_f32 v116, v116\n"
      "v_add_f32 v16, v16, v115\n"
      "v_cvt_pk_bf16_f32 v113, v114, v115\n"
      "ds_read_b128 v[48:51], v0 offset:12288\n"
      "v_add_f32 v16, v16, v116\n"
      "v_exp_f32 v117, v117\n"
      "v_exp_f32 v118, v118\n"
      "v_add_f32 v16, v16, v117\n"
      "v_cvt_pk_bf16_f32 v114, v116, v117\n"
      "v_add_f32 v16, v16, v118\n"
      "buffer_load_dwordx4 v9, s[40:43], s55 offen offset:3072 lds\n"
      "v_exp_f32 v119, v119\n"
      "s_add_u32 s55, s55, 0x2000\n"
      "v_add_f32 v16, v16, v119\n"
      "v_cvt_pk_bf16_f32 v115, v118, v119\n"
      "v_exp_f32 v120, v120\n"
      "ds_read_b128 v[52:55], v1 offset:12288\n"
      "v_add_f32 v17, v17, v120\n"
      "v_exp_f32 v121, v121\n"
      "v_exp_f32 v122, v122\n"
      "v_add_f32 v17, v17, v121\n"
      "v_cvt_pk_bf16_f32 v120, v120, v121\n"
      "ds_read_b128 v[56:59], v2 offset:12288\n"
      "v_add_f32 v17, v17, v122\n"
      "v_exp_f32 v123, v123\n"
      "v_exp_f32 v124, v124\n"
      "v_add_f32 v17, v17, v123\n"
      "v_cvt_pk_bf16_f32 v121, v122, v123\n"
      "ds_read_b128 v[60:63], v3 offset:12288\n"
      "v_add_f32 v17, v17, v124\n"
      "v_exp_f32 v125, v125\n"
      "v_exp_f32 v126, v126\n"
      "v_add_f32 v17, v17, v125\n"
      "v_cvt_pk_bf16_f32 v122, v124, v125\n"
      "v_add_f32 v17, v17, v126\n"
      "v_exp_f32 v127, v127\n"
      "v_exp_f32 v128, v128\n"
      "v_add_f32 v17, v17, v127\n"
      "v_cvt_pk_bf16_f32 v123, v126, v127\n"
      "v_add_f32 v18, v18, v128\n"
      "v_exp_f32 v129, v129\n"
      "s_nop 0\n"
      "v_add_f32 v18, v18, v129\n"
      "v_cvt_pk_bf16_f32 v128, v128, v129\n"
      "s_waitcnt lgkmcnt(0)\n"
      "s_waitcnt vmcnt(16)\n"
      "s_barrier\n"
      "s_branch L_entry_w3\n"
      "L_loop_w3:\n"
      "v_mfma_f32_16x16x32_bf16 v[96:99], v[32:35], a[192:195], v[192:195]\n"
      "v_exp_f32 v171, v171\n"
      "v_mfma_f32_16x16x32_bf16 v[96:99], v[36:39], a[196:199], v[96:99]\n"
      "v_add_f32 v18, v18, v171\n"
      "v_cvt_pk_bf16_f32 v169, v170, v171\n"
      "v_mfma_f32_16x16x32_bf16 v[96:99], v[40:43], a[200:203], v[96:99]\n"
      "v_exp_f32 v172, v172\n"
      "v_mfma_f32_16x16x32_bf16 v[96:99], v[44:47], a[204:207], v[96:99]\n"
      "ds_read_b64_tr_b16 v[64:65], v4 offset:24576\n"
      "v_add_f32 v18, v18, v172\n"
      "v_mfma_f32_16x16x32_bf16 v[100:103], v[48:51], a[192:195], v[192:195]\n"
      "v_exp_f32 v173, v173\n"
      "v_mfma_f32_16x16x32_bf16 v[100:103], v[52:55], a[196:199], v[100:103]\n"
      "v_add_f32 v18, v18, v173\n"
      "v_cvt_pk_bf16_f32 v170, v172, v173\n"
      "v_mfma_f32_16x16x32_bf16 v[100:103], v[56:59], a[200:203], v[100:103]\n"
      "v_exp_f32 v174, v174\n"
      "v_mfma_f32_16x16x32_bf16 v[100:103], v[60:63], a[204:207], v[100:103]\n"
      "ds_read_b64_tr_b16 v[66:67], v4 offset:28672\n"
      "v_add_f32 v18, v18, v174\n"
      "v_mfma_f32_16x16x32_bf16 v[104:107], v[32:35], a[208:211], v[196:199]\n"
      "v_exp_f32 v175, v175\n"
      "v_mfma_f32_16x16x32_bf16 v[104:107], v[36:39], a[212:215], v[104:107]\n"
      "v_add_f32 v18, v18, v175\n"
      "v_cvt_pk_bf16_f32 v171, v174, v175\n"
      "v_mfma_f32_16x16x32_bf16 v[104:107], v[40:43], a[216:219], v[104:107]\n"
      "v_exp_f32 v176, v176\n"
      "v_mfma_f32_16x16x32_bf16 v[104:107], v[44:47], a[220:223], v[104:107]\n"
      "ds_read_b64_tr_b16 v[68:69], v5 offset:24576\n"
      "v_add_f32 v19, v19, v176\n"
      "v_mfma_f32_16x16x32_bf16 v[108:111], v[48:51], a[208:211], v[196:199]\n"
      "v_exp_f32 v177, v177\n"
      "v_mfma_f32_16x16x32_bf16 v[108:111], v[52:55], a[212:215], v[108:111]\n"
      "v_add_f32 v19, v19, v177\n"
      "v_cvt_pk_bf16_f32 v176, v176, v177\n"
      "v_mfma_f32_16x16x32_bf16 v[108:111], v[56:59], a[216:219], v[108:111]\n"
      "v_exp_f32 v178, v178\n"
      "v_mfma_f32_16x16x32_bf16 v[108:111], v[60:63], a[220:223], v[108:111]\n"
      "ds_read_b64_tr_b16 v[70:71], v5 offset:28672\n"
      "v_add_f32 v19, v19, v178\n"
      "v_mfma_f32_16x16x32_bf16 v[112:115], v[32:35], a[224:227], v[200:203]\n"
      "v_exp_f32 v179, v179\n"
      "v_mfma_f32_16x16x32_bf16 v[112:115], v[36:39], a[228:231], v[112:115]\n"
      "v_add_f32 v19, v19, v179\n"
      "v_cvt_pk_bf16_f32 v177, v178, v179\n"
      "v_mfma_f32_16x16x32_bf16 v[112:115], v[40:43], a[232:235], v[112:115]\n"
      "ds_read_b64_tr_b16 v[72:73], v4 offset:24832\n"
      "ds_read_b64_tr_b16 v[74:75], v4 offset:28928\n"
      "v_mfma_f32_16x16x32_bf16 v[112:115], v[44:47], a[236:239], v[112:115]\n"
      "ds_read_b64_tr_b16 v[76:77], v5 offset:24832\n"
      "ds_read_b64_tr_b16 v[78:79], v5 offset:28928\n"
      "v_mfma_f32_16x16x32_bf16 v[116:119], v[48:51], a[224:227], v[200:203]\n"
      "v_exp_f32 v96, v96\n"
      "v_mfma_f32_16x16x32_bf16 v[116:119], v[52:55], a[228:231], v[116:119]\n"
      "ds_read_b64_tr_b16 v[80:81], v4 offset:25088\n"
      "v_add_f32 v14, v14, v96\n"
      "v_mfma_f32_16x16x32_bf16 v[116:119], v[56:59], a[232:235], v[116:119]\n"
      "v_exp_f32 v97, v97\n"
      "v_mfma_f32_16x16x32_bf16 v[116:119], v[60:63], a[236:239], v[116:119]\n"
      "v_add_f32 v14, v14, v97\n"
      "v_cvt_pk_bf16_f32 v96, v96, v97\n"
      "v_mfma_f32_16x16x32_bf16 v[120:123], v[32:35], a[240:243], v[204:207]\n"
      "v_exp_f32 v98, v98\n"
      "v_mfma_f32_16x16x32_bf16 v[120:123], v[36:39], a[244:247], v[120:123]\n"
      "ds_read_b64_tr_b16 v[82:83], v4 offset:29184\n"
      "v_add_f32 v14, v14, v98\n"
      "v_mfma_f32_16x16x32_bf16 v[120:123], v[40:43], a[248:251], v[120:123]\n"
      "v_exp_f32 v99, v99\n"
      "v_mfma_f32_16x16x32_bf16 v[120:123], v[44:47], a[252:255], v[120:123]\n"
      "v_add_f32 v14, v14, v99\n"
      "v_cvt_pk_bf16_f32 v97, v98, v99\n"
      "v_mfma_f32_16x16x32_bf16 v[124:127], v[48:51], a[240:243], v[204:207]\n"
      "v_exp_f32 v100, v100\n"
      "v_mfma_f32_16x16x32_bf16 v[124:127], v[52:55], a[244:247], v[124:127]\n"
      "ds_read_b64_tr_b16 v[84:85], v5 offset:25088\n"
      "ds_read_b64_tr_b16 v[86:87], v5 offset:29184\n"
      "v_add_f32 v14, v14, v100\n"
      "v_mfma_f32_16x16x32_bf16 v[124:127], v[56:59], a[248:251], v[124:127]\n"
      "v_exp_f32 v101, v101\n"
      "v_mfma_f32_16x16x32_bf16 v[124:127], v[60:63], a[252:255], v[124:127]\n"
      "v_add_f32 v14, v14, v101\n"
      "v_cvt_pk_bf16_f32 v98, v100, v101\n"
      "v_mfma_f32_16x16x32_bf16 v[128:131], v[32:35], v[216:219], v[208:211]\n"
      "v_exp_f32 v102, v102\n"
      "v_mfma_f32_16x16x32_bf16 v[128:131], v[36:39], v[220:223], v[128:131]\n"
      "ds_read_b64_tr_b16 v[88:89], v4 offset:25344\n"
      "ds_read_b64_tr_b16 v[90:91], v4 offset:29440\n"
      "v_add_f32 v14, v14, v102\n"
      "v_mfma_f32_16x16x32_bf16 v[128:131], v[40:43], v[224:227], v[128:131]\n"
      "v_exp_f32 v103, v103\n"
      "v_mfma_f32_16x16x32_bf16 v[128:131], v[44:47], v[228:231], v[128:131]\n"
      "v_add_f32 v14, v14, v103\n"
      "v_cvt_pk_bf16_f32 v99, v102, v103\n"
      "v_mfma_f32_16x16x32_bf16 v[132:135], v[48:51], v[216:219], v[208:211]\n"
      "v_exp_f32 v104, v104\n"
      "v_mfma_f32_16x16x32_bf16 v[132:135], v[52:55], v[220:223], v[132:135]\n"
      "ds_read_b64_tr_b16 v[92:93], v5 offset:25344\n"
      "ds_read_b64_tr_b16 v[94:95], v5 offset:29440\n"
      "v_add_f32 v15, v15, v104\n"
      "v_xor_b32 v4, 0x8000, v4\n"
      "v_mfma_f32_16x16x32_bf16 v[132:135], v[56:59], v[224:227], v[132:135]\n"
      "v_exp_f32 v105, v105\n"
      "v_xor_b32 v5, 0x8000, v5\n"
      "v_mfma_f32_16x16x32_bf16 v[132:135], v[60:63], v[228:231], v[132:135]\n"
      "v_add_f32 v15, v15, v105\n"
      "v_cvt_pk_bf16_f32 v104, v104, v105\n"
      "s_waitcnt lgkmcnt(0)\n"
      "v_mfma_f32_16x16x32_bf16 a[0:3], v[136:139], v[64:67], a[0:3]\n"
      "v_exp_f32 v106, v106\n"
      "v_mfma_f32_16x16x32_bf16 a[4:7], v[136:139], v[68:71], a[4:7]\n"
      "ds_read_b128 v[32:35], v0 offset:8192\n"
      "v_add_f32 v15, v15, v106\n"
      "v_mfma_f32_16x16x32_bf16 a[8:11], v[136:139], v[72:75], a[8:11]\n"
      "v_exp_f32 v107, v107\n"
      "v_mfma_f32_16x16x32_bf16 a[12:15], v[136:139], v[76:79], a[12:15]\n"
      "v_add_f32 v15, v15, v107\n"
      "v_cvt_pk_bf16_f32 v105, v106, v107\n"
      "v_mfma_f32_16x16x32_bf16 a[16:19], v[136:139], v[80:83], a[16:19]\n"
      "v_exp_f32 v108, v108\n"
      "v_mfma_f32_16x16x32_bf16 a[20:23], v[136:139], v[84:87], a[20:23]\n"
      "v_add_f32 v15, v15, v108\n"
      "v_mfma_f32_16x16x32_bf16 a[24:27], v[136:139], v[88:91], a[24:27]\n"
      "v_exp_f32 v109, v109\n"
      "v_mfma_f32_16x16x32_bf16 a[28:31], v[136:139], v[92:95], a[28:31]\n"
      "v_add_f32 v15, v15, v109\n"
      "v_cvt_pk_bf16_f32 v106, v108, v109\n"
      "v_mfma_f32_16x16x32_bf16 a[32:35], v[144:147], v[64:67], a[32:35]\n"
      "v_exp_f32 v110, v110\n"
      "v_mfma_f32_16x16x32_bf16 a[36:39], v[144:147], v[68:71], a[36:39]\n"
      "ds_read_b128 v[36:39], v1 offset:8192\n"
      "v_add_f32 v15, v15, v110\n"
      "v_mfma_f32_16x16x32_bf16 a[40:43], v[144:147], v[72:75], a[40:43]\n"
      "v_exp_f32 v111, v111\n"
      "v_mfma_f32_16x16x32_bf16 a[44:47], v[144:147], v[76:79], a[44:47]\n"
      "v_add_f32 v15, v15, v111\n"
      "v_cvt_pk_bf16_f32 v107, v110, v111\n"
      "s_xor_b32 m0, s56, 0xd000\n"
      "v_mfma_f32_16x16x32_bf16 a[48:51], v[144:147], v[80:83], a[48:51]\n"
      "buffer_load_dwordx4 v6, s[40:43], s55 offen offset:0 lds\n"
      "v_exp_f32 v112, v112\n"
      "v_mfma_f32_16x16x32_bf16 a[52:55], v[144:147], v[84:87], a[52:55]\n"
      "ds_read_b128 v[40:43], v2 offset:8192\n"
      "v_add_f32 v16, v16, v112\n"
      "v_mfma_f32_16x16x32_bf16 a[56:59], v[144:147], v[88:91], a[56:59]\n"
      "v_exp_f32 v113, v113\n"
      "v_mfma_f32_16x16x32_bf16 a[60:63], v[144:147], v[92:95], a[60:63]\n"
      "v_add_f32 v16, v16, v113\n"
      "v_cvt_pk_bf16_f32 v112, v112, v113\n"
      "v_mfma_f32_16x16x32_bf16 a[64:67], v[152:155], v[64:67], a[64:67]\n"
      "v_exp_f32 v114, v114\n"
      "v_mfma_f32_16x16x32_bf16 a[68:71], v[152:155], v[68:71], a[68:71]\n"
      "ds_read_b128 v[44:47], v3 offset:8192\n"
      "v_add_f32 v16, v16, v114\n"
      "v_mfma_f32_16x16x32_bf16 a[72:75], v[152:155], v[72:75], a[72:75]\n"
      "v_exp_f32 v115, v115\n"
      "v_mfma_f32_16x16x32_bf16 a[76:79], v[152:155], v[76:79], a[76:79]\n"
      "v_add_f32 v16, v16, v115\n"
      "v_cvt_pk_bf16_f32 v113, v114, v115\n"
      "v_mfma_f32_16x16x32_bf16 a[80:83], v[152:155], v[80:83], a[80:83]\n"
      "buffer_load_dwordx4 v7, s[40:43], s55 offen offset:1024 lds\n"
      "v_exp_f32 v116, v116\n"
      "v_mfma_f32_16x16x32_bf16 a[84:87], v[152:155], v[84:87], a[84:87]\n"
      "ds_read_b128 v[48:51], v0 offset:12288\n"
      "v_add_f32 v16, v16, v116\n"
      "v_mfma_f32_16x16x32_bf16 a[88:91], v[152:155], v[88:91], a[88:91]\n"
      "v_exp_f32 v117, v117\n"
      "v_mfma_f32_16x16x32_bf16 a[92:95], v[152:155], v[92:95], a[92:95]\n"
      "v_add_f32 v16, v16, v117\n"
      "v_cvt_pk_bf16_f32 v114, v116, v117\n"
      "v_mfma_f32_16x16x32_bf16 a[96:99], v[160:163], v[64:67], a[96:99]\n"
      "v_exp_f32 v118, v118\n"
      "v_mfma_f32_16x16x32_bf16 a[100:103], v[160:163], v[68:71], a[100:103]\n"
      "v_add_f32 v16, v16, v118\n"
      "v_mfma_f32_16x16x32_bf16 a[104:107], v[160:163], v[72:75], a[104:107]\n"
      "v_exp_f32 v119, v119\n"
      "v_mfma_f32_16x16x32_bf16 a[108:111], v[160:163], v[76:79], a[108:111]\n"
      "v_add_f32 v16, v16, v119\n"
      "v_cvt_pk_bf16_f32 v115, v118, v119\n"
      "v_mfma_f32_16x16x32_bf16 a[112:115], v[160:163], v[80:83], a[112:115]\n"
      "buffer_load_dwordx4 v8, s[40:43], s55 offen offset:2048 lds\n"
      "v_exp_f32 v120, v120\n"
      "v_mfma_f32_16x16x32_bf16 a[116:119], v[160:163], v[84:87], a[116:119]\n"
      "ds_read_b128 v[52:55], v1 offset:12288\n"
      "v_add_f32 v17, v17, v120\n"
      "v_mfma_f32_16x16x32_bf16 a[120:123], v[160:163], v[88:91], a[120:123]\n"
      "v_exp_f32 v121, v121\n"
      "v_mfma_f32_16x16x32_bf16 a[124:127], v[160:163], v[92:95], a[124:127]\n"
      "v_add_f32 v17, v17, v121\n"
      "v_cvt_pk_bf16_f32 v120, v120, v121\n"
      "v_mfma_f32_16x16x32_bf16 a[128:131], v[168:171], v[64:67], a[128:131]\n"
      "v_exp_f32 v122, v122\n"
      "v_mfma_f32_16x16x32_bf16 a[132:135], v[168:171], v[68:71], a[132:135]\n"
      "ds_read_b128 v[56:59], v2 offset:12288\n"
      "v_add_f32 v17, v17, v122\n"
      "v_mfma_f32_16x16x32_bf16 a[136:139], v[168:171], v[72:75], a[136:139]\n"
      "v_exp_f32 v123, v123\n"
      "v_mfma_f32_16x16x32_bf16 a[140:143], v[168:171], v[76:79], a[140:143]\n"
      "v_add_f32 v17, v17, v123\n"
      "v_cvt_pk_bf16_f32 v121, v122, v123\n"
      "v_mfma_f32_16x16x32_bf16 a[144:147], v[168:171], v[80:83], a[144:147]\n"
      "buffer_load_dwordx4 v9, s[40:43], s55 offen offset:3072 lds\n"
      "v_exp_f32 v124, v124\n"
      "s_add_u32 s55, s55, 0x2000\n"
      "v_mfma_f32_16x16x32_bf16 a[148:151], v[168:171], v[84:87], a[148:151]\n"
      "ds_read_b128 v[60:63], v3 offset:12288\n"
      "v_add_f32 v17, v17, v124\n"
      "v_mfma_f32_16x16x32_bf16 a[152:155], v[168:171], v[88:91], a[152:155]\n"
      "v_exp_f32 v125, v125\n"
      "v_mfma_f32_16x16x32_bf16 a[156:159], v[168:171], v[92:95], a[156:159]\n"
      "v_add_f32 v17, v17, v125\n"
      "v_cvt_pk_bf16_f32 v122, v124, v125\n"
      "v_mfma_f32_16x16x16_bf16 a[160:163], v[176:177], v[66:67], a[160:163]\n"
      "v_exp_f32 v126, v126\n"
      "v_mfma_f32_16x16x16_bf16 a[164:167], v[176:177], v[70:71], a[164:167]\n"
      "v_add_f32 v17, v17, v126\n"
      "v_mfma_f32_16x16x16_bf16 a[168:171], v[176:177], v[74:75], a[168:171]\n"
      "v_exp_f32 v127, v127\n"
      "v_mfma_f32_16x16x16_bf16 a[172:175], v[176:177], v[78:79], a[172:175]\n"
      "v_add_f32 v17, v17, v127\n"
      "v_cvt_pk_bf16_f32 v123, v126, v127\n"
      "v_mfma_f32_16x16x16_bf16 a[176:179], v[176:177], v[82:83], a[176:179]\n"
      "v_exp_f32 v128, v128\n"
      "v_mfma_f32_16x16x16_bf16 a[180:183], v[176:177], v[86:87], a[180:183]\n"
      "v_add_f32 v18, v18, v128\n"
      "v_mfma_f32_16x16x16_bf16 a[184:187], v[176:177], v[90:91], a[184:187]\n"
      "v_exp_f32 v129, v129\n"
      "v_mfma_f32_16x16x16_bf16 a[188:191], v[176:177], v[94:95], a[188:191]\n"
      "v_add_f32 v18, v18, v129\n"
      "v_cvt_pk_bf16_f32 v128, v128, v129\n"
      "s_waitcnt lgkmcnt(0)\n"
      "L_entry_w3:\n"
      "v_mfma_f32_16x16x32_bf16 v[136:139], v[32:35], a[192:195], v[192:195]\n"
      "v_exp_f32 v130, v130\n"
      "v_mfma_f32_16x16x32_bf16 v[136:139], v[36:39], a[196:199], v[136:139]\n"
      "ds_read_b64_tr_b16 v[64:65], v4 offset:0\n"
      "v_add_f32 v18, v18, v130\n"
      "v_mfma_f32_16x16x32_bf16 v[136:139], v[40:43], a[200:203], v[136:139]\n"
      "v_exp_f32 v131, v131\n"
      "v_mfma_f32_16x16x32_bf16 v[136:139], v[44:47], a[204:207], v[136:139]\n"
      "v_add_f32 v18, v18, v131\n"
      "v_cvt_pk_bf16_f32 v129, v130, v131\n"
      "v_mfma_f32_16x16x32_bf16 v[140:143], v[48:51], a[192:195], v[192:195]\n"
      "v_exp_f32 v132, v132\n"
      "v_mfma_f32_16x16x32_bf16 v[140:143], v[52:55], a[196:199], v[140:143]\n"
      "ds_read_b64_tr_b16 v[66:67], v4 offset:4096\n"
      "v_add_f32 v18, v18, v132\n"
      "v_mfma_f32_16x16x32_bf16 v[140:143], v[56:59], a[200:203], v[140:143]\n"
      "v_exp_f32 v133, v133\n"
      "v_mfma_f32_16x16x32_bf16 v[140:143], v[60:63], a[204:207], v[140:143]\n"
      "v_add_f32 v18, v18, v133\n"
      "v_cvt_pk_bf16_f32 v130, v132, v133\n"
      "v_mfma_f32_16x16x32_bf16 v[144:147], v[32:35], a[208:211], v[196:199]\n"
      "v_exp_f32 v134, v134\n"
      "v_mfma_f32_16x16x32_bf16 v[144:147], v[36:39], a[212:215], v[144:147]\n"
      "ds_read_b64_tr_b16 v[68:69], v5 offset:0\n"
      "v_add_f32 v18, v18, v134\n"
      "v_mfma_f32_16x16x32_bf16 v[144:147], v[40:43], a[216:219], v[144:147]\n"
      "v_exp_f32 v135, v135\n"
      "v_mfma_f32_16x16x32_bf16 v[144:147], v[44:47], a[220:223], v[144:147]\n"
      "v_add_f32 v18, v18, v135\n"
      "v_cvt_pk_bf16_f32 v131, v134, v135\n"
      "v_mfma_f32_16x16x32_bf16 v[148:151], v[48:51], a[208:211], v[196:199]\n"
      "ds_read_b64_tr_b16 v[70:71], v5 offset:4096\n"
      "ds_read_b64_tr_b16 v[72:73], v4 offset:256\n"
      "v_mfma_f32_16x16x32_bf16 v[148:151], v[52:55], a[212:215], v[148:151]\n"
      "v_mfma_f32_16x16x32_bf16 v[148:151], v[56:59], a[216:219], v[148:151]\n"
      "v_exp_f32 v136, v136\n"
      "v_mfma_f32_16x16x32_bf16 v[148:151], v[60:63], a[220:223], v[148:151]\n"
      "ds_read_b64_tr_b16 v[74:75], v4 offset:4352\n"
      "v_add_f32 v14, v14, v136\n"
      "v_mfma_f32_16x16x32_bf16 v[152:155], v[32:35], a[224:227], v[200:203]\n"
      "v_exp_f32 v137, v137\n"
      "v_mfma_f32_16x16x32_bf16 v[152:155], v[36:39], a[228:231], v[152:155]\n"
      "v_add_f32 v14, v14, v137\n"
      "v_cvt_pk_bf16_f32 v136, v136, v137\n"
      "v_mfma_f32_16x16x32_bf16 v[152:155], v[40:43], a[232:235], v[152:155]\n"
      "v_exp_f32 v138, v138\n"
      "v_mfma_f32_16x16x32_bf16 v[152:155], v[44:47], a[236:239], v[152:155]\n"
      "ds_read_b64_tr_b16 v[76:77], v5 offset:256\n"
      "v_add_f32 v14, v14, v138\n"
      "v_mfma_f32_16x16x32_bf16 v[156:159], v[48:51], a[224:227], v[200:203]\n"
      "v_exp_f32 v139, v139\n"
      "v_mfma_f32_16x16x32_bf16 v[156:159], v[52:55], a[228:231], v[156:159]\n"
      "v_add_f32 v14, v14, v139\n"
      "v_cvt_pk_bf16_f32 v137, v138, v139\n"
      "v_mfma_f32_16x16x32_bf16 v[156:159], v[56:59], a[232:235], v[156:159]\n"
      "v_exp_f32 v140, v140\n"
      "v_mfma_f32_16x16x32_bf16 v[156:159], v[60:63], a[236:239], v[156:159]\n"
      "ds_read_b64_tr_b16 v[78:79], v5 offset:4352\n"
      "v_add_f32 v14, v14, v140\n"
      "v_mfma_f32_16x16x32_bf16 v[160:163], v[32:35], a[240:243], v[204:207]\n"
      "v_exp_f32 v141, v141\n"
      "v_mfma_f32_16x16x32_bf16 v[160:163], v[36:39], a[244:247], v[160:163]\n"
      "v_add_f32 v14, v14, v141\n"
      "v_cvt_pk_bf16_f32 v138, v140, v141\n"
      "v_mfma_f32_16x16x32_bf16 v[160:163], v[40:43], a[248:251], v[160:163]\n"
      "v_exp_f32 v142, v142\n"
      "v_mfma_f32_16x16x32_bf16 v[160:163], v[44:47], a[252:255], v[160:163]\n"
      "ds_read_b64_tr_b16 v[80:81], v4 offset:512\n"
      "ds_read_b64_tr_b16 v[82:83], v4 offset:4608\n"
      "v_add_f32 v14, v14, v142\n"
      "v_mfma_f32_16x16x32_bf16 v[164:167], v[48:51], a[240:243], v[204:207]\n"
      "v_exp_f32 v143, v143\n"
      "v_mfma_f32_16x16x32_bf16 v[164:167], v[52:55], a[244:247], v[164:167]\n"
      "v_add_f32 v14, v14, v143\n"
      "v_cvt_pk_bf16_f32 v139, v142, v143\n"
      "v_mfma_f32_16x16x32_bf16 v[164:167], v[56:59], a[248:251], v[164:167]\n"
      "v_exp_f32 v144, v144\n"
      "v_mfma_f32_16x16x32_bf16 v[164:167], v[60:63], a[252:255], v[164:167]\n"
      "ds_read_b64_tr_b16 v[84:85], v5 offset:512\n"
      "ds_read_b64_tr_b16 v[86:87], v5 offset:4608\n"
      "v_add_f32 v15, v15, v144\n"
      "v_mfma_f32_16x16x32_bf16 v[168:171], v[32:35], v[216:219], v[208:211]\n"
      "v_exp_f32 v145, v145\n"
      "v_mfma_f32_16x16x32_bf16 v[168:171], v[36:39], v[220:223], v[168:171]\n"
      "v_add_f32 v15, v15, v145\n"
      "v_cvt_pk_bf16_f32 v144, v144, v145\n"
      "v_mfma_f32_16x16x32_bf16 v[168:171], v[40:43], v[224:227], v[168:171]\n"
      "v_exp_f32 v146, v146\n"
      "v_mfma_f32_16x16x32_bf16 v[168:171], v[44:47], v[228:231], v[168:171]\n"
      "ds_read_b64_tr_b16 v[88:89], v4 offset:768\n"
      "ds_read_b64_tr_b16 v[90:91], v4 offset:4864\n"
      "v_add_f32 v15, v15, v146\n"
      "v_mfma_f32_16x16x32_bf16 v[172:175], v[48:51], v[216:219], v[208:211]\n"
      "v_exp_f32 v147, v147\n"
      "v_mfma_f32_16x16x32_bf16 v[172:175], v[52:55], v[220:223], v[172:175]\n"
      "v_add_f32 v15, v15, v147\n"
      "v_cvt_pk_bf16_f32 v145, v146, v147\n"
      "v_mfma_f32_16x16x32_bf16 v[172:175], v[56:59], v[224:227], v[172:175]\n"
      "v_exp_f32 v148, v148\n"
      "v_mfma_f32_16x16x32_bf16 v[172:175], v[60:63], v[228:231], v[172:175]\n"
      "ds_read_b64_tr_b16 v[92:93], v5 offset:768\n"
      "ds_read_b64_tr_b16 v[94:95], v5 offset:4864\n"
      "v_add_f32 v15, v15, v148\n"
      "v_mfma_f32_16x16x32_bf16 v[176:179], v[48:51], v[232:235], v[212:215]\n"
      "v_exp_f32 v149, v149\n"
      "v_mfma_f32_16x16x32_bf16 v[176:179], v[52:55], v[236:239], v[176:179]\n"
      "v_add_f32 v15, v15, v149\n"
      "v_cvt_pk_bf16_f32 v146, v148, v149\n"
      "v_mfma_f32_16x16x32_bf16 v[176:179], v[56:59], v[240:243], v[176:179]\n"
      "v_exp_f32 v150, v150\n"
      "v_mfma_f32_16x16x32_bf16 v[176:179], v[60:63], v[244:247], v[176:179]\n"
      "v_add_f32 v15, v15, v150\n"
      "s_waitcnt lgkmcnt(0)\n"
      "v_mfma_f32_16x16x32_bf16 a[0:3], v[96:99], v[64:67], a[0:3]\n"
      "v_exp_f32 v151, v151\n"
      "v_mfma_f32_16x16x32_bf16 a[4:7], v[96:99], v[68:71], a[4:7]\n"
      "v_add_f32 v15, v15, v151\n"
      "v_cvt_pk_bf16_f32 v147, v150, v151\n"
      "v_mfma_f32_16x16x32_bf16 a[8:11], v[96:99], v[72:75], a[8:11]\n"
      "v_exp_f32 v152, v152\n"
      "v_mfma_f32_16x16x32_bf16 a[12:15], v[96:99], v[76:79], a[12:15]\n"
      "ds_read_b128 v[32:35], v0 offset:16384\n"
      "v_add_f32 v16, v16, v152\n"
      "v_mfma_f32_16x16x32_bf16 a[16:19], v[96:99], v[80:83], a[16:19]\n"
      "v_exp_f32 v153, v153\n"
      "v_mfma_f32_16x16x32_bf16 a[20:23], v[96:99], v[84:87], a[20:23]\n"
      "v_add_f32 v16, v16, v153\n"
      "v_cvt_pk_bf16_f32 v152, v152, v153\n"
      "v_mfma_f32_16x16x32_bf16 a[24:27], v[96:99], v[88:91], a[24:27]\n"
      "v_exp_f32 v154, v154\n"
      "v_mfma_f32_16x16x32_bf16 a[28:31], v[96:99], v[92:95], a[28:31]\n"
      "ds_read_b128 v[36:39], v1 offset:16384\n"
      "v_add_f32 v16, v16, v154\n"
      "s_xor_b32 m0, s56, 0xf000\n"
      "v_mfma_f32_16x16x32_bf16 a[32:35], v[104:107], v[64:67], a[32:35]\n"
      "buffer_load_dwordx4 v6, s[40:43], s55 offen offset:0 nt lds\n"
      "v_exp_f32 v155, v155\n"
      "v_mfma_f32_16x16x32_bf16 a[36:39], v[104:107], v[68:71], a[36:39]\n"
      "v_add_f32 v16, v16, v155\n"
      "v_cvt_pk_bf16_f32 v153, v154, v155\n"
      "v_mfma_f32_16x16x32_bf16 a[40:43], v[104:107], v[72:75], a[40:43]\n"
      "v_exp_f32 v156, v156\n"
      "v_mfma_f32_16x16x32_bf16 a[44:47], v[104:107], v[76:79], a[44:47]\n"
      "ds_read_b128 v[40:43], v2 offset:16384\n"
      "v_add_f32 v16, v16, v156\n"
      "v_mfma_f32_16x16x32_bf16 a[48:51], v[104:107], v[80:83], a[48:51]\n"
      "v_exp_f32 v157, v157\n"
      "v_mfma_f32_16x16x32_bf16 a[52:55], v[104:107], v[84:87], a[52:55]\n"
      "v_add_f32 v16, v16, v157\n"
      "v_cvt_pk_bf16_f32 v154, v156, v157\n"
      "v_mfma_f32_16x16x32_bf16 a[56:59], v[104:107], v[88:91], a[56:59]\n"
      "v_exp_f32 v158, v158\n"
      "v_mfma_f32_16x16x32_bf16 a[60:63], v[104:107], v[92:95], a[60:63]\n"
      "ds_read_b128 v[44:47], v3 offset:16384\n"
      "v_add_f32 v16, v16, v158\n"
      "v_mfma_f32_16x16x32_bf16 a[64:67], v[112:115], v[64:67], a[64:67]\n"
      "buffer_load_dwordx4 v7, s[40:43], s55 offen offset:1024 nt lds\n"
      "v_exp_f32 v159, v159\n"
      "v_mfma_f32_16x16x32_bf16 a[68:71], v[112:115], v[68:71], a[68:71]\n"
      "v_add_f32 v16, v16, v159\n"
      "v_cvt_pk_bf16_f32 v155, v158, v159\n"
      "v_mfma_f32_16x16x32_bf16 a[72:75], v[112:115], v[72:75], a[72:75]\n"
      "v_exp_f32 v160, v160\n"
      "v_mfma_f32_16x16x32_bf16 a[76:79], v[112:115], v[76:79], a[76:79]\n"
      "ds_read_b128 v[48:51], v0 offset:20480\n"
      "v_add_f32 v17, v17, v160\n"
      "v_mfma_f32_16x16x32_bf16 a[80:83], v[112:115], v[80:83], a[80:83]\n"
      "v_exp_f32 v161, v161\n"
      "v_mfma_f32_16x16x32_bf16 a[84:87], v[112:115], v[84:87], a[84:87]\n"
      "v_add_f32 v17, v17, v161\n"
      "v_cvt_pk_bf16_f32 v160, v160, v161\n"
      "v_mfma_f32_16x16x32_bf16 a[88:91], v[112:115], v[88:91], a[88:91]\n"
      "v_exp_f32 v162, v162\n"
      "v_mfma_f32_16x16x32_bf16 a[92:95], v[112:115], v[92:95], a[92:95]\n"
      "ds_read_b128 v[52:55], v1 offset:20480\n"
      "v_add_f32 v17, v17, v162\n"
      "v_mfma_f32_16x16x32_bf16 a[96:99], v[120:123], v[64:67], a[96:99]\n"
      "buffer_load_dwordx4 v8, s[40:43], s55 offen offset:2048 nt lds\n"
      "v_exp_f32 v163, v163\n"
      "v_mfma_f32_16x16x32_bf16 a[100:103], v[120:123], v[68:71], a[100:103]\n"
      "v_add_f32 v17, v17, v163\n"
      "v_cvt_pk_bf16_f32 v161, v162, v163\n"
      "v_mfma_f32_16x16x32_bf16 a[104:107], v[120:123], v[72:75], a[104:107]\n"
      "v_exp_f32 v164, v164\n"
      "v_mfma_f32_16x16x32_bf16 a[108:111], v[120:123], v[76:79], a[108:111]\n"
      "ds_read_b128 v[56:59], v2 offset:20480\n"
      "v_add_f32 v17, v17, v164\n"
      "v_mfma_f32_16x16x32_bf16 a[112:115], v[120:123], v[80:83], a[112:115]\n"
      "v_exp_f32 v165, v165\n"
      "v_mfma_f32_16x16x32_bf16 a[116:119], v[120:123], v[84:87], a[116:119]\n"
      "v_add_f32 v17, v17, v165\n"
      "v_cvt_pk_bf16_f32 v162, v164, v165\n"
      "v_mfma_f32_16x16x32_bf16 a[120:123], v[120:123], v[88:91], a[120:123]\n"
      "v_exp_f32 v166, v166\n"
      "v_mfma_f32_16x16x32_bf16 a[124:127], v[120:123], v[92:95], a[124:127]\n"
      "ds_read_b128 v[60:63], v3 offset:20480\n"
      "v_add_f32 v17, v17, v166\n"
      "v_mfma_f32_16x16x32_bf16 a[128:131], v[128:131], v[64:67], a[128:131]\n"
      "buffer_load_dwordx4 v9, s[40:43], s55 offen offset:3072 nt lds\n"
      "v_exp_f32 v167, v167\n"
      "s_add_u32 s55, s55, 0x2000\n"
      "v_mfma_f32_16x16x32_bf16 a[132:135], v[128:131], v[68:71], a[132:135]\n"
      "v_add_f32 v17, v17, v167\n"
      "v_cvt_pk_bf16_f32 v163, v166, v167\n"
      "v_mfma_f32_16x16x32_bf16 a[136:139], v[128:131], v[72:75], a[136:139]\n"
      "v_exp_f32 v168, v168\n"
      "v_mfma_f32_16x16x32_bf16 a[140:143], v[128:131], v[76:79], a[140:143]\n"
      "v_add_f32 v18, v18, v168\n"
      "v_mfma_f32_16x16x32_bf16 a[144:147], v[128:131], v[80:83], a[144:147]\n"
      "v_exp_f32 v169, v169\n"
      "v_mfma_f32_16x16x32_bf16 a[148:151], v[128:131], v[84:87], a[148:151]\n"
      "v_add_f32 v18, v18, v169\n"
      "v_cvt_pk_bf16_f32 v168, v168, v169\n"
      "v_mfma_f32_16x16x32_bf16 a[152:155], v[128:131], v[88:91], a[152:155]\n"
      "v_exp_f32 v170, v170\n"
      "v_mfma_f32_16x16x32_bf16 a[156:159], v[128:131], v[92:95], a[156:159]\n"
      "v_add_f32 v18, v18, v170\n"
      "s_waitcnt lgkmcnt(0)\n"
      "s_waitcnt vmcnt(12)\n"
      "s_barrier\n"
      "v_mfma_f32_16x16x32_bf16 v[96:99], v[32:35], a[192:195], v[192:195]\n"
      "v_exp_f32 v171, v171\n"
      "v_mfma_f32_16x16x32_bf16 v[96:99], v[36:39], a[196:199], v[96:99]\n"
      "v_add_f32 v18, v18, v171\n"
      "v_cvt_pk_bf16_f32 v169, v170, v171\n"
      "v_mfma_f32_16x16x32_bf16 v[96:99], v[40:43], a[200:203], v[96:99]\n"
      "v_exp_f32 v172, v172\n"
      "v_mfma_f32_16x16x32_bf16 v[96:99], v[44:47], a[204:207], v[96:99]\n"
      "ds_read_b64_tr_b16 v[64:65], v4 offset:8192\n"
      "v_add_f32 v18, v18, v172\n"
      "v_mfma_f32_16x16x32_bf16 v[100:103], v[48:51], a[192:195], v[192:195]\n"
      "v_exp_f32 v173, v173\n"
      "v_mfma_f32_16x16x32_bf16 v[100:103], v[52:55], a[196:199], v[100:103]\n"
      "v_add_f32 v18, v18, v173\n"
      "v_cvt_pk_bf16_f32 v170, v172, v173\n"
      "v_mfma_f32_16x16x32_bf16 v[100:103], v[56:59], a[200:203], v[100:103]\n"
      "v_exp_f32 v174, v174\n"
      "v_mfma_f32_16x16x32_bf16 v[100:103], v[60:63], a[204:207], v[100:103]\n"
      "ds_read_b64_tr_b16 v[66:67], v4 offset:12288\n"
      "v_add_f32 v18, v18, v174\n"
      "v_mfma_f32_16x16x32_bf16 v[104:107], v[32:35], a[208:211], v[196:199]\n"
      "v_exp_f32 v175, v175\n"
      "v_mfma_f32_16x16x32_bf16 v[104:107], v[36:39], a[212:215], v[104:107]\n"
      "v_add_f32 v18, v18, v175\n"
      "v_cvt_pk_bf16_f32 v171, v174, v175\n"
      "v_mfma_f32_16x16x32_bf16 v[104:107], v[40:43], a[216:219], v[104:107]\n"
      "v_exp_f32 v176, v176\n"
      "v_mfma_f32_16x16x32_bf16 v[104:107], v[44:47], a[220:223], v[104:107]\n"
      "ds_read_b64_tr_b16 v[68:69], v5 offset:8192\n"
      "v_add_f32 v19, v19, v176\n"
      "v_mfma_f32_16x16x32_bf16 v[108:111], v[48:51], a[208:211], v[196:199]\n"
      "v_exp_f32 v177, v177\n"
      "v_mfma_f32_16x16x32_bf16 v[108:111], v[52:55], a[212:215], v[108:111]\n"
      "v_add_f32 v19, v19, v177\n"
      "v_cvt_pk_bf16_f32 v176, v176, v177\n"
      "v_mfma_f32_16x16x32_bf16 v[108:111], v[56:59], a[216:219], v[108:111]\n"
      "v_exp_f32 v178, v178\n"
      "v_mfma_f32_16x16x32_bf16 v[108:111], v[60:63], a[220:223], v[108:111]\n"
      "ds_read_b64_tr_b16 v[70:71], v5 offset:12288\n"
      "v_add_f32 v19, v19, v178\n"
      "v_mfma_f32_16x16x32_bf16 v[112:115], v[32:35], a[224:227], v[200:203]\n"
      "v_exp_f32 v179, v179\n"
      "v_mfma_f32_16x16x32_bf16 v[112:115], v[36:39], a[228:231], v[112:115]\n"
      "v_add_f32 v19, v19, v179\n"
      "v_cvt_pk_bf16_f32 v177, v178, v179\n"
      "v_mfma_f32_16x16x32_bf16 v[112:115], v[40:43], a[232:235], v[112:115]\n"
      "ds_read_b64_tr_b16 v[72:73], v4 offset:8448\n"
      "ds_read_b64_tr_b16 v[74:75], v4 offset:12544\n"
      "v_mfma_f32_16x16x32_bf16 v[112:115], v[44:47], a[236:239], v[112:115]\n"
      "ds_read_b64_tr_b16 v[76:77], v5 offset:8448\n"
      "ds_read_b64_tr_b16 v[78:79], v5 offset:12544\n"
      "v_mfma_f32_16x16x32_bf16 v[116:119], v[48:51], a[224:227], v[200:203]\n"
      "v_exp_f32 v96, v96\n"
      "v_mfma_f32_16x16x32_bf16 v[116:119], v[52:55], a[228:231], v[116:119]\n"
      "ds_read_b64_tr_b16 v[80:81], v4 offset:8704\n"
      "v_add_f32 v14, v14, v96\n"
      "v_mfma_f32_16x16x32_bf16 v[116:119], v[56:59], a[232:235], v[116:119]\n"
      "v_exp_f32 v97, v97\n"
      "v_mfma_f32_16x16x32_bf16 v[116:119], v[60:63], a[236:239], v[116:119]\n"
      "v_add_f32 v14, v14, v97\n"
      "v_cvt_pk_bf16_f32 v96, v96, v97\n"
      "v_mfma_f32_16x16x32_bf16 v[120:123], v[32:35], a[240:243], v[204:207]\n"
      "v_exp_f32 v98, v98\n"
      "v_mfma_f32_16x16x32_bf16 v[120:123], v[36:39], a[244:247], v[120:123]\n"
      "ds_read_b64_tr_b16 v[82:83], v4 offset:12800\n"
      "v_add_f32 v14, v14, v98\n"
      "v_mfma_f32_16x16x32_bf16 v[120:123], v[40:43], a[248:251], v[120:123]\n"
      "v_exp_f32 v99, v99\n"
      "v_mfma_f32_16x16x32_bf16 v[120:123], v[44:47], a[252:255], v[120:123]\n"
      "v_add_f32 v14, v14, v99\n"
      "v_cvt_pk_bf16_f32 v97, v98, v99\n"
      "v_mfma_f32_16x16x32_bf16 v[124:127], v[48:51], a[240:243], v[204:207]\n"
      "v_exp_f32 v100, v100\n"
      "v_mfma_f32_16x16x32_bf16 v[124:127], v[52:55], a[244:247], v[124:127]\n"
      "ds_read_b64_tr_b16 v[84:85], v5 offset:8704\n"
      "ds_read_b64_tr_b16 v[86:87], v5 offset:12800\n"
      "v_add_f32 v14, v14, v100\n"
      "v_mfma_f32_16x16x32_bf16 v[124:127], v[56:59], a[248:251], v[124:127]\n"
      "v_exp_f32 v101, v101\n"
      "v_mfma_f32_16x16x32_bf16 v[124:127], v[60:63], a[252:255], v[124:127]\n"
      "v_add_f32 v14, v14, v101\n"
      "v_cvt_pk_bf16_f32 v98, v100, v101\n"
      "v_mfma_f32_16x16x32_bf16 v[128:131], v[32:35], v[216:219], v[208:211]\n"
      "v_exp_f32 v102, v102\n"
      "v_mfma_f32_16x16x32_bf16 v[128:131], v[36:39], v[220:223], v[128:131]\n"
      "ds_read_b64_tr_b16 v[88:89], v4 offset:8960\n"
      "ds_read_b64_tr_b16 v[90:91], v4 offset:13056\n"
      "v_add_f32 v14, v14, v102\n"
      "v_mfma_f32_16x16x32_bf16 v[128:131], v[40:43], v[224:227], v[128:131]\n"
      "v_exp_f32 v103, v103\n"
      "v_mfma_f32_16x16x32_bf16 v[128:131], v[44:47], v[228:231], v[128:131]\n"
      "v_add_f32 v14, v14, v103\n"
      "v_cvt_pk_bf16_f32 v99, v102, v103\n"
      "v_mfma_f32_16x16x32_bf16 v[132:135], v[48:51], v[216:219], v[208:211]\n"
      "v_exp_f32 v104, v104\n"
      "v_mfma_f32_16x16x32_bf16 v[132:135], v[52:55], v[220:223], v[132:135]\n"
      "ds_read_b64_tr_b16 v[92:93], v5 offset:8960\n"
      "ds_read_b64_tr_b16 v[94:95], v5 offset:13056\n"
      "v_add_f32 v15, v15, v104\n"
      "v_mfma_f32_16x16x32_bf16 v[132:135], v[56:59], v[224:227], v[132:135]\n"
      "v_exp_f32 v105, v105\n"
      "v_mfma_f32_16x16x32_bf16 v[132:135], v[60:63], v[228:231], v[132:135]\n"
      "v_add_f32 v15, v15, v105\n"
      "v_cvt_pk_bf16_f32 v104, v104, v105\n"
      "s_waitcnt lgkmcnt(0)\n"
      "v_mfma_f32_16x16x32_bf16 a[0:3], v[136:139], v[64:67], a[0:3]\n"
      "v_exp_f32 v106, v106\n"
      "v_mfma_f32_16x16x32_bf16 a[4:7], v[136:139], v[68:71], a[4:7]\n"
      "ds_read_b128 v[32:35], v0 offset:24576\n"
      "v_add_f32 v15, v15, v106\n"
      "v_mfma_f32_16x16x32_bf16 a[8:11], v[136:139], v[72:75], a[8:11]\n"
      "v_exp_f32 v107, v107\n"
      "v_mfma_f32_16x16x32_bf16 a[12:15], v[136:139], v[76:79], a[12:15]\n"
      "v_add_f32 v15, v15, v107\n"
      "v_cvt_pk_bf16_f32 v105, v106, v107\n"
      "v_mfma_f32_16x16x32_bf16 a[16:19], v[136:139], v[80:83], a[16:19]\n"
      "v_exp_f32 v108, v108\n"
      "v_mfma_f32_16x16x32_bf16 a[20:23], v[136:139], v[84:87], a[20:23]\n"
      "v_add_f32 v15, v15, v108\n"
      "v_mfma_f32_16x16x32_bf16 a[24:27], v[136:139], v[88:91], a[24:27]\n"
      "v_exp_f32 v109, v109\n"
      "v_mfma_f32_16x16x32_bf16 a[28:31], v[136:139], v[92:95], a[28:31]\n"
      "v_add_f32 v15, v15, v109\n"
      "v_cvt_pk_bf16_f32 v106, v108, v109\n"
      "v_mfma_f32_16x16x32_bf16 a[32:35], v[144:147], v[64:67], a[32:35]\n"
      "v_exp_f32 v110, v110\n"
      "v_mfma_f32_16x16x32_bf16 a[36:39], v[144:147], v[68:71], a[36:39]\n"
      "ds_read_b128 v[36:39], v1 offset:24576\n"
      "v_add_f32 v15, v15, v110\n"
      "v_mfma_f32_16x16x32_bf16 a[40:43], v[144:147], v[72:75], a[40:43]\n"
      "v_exp_f32 v111, v111\n"
      "v_mfma_f32_16x16x32_bf16 a[44:47], v[144:147], v[76:79], a[44:47]\n"
      "v_add_f32 v15, v15, v111\n"
      "v_cvt_pk_bf16_f32 v107, v110, v111\n"
      "s_xor_b32 m0, s56, 0x1000\n"
      "v_mfma_f32_16x16x32_bf16 a[48:51], v[144:147], v[80:83], a[48:51]\n"
      "buffer_load_dwordx4 v6, s[40:43], s55 offen offset:0 lds\n"
      "v_exp_f32 v112, v112\n"
      "v_mfma_f32_16x16x32_bf16 a[52:55], v[144:147], v[84:87], a[52:55]\n"
      "ds_read_b128 v[40:43], v2 offset:24576\n"
      "v_add_f32 v16, v16, v112\n"
      "v_mfma_f32_16x16x32_bf16 a[56:59], v[144:147], v[88:91], a[56:59]\n"
      "v_exp_f32 v113, v113\n"
      "v_mfma_f32_16x16x32_bf16 a[60:63], v[144:147], v[92:95], a[60:63]\n"
      "v_add_f32 v16, v16, v113\n"
      "v_cvt_pk_bf16_f32 v112, v112, v113\n"
      "v_mfma_f32_16x16x32_bf16 a[64:67], v[152:155], v[64:67], a[64:67]\n"
      "v_exp_f32 v114, v114\n"
      "v_mfma_f32_16x16x32_bf16 a[68:71], v[152:155], v[68:71], a[68:71]\n"
      "ds_read_b128 v[44:47], v3 offset:24576\n"
      "v_add_f32 v16, v16, v114\n"
      "v_mfma_f32_16x16x32_bf16 a[72:75], v[152:155], v[72:75], a[72:75]\n"
      "v_exp_f32 v115, v115\n"
      "v_mfma_f32_16x16x32_bf16 a[76:79], v[152:155], v[76:79], a[76:79]\n"
      "v_add_f32 v16, v16, v115\n"
      "v_cvt_pk_bf16_f32 v113, v114, v115\n"
      "v_mfma_f32_16x16x32_bf16 a[80:83], v[152:155], v[80:83], a[80:83]\n"
      "buffer_load_dwordx4 v7, s[40:43], s55 offen offset:1024 lds\n"
      "v_exp_f32 v116, v116\n"
      "v_mfma_f32_16x16x32_bf16 a[84:87], v[152:155], v[84:87], a[84:87]\n"
      "ds_read_b128 v[48:51], v0 offset:28672\n"
      "v_add_f32 v16, v16, v116\n"
      "v_mfma_f32_16x16x32_bf16 a[88:91], v[152:155], v[88:91], a[88:91]\n"
      "v_exp_f32 v117, v117\n"
      "v_mfma_f32_16x16x32_bf16 a[92:95], v[152:155], v[92:95], a[92:95]\n"
      "v_add_f32 v16, v16, v117\n"
      "v_cvt_pk_bf16_f32 v114, v116, v117\n"
      "v_mfma_f32_16x16x32_bf16 a[96:99], v[160:163], v[64:67], a[96:99]\n"
      "v_exp_f32 v118, v118\n"
      "v_mfma_f32_16x16x32_bf16 a[100:103], v[160:163], v[68:71], a[100:103]\n"
      "v_add_f32 v16, v16, v118\n"
      "v_mfma_f32_16x16x32_bf16 a[104:107], v[160:163], v[72:75], a[104:107]\n"
      "v_exp_f32 v119, v119\n"
      "v_mfma_f32_16x16x32_bf16 a[108:111], v[160:163], v[76:79], a[108:111]\n"
      "v_add_f32 v16, v16, v119\n"
      "v_cvt_pk_bf16_f32 v115, v118, v119\n"
      "v_mfma_f32_16x16x32_bf16 a[112:115], v[160:163], v[80:83], a[112:115]\n"
      "buffer_load_dwordx4 v8, s[40:43], s55 offen offset:2048 lds\n"
      "v_exp_f32 v120, v120\n"
      "v_mfma_f32_16x16x32_bf16 a[116:119], v[160:163], v[84:87], a[116:119]\n"
      "ds_read_b128 v[52:55], v1 offset:28672\n"
      "v_add_f32 v17, v17, v120\n"
      "v_mfma_f32_16x16x32_bf16 a[120:123], v[160:163], v[88:91], a[120:123]\n"
      "v_exp_f32 v121, v121\n"
      "v_mfma_f32_16x16x32_bf16 a[124:127], v[160:163], v[92:95], a[124:127]\n"
      "v_add_f32 v17, v17, v121\n"
      "v_cvt_pk_bf16_f32 v120, v120, v121\n"
      "v_mfma_f32_16x16x32_bf16 a[128:131], v[168:171], v[64:67], a[128:131]\n"
      "v_exp_f32 v122, v122\n"
      "v_mfma_f32_16x16x32_bf16 a[132:135], v[168:171], v[68:71], a[132:135]\n"
      "ds_read_b128 v[56:59], v2 offset:28672\n"
      "v_add_f32 v17, v17, v122\n"
      "v_mfma_f32_16x16x32_bf16 a[136:139], v[168:171], v[72:75], a[136:139]\n"
      "v_exp_f32 v123, v123\n"
      "v_mfma_f32_16x16x32_bf16 a[140:143], v[168:171], v[76:79], a[140:143]\n"
      "v_add_f32 v17, v17, v123\n"
      "v_cvt_pk_bf16_f32 v121, v122, v123\n"
      "v_mfma_f32_16x16x32_bf16 a[144:147], v[168:171], v[80:83], a[144:147]\n"
      "buffer_load_dwordx4 v9, s[40:43], s55 offen offset:3072 lds\n"
      "v_exp_f32 v124, v124\n"
      "s_add_u32 s55, s55, 0x2000\n"
      "v_mfma_f32_16x16x32_bf16 a[148:151], v[168:171], v[84:87], a[148:151]\n"
      "ds_read_b128 v[60:63], v3 offset:28672\n"
      "v_add_f32 v17, v17, v124\n"
      "v_mfma_f32_16x16x32_bf16 a[152:155], v[168:171], v[88:91], a[152:155]\n"
      "v_exp_f32 v125, v125\n"
      "v_mfma_f32_16x16x32_bf16 a[156:159], v[168:171], v[92:95], a[156:159]\n"
      "v_add_f32 v17, v17, v125\n"
      "v_cvt_pk_bf16_f32 v122, v124, v125\n"
      "v_mfma_f32_16x16x16_bf16 a[160:163], v[176:177], v[66:67], a[160:163]\n"
      "v_exp_f32 v126, v126\n"
      "v_mfma_f32_16x16x16_bf16 a[164:167], v[176:177], v[70:71], a[164:167]\n"
      "v_add_f32 v17, v17, v126\n"
      "v_mfma_f32_16x16x16_bf16 a[168:171], v[176:177], v[74:75], a[168:171]\n"
      "v_exp_f32 v127, v127\n"
      "v_mfma_f32_16x16x16_bf16 a[172:175], v[176:177], v[78:79], a[172:175]\n"
      "v_add_f32 v17, v17, v127\n"
      "v_cvt_pk_bf16_f32 v123, v126, v127\n"
      "v_mfma_f32_16x16x16_bf16 a[176:179], v[176:177], v[82:83], a[176:179]\n"
      "v_exp_f32 v128, v128\n"
      "v_mfma_f32_16x16x16_bf16 a[180:183], v[176:177], v[86:87], a[180:183]\n"
      "v_add_f32 v18, v18, v128\n"
      "v_mfma_f32_16x16x16_bf16 a[184:187], v[176:177], v[90:91], a[184:187]\n"
      "v_exp_f32 v129, v129\n"
      "v_mfma_f32_16x16x16_bf16 a[188:191], v[176:177], v[94:95], a[188:191]\n"
      "v_add_f32 v18, v18, v129\n"
      "v_cvt_pk_bf16_f32 v128, v128, v129\n"
      "s_waitcnt lgkmcnt(0)\n"
      "v_mfma_f32_16x16x32_bf16 v[136:139], v[32:35], a[192:195], v[192:195]\n"
      "v_exp_f32 v130, v130\n"
      "v_mfma_f32_16x16x32_bf16 v[136:139], v[36:39], a[196:199], v[136:139]\n"
      "ds_read_b64_tr_b16 v[64:65], v4 offset:16384\n"
      "v_add_f32 v18, v18, v130\n"
      "v_mfma_f32_16x16x32_bf16 v[136:139], v[40:43], a[200:203], v[136:139]\n"
      "v_exp_f32 v131, v131\n"
      "v_mfma_f32_16x16x32_bf16 v[136:139], v[44:47], a[204:207], v[136:139]\n"
      "v_add_f32 v18, v18, v131\n"
      "v_cvt_pk_bf16_f32 v129, v130, v131\n"
      "v_mfma_f32_16x16x32_bf16 v[140:143], v[48:51], a[192:195], v[192:195]\n"
      "v_exp_f32 v132, v132\n"
      "v_mfma_f32_16x16x32_bf16 v[140:143], v[52:55], a[196:199], v[140:143]\n"
      "ds_read_b64_tr_b16 v[66:67], v4 offset:20480\n"
      "v_add_f32 v18, v18, v132\n"
      "v_mfma_f32_16x16x32_bf16 v[140:143], v[56:59], a[200:203], v[140:143]\n"
      "v_exp_f32 v133, v133\n"
      "v_mfma_f32_16x16x32_bf16 v[140:143], v[60:63], a[204:207], v[140:143]\n"
      "v_add_f32 v18, v18, v133\n"
      "v_cvt_pk_bf16_f32 v130, v132, v133\n"
      "v_mfma_f32_16x16x32_bf16 v[144:147], v[32:35], a[208:211], v[196:199]\n"
      "v_exp_f32 v134, v134\n"
      "v_mfma_f32_16x16x32_bf16 v[144:147], v[36:39], a[212:215], v[144:147]\n"
      "ds_read_b64_tr_b16 v[68:69], v5 offset:16384\n"
      "v_add_f32 v18, v18, v134\n"
      "v_mfma_f32_16x16x32_bf16 v[144:147], v[40:43], a[216:219], v[144:147]\n"
      "v_exp_f32 v135, v135\n"
      "v_mfma_f32_16x16x32_bf16 v[144:147], v[44:47], a[220:223], v[144:147]\n"
      "v_add_f32 v18, v18, v135\n"
      "v_cvt_pk_bf16_f32 v131, v134, v135\n"
      "v_mfma_f32_16x16x32_bf16 v[148:151], v[48:51], a[208:211], v[196:199]\n"
      "ds_read_b64_tr_b16 v[70:71], v5 offset:20480\n"
      "ds_read_b64_tr_b16 v[72:73], v4 offset:16640\n"
      "v_mfma_f32_16x16x32_bf16 v[148:151], v[52:55], a[212:215], v[148:151]\n"
      "v_mfma_f32_16x16x32_bf16 v[148:151], v[56:59], a[216:219], v[148:151]\n"
      "v_exp_f32 v136, v136\n"
      "v_mfma_f32_16x16x32_bf16 v[148:151], v[60:63], a[220:223], v[148:151]\n"
      "ds_read_b64_tr_b16 v[74:75], v4 offset:20736\n"
      "v_add_f32 v14, v14, v136\n"
      "v_mfma_f32_16x16x32_bf16 v[152:155], v[32:35], a[224:227], v[200:203]\n"
      "v_exp_f32 v137, v137\n"
      "v_mfma_f32_16x16x32_bf16 v[152:155], v[36:39], a[228:231], v[152:155]\n"
      "v_add_f32 v14, v14, v137\n"
      "v_cvt_pk_bf16_f32 v136, v136, v137\n"
      "v_mfma_f32_16x16x32_bf16 v[152:155], v[40:43], a[232:235], v[152:155]\n"
      "v_exp_f32 v138, v138\n"
      "v_mfma_f32_16x16x32_bf16 v[152:155], v[44:47], a[236:239], v[152:155]\n"
      "ds_read_b64_tr_b16 v[76:77], v5 offset:16640\n"
      "v_add_f32 v14, v14, v138\n"
      "v_mfma_f32_16x16x32_bf16 v[156:159], v[48:51], a[224:227], v[200:203]\n"
      "v_exp_f32 v139, v139\n"
      "v_mfma_f32_16x16x32_bf16 v[156:159], v[52:55], a[228:231], v[156:159]\n"
      "v_add_f32 v14, v14, v139\n"
      "v_cvt_pk_bf16_f32 v137, v138, v139\n"
      "v_mfma_f32_16x16x32_bf16 v[156:159], v[56:59], a[232:235], v[156:159]\n"
      "v_exp_f32 v140, v140\n"
      "v_mfma_f32_16x16x32_bf16 v[156:159], v[60:63], a[236:239], v[156:159]\n"
      "ds_read_b64_tr_b16 v[78:79], v5 offset:20736\n"
      "v_add_f32 v14, v14, v140\n"
      "v_mfma_f32_16x16x32_bf16 v[160:163], v[32:35], a[240:243], v[204:207]\n"
      "v_exp_f32 v141, v141\n"
      "v_mfma_f32_16x16x32_bf16 v[160:163], v[36:39], a[244:247], v[160:163]\n"
      "v_add_f32 v14, v14, v141\n"
      "v_cvt_pk_bf16_f32 v138, v140, v141\n"
      "v_mfma_f32_16x16x32_bf16 v[160:163], v[40:43], a[248:251], v[160:163]\n"
      "v_exp_f32 v142, v142\n"
      "v_mfma_f32_16x16x32_bf16 v[160:163], v[44:47], a[252:255], v[160:163]\n"
      "ds_read_b64_tr_b16 v[80:81], v4 offset:16896\n"
      "ds_read_b64_tr_b16 v[82:83], v4 offset:20992\n"
      "v_add_f32 v14, v14, v142\n"
      "v_mfma_f32_16x16x32_bf16 v[164:167], v[48:51], a[240:243], v[204:207]\n"
      "v_exp_f32 v143, v143\n"
      "v_mfma_f32_16x16x32_bf16 v[164:167], v[52:55], a[244:247], v[164:167]\n"
      "v_add_f32 v14, v14, v143\n"
      "v_cvt_pk_bf16_f32 v139, v142, v143\n"
      "v_mfma_f32_16x16x32_bf16 v[164:167], v[56:59], a[248:251], v[164:167]\n"
      "v_exp_f32 v144, v144\n"
      "v_mfma_f32_16x16x32_bf16 v[164:167], v[60:63], a[252:255], v[164:167]\n"
      "ds_read_b64_tr_b16 v[84:85], v5 offset:16896\n"
      "ds_read_b64_tr_b16 v[86:87], v5 offset:20992\n"
      "v_add_f32 v15, v15, v144\n"
      "v_mfma_f32_16x16x32_bf16 v[168:171], v[32:35], v[216:219], v[208:211]\n"
      "v_exp_f32 v145, v145\n"
      "v_mfma_f32_16x16x32_bf16 v[168:171], v[36:39], v[220:223], v[168:171]\n"
      "v_add_f32 v15, v15, v145\n"
      "v_cvt_pk_bf16_f32 v144, v144, v145\n"
      "v_mfma_f32_16x16x32_bf16 v[168:171], v[40:43], v[224:227], v[168:171]\n"
      "v_exp_f32 v146, v146\n"
      "v_mfma_f32_16x16x32_bf16 v[168:171], v[44:47], v[228:231], v[168:171]\n"
      "ds_read_b64_tr_b16 v[88:89], v4 offset:17152\n"
      "ds_read_b64_tr_b16 v[90:91], v4 offset:21248\n"
      "v_add_f32 v15, v15, v146\n"
      "v_mfma_f32_16x16x32_bf16 v[172:175], v[48:51], v[216:219], v[208:211]\n"
      "v_exp_f32 v147, v147\n"
      "v_mfma_f32_16x16x32_bf16 v[172:175], v[52:55], v[220:223], v[172:175]\n"
      "v_add_f32 v15, v15, v147\n"
      "v_cvt_pk_bf16_f32 v145, v146, v147\n"
      "v_mfma_f32_16x16x32_bf16 v[172:175], v[56:59], v[224:227], v[172:175]\n"
      "v_exp_f32 v148, v148\n"
      "v_mfma_f32_16x16x32_bf16 v[172:175], v[60:63], v[228:231], v[172:175]\n"
      "ds_read_b64_tr_b16 v[92:93], v5 offset:17152\n"
      "ds_read_b64_tr_b16 v[94:95], v5 offset:21248\n"
      "v_add_f32 v15, v15, v148\n"
      "v_mfma_f32_16x16x32_bf16 v[176:179], v[48:51], v[232:235], v[212:215]\n"
      "v_exp_f32 v149, v149\n"
      "v_xor_b32 v3, 0x8000, v3\n"
      "v_mfma_f32_16x16x32_bf16 v[176:179], v[52:55], v[236:239], v[176:179]\n"
      "v_add_f32 v15, v15, v149\n"
      "v_cvt_pk_bf16_f32 v146, v148, v149\n"
      "v_mfma_f32_16x16x32_bf16 v[176:179], v[56:59], v[240:243], v[176:179]\n"
      "v_exp_f32 v150, v150\n"
      "v_xor_b32 v2, 0x8000, v2\n"
      "v_mfma_f32_16x16x32_bf16 v[176:179], v[60:63], v[244:247], v[176:179]\n"
      "v_add_f32 v15, v15, v150\n"
      "s_waitcnt lgkmcnt(0)\n"
      "v_mfma_f32_16x16x32_bf16 a[0:3], v[96:99], v[64:67], a[0:3]\n"
      "v_exp_f32 v151, v151\n"
      "v_xor_b32 v1, 0x8000, v1\n"
      "v_mfma_f32_16x16x32_bf16 a[4:7], v[96:99], v[68:71], a[4:7]\n"
      "v_add_f32 v15, v15, v151\n"
      "v_cvt_pk_bf16_f32 v147, v150, v151\n"
      "v_mfma_f32_16x16x32_bf16 a[8:11], v[96:99], v[72:75], a[8:11]\n"
      "v_exp_f32 v152, v152\n"
      "v_xor_b32 v0, 0x8000, v0\n"
      "v_mfma_f32_16x16x32_bf16 a[12:15], v[96:99], v[76:79], a[12:15]\n"
      "ds_read_b128 v[32:35], v0 offset:0\n"
      "v_add_f32 v16, v16, v152\n"
      "v_mfma_f32_16x16x32_bf16 a[16:19], v[96:99], v[80:83], a[16:19]\n"
      "v_exp_f32 v153, v153\n"
      "v_mfma_f32_16x16x32_bf16 a[20:23], v[96:99], v[84:87], a[20:23]\n"
      "v_add_f32 v16, v16, v153\n"
      "v_cvt_pk_bf16_f32 v152, v152, v153\n"
      "v_mfma_f32_16x16x32_bf16 a[24:27], v[96:99], v[88:91], a[24:27]\n"
      "v_exp_f32 v154, v154\n"
      "v_mfma_f32_16x16x32_bf16 a[28:31], v[96:99], v[92:95], a[28:31]\n"
      "ds_read_b128 v[36:39], v1 offset:0\n"
      "v_add_f32 v16, v16, v154\n"
      "s_xor_b32 m0, s56, 0x3000\n"
      "v_mfma_f32_16x16x32_bf16 a[32:35], v[104:107], v[64:67], a[32:35]\n"
      "buffer_load_dwordx4 v6, s[40:43], s55 offen offset:0 nt lds\n"
      "v_exp_f32 v155, v155\n"
      "v_mfma_f32_16x16x32_bf16 a[36:39], v[104:107], v[68:71], a[36:39]\n"
      "v_add_f32 v16, v16, v155\n"
      "v_cvt_pk_bf16_f32 v153, v154, v155\n"
      "v_mfma_f32_16x16x32_bf16 a[40:43], v[104:107], v[72:75], a[40:43]\n"
      "v_exp_f32 v156, v156\n"
      "v_mfma_f32_16x16x32_bf16 a[44:47], v[104:107], v[76:79], a[44:47]\n"
      "ds_read_b128 v[40:43], v2 offset:0\n"
      "v_add_f32 v16, v16, v156\n"
      "v_mfma_f32_16x16x32_bf16 a[48:51], v[104:107], v[80:83], a[48:51]\n"
      "v_exp_f32 v157, v157\n"
      "v_mfma_f32_16x16x32_bf16 a[52:55], v[104:107], v[84:87], a[52:55]\n"
      "v_add_f32 v16, v16, v157\n"
      "v_cvt_pk_bf16_f32 v154, v156, v157\n"
      "v_mfma_f32_16x16x32_bf16 a[56:59], v[104:107], v[88:91], a[56:59]\n"
      "v_exp_f32 v158, v158\n"
      "v_mfma_f32_16x16x32_bf16 a[60:63], v[104:107], v[92:95], a[60:63]\n"
      "ds_read_b128 v[44:47], v3 offset:0\n"
      "v_add_f32 v16, v16, v158\n"
      "v_mfma_f32_16x16x32_bf16 a[64:67], v[112:115], v[64:67], a[64:67]\n"
      "buffer_load_dwordx4 v7, s[40:43], s55 offen offset:1024 nt lds\n"
      "v_exp_f32 v159, v159\n"
      "v_mfma_f32_16x16x32_bf16 a[68:71], v[112:115], v[68:71], a[68:71]\n"
      "v_add_f32 v16, v16, v159\n"
      "v_cvt_pk_bf16_f32 v155, v158, v159\n"
      "v_mfma_f32_16x16x32_bf16 a[72:75], v[112:115], v[72:75], a[72:75]\n"
      "v_exp_f32 v160, v160\n"
      "v_mfma_f32_16x16x32_bf16 a[76:79], v[112:115], v[76:79], a[76:79]\n"
      "ds_read_b128 v[48:51], v0 offset:4096\n"
      "v_add_f32 v17, v17, v160\n"
      "v_mfma_f32_16x16x32_bf16 a[80:83], v[112:115], v[80:83], a[80:83]\n"
      "v_exp_f32 v161, v161\n"
      "v_mfma_f32_16x16x32_bf16 a[84:87], v[112:115], v[84:87], a[84:87]\n"
      "v_add_f32 v17, v17, v161\n"
      "v_cvt_pk_bf16_f32 v160, v160, v161\n"
      "v_mfma_f32_16x16x32_bf16 a[88:91], v[112:115], v[88:91], a[88:91]\n"
      "v_exp_f32 v162, v162\n"
      "v_mfma_f32_16x16x32_bf16 a[92:95], v[112:115], v[92:95], a[92:95]\n"
      "ds_read_b128 v[52:55], v1 offset:4096\n"
      "v_add_f32 v17, v17, v162\n"
      "v_mfma_f32_16x16x32_bf16 a[96:99], v[120:123], v[64:67], a[96:99]\n"
      "buffer_load_dwordx4 v8, s[40:43], s55 offen offset:2048 nt lds\n"
      "v_exp_f32 v163, v163\n"
      "v_mfma_f32_16x16x32_bf16 a[100:103], v[120:123], v[68:71], a[100:103]\n"
      "v_add_f32 v17, v17, v163\n"
      "v_cvt_pk_bf16_f32 v161, v162, v163\n"
      "v_mfma_f32_16x16x32_bf16 a[104:107], v[120:123], v[72:75], a[104:107]\n"
      "v_exp_f32 v164, v164\n"
      "v_mfma_f32_16x16x32_bf16 a[108:111], v[120:123], v[76:79], a[108:111]\n"
      "ds_read_b128 v[56:59], v2 offset:4096\n"
      "v_add_f32 v17, v17, v164\n"
      "v_mfma_f32_16x16x32_bf16 a[112:115], v[120:123], v[80:83], a[112:115]\n"
      "v_exp_f32 v165, v165\n"
      "v_mfma_f32_16x16x32_bf16 a[116:119], v[120:123], v[84:87], a[116:119]\n"
      "v_add_f32 v17, v17, v165\n"
      "v_cvt_pk_bf16_f32 v162, v164, v165\n"
      "v_mfma_f32_16x16x32_bf16 a[120:123], v[120:123], v[88:91], a[120:123]\n"
      "v_exp_f32 v166, v166\n"
      "v_mfma_f32_16x16x32_bf16 a[124:127], v[120:123], v[92:95], a[124:127]\n"
      "ds_read_b128 v[60:63], v3 offset:4096\n"
      "v_add_f32 v17, v17, v166\n"
      "v_mfma_f32_16x16x32_bf16 a[128:131], v[128:131], v[64:67], a[128:131]\n"
      "buffer_load_dwordx4 v9, s[40:43], s55 offen offset:3072 nt lds\n"
      "v_exp_f32 v167, v167\n"
      "s_add_u32 s55, s55, 0x2000\n"
      "s_xor_b32 s56, s56, 0x8000\n"
      "v_mfma_f32_16x16x32_bf16 a[132:135], v[128:131], v[68:71], a[132:135]\n"
      "v_add_f32 v17, v17, v167\n"
      "v_cvt_pk_bf16_f32 v163, v166, v167\n"
      "v_mfma_f32_16x16x32_bf16 a[136:139], v[128:131], v[72:75], a[136:139]\n"
      "v_exp_f32 v168, v168\n"
      "v_mfma_f32_16x16x32_bf16 a[140:143], v[128:131], v[76:79], a[140:143]\n"
      "v_add_f32 v18, v18, v168\n"
      "v_mfma_f32_16x16x32_bf16 a[144:147], v[128:131], v[80:83], a[144:147]\n"
      "v_exp_f32 v169, v169\n"
      "v_mfma_f32_16x16x32_bf16 a[148:151], v[128:131], v[84:87], a[148:151]\n"
      "v_add_f32 v18, v18, v169\n"
      "v_cvt_pk_bf16_f32 v168, v168, v169\n"
      "v_mfma_f32_16x16x32_bf16 a[152:155], v[128:131], v[88:91], a[152:155]\n"
      "v_exp_f32 v170, v170\n"
      "v_mfma_f32_16x16x32_bf16 a[156:159], v[128:131], v[92:95], a[156:159]\n"
      "v_add_f32 v18, v18, v170\n"
      "s_waitcnt lgkmcnt(0)\n"
      "s_waitcnt vmcnt(12)\n"
      "s_barrier\n"
      "s_sub_u32 s57, s57, 1\n"
      "s_cmp_lg_u32 s57, 0\n"
      "s_cbranch_scc1 L_loop_w3\n"
      "v_exp_f32 v171, v171\n"
      "v_exp_f32 v172, v172\n"
      "v_add_f32 v18, v18, v171\n"
      "v_cvt_pk_bf16_f32 v169, v170, v171\n"
      "ds_read_b64_tr_b16 v[64:65], v4 offset:24576\n"
      "v_add_f32 v18, v18, v172\n"
      "v_exp_f32 v173, v173\n"
      "v_exp_f32 v174, v174\n"
      "v_add_f32 v18, v18, v173\n"
      "v_cvt_pk_bf16_f32 v170, v172, v173\n"
      "ds_read_b64_tr_b16 v[66:67], v4 offset:28672\n"
      "v_add_f32 v18, v18, v174\n"
      "v_exp_f32 v175, v175\n"
      "v_exp_f32 v176, v176\n"
      "v_add_f32 v18, v18, v175\n"
      "v_cvt_pk_bf16_f32 v171, v174, v175\n"
      "ds_read_b64_tr_b16 v[68:69], v5 offset:24576\n"
      "v_add_f32 v19, v19, v176\n"
      "v_exp_f32 v177, v177\n"
      "v_exp_f32 v178, v178\n"
      "v_add_f32 v19, v19, v177\n"
      "v_cvt_pk_bf16_f32 v176, v176, v177\n"
      "ds_read_b64_tr_b16 v[70:71], v5 offset:28672\n"
      "v_add_f32 v19, v19, v178\n"
      "v_exp_f32 v179, v179\n"
      "s_nop 0\n"
      "v_add_f32 v19, v19, v179\n"
      "v_cvt_pk_bf16_f32 v177, v178, v179\n"
      "ds_read_b64_tr_b16 v[72:73], v4 offset:24832\n"
      "ds_read_b64_tr_b16 v[74:75], v4 offset:28928\n"
      "ds_read_b64_tr_b16 v[76:77], v5 offset:24832\n"
      "ds_read_b64_tr_b16 v[78:79], v5 offset:28928\n"
      "ds_read_b64_tr_b16 v[80:81], v4 offset:25088\n"
      "ds_read_b64_tr_b16 v[82:83], v4 offset:29184\n"
      "ds_read_b64_tr_b16 v[84:85], v5 offset:25088\n"
      "ds_read_b64_tr_b16 v[86:87], v5 offset:29184\n"
      "ds_read_b64_tr_b16 v[88:89], v4 offset:25344\n"
      "ds_read_b64_tr_b16 v[90:91], v4 offset:29440\n"
      "ds_read_b64_tr_b16 v[92:93], v5 offset:25344\n"
      "ds_read_b64_tr_b16 v[94:95], v5 offset:29440\n"
      "v_and_b32 v22, 15, v13\n"
      "v_lshrrev_b32 v23, 4, v13\n"
      "v_lshrrev_b32 v29, 2, v22\n"
      "v_xor_b32 v29, v29, v23\n"
      "v_lshlrev_b32 v29, 3, v29\n"
      "v_lshl_or_b32 v29, v22, 5, v29\n"
      "v_add_u32 v24, s64, v29\n"
      "v_bfe_u32 v29, v13, 2, 2\n"
      "v_lshl_or_b32 v29, v23, 3, v29\n"
      "v_lshlrev_b32 v29, 5, v29\n"
      "v_add_u32 v29, s64, v29\n"
      "v_lshlrev_b32 v30, 1, v23\n"
      "v_xor_b32 v30, v30, v13\n"
      "v_and_b32 v30, 3, v30\n"
      "v_lshl_add_u32 v25, v30, 3, v29\n"
      "v_xor_b32 v30, 1, v30\n"
      "v_lshl_add_u32 v26, v30, 3, v29\n"
      "v_add_u32 v26, 128, v26\n"
      "v_lshlrev_b32 v29, 4, v23\n"
      "v_lshl_or_b32 v253, v22, 8, v29\n"
      "v_add_u32 v27, 0x21200, v29\n"
      "v_add_u32 v250, 0x20000, v29\n"
      "v_lshlrev_b32 v29, 2, v22\n"
      "v_add_u32 v251, 0x21200, v29\n"
      "v_add_u32 v248, 0x1a000, v12\n"
      "v_add_u32 v249, 0x15800, v12\n"
      "v_lshlrev_b32 v21, 2, v13\n"
      "v_add_u32 v21, 0x20000, v21\n"
      "v_add_u32 v0, s64, v0\n"
      "v_add_u32 v6, s64, v6\n"
      "v_add_u32 v1, s64, v1\n"
      "v_add_u32 v7, s64, v7\n"
      "v_add_u32 v2, s64, v2\n"
      "v_add_u32 v8, s64, v8\n"
      "v_add_u32 v3, s64, v3\n"
      "v_add_u32 v9, s64, v9\n"
      "s_waitcnt vmcnt(0)\n"
      "s_barrier\n"
      "s_waitcnt lgkmcnt(0)\n"
      "v_mfma_f32_16x16x16_bf16 a[160:163], v[176:177], v[66:67], a[160:163]\n"
      "v_mov_b32 v50, v14\n"
      "v_mov_b32 v51, v15\n"
      "v_mfma_f32_16x16x16_bf16 a[164:167], v[176:177], v[70:71], a[164:167]\n"
      "v_mov_b32 v52, v16\n"
      "v_mov_b32 v53, v17\n"
      "v_mfma_f32_16x16x16_bf16 a[168:171], v[176:177], v[74:75], a[168:171]\n"
      "v_mov_b32 v54, v18\n"
      "v_mov_b32 v55, v19\n"
      "v_mfma_f32_16x16x16_bf16 a[172:175], v[176:177], v[78:79], a[172:175]\n"
      "s_nop 1\n"
      "v_mfma_f32_16x16x16_bf16 a[176:179], v[176:177], v[82:83], a[176:179]\n"
      "v_permlane16_swap_b32 v14, v50\n"
      "v_permlane16_swap_b32 v15, v51\n"
      "v_mfma_f32_16x16x16_bf16 a[180:183], v[176:177], v[86:87], a[180:183]\n"
      "v_permlane16_swap_b32 v16, v52\n"
      "v_permlane16_swap_b32 v17, v53\n"
      "v_mfma_f32_16x16x16_bf16 a[184:187], v[176:177], v[90:91], a[184:187]\n"
      "v_permlane16_swap_b32 v18, v54\n"
      "v_permlane16_swap_b32 v19, v55\n"
      "v_mfma_f32_16x16x16_bf16 a[188:191], v[176:177], v[94:95], a[188:191]\n"
      "s_nop 1\n"
      "v_mfma_f32_16x16x32_bf16 a[0:3], v[136:139], v[64:67], a[0:3]\n"
      "v_add_f32 v14, v14, v50\n"
      "v_add_f32 v15, v15, v51\n"
      "v_mfma_f32_16x16x32_bf16 a[4:7], v[136:139], v[68:71], a[4:7]\n"
      "v_add_f32 v16, v16, v52\n"
      "v_add_f32 v17, v17, v53\n"
      "v_mfma_f32_16x16x32_bf16 a[8:11], v[136:139], v[72:75], a[8:11]\n"
      "v_add_f32 v18, v18, v54\n"
      "v_add_f32 v19, v19, v55\n"
      "v_mfma_f32_16x16x32_bf16 a[12:15], v[136:139], v[76:79], a[12:15]\n"
      "v_mov_b32 v50, v14\n"
      "v_mov_b32 v51, v15\n"
      "v_mfma_f32_16x16x32_bf16 a[16:19], v[136:139], v[80:83], a[16:19]\n"
      "v_mov_b32 v52, v16\n"
      "v_mov_b32 v53, v17\n"
      "v_mfma_f32_16x16x32_bf16 a[20:23], v[136:139], v[84:87], a[20:23]\n"
      "v_mov_b32 v54, v18\n"
      "v_mov_b32 v55, v19\n"
      "v_mfma_f32_16x16x32_bf16 a[24:27], v[136:139], v[88:91], a[24:27]\n"
      "s_nop 1\n"
      "v_mfma_f32_16x16x32_bf16 a[28:31], v[136:139], v[92:95], a[28:31]\n"
      "v_permlane32_swap_b32 v14, v50\n"
      "v_permlane32_swap_b32 v15, v51\n"
      "v_mfma_f32_16x16x32_bf16 a[32:35], v[144:147], v[64:67], a[32:35]\n"
      "v_permlane32_swap_b32 v16, v52\n"
      "v_permlane32_swap_b32 v17, v53\n"
      "v_mfma_f32_16x16x32_bf16 a[36:39], v[144:147], v[68:71], a[36:39]\n"
      "v_permlane32_swap_b32 v18, v54\n"
      "v_permlane32_swap_b32 v19, v55\n"
      "v_mfma_f32_16x16x32_bf16 a[40:43], v[144:147], v[72:75], a[40:43]\n"
      "s_nop 1\n"
      "v_mfma_f32_16x16x32_bf16 a[44:47], v[144:147], v[76:79], a[44:47]\n"
      "v_add_f32 v14, v14, v50\n"
      "v_add_f32 v15, v15, v51\n"
      "v_mfma_f32_16x16x32_bf16 a[48:51], v[144:147], v[80:83], a[48:51]\n"
      "v_add_f32 v16, v16, v52\n"
      "v_add_f32 v17, v17, v53\n"
      "v_mfma_f32_16x16x32_bf16 a[52:55], v[144:147], v[84:87], a[52:55]\n"
      "v_add_f32 v18, v18, v54\n"
      "v_add_f32 v19, v19, v55\n"
      "v_mfma_f32_16x16x32_bf16 a[56:59], v[144:147], v[88:91], a[56:59]\n"
      "s_mov_b64 s[98:99], 0\n"
      "v_cmp_nge_f32 vcc, s96, v14\n"
      "v_mfma_f32_16x16x32_bf16 a[60:63], v[144:147], v[92:95], a[60:63]\n"
      "v_rcp_f32 v40, v14\n"
      "s_nop 1\n"
      "v_mfma_f32_16x16x32_bf16 a[64:67], v[152:155], v[64:67], a[64:67]\n"
      "s_or_b64 s[98:99], s[98:99], vcc\n"
      "v_cmp_nge_f32 vcc, s96, v15\n"
      "v_mfma_f32_16x16x32_bf16 a[68:71], v[152:155], v[68:71], a[68:71]\n"
      "v_rcp_f32 v41, v15\n"
      "s_nop 1\n"
      "v_mfma_f32_16x16x32_bf16 a[72:75], v[152:155], v[72:75], a[72:75]\n"
      "s_or_b64 s[98:99], s[98:99], vcc\n"
      "v_cmp_nge_f32 vcc, s96, v16\n"
      "v_mfma_f32_16x16x32_bf16 a[76:79], v[152:155], v[76:79], a[76:79]\n"
      "v_rcp_f32 v42, v16\n"
      "s_nop 1\n"
      "v_mfma_f32_16x16x32_bf16 a[80:83], v[152:155], v[80:83], a[80:83]\n"
      "s_or_b64 s[98:99], s[98:99], vcc\n"
      "v_cmp_nge_f32 vcc, s96, v17\n"
      "v_mfma_f32_16x16x32_bf16 a[84:87], v[152:155], v[84:87], a[84:87]\n"
      "v_rcp_f32 v43, v17\n"
      "s_nop 1\n"
      "v_mfma_f32_16x16x32_bf16 a[88:91], v[152:155], v[88:91], a[88:91]\n"
      "s_or_b64 s[98:99], s[98:99], vcc\n"
      "v_cmp_nge_f32 vcc, s96, v18\n"
      "v_mfma_f32_16x16x32_bf16 a[92:95], v[152:155], v[92:95], a[92:95]\n"
      "v_rcp_f32 v44, v18\n"
      "s_nop 1\n"
      "v_mfma_f32_16x16x32_bf16 a[96:99], v[160:163], v[64:67], a[96:99]\n"
      "s_or_b64 s[98:99], s[98:99], vcc\n"
      "v_cmp_nge_f32 vcc, s96, v19\n"
      "v_mfma_f32_16x16x32_bf16 a[100:103], v[160:163], v[68:71], a[100:103]\n"
      "s_nop 1\n"
      "v_mfma_f32_16x16x32_bf16 a[104:107], v[160:163], v[72:75], a[104:107]\n"
      "s_or_b64 s[98:99], s[98:99], vcc\n"
      "ds_write_b32 v251, v40 offset:0\n"
      "v_mfma_f32_16x16x32_bf16 a[108:111], v[160:163], v[76:79], a[108:111]\n"
      "ds_write_b32 v251, v41 offset:64\n"
      "ds_write_b32 v251, v42 offset:128\n"
      "v_mfma_f32_16x16x32_bf16 a[112:115], v[160:163], v[80:83], a[112:115]\n"
      "ds_write_b32 v251, v43 offset:192\n"
      "ds_write_b32 v251, v44 offset:256\n"
      "v_mfma_f32_16x16x32_bf16 a[116:119], v[160:163], v[84:87], a[116:119]\n"
      "ds_read_b128 v[196:199], v27 offset:0\n"
      "ds_read_b128 v[200:203], v27 offset:64\n"
      "v_mfma_f32_16x16x32_bf16 a[120:123], v[160:163], v[88:91], a[120:123]\n"
      "ds_read_b128 v[204:207], v27 offset:128\n"
      "ds_read_b128 v[208:211], v27 offset:192\n"
      "v_mfma_f32_16x16x32_bf16 a[124:127], v[160:163], v[92:95], a[124:127]\n"
      "ds_read_b128 v[212:215], v27 offset:256\n"
      "s_cmp_lg_u64 s[98:99], 0\n"
      "v_mfma_f32_16x16x32_bf16 a[128:131], v[168:171], v[64:67], a[128:131]\n"
      "s_cselect_b32 s59, 1, 0\n"
      "v_mov_b32 v28, s59\n"
      "v_mfma_f32_16x16x32_bf16 a[132:135], v[168:171], v[68:71], a[132:135]\n"
      "v_mov_b32 v20, 0x2080c\n"
      "ds_write_b128 v248, a[160:163] offset:0\n"
      "v_mfma_f32_16x16x32_bf16 a[136:139], v[168:171], v[72:75], a[136:139]\n"
      "ds_write_b128 v248, a[164:167] offset:1024\n"
      "ds_write_b128 v248, a[168:171] offset:2048\n"
      "v_mfma_f32_16x16x32_bf16 a[140:143], v[168:171], v[76:79], a[140:143]\n"
      "ds_write_b128 v248, a[172:175] offset:3072\n"
      "ds_write_b128 v248, a[176:179] offset:4096\n"
      "v_mfma_f32_16x16x32_bf16 a[144:147], v[168:171], v[80:83], a[144:147]\n"
      "ds_write_b128 v248, a[180:183] offset:5120\n"
      "ds_write_b128 v248, a[184:187] offset:6144\n"
      "v_mfma_f32_16x16x32_bf16 a[148:151], v[168:171], v[84:87], a[148:151]\n"
      "ds_write_b128 v248, a[188:191] offset:7168\n"
      "ds_write_b32 v21, v19 offset:768\n"
      "v_mfma_f32_16x16x32_bf16 a[152:155], v[168:171], v[88:91], a[152:155]\n"
      "ds_write_b32 v20, v28 offset:0\n"
      "s_waitcnt lgkmcnt(10)\n"
      "v_mfma_f32_16x16x32_bf16 a[156:159], v[168:171], v[92:95], a[156:159]\n"
      "v_accvgpr_read_b32 v96, a0\n"
      "v_accvgpr_read_b32 v97, a1\n"
      "v_accvgpr_read_b32 v98, a2\n"
      "v_accvgpr_read_b32 v99, a3\n"
      "v_mul_f32 v96, v196, v96\n"
      "v_mul_f32 v97, v197, v97\n"
      "v_mul_f32 v98, v198, v98\n"
      "v_mul_f32 v99, v199, v99\n"
      "v_cvt_pk_bf16_f32 v96, v96, v97\n"
      "v_cvt_pk_bf16_f32 v97, v98, v99\n"
      "ds_write_b64 v24, v[96:97] offset:0\n"
      "v_accvgpr_read_b32 v100, a4\n"
      "v_accvgpr_read_b32 v101, a5\n"
      "v_accvgpr_read_b32 v102, a6\n"
      "v_accvgpr_read_b32 v103, a7\n"
      "v_mul_f32 v100, v196, v100\n"
      "v_mul_f32 v101, v197, v101\n"
      "v_mul_f32 v102, v198, v102\n"
      "v_mul_f32 v103, v199, v103\n"
      "v_cvt_pk_bf16_f32 v100, v100, v101\n"
      "v_cvt_pk_bf16_f32 v101, v102, v103\n"
      "ds_write_b64 v24, v[100:101] offset:512\n"
      "v_accvgpr_read_b32 v104, a8\n"
      "v_accvgpr_read_b32 v105, a9\n"
      "v_accvgpr_read_b32 v106, a10\n"
      "v_accvgpr_read_b32 v107, a11\n"
      "v_mul_f32 v104, v196, v104\n"
      "v_mul_f32 v105, v197, v105\n"
      "v_mul_f32 v106, v198, v106\n"
      "v_mul_f32 v107, v199, v107\n"
      "v_cvt_pk_bf16_f32 v104, v104, v105\n"
      "v_cvt_pk_bf16_f32 v105, v106, v107\n"
      "ds_write_b64 v24, v[104:105] offset:1024\n"
      "v_accvgpr_read_b32 v108, a12\n"
      "v_accvgpr_read_b32 v109, a13\n"
      "v_accvgpr_read_b32 v110, a14\n"
      "v_accvgpr_read_b32 v111, a15\n"
      "v_mul_f32 v108, v196, v108\n"
      "v_mul_f32 v109, v197, v109\n"
      "v_mul_f32 v110, v198, v110\n"
      "v_mul_f32 v111, v199, v111\n"
      "v_cvt_pk_bf16_f32 v108, v108, v109\n"
      "v_cvt_pk_bf16_f32 v109, v110, v111\n"
      "ds_write_b64 v24, v[108:109] offset:1536\n"
      "v_accvgpr_read_b32 v112, a16\n"
      "v_accvgpr_read_b32 v113, a17\n"
      "v_accvgpr_read_b32 v114, a18\n"
      "v_accvgpr_read_b32 v115, a19\n"
      "v_mul_f32 v112, v196, v112\n"
      "v_mul_f32 v113, v197, v113\n"
      "v_mul_f32 v114, v198, v114\n"
      "v_mul_f32 v115, v199, v115\n"
      "v_cvt_pk_bf16_f32 v112, v112, v113\n"
      "v_cvt_pk_bf16_f32 v113, v114, v115\n"
      "ds_write_b64 v24, v[112:113] offset:2048\n"
      "v_accvgpr_read_b32 v116, a20\n"
      "v_accvgpr_read_b32 v117, a21\n"
      "v_accvgpr_read_b32 v118, a22\n"
      "v_accvgpr_read_b32 v119, a23\n"
      "v_mul_f32 v116, v196, v116\n"
      "v_mul_f32 v117, v197, v117\n"
      "v_mul_f32 v118, v198, v118\n"
      "v_mul_f32 v119, v199, v119\n"
      "v_cvt_pk_bf16_f32 v116, v116, v117\n"
      "v_cvt_pk_bf16_f32 v117, v118, v119\n"
      "ds_write_b64 v24, v[116:117] offset:2560\n"
      "v_accvgpr_read_b32 v120, a24\n"
      "v_accvgpr_read_b32 v121, a25\n"
      "v_accvgpr_read_b32 v122, a26\n"
      "v_accvgpr_read_b32 v123, a27\n"
      "v_mul_f32 v120, v196, v120\n"
      "v_mul_f32 v121, v197, v121\n"
      "v_mul_f32 v122, v198, v122\n"
      "v_mul_f32 v123, v199, v123\n"
      "v_cvt_pk_bf16_f32 v120, v120, v121\n"
      "v_cvt_pk_bf16_f32 v121, v122, v123\n"
      "ds_write_b64 v24, v[120:121] offset:3072\n"
      "v_accvgpr_read_b32 v124, a28\n"
      "v_accvgpr_read_b32 v125, a29\n"
      "v_accvgpr_read_b32 v126, a30\n"
      "v_accvgpr_read_b32 v127, a31\n"
      "v_mul_f32 v124, v196, v124\n"
      "v_mul_f32 v125, v197, v125\n"
      "v_mul_f32 v126, v198, v126\n"
      "v_mul_f32 v127, v199, v127\n"
      "v_cvt_pk_bf16_f32 v124, v124, v125\n"
      "v_cvt_pk_bf16_f32 v125, v126, v127\n"
      "ds_write_b64 v24, v[124:125] offset:3584\n"
      "ds_read_b64_tr_b16 v[64:65], v25 offset:0\n"
      "ds_read_b64_tr_b16 v[66:67], v26 offset:0\n"
      "ds_read_b64_tr_b16 v[68:69], v25 offset:1024\n"
      "ds_read_b64_tr_b16 v[70:71], v26 offset:1024\n"
      "ds_read_b64_tr_b16 v[72:73], v25 offset:2048\n"
      "ds_read_b64_tr_b16 v[74:75], v26 offset:2048\n"
      "ds_read_b64_tr_b16 v[76:77], v25 offset:3072\n"
      "ds_read_b64_tr_b16 v[78:79], v26 offset:3072\n"
      "v_accvgpr_read_b32 v128, a32\n"
      "v_accvgpr_read_b32 v129, a33\n"
      "v_accvgpr_read_b32 v130, a34\n"
      "v_accvgpr_read_b32 v131, a35\n"
      "v_pk_mul_f32 v[128:129], v[128:129], v[200:201]\n"
      "v_pk_mul_f32 v[130:131], v[130:131], v[202:203]\n"
      "v_cvt_pk_bf16_f32 v128, v128, v129\n"
      "v_cvt_pk_bf16_f32 v129, v130, v131\n"
      "ds_write_b64 v24, v[128:129] offset:4096\n"
      "v_accvgpr_read_b32 v132, a36\n"
      "v_accvgpr_read_b32 v133, a37\n"
      "v_accvgpr_read_b32 v134, a38\n"
      "v_accvgpr_read_b32 v135, a39\n"
      "v_pk_mul_f32 v[132:133], v[132:133], v[200:201]\n"
      "v_pk_mul_f32 v[134:135], v[134:135], v[202:203]\n"
      "v_cvt_pk_bf16_f32 v132, v132, v133\n"
      "v_cvt_pk_bf16_f32 v133, v134, v135\n"
      "ds_write_b64 v24, v[132:133] offset:4608\n"
      "v_accvgpr_read_b32 v136, a40\n"
      "v_accvgpr_read_b32 v137, a41\n"
      "v_accvgpr_read_b32 v138, a42\n"
      "v_accvgpr_read_b32 v139, a43\n"
      "v_pk_mul_f32 v[136:137], v[136:137], v[200:201]\n"
      "v_pk_mul_f32 v[138:139], v[138:139], v[202:203]\n"
      "v_cvt_pk_bf16_f32 v136, v136, v137\n"
      "v_cvt_pk_bf16_f32 v137, v138, v139\n"
      "ds_write_b64 v24, v[136:137] offset:5120\n"
      "v_accvgpr_read_b32 v140, a44\n"
      "v_accvgpr_read_b32 v141, a45\n"
      "v_accvgpr_read_b32 v142, a46\n"
      "v_accvgpr_read_b32 v143, a47\n"
      "v_pk_mul_f32 v[140:141], v[140:141], v[200:201]\n"
      "v_pk_mul_f32 v[142:143], v[142:143], v[202:203]\n"
      "v_cvt_pk_bf16_f32 v140, v140, v141\n"
      "v_cvt_pk_bf16_f32 v141, v142, v143\n"
      "ds_write_b64 v24, v[140:141] offset:5632\n"
      "s_waitcnt lgkmcnt(4)\n"
      "ds_write_b128 v0, v[64:67] offset:0\n"
      "ds_write_b128 v1, v[68:71] offset:0\n"
      "ds_write_b128 v2, v[72:75] offset:0\n"
      "ds_write_b128 v3, v[76:79] offset:0\n"
      "ds_read_b128 v[64:67], v6 offset:0\n"
      "ds_read_b128 v[68:71], v7 offset:1024\n"
      "ds_read_b128 v[72:75], v8 offset:2048\n"
      "ds_read_b128 v[76:79], v9 offset:3072\n"
      "v_accvgpr_read_b32 v144, a48\n"
      "v_accvgpr_read_b32 v145, a49\n"
      "v_accvgpr_read_b32 v146, a50\n"
      "v_accvgpr_read_b32 v147, a51\n"
      "v_pk_mul_f32 v[144:145], v[144:145], v[200:201]\n"
      "v_pk_mul_f32 v[146:147], v[146:147], v[202:203]\n"
      "v_cvt_pk_bf16_f32 v144, v144, v145\n"
      "v_cvt_pk_bf16_f32 v145, v146, v147\n"
      "ds_write_b64 v24, v[144:145] offset:6144\n"
      "v_accvgpr_read_b32 v148, a52\n"
      "v_accvgpr_read_b32 v149, a53\n"
      "v_accvgpr_read_b32 v150, a54\n"
      "v_accvgpr_read_b32 v151, a55\n"
      "v_pk_mul_f32 v[148:149], v[148:149], v[200:201]\n"
      "v_pk_mul_f32 v[150:151], v[150:151], v[202:203]\n"
      "v_cvt_pk_bf16_f32 v148, v148, v149\n"
      "v_cvt_pk_bf16_f32 v149, v150, v151\n"
      "ds_write_b64 v24, v[148:149] offset:6656\n"
      "v_accvgpr_read_b32 v152, a56\n"
      "v_accvgpr_read_b32 v153, a57\n"
      "v_accvgpr_read_b32 v154, a58\n"
      "v_accvgpr_read_b32 v155, a59\n"
      "v_pk_mul_f32 v[152:153], v[152:153], v[200:201]\n"
      "v_pk_mul_f32 v[154:155], v[154:155], v[202:203]\n"
      "v_cvt_pk_bf16_f32 v152, v152, v153\n"
      "v_cvt_pk_bf16_f32 v153, v154, v155\n"
      "ds_write_b64 v24, v[152:153] offset:7168\n"
      "v_accvgpr_read_b32 v156, a60\n"
      "v_accvgpr_read_b32 v157, a61\n"
      "v_accvgpr_read_b32 v158, a62\n"
      "v_accvgpr_read_b32 v159, a63\n"
      "v_pk_mul_f32 v[156:157], v[156:157], v[200:201]\n"
      "v_pk_mul_f32 v[158:159], v[158:159], v[202:203]\n"
      "v_cvt_pk_bf16_f32 v156, v156, v157\n"
      "v_cvt_pk_bf16_f32 v157, v158, v159\n"
      "ds_write_b64 v24, v[156:157] offset:7680\n"
      "s_waitcnt lgkmcnt(4)\n"
      "s_add_u32 s58, s62, 0\n"
      "buffer_store_dwordx4 v[64:67], v12, s[48:51], s58 offen offset:0 nt\n"
      "buffer_store_dwordx4 v[68:71], v12, s[48:51], s58 offen offset:1024 nt\n"
      "buffer_store_dwordx4 v[72:75], v12, s[48:51], s58 offen offset:2048 nt\n"
      "buffer_store_dwordx4 v[76:79], v12, s[48:51], s58 offen offset:3072 nt\n"
      "v_mov_b32 v20, 0x20800\n"
      "ds_read_b64_tr_b16 v[80:81], v25 offset:4096\n"
      "ds_read_b64_tr_b16 v[82:83], v26 offset:4096\n"
      "ds_read_b64_tr_b16 v[84:85], v25 offset:5120\n"
      "ds_read_b64_tr_b16 v[86:87], v26 offset:5120\n"
      "ds_read_b64_tr_b16 v[88:89], v25 offset:6144\n"
      "ds_read_b64_tr_b16 v[90:91], v26 offset:6144\n"
      "ds_read_b64_tr_b16 v[92:93], v25 offset:7168\n"
      "ds_read_b64_tr_b16 v[94:95], v26 offset:7168\n"
      "v_accvgpr_read_b32 v96, a64\n"
      "v_accvgpr_read_b32 v97, a65\n"
      "v_accvgpr_read_b32 v98, a66\n"
      "v_accvgpr_read_b32 v99, a67\n"
      "v_pk_mul_f32 v[96:97], v[96:97], v[204:205]\n"
      "v_pk_mul_f32 v[98:99], v[98:99], v[206:207]\n"
      "v_cvt_pk_bf16_f32 v96, v96, v97\n"
      "v_cvt_pk_bf16_f32 v97, v98, v99\n"
      "ds_write_b64 v24, v[96:97] offset:8192\n"
      "v_accvgpr_read_b32 v100, a68\n"
      "v_accvgpr_read_b32 v101, a69\n"
      "v_accvgpr_read_b32 v102, a70\n"
      "v_accvgpr_read_b32 v103, a71\n"
      "v_pk_mul_f32 v[100:101], v[100:101], v[204:205]\n"
      "v_pk_mul_f32 v[102:103], v[102:103], v[206:207]\n"
      "v_cvt_pk_bf16_f32 v100, v100, v101\n"
      "v_cvt_pk_bf16_f32 v101, v102, v103\n"
      "ds_write_b64 v24, v[100:101] offset:8704\n"
      "v_accvgpr_read_b32 v104, a72\n"
      "v_accvgpr_read_b32 v105, a73\n"
      "v_accvgpr_read_b32 v106, a74\n"
      "v_accvgpr_read_b32 v107, a75\n"
      "v_pk_mul_f32 v[104:105], v[104:105], v[204:205]\n"
      "v_pk_mul_f32 v[106:107], v[106:107], v[206:207]\n"
      "v_cvt_pk_bf16_f32 v104, v104, v105\n"
      "v_cvt_pk_bf16_f32 v105, v106, v107\n"
      "ds_write_b64 v24, v[104:105] offset:9216\n"
      "v_accvgpr_read_b32 v108, a76\n"
      "v_accvgpr_read_b32 v109, a77\n"
      "v_accvgpr_read_b32 v110, a78\n"
      "v_accvgpr_read_b32 v111, a79\n"
      "v_pk_mul_f32 v[108:109], v[108:109], v[204:205]\n"
      "v_pk_mul_f32 v[110:111], v[110:111], v[206:207]\n"
      "v_cvt_pk_bf16_f32 v108, v108, v109\n"
      "v_cvt_pk_bf16_f32 v109, v110, v111\n"
      "ds_write_b64 v24, v[108:109] offset:9728\n"
      "s_waitcnt lgkmcnt(4)\n"
      "ds_write_b128 v0, v[80:83] offset:4096\n"
      "ds_write_b128 v1, v[84:87] offset:4096\n"
      "ds_write_b128 v2, v[88:91] offset:4096\n"
      "ds_write_b128 v3, v[92:95] offset:4096\n"
      "ds_read_b128 v[80:83], v6 offset:4096\n"
      "ds_read_b128 v[84:87], v7 offset:5120\n"
      "ds_read_b128 v[88:91], v8 offset:6144\n"
      "ds_read_b128 v[92:95], v9 offset:7168\n"
      "v_accvgpr_read_b32 v112, a80\n"
      "v_accvgpr_read_b32 v113, a81\n"
      "v_accvgpr_read_b32 v114, a82\n"
      "v_accvgpr_read_b32 v115, a83\n"
      "v_pk_mul_f32 v[112:113], v[112:113], v[204:205]\n"
      "v_pk_mul_f32 v[114:115], v[114:115], v[206:207]\n"
      "v_cvt_pk_bf16_f32 v112, v112, v113\n"
      "v_cvt_pk_bf16_f32 v113, v114, v115\n"
      "ds_write_b64 v24, v[112:113] offset:10240\n"
      "v_accvgpr_read_b32 v116, a84\n"
      "v_accvgpr_read_b32 v117, a85\n"
      "v_accvgpr_read_b32 v118, a86\n"
      "v_accvgpr_read_b32 v119, a87\n"
      "v_pk_mul_f32 v[116:117], v[116:117], v[204:205]\n"
      "v_pk_mul_f32 v[118:119], v[118:119], v[206:207]\n"
      "v_cvt_pk_bf16_f32 v116, v116, v117\n"
      "v_cvt_pk_bf16_f32 v117, v118, v119\n"
      "ds_write_b64 v24, v[116:117] offset:10752\n"
      "v_accvgpr_read_b32 v120, a88\n"
      "v_accvgpr_read_b32 v121, a89\n"
      "v_accvgpr_read_b32 v122, a90\n"
      "v_accvgpr_read_b32 v123, a91\n"
      "v_pk_mul_f32 v[120:121], v[120:121], v[204:205]\n"
      "v_pk_mul_f32 v[122:123], v[122:123], v[206:207]\n"
      "v_cvt_pk_bf16_f32 v120, v120, v121\n"
      "v_cvt_pk_bf16_f32 v121, v122, v123\n"
      "ds_write_b64 v24, v[120:121] offset:11264\n"
      "v_accvgpr_read_b32 v124, a92\n"
      "v_accvgpr_read_b32 v125, a93\n"
      "v_accvgpr_read_b32 v126, a94\n"
      "v_accvgpr_read_b32 v127, a95\n"
      "v_pk_mul_f32 v[124:125], v[124:125], v[204:205]\n"
      "v_pk_mul_f32 v[126:127], v[126:127], v[206:207]\n"
      "v_cvt_pk_bf16_f32 v124, v124, v125\n"
      "v_cvt_pk_bf16_f32 v125, v126, v127\n"
      "ds_write_b64 v24, v[124:125] offset:11776\n"
      "s_waitcnt lgkmcnt(4)\n"
      "s_add_u32 s58, s62, 4096\n"
      "buffer_store_dwordx4 v[80:83], v12, s[48:51], s58 offen offset:0 nt\n"
      "buffer_store_dwordx4 v[84:87], v12, s[48:51], s58 offen offset:1024 nt\n"
      "buffer_store_dwordx4 v[88:91], v12, s[48:51], s58 offen offset:2048 nt\n"
      "buffer_store_dwordx4 v[92:95], v12, s[48:51], s58 offen offset:3072 nt\n"
      "s_barrier\n"
      "ds_read_b64_tr_b16 v[64:65], v25 offset:8192\n"
      "ds_read_b64_tr_b16 v[66:67], v26 offset:8192\n"
      "ds_read_b64_tr_b16 v[68:69], v25 offset:9216\n"
      "ds_read_b64_tr_b16 v[70:71], v26 offset:9216\n"
      "ds_read_b64_tr_b16 v[72:73], v25 offset:10240\n"
      "ds_read_b64_tr_b16 v[74:75], v26 offset:10240\n"
      "ds_read_b64_tr_b16 v[76:77], v25 offset:11264\n"
      "ds_read_b64_tr_b16 v[78:79], v26 offset:11264\n"
      "ds_read_b128 v[160:163], v249 offset:0\n"
      "ds_read_b128 v[164:167], v249 offset:1024\n"
      "ds_read_b128 v[168:171], v249 offset:8192\n"
      "ds_read_b128 v[172:175], v249 offset:9216\n"
      "ds_read_b128 v[176:179], v249 offset:16384\n"
      "ds_read_b128 v[180:183], v249 offset:17408\n"
      "ds_read_b128 v[184:187], v249 offset:24576\n"
      "ds_read_b128 v[188:191], v249 offset:25600\n"
      "ds_read_b128 v[32:35], v250 offset:0\n"
      "ds_read_b128 v[36:39], v250 offset:256\n"
      "ds_read_b128 v[40:43], v250 offset:512\n"
      "ds_read_b128 v[44:47], v250 offset:768\n"
      "ds_read_b128 v[48:51], v20 offset:0\n"
      "v_accvgpr_read_b32 v128, a96\n"
      "v_accvgpr_read_b32 v129, a97\n"
      "v_accvgpr_read_b32 v130, a98\n"
      "v_accvgpr_read_b32 v131, a99\n"
      "v_pk_mul_f32 v[128:129], v[128:129], v[208:209]\n"
      "v_pk_mul_f32 v[130:131], v[130:131], v[210:211]\n"
      "v_cvt_pk_bf16_f32 v128, v128, v129\n"
      "v_cvt_pk_bf16_f32 v129, v130, v131\n"
      "ds_write_b64 v24, v[128:129] offset:12288\n"
      "v_accvgpr_read_b32 v132, a100\n"
      "v_accvgpr_read_b32 v133, a101\n"
      "v_accvgpr_read_b32 v134, a102\n"
      "v_accvgpr_read_b32 v135, a103\n"
      "v_pk_mul_f32 v[132:133], v[132:133], v[208:209]\n"
      "v_pk_mul_f32 v[134:135], v[134:135], v[210:211]\n"
      "v_cvt_pk_bf16_f32 v132, v132, v133\n"
      "v_cvt_pk_bf16_f32 v133, v134, v135\n"
      "ds_write_b64 v24, v[132:133] offset:12800\n"
      "v_accvgpr_read_b32 v136, a104\n"
      "v_accvgpr_read_b32 v137, a105\n"
      "v_accvgpr_read_b32 v138, a106\n"
      "v_accvgpr_read_b32 v139, a107\n"
      "v_pk_mul_f32 v[136:137], v[136:137], v[208:209]\n"
      "v_pk_mul_f32 v[138:139], v[138:139], v[210:211]\n"
      "v_cvt_pk_bf16_f32 v136, v136, v137\n"
      "v_cvt_pk_bf16_f32 v137, v138, v139\n"
      "ds_write_b64 v24, v[136:137] offset:13312\n"
      "v_accvgpr_read_b32 v140, a108\n"
      "v_accvgpr_read_b32 v141, a109\n"
      "v_accvgpr_read_b32 v142, a110\n"
      "v_accvgpr_read_b32 v143, a111\n"
      "v_pk_mul_f32 v[140:141], v[140:141], v[208:209]\n"
      "v_pk_mul_f32 v[142:143], v[142:143], v[210:211]\n"
      "v_cvt_pk_bf16_f32 v140, v140, v141\n"
      "v_cvt_pk_bf16_f32 v141, v142, v143\n"
      "ds_write_b64 v24, v[140:141] offset:13824\n"
      "s_waitcnt lgkmcnt(15)\n"
      "ds_write_b128 v0, v[64:67] offset:8192\n"
      "ds_write_b128 v1, v[68:71] offset:8192\n"
      "ds_write_b128 v2, v[72:75] offset:8192\n"
      "ds_write_b128 v3, v[76:79] offset:8192\n"
      "ds_read_b128 v[64:67], v6 offset:8192\n"
      "ds_read_b128 v[68:71], v7 offset:9216\n"
      "ds_read_b128 v[72:75], v8 offset:10240\n"
      "ds_read_b128 v[76:79], v9 offset:11264\n"
      "v_accvgpr_read_b32 v144, a112\n"
      "v_accvgpr_read_b32 v145, a113\n"
      "v_accvgpr_read_b32 v146, a114\n"
      "v_accvgpr_read_b32 v147, a115\n"
      "v_pk_mul_f32 v[144:145], v[144:145], v[208:209]\n"
      "v_pk_mul_f32 v[146:147], v[146:147], v[210:211]\n"
      "v_cvt_pk_bf16_f32 v144, v144, v145\n"
      "v_cvt_pk_bf16_f32 v145, v146, v147\n"
      "ds_write_b64 v24, v[144:145] offset:14336\n"
      "v_accvgpr_read_b32 v148, a116\n"
      "v_accvgpr_read_b32 v149, a117\n"
      "v_accvgpr_read_b32 v150, a118\n"
      "v_accvgpr_read_b32 v151, a119\n"
      "v_pk_mul_f32 v[148:149], v[148:149], v[208:209]\n"
      "v_pk_mul_f32 v[150:151], v[150:151], v[210:211]\n"
      "v_cvt_pk_bf16_f32 v148, v148, v149\n"
      "v_cvt_pk_bf16_f32 v149, v150, v151\n"
      "ds_write_b64 v24, v[148:149] offset:14848\n"
      "v_accvgpr_read_b32 v152, a120\n"
      "v_accvgpr_read_b32 v153, a121\n"
      "v_accvgpr_read_b32 v154, a122\n"
      "v_accvgpr_read_b32 v155, a123\n"
      "v_pk_mul_f32 v[152:153], v[152:153], v[208:209]\n"
      "v_pk_mul_f32 v[154:155], v[154:155], v[210:211]\n"
      "v_cvt_pk_bf16_f32 v152, v152, v153\n"
      "v_cvt_pk_bf16_f32 v153, v154, v155\n"
      "ds_write_b64 v24, v[152:153] offset:15360\n"
      "v_accvgpr_read_b32 v156, a124\n"
      "v_accvgpr_read_b32 v157, a125\n"
      "v_accvgpr_read_b32 v158, a126\n"
      "v_accvgpr_read_b32 v159, a127\n"
      "v_pk_mul_f32 v[156:157], v[156:157], v[208:209]\n"
      "v_pk_mul_f32 v[158:159], v[158:159], v[210:211]\n"
      "v_cvt_pk_bf16_f32 v156, v156, v157\n"
      "v_cvt_pk_bf16_f32 v157, v158, v159\n"
      "ds_write_b64 v24, v[156:157] offset:15872\n"
      "s_waitcnt lgkmcnt(4)\n"
      "s_add_u32 s58, s62, 8192\n"
      "buffer_store_dwordx4 v[64:67], v12, s[48:51], s58 offen offset:0 nt\n"
      "buffer_store_dwordx4 v[68:71], v12, s[48:51], s58 offen offset:1024 nt\n"
      "buffer_store_dwordx4 v[72:75], v12, s[48:51], s58 offen offset:2048 nt\n"
      "buffer_store_dwordx4 v[76:79], v12, s[48:51], s58 offen offset:3072 nt\n"
      "v_add_f32 v32, v32, v36\n"
      "v_add_f32 v40, v40, v44\n"
      "v_add_f32 v33, v33, v37\n"
      "v_add_f32 v41, v41, v45\n"
      "v_add_f32 v34, v34, v38\n"
      "v_add_f32 v42, v42, v46\n"
      "v_add_f32 v35, v35, v39\n"
      "v_add_f32 v43, v43, v47\n"
      "v_add_f32 v160, v160, v168\n"
      "v_add_f32 v176, v176, v184\n"
      "v_add_f32 v161, v161, v169\n"
      "v_add_f32 v177, v177, v185\n"
      "v_add_f32 v162, v162, v170\n"
      "v_add_f32 v178, v178, v186\n"
      "v_add_f32 v163, v163, v171\n"
      "v_add_f32 v179, v179, v187\n"
      "v_add_f32 v164, v164, v172\n"
      "v_add_f32 v180, v180, v188\n"
      "v_add_f32 v165, v165, v173\n"
      "v_add_f32 v181, v181, v189\n"
      "v_add_f32 v166, v166, v174\n"
      "v_add_f32 v182, v182, v190\n"
      "v_add_f32 v167, v167, v175\n"
      "v_add_f32 v183, v183, v191\n"
      "v_add_f32 v32, v32, v40\n"
      "v_add_f32 v33, v33, v41\n"
      "v_add_f32 v34, v34, v42\n"
      "v_add_f32 v35, v35, v43\n"
      "v_add_f32 v160, v160, v176\n"
      "v_add_f32 v161, v161, v177\n"
      "v_add_f32 v162, v162, v178\n"
      "v_add_f32 v163, v163, v179\n"
      "v_add_f32 v164, v164, v180\n"
      "v_add_f32 v165, v165, v181\n"
      "v_add_f32 v166, v166, v182\n"
      "v_add_f32 v167, v167, v183\n"
      "v_rcp_f32 v52, v32\n"
      "v_rcp_f32 v53, v33\n"
      "v_rcp_f32 v54, v34\n"
      "v_rcp_f32 v55, v35\n"
      "v_or3_b32 v48, v48, v49, v50\n"
      "v_or_b32 v48, v48, v51\n"
      "v_mul_f32 v160, v52, v160\n"
      "v_mul_f32 v161, v53, v161\n"
      "v_mul_f32 v162, v54, v162\n"
      "v_mul_f32 v163, v55, v163\n"
      "v_mul_f32 v164, v52, v164\n"
      "v_mul_f32 v165, v53, v165\n"
      "v_mul_f32 v166, v54, v166\n"
      "v_mul_f32 v167, v55, v167\n"
      "v_cvt_pk_bf16_f32 v56, v160, v161\n"
      "v_cvt_pk_bf16_f32 v57, v162, v163\n"
      "v_cvt_pk_bf16_f32 v58, v164, v165\n"
      "v_cvt_pk_bf16_f32 v59, v166, v167\n"
      "ds_write_b64 v24, v[56:57] offset:0\n"
      "ds_write_b64 v24, v[58:59] offset:512\n"
      "ds_read_b64_tr_b16 v[60:61], v25 offset:0\n"
      "ds_read_b64_tr_b16 v[62:63], v26 offset:0\n"
      "v_readfirstlane_b32 s60, v48\n"
      "s_mov_b32 s59, 0x140c0\n"
      "s_waitcnt lgkmcnt(0)\n"
      "buffer_store_dwordx4 v[60:63], v253, s[48:51], s59 offen offset:0 nt\n"
      "ds_read_b64_tr_b16 v[80:81], v25 offset:12288\n"
      "ds_read_b64_tr_b16 v[82:83], v26 offset:12288\n"
      "ds_read_b64_tr_b16 v[84:85], v25 offset:13312\n"
      "ds_read_b64_tr_b16 v[86:87], v26 offset:13312\n"
      "ds_read_b64_tr_b16 v[88:89], v25 offset:14336\n"
      "ds_read_b64_tr_b16 v[90:91], v26 offset:14336\n"
      "ds_read_b64_tr_b16 v[92:93], v25 offset:15360\n"
      "ds_read_b64_tr_b16 v[94:95], v26 offset:15360\n"
      "v_accvgpr_read_b32 v96, a128\n"
      "v_accvgpr_read_b32 v97, a129\n"
      "v_accvgpr_read_b32 v98, a130\n"
      "v_accvgpr_read_b32 v99, a131\n"
      "v_pk_mul_f32 v[96:97], v[96:97], v[212:213]\n"
      "v_pk_mul_f32 v[98:99], v[98:99], v[214:215]\n"
      "v_cvt_pk_bf16_f32 v96, v96, v97\n"
      "v_cvt_pk_bf16_f32 v97, v98, v99\n"
      "ds_write_b64 v24, v[96:97] offset:16384\n"
      "v_accvgpr_read_b32 v100, a132\n"
      "v_accvgpr_read_b32 v101, a133\n"
      "v_accvgpr_read_b32 v102, a134\n"
      "v_accvgpr_read_b32 v103, a135\n"
      "v_pk_mul_f32 v[100:101], v[100:101], v[212:213]\n"
      "v_pk_mul_f32 v[102:103], v[102:103], v[214:215]\n"
      "v_cvt_pk_bf16_f32 v100, v100, v101\n"
      "v_cvt_pk_bf16_f32 v101, v102, v103\n"
      "ds_write_b64 v24, v[100:101] offset:16896\n"
      "v_accvgpr_read_b32 v104, a136\n"
      "v_accvgpr_read_b32 v105, a137\n"
      "v_accvgpr_read_b32 v106, a138\n"
      "v_accvgpr_read_b32 v107, a139\n"
      "v_pk_mul_f32 v[104:105], v[104:105], v[212:213]\n"
      "v_pk_mul_f32 v[106:107], v[106:107], v[214:215]\n"
      "v_cvt_pk_bf16_f32 v104, v104, v105\n"
      "v_cvt_pk_bf16_f32 v105, v106, v107\n"
      "ds_write_b64 v24, v[104:105] offset:17408\n"
      "v_accvgpr_read_b32 v108, a140\n"
      "v_accvgpr_read_b32 v109, a141\n"
      "v_accvgpr_read_b32 v110, a142\n"
      "v_accvgpr_read_b32 v111, a143\n"
      "v_pk_mul_f32 v[108:109], v[108:109], v[212:213]\n"
      "v_pk_mul_f32 v[110:111], v[110:111], v[214:215]\n"
      "v_cvt_pk_bf16_f32 v108, v108, v109\n"
      "v_cvt_pk_bf16_f32 v109, v110, v111\n"
      "ds_write_b64 v24, v[108:109] offset:17920\n"
      "s_waitcnt lgkmcnt(4)\n"
      "ds_write_b128 v0, v[80:83] offset:12288\n"
      "ds_write_b128 v1, v[84:87] offset:12288\n"
      "ds_write_b128 v2, v[88:91] offset:12288\n"
      "ds_write_b128 v3, v[92:95] offset:12288\n"
      "ds_read_b128 v[80:83], v6 offset:12288\n"
      "ds_read_b128 v[84:87], v7 offset:13312\n"
      "ds_read_b128 v[88:91], v8 offset:14336\n"
      "ds_read_b128 v[92:95], v9 offset:15360\n"
      "v_accvgpr_read_b32 v112, a144\n"
      "v_accvgpr_read_b32 v113, a145\n"
      "v_accvgpr_read_b32 v114, a146\n"
      "v_accvgpr_read_b32 v115, a147\n"
      "v_pk_mul_f32 v[112:113], v[112:113], v[212:213]\n"
      "v_pk_mul_f32 v[114:115], v[114:115], v[214:215]\n"
      "v_cvt_pk_bf16_f32 v112, v112, v113\n"
      "v_cvt_pk_bf16_f32 v113, v114, v115\n"
      "ds_write_b64 v24, v[112:113] offset:18432\n"
      "v_accvgpr_read_b32 v116, a148\n"
      "v_accvgpr_read_b32 v117, a149\n"
      "v_accvgpr_read_b32 v118, a150\n"
      "v_accvgpr_read_b32 v119, a151\n"
      "v_pk_mul_f32 v[116:117], v[116:117], v[212:213]\n"
      "v_pk_mul_f32 v[118:119], v[118:119], v[214:215]\n"
      "v_cvt_pk_bf16_f32 v116, v116, v117\n"
      "v_cvt_pk_bf16_f32 v117, v118, v119\n"
      "ds_write_b64 v24, v[116:117] offset:18944\n"
      "v_accvgpr_read_b32 v120, a152\n"
      "v_accvgpr_read_b32 v121, a153\n"
      "v_accvgpr_read_b32 v122, a154\n"
      "v_accvgpr_read_b32 v123, a155\n"
      "v_pk_mul_f32 v[120:121], v[120:121], v[212:213]\n"
      "v_pk_mul_f32 v[122:123], v[122:123], v[214:215]\n"
      "v_cvt_pk_bf16_f32 v120, v120, v121\n"
      "v_cvt_pk_bf16_f32 v121, v122, v123\n"
      "ds_write_b64 v24, v[120:121] offset:19456\n"
      "v_accvgpr_read_b32 v124, a156\n"
      "v_accvgpr_read_b32 v125, a157\n"
      "v_accvgpr_read_b32 v126, a158\n"
      "v_accvgpr_read_b32 v127, a159\n"
      "v_pk_mul_f32 v[124:125], v[124:125], v[212:213]\n"
      "v_pk_mul_f32 v[126:127], v[126:127], v[214:215]\n"
      "v_cvt_pk_bf16_f32 v124, v124, v125\n"
      "v_cvt_pk_bf16_f32 v125, v126, v127\n"
      "ds_write_b64 v24, v[124:125] offset:19968\n"
      "s_waitcnt lgkmcnt(4)\n"
      "s_add_u32 s58, s62, 12288\n"
      "buffer_store_dwordx4 v[80:83], v12, s[48:51], s58 offen offset:0 nt\n"
      "buffer_store_dwordx4 v[84:87], v12, s[48:51], s58 offen offset:1024 nt\n"
      "buffer_store_dwordx4 v[88:91], v12, s[48:51], s58 offen offset:2048 nt\n"
      "buffer_store_dwordx4 v[92:95], v12, s[48:51], s58 offen offset:3072 nt\n"
      "ds_read_b64_tr_b16 v[64:65], v25 offset:16384\n"
      "ds_read_b64_tr_b16 v[66:67], v26 offset:16384\n"
      "ds_read_b64_tr_b16 v[68:69], v25 offset:17408\n"
      "ds_read_b64_tr_b16 v[70:71], v26 offset:17408\n"
      "ds_read_b64_tr_b16 v[72:73], v25 offset:18432\n"
      "ds_read_b64_tr_b16 v[74:75], v26 offset:18432\n"
      "ds_read_b64_tr_b16 v[76:77], v25 offset:19456\n"
      "ds_read_b64_tr_b16 v[78:79], v26 offset:19456\n"
      "s_waitcnt lgkmcnt(0)\n"
      "s_add_u32 s58, s62, 16384\n"
      "ds_write_b128 v0, v[64:67] offset:16384\n"
      "ds_write_b128 v1, v[68:71] offset:16384\n"
      "ds_write_b128 v2, v[72:75] offset:16384\n"
      "ds_write_b128 v3, v[76:79] offset:16384\n"
      "ds_read_b128 v[64:67], v6 offset:16384\n"
      "ds_read_b128 v[68:71], v7 offset:17408\n"
      "ds_read_b128 v[72:75], v8 offset:18432\n"
      "ds_read_b128 v[76:79], v9 offset:19456\n"
      "s_waitcnt lgkmcnt(0)\n"
      "buffer_store_dwordx4 v[64:67], v12, s[48:51], s58 offen offset:0 nt\n"
      "buffer_store_dwordx4 v[68:71], v12, s[48:51], s58 offen offset:1024 nt\n"
      "buffer_store_dwordx4 v[72:75], v12, s[48:51], s58 offen offset:2048 nt\n"
      "buffer_store_dwordx4 v[76:79], v12, s[48:51], s58 offen offset:3072 nt\n"
      "s_andn2_b32 s60, s60, s65\n"
      "s_cmp_lg_u32 s60, 0\n"
      "s_cbranch_scc1 L_fallback_w3\n"
      "s_branch L_end\n"
      "L_fallback_w3:\n"
      "s_mov_b32 s65, 1\n"
      "s_waitcnt vmcnt(0) lgkmcnt(0)\n"
      "s_barrier\n"
      "v_subrev_u32 v0, s64, v0\n"
      "v_subrev_u32 v6, s64, v6\n"
      "v_subrev_u32 v1, s64, v1\n"
      "v_subrev_u32 v7, s64, v7\n"
      "v_subrev_u32 v2, s64, v2\n"
      "v_subrev_u32 v8, s64, v8\n"
      "v_subrev_u32 v3, s64, v3\n"
      "v_subrev_u32 v9, s64, v9\n"
      "v_and_b32 v20, 15, v13\n"
      "v_lshrrev_b32 v21, 4, v13\n"
      "v_lshlrev_b32 v22, 4, v21\n"
      "v_lshl_or_b32 v22, v20, 8, v22\n"
      "v_mov_b32 v64, 0xff800000\n"
      "v_mov_b32 v65, 0xff800000\n"
      "v_mov_b32 v66, 0xff800000\n"
      "v_mov_b32 v67, 0xff800000\n"
      "v_mov_b32 v68, 0xff800000\n"
      "v_mov_b32 v69, 0xff800000\n"
      "s_mov_b32 s60, 0\n"
      "L_fb_loop_w3:\n"
      "s_add_u32 s61, s60, 4096\n"
      "buffer_load_dwordx4 v[32:35], v22, s[40:43], s60 offen offset:0\n"
      "buffer_load_dwordx4 v[36:39], v22, s[40:43], s60 offen offset:64\n"
      "buffer_load_dwordx4 v[40:43], v22, s[40:43], s60 offen offset:128\n"
      "buffer_load_dwordx4 v[44:47], v22, s[40:43], s60 offen offset:192\n"
      "buffer_load_dwordx4 v[48:51], v22, s[40:43], s61 offen offset:0\n"
      "buffer_load_dwordx4 v[52:55], v22, s[40:43], s61 offen offset:64\n"
      "buffer_load_dwordx4 v[56:59], v22, s[40:43], s61 offen offset:128\n"
      "buffer_load_dwordx4 v[60:63], v22, s[40:43], s61 offen offset:192\n"
      "s_waitcnt vmcnt(0)\n"
      "v_mfma_f32_16x16x32_bf16 v[96:99], v[32:35], a[192:195], 0\n"
      "v_mfma_f32_16x16x32_bf16 v[96:99], v[36:39], a[196:199], v[96:99]\n"
      "v_mfma_f32_16x16x32_bf16 v[96:99], v[40:43], a[200:203], v[96:99]\n"
      "v_mfma_f32_16x16x32_bf16 v[96:99], v[44:47], a[204:207], v[96:99]\n"
      "v_mfma_f32_16x16x32_bf16 v[100:103], v[48:51], a[192:195], 0\n"
      "v_mfma_f32_16x16x32_bf16 v[100:103], v[52:55], a[196:199], v[100:103]\n"
      "v_mfma_f32_16x16x32_bf16 v[100:103], v[56:59], a[200:203], v[100:103]\n"
      "v_mfma_f32_16x16x32_bf16 v[100:103], v[60:63], a[204:207], v[100:103]\n"
      "s_nop 7\n"
      "v_max3_f32 v64, v64, v96, v97\n"
      "v_max3_f32 v64, v64, v98, v99\n"
      "v_max3_f32 v64, v64, v100, v101\n"
      "v_max3_f32 v64, v64, v102, v103\n"
      "v_mfma_f32_16x16x32_bf16 v[96:99], v[32:35], a[208:211], 0\n"
      "v_mfma_f32_16x16x32_bf16 v[96:99], v[36:39], a[212:215], v[96:99]\n"
      "v_mfma_f32_16x16x32_bf16 v[96:99], v[40:43], a[216:219], v[96:99]\n"
      "v_mfma_f32_16x16x32_bf16 v[96:99], v[44:47], a[220:223], v[96:99]\n"
      "v_mfma_f32_16x16x32_bf16 v[100:103], v[48:51], a[208:211], 0\n"
      "v_mfma_f32_16x16x32_bf16 v[100:103], v[52:55], a[212:215], v[100:103]\n"
      "v_mfma_f32_16x16x32_bf16 v[100:103], v[56:59], a[216:219], v[100:103]\n"
      "v_mfma_f32_16x16x32_bf16 v[100:103], v[60:63], a[220:223], v[100:103]\n"
      "s_nop 7\n"
      "v_max3_f32 v65, v65, v96, v97\n"
      "v_max3_f32 v65, v65, v98, v99\n"
      "v_max3_f32 v65, v65, v100, v101\n"
      "v_max3_f32 v65, v65, v102, v103\n"
      "v_mfma_f32_16x16x32_bf16 v[96:99], v[32:35], a[224:227], 0\n"
      "v_mfma_f32_16x16x32_bf16 v[96:99], v[36:39], a[228:231], v[96:99]\n"
      "v_mfma_f32_16x16x32_bf16 v[96:99], v[40:43], a[232:235], v[96:99]\n"
      "v_mfma_f32_16x16x32_bf16 v[96:99], v[44:47], a[236:239], v[96:99]\n"
      "v_mfma_f32_16x16x32_bf16 v[100:103], v[48:51], a[224:227], 0\n"
      "v_mfma_f32_16x16x32_bf16 v[100:103], v[52:55], a[228:231], v[100:103]\n"
      "v_mfma_f32_16x16x32_bf16 v[100:103], v[56:59], a[232:235], v[100:103]\n"
      "v_mfma_f32_16x16x32_bf16 v[100:103], v[60:63], a[236:239], v[100:103]\n"
      "s_nop 7\n"
      "v_max3_f32 v66, v66, v96, v97\n"
      "v_max3_f32 v66, v66, v98, v99\n"
      "v_max3_f32 v66, v66, v100, v101\n"
      "v_max3_f32 v66, v66, v102, v103\n"
      "v_mfma_f32_16x16x32_bf16 v[96:99], v[32:35], a[240:243], 0\n"
      "v_mfma_f32_16x16x32_bf16 v[96:99], v[36:39], a[244:247], v[96:99]\n"
      "v_mfma_f32_16x16x32_bf16 v[96:99], v[40:43], a[248:251], v[96:99]\n"
      "v_mfma_f32_16x16x32_bf16 v[96:99], v[44:47], a[252:255], v[96:99]\n"
      "v_mfma_f32_16x16x32_bf16 v[100:103], v[48:51], a[240:243], 0\n"
      "v_mfma_f32_16x16x32_bf16 v[100:103], v[52:55], a[244:247], v[100:103]\n"
      "v_mfma_f32_16x16x32_bf16 v[100:103], v[56:59], a[248:251], v[100:103]\n"
      "v_mfma_f32_16x16x32_bf16 v[100:103], v[60:63], a[252:255], v[100:103]\n"
      "s_nop 7\n"
      "v_max3_f32 v67, v67, v96, v97\n"
      "v_max3_f32 v67, v67, v98, v99\n"
      "v_max3_f32 v67, v67, v100, v101\n"
      "v_max3_f32 v67, v67, v102, v103\n"
      "v_mfma_f32_16x16x32_bf16 v[96:99], v[32:35], v[216:219], 0\n"
      "v_mfma_f32_16x16x32_bf16 v[96:99], v[36:39], v[220:223], v[96:99]\n"
      "v_mfma_f32_16x16x32_bf16 v[96:99], v[40:43], v[224:227], v[96:99]\n"
      "v_mfma_f32_16x16x32_bf16 v[96:99], v[44:47], v[228:231], v[96:99]\n"
      "v_mfma_f32_16x16x32_bf16 v[100:103], v[48:51], v[216:219], 0\n"
      "v_mfma_f32_16x16x32_bf16 v[100:103], v[52:55], v[220:223], v[100:103]\n"
      "v_mfma_f32_16x16x32_bf16 v[100:103], v[56:59], v[224:227], v[100:103]\n"
      "v_mfma_f32_16x16x32_bf16 v[100:103], v[60:63], v[228:231], v[100:103]\n"
      "s_nop 7\n"
      "v_max3_f32 v68, v68, v96, v97\n"
      "v_max3_f32 v68, v68, v98, v99\n"
      "v_max3_f32 v68, v68, v100, v101\n"
      "v_max3_f32 v68, v68, v102, v103\n"
      "v_mfma_f32_16x16x32_bf16 v[96:99], v[32:35], v[232:235], 0\n"
      "v_mfma_f32_16x16x32_bf16 v[96:99], v[36:39], v[236:239], v[96:99]\n"
      "v_mfma_f32_16x16x32_bf16 v[96:99], v[40:43], v[240:243], v[96:99]\n"
      "v_mfma_f32_16x16x32_bf16 v[96:99], v[44:47], v[244:247], v[96:99]\n"
      "v_mfma_f32_16x16x32_bf16 v[100:103], v[48:51], v[232:235], 0\n"
      "v_mfma_f32_16x16x32_bf16 v[100:103], v[52:55], v[236:239], v[100:103]\n"
      "v_mfma_f32_16x16x32_bf16 v[100:103], v[56:59], v[240:243], v[100:103]\n"
      "v_mfma_f32_16x16x32_bf16 v[100:103], v[60:63], v[244:247], v[100:103]\n"
      "s_nop 7\n"
      "v_max3_f32 v69, v69, v96, v97\n"
      "v_max3_f32 v69, v69, v98, v99\n"
      "v_max3_f32 v69, v69, v100, v101\n"
      "v_max3_f32 v69, v69, v102, v103\n"
      "s_add_u32 s60, s60, 8192\n"
      "s_cmp_lt_u32 s60, 0x80000\n"
      "s_cbranch_scc1 L_fb_loop_w3\n"
      "v_mov_b32 v29, v64\n"
      "s_nop 1\n"
      "v_permlane16_swap_b32 v64, v29\n"
      "s_nop 0\n"
      "v_max_f32 v64, v64, v29\n"
      "v_mov_b32 v29, v64\n"
      "s_nop 1\n"
      "v_permlane32_swap_b32 v64, v29\n"
      "s_nop 0\n"
      "v_max_f32 v64, v64, v29\n"
      "v_sub_f32 v192, 0, v64\n"
      "v_mov_b32 v193, v192\n"
      "v_mov_b32 v194, v192\n"
      "v_mov_b32 v195, v192\n"
      "v_mov_b32 v29, v65\n"
      "s_nop 1\n"
      "v_permlane16_swap_b32 v65, v29\n"
      "s_nop 0\n"
      "v_max_f32 v65, v65, v29\n"
      "v_mov_b32 v29, v65\n"
      "s_nop 1\n"
      "v_permlane32_swap_b32 v65, v29\n"
      "s_nop 0\n"
      "v_max_f32 v65, v65, v29\n"
      "v_sub_f32 v196, 0, v65\n"
      "v_mov_b32 v197, v196\n"
      "v_mov_b32 v198, v196\n"
      "v_mov_b32 v199, v196\n"
      "v_mov_b32 v29, v66\n"
      "s_nop 1\n"
      "v_permlane16_swap_b32 v66, v29\n"
      "s_nop 0\n"
      "v_max_f32 v66, v66, v29\n"
      "v_mov_b32 v29, v66\n"
      "s_nop 1\n"
      "v_permlane32_swap_b32 v66, v29\n"
      "s_nop 0\n"
      "v_max_f32 v66, v66, v29\n"
      "v_sub_f32 v200, 0, v66\n"
      "v_mov_b32 v201, v200\n"
      "v_mov_b32 v202, v200\n"
      "v_mov_b32 v203, v200\n"
      "v_mov_b32 v29, v67\n"
      "s_nop 1\n"
      "v_permlane16_swap_b32 v67, v29\n"
      "s_nop 0\n"
      "v_max_f32 v67, v67, v29\n"
      "v_mov_b32 v29, v67\n"
      "s_nop 1\n"
      "v_permlane32_swap_b32 v67, v29\n"
      "s_nop 0\n"
      "v_max_f32 v67, v67, v29\n"
      "v_sub_f32 v204, 0, v67\n"
      "v_mov_b32 v205, v204\n"
      "v_mov_b32 v206, v204\n"
      "v_mov_b32 v207, v204\n"
      "v_mov_b32 v29, v68\n"
      "s_nop 1\n"
      "v_permlane16_swap_b32 v68, v29\n"
      "s_nop 0\n"
      "v_max_f32 v68, v68, v29\n"
      "v_mov_b32 v29, v68\n"
      "s_nop 1\n"
      "v_permlane32_swap_b32 v68, v29\n"
      "s_nop 0\n"
      "v_max_f32 v68, v68, v29\n"
      "v_sub_f32 v208, 0, v68\n"
      "v_mov_b32 v209, v208\n"
      "v_mov_b32 v210, v208\n"
      "v_mov_b32 v211, v208\n"
      "v_mov_b32 v29, v69\n"
      "s_nop 1\n"
      "v_permlane16_swap_b32 v69, v29\n"
      "s_nop 0\n"
      "v_max_f32 v69, v69, v29\n"
      "v_mov_b32 v29, v69\n"
      "s_nop 1\n"
      "v_permlane32_swap_b32 v69, v29\n"
      "s_nop 0\n"
      "v_max_f32 v69, v69, v29\n"
      "v_sub_f32 v212, 0, v69\n"
      "v_mov_b32 v213, v212\n"
      "v_mov_b32 v214, v212\n"
      "v_mov_b32 v215, v212\n"
      "v_mov_b32 v64, 0\n"
      "v_mov_b32 v65, 0\n"
      "v_mov_b32 v66, 0\n"
      "v_mov_b32 v67, 0\n"
      "v_mov_b32 v14, 0\n"
      "v_mov_b32 v15, 0\n"
      "v_mov_b32 v16, 0\n"
      "v_mov_b32 v17, 0\n"
      "v_mov_b32 v18, 0\n"
      "v_mov_b32 v19, 0\n"
      "v_mfma_f32_32x32x16_bf16 a[0:15], v[64:67], v[64:67], 0\n"
      "v_mfma_f32_32x32x16_bf16 a[16:31], v[64:67], v[64:67], 0\n"
      "v_mfma_f32_32x32x16_bf16 a[32:47], v[64:67], v[64:67], 0\n"
      "v_mfma_f32_32x32x16_bf16 a[48:63], v[64:67], v[64:67], 0\n"
      "v_mfma_f32_32x32x16_bf16 a[64:79], v[64:67], v[64:67], 0\n"
      "v_mfma_f32_32x32x16_bf16 a[80:95], v[64:67], v[64:67], 0\n"
      "v_mfma_f32_32x32x16_bf16 a[96:111], v[64:67], v[64:67], 0\n"
      "v_mfma_f32_32x32x16_bf16 a[112:127], v[64:67], v[64:67], 0\n"
      "v_mfma_f32_32x32x16_bf16 a[128:143], v[64:67], v[64:67], 0\n"
      "v_mfma_f32_32x32x16_bf16 a[144:159], v[64:67], v[64:67], 0\n"
      "v_mfma_f32_32x32x16_bf16 a[160:175], v[64:67], v[64:67], 0\n"
      "v_mfma_f32_32x32x16_bf16 a[176:191], v[64:67], v[64:67], 0\n"
      "v_xor_b32 v4, 0x8000, v4\n"
      "v_xor_b32 v5, 0x8000, v5\n"
      "s_mov_b32 s58, 0x1000\n"
      "s_mov_b32 m0, 0x1000\n"
      "s_nop 0\n"
      "buffer_load_dwordx4 v6, s[40:43], s58 offen offset:0 lds\n"
      "buffer_load_dwordx4 v7, s[40:43], s58 offen offset:1024 lds\n"
      "buffer_load_dwordx4 v8, s[40:43], s58 offen offset:2048 lds\n"
      "buffer_load_dwordx4 v9, s[40:43], s58 offen offset:3072 lds\n"
      "s_mov_b32 s58, 0x3000\n"
      "s_mov_b32 m0, 0x3000\n"
      "s_nop 0\n"
      "buffer_load_dwordx4 v6, s[40:43], s58 offen offset:0 lds\n"
      "buffer_load_dwordx4 v7, s[40:43], s58 offen offset:1024 lds\n"
      "buffer_load_dwordx4 v8, s[40:43], s58 offen offset:2048 lds\n"
      "buffer_load_dwordx4 v9, s[40:43], s58 offen offset:3072 lds\n"
      "s_mov_b32 s58, 0x5000\n"
      "s_mov_b32 m0, 0x5000\n"
      "s_nop 0\n"
      "buffer_load_dwordx4 v6, s[40:43], s58 offen offset:0 lds\n"
      "buffer_load_dwordx4 v7, s[40:43], s58 offen offset:1024 lds\n"
      "buffer_load_dwordx4 v8, s[40:43], s58 offen offset:2048 lds\n"
      "buffer_load_dwordx4 v9, s[40:43], s58 offen offset:3072 lds\n"
      "s_mov_b32 s58, 0x7000\n"
      "s_mov_b32 m0, 0x7000\n"
      "s_nop 0\n"
      "buffer_load_dwordx4 v6, s[40:43], s58 offen offset:0 lds\n"
      "buffer_load_dwordx4 v7, s[40:43], s58 offen offset:1024 lds\n"
      "buffer_load_dwordx4 v8, s[40:43], s58 offen offset:2048 lds\n"
      "buffer_load_dwordx4 v9, s[40:43], s58 offen offset:3072 lds\n"
      "s_mov_b32 s58, 0x9000\n"
      "s_mov_b32 m0, 0x9000\n"
      "s_nop 0\n"
      "buffer_load_dwordx4 v6, s[40:43], s58 offen offset:0 lds\n"
      "buffer_load_dwordx4 v7, s[40:43], s58 offen offset:1024 lds\n"
      "buffer_load_dwordx4 v8, s[40:43], s58 offen offset:2048 lds\n"
      "buffer_load_dwordx4 v9, s[40:43], s58 offen offset:3072 lds\n"
      "s_waitcnt vmcnt(8)\n"
      "s_barrier\n"
      "ds_read_b128 v[32:35], v0 offset:0\n"
      "ds_read_b128 v[36:39], v1 offset:0\n"
      "ds_read_b128 v[40:43], v2 offset:0\n"
      "ds_read_b128 v[44:47], v3 offset:0\n"
      "ds_read_b128 v[48:51], v0 offset:4096\n"
      "ds_read_b128 v[52:55], v1 offset:4096\n"
      "ds_read_b128 v[56:59], v2 offset:4096\n"
      "ds_read_b128 v[60:63], v3 offset:4096\n"
      "s_waitcnt lgkmcnt(0)\n"
      "v_mfma_f32_16x16x32_bf16 v[96:99], v[32:35], a[192:195], v[192:195]\n"
      "v_mfma_f32_16x16x32_bf16 v[96:99], v[36:39], a[196:199], v[96:99]\n"
      "v_mfma_f32_16x16x32_bf16 v[96:99], v[40:43], a[200:203], v[96:99]\n"
      "v_mfma_f32_16x16x32_bf16 v[96:99], v[44:47], a[204:207], v[96:99]\n"
      "v_mfma_f32_16x16x32_bf16 v[100:103], v[48:51], a[192:195], v[192:195]\n"
      "v_mfma_f32_16x16x32_bf16 v[100:103], v[52:55], a[196:199], v[100:103]\n"
      "v_mfma_f32_16x16x32_bf16 v[100:103], v[56:59], a[200:203], v[100:103]\n"
      "v_mfma_f32_16x16x32_bf16 v[100:103], v[60:63], a[204:207], v[100:103]\n"
      "v_mfma_f32_16x16x32_bf16 v[104:107], v[32:35], a[208:211], v[196:199]\n"
      "v_mfma_f32_16x16x32_bf16 v[104:107], v[36:39], a[212:215], v[104:107]\n"
      "v_mfma_f32_16x16x32_bf16 v[104:107], v[40:43], a[216:219], v[104:107]\n"
      "v_mfma_f32_16x16x32_bf16 v[104:107], v[44:47], a[220:223], v[104:107]\n"
      "v_mfma_f32_16x16x32_bf16 v[108:111], v[48:51], a[208:211], v[196:199]\n"
      "v_mfma_f32_16x16x32_bf16 v[108:111], v[52:55], a[212:215], v[108:111]\n"
      "v_mfma_f32_16x16x32_bf16 v[108:111], v[56:59], a[216:219], v[108:111]\n"
      "v_mfma_f32_16x16x32_bf16 v[108:111], v[60:63], a[220:223], v[108:111]\n"
      "v_mfma_f32_16x16x32_bf16 v[112:115], v[32:35], a[224:227], v[200:203]\n"
      "v_mfma_f32_16x16x32_bf16 v[112:115], v[36:39], a[228:231], v[112:115]\n"
      "v_mfma_f32_16x16x32_bf16 v[112:115], v[40:43], a[232:235], v[112:115]\n"
      "v_mfma_f32_16x16x32_bf16 v[112:115], v[44:47], a[236:239], v[112:115]\n"
      "v_mfma_f32_16x16x32_bf16 v[116:119], v[48:51], a[224:227], v[200:203]\n"
      "v_mfma_f32_16x16x32_bf16 v[116:119], v[52:55], a[228:231], v[116:119]\n"
      "v_mfma_f32_16x16x32_bf16 v[116:119], v[56:59], a[232:235], v[116:119]\n"
      "v_mfma_f32_16x16x32_bf16 v[116:119], v[60:63], a[236:239], v[116:119]\n"
      "v_mfma_f32_16x16x32_bf16 v[120:123], v[32:35], a[240:243], v[204:207]\n"
      "v_mfma_f32_16x16x32_bf16 v[120:123], v[36:39], a[244:247], v[120:123]\n"
      "v_mfma_f32_16x16x32_bf16 v[120:123], v[40:43], a[248:251], v[120:123]\n"
      "v_mfma_f32_16x16x32_bf16 v[120:123], v[44:47], a[252:255], v[120:123]\n"
      "v_mfma_f32_16x16x32_bf16 v[124:127], v[48:51], a[240:243], v[204:207]\n"
      "v_mfma_f32_16x16x32_bf16 v[124:127], v[52:55], a[244:247], v[124:127]\n"
      "v_mfma_f32_16x16x32_bf16 v[124:127], v[56:59], a[248:251], v[124:127]\n"
      "v_mfma_f32_16x16x32_bf16 v[124:127], v[60:63], a[252:255], v[124:127]\n"
      "v_mfma_f32_16x16x32_bf16 v[128:131], v[32:35], v[216:219], v[208:211]\n"
      "v_mfma_f32_16x16x32_bf16 v[128:131], v[36:39], v[220:223], v[128:131]\n"
      "v_mfma_f32_16x16x32_bf16 v[128:131], v[40:43], v[224:227], v[128:131]\n"
      "v_mfma_f32_16x16x32_bf16 v[128:131], v[44:47], v[228:231], v[128:131]\n"
      "v_mfma_f32_16x16x32_bf16 v[132:135], v[48:51], v[216:219], v[208:211]\n"
      "v_mfma_f32_16x16x32_bf16 v[132:135], v[52:55], v[220:223], v[132:135]\n"
      "v_mfma_f32_16x16x32_bf16 v[132:135], v[56:59], v[224:227], v[132:135]\n"
      "v_mfma_f32_16x16x32_bf16 v[132:135], v[60:63], v[228:231], v[132:135]\n"
      "v_mfma_f32_16x16x32_bf16 v[168:171], v[32:35], v[232:235], v[212:215]\n"
      "v_mfma_f32_16x16x32_bf16 v[168:171], v[36:39], v[236:239], v[168:171]\n"
      "v_mfma_f32_16x16x32_bf16 v[168:171], v[40:43], v[240:243], v[168:171]\n"
      "v_mfma_f32_16x16x32_bf16 v[168:171], v[44:47], v[244:247], v[168:171]\n"
      "v_mfma_f32_16x16x32_bf16 v[176:179], v[48:51], v[232:235], v[212:215]\n"
      "v_mfma_f32_16x16x32_bf16 v[176:179], v[52:55], v[236:239], v[176:179]\n"
      "v_mfma_f32_16x16x32_bf16 v[176:179], v[56:59], v[240:243], v[176:179]\n"
      "v_mfma_f32_16x16x32_bf16 v[176:179], v[60:63], v[244:247], v[176:179]\n"
      "s_nop 7\n"
      "s_branch L_restart_w3\n"
      "L_end:\n"
      "L_end2:\n"
      :
      : "s"(q), "s"(k), "s"(v), "s"(o), "s"(bh), "s"(wid), "s"(bh), "s"(ldsbase)
      : "memory", "vcc", "scc", "s36", "s37", "s38", "s39", "s40", "s41", "s42", "s43", "s44", "s45", "s46", "s47", "s48",
        "s49", "s50", "s51", "s52", "s53", "s54", "s55", "s56", "s57", "s58", "s59", "s60", "s61", "s62", "s63", "s64",
        "s65", "s66", "s67", "s68", "s69", "s70", "s71", "s72", "s73", "s74", "s75", "s76", "s77", "s78", "s79", "s80",
        "s81", "s82", "s83", "s84", "s85", "s86", "s87", "s88", "s89", "s90", "s91", "s92", "s93", "s94", "s95", "s96",
        "s97", "s98", "s99", "v0", "v1", "v2", "v3", "v4", "v5", "v6", "v7", "v8", "v9", "v10", "v11", "v12",
        "v13", "v14", "v15", "v16", "v17", "v18", "v19", "v20", "v21", "v22", "v23", "v24", "v25", "v26", "v27", "v28",
        "v29", "v30", "v31", "v32", "v33", "v34", "v35", "v36", "v37", "v38", "v39", "v40", "v41", "v42", "v43", "v44",
        "v45", "v46", "v47", "v48", "v49", "v50", "v51", "v52", "v53", "v54", "v55", "v56", "v57", "v58", "v59", "v60",
        "v61", "v62", "v63", "v64", "v65", "v66", "v67", "v68", "v69", "v70", "v71", "v72", "v73", "v74", "v75", "v76",
        "v77", "v78", "v79", "v80", "v81", "v82", "v83", "v84", "v85", "v86", "v87", "v88", "v89", "v90", "v91", "v92",
        "v93", "v94", "v95", "v96", "v97", "v98", "v99", "v100", "v101", "v102", "v103", "v104", "v105", "v106", "v107", "v108",
        "v109", "v110", "v111", "v112", "v113", "v114", "v115", "v116", "v117", "v118", "v119", "v120", "v121", "v122", "v123", "v124",
        "v125", "v126", "v127", "v128", "v129", "v130", "v131", "v132", "v133", "v134", "v135", "v136", "v137", "v138", "v139", "v140",
        "v141", "v142", "v143", "v144", "v145", "v146", "v147", "v148", "v149", "v150", "v151", "v152", "v153", "v154", "v155", "v156",
        "v157", "v158", "v159", "v160", "v161", "v162", "v163", "v164", "v165", "v166", "v167", "v168", "v169", "v170", "v171", "v172",
        "v173", "v174", "v175", "v176", "v177", "v178", "v179", "v180", "v181", "v182", "v183", "v184", "v185", "v186", "v187", "v188",
        "v189", "v190", "v191", "v192", "v193", "v194", "v195", "v196", "v197", "v198", "v199", "v200", "v201", "v202", "v203", "v204",
        "v205", "v206", "v207", "v208", "v209", "v210", "v211", "v212", "v213", "v214", "v215", "v216", "v217", "v218", "v219", "v220",
        "v221", "v222", "v223", "v224", "v225", "v226", "v227", "v228", "v229", "v230", "v231", "v232", "v233", "v234", "v235", "v236",
        "v237", "v238", "v239", "v240", "v241", "v242", "v243", "v244", "v245", "v246", "v247", "v248", "v249", "v250", "v251", "v252",
        "v253", "v254", "v255", "a0", "a1", "a2", "a3", "a4", "a5", "a6", "a7", "a8", "a9", "a10", "a11", "a12",
        "a13", "a14", "a15", "a16", "a17", "a18", "a19", "a20", "a21", "a22", "a23", "a24", "a25", "a26", "a27", "a28",
        "a29", "a30", "a31", "a32", "a33", "a34", "a35", "a36", "a37", "a38", "a39", "a40", "a41", "a42", "a43", "a44",
        "a45", "a46", "a47", "a48", "a49", "a50", "a51", "a52", "a53", "a54", "a55", "a56", "a57", "a58", "a59", "a60",
        "a61", "a62", "a63", "a64", "a65", "a66", "a67", "a68", "a69", "a70", "a71", "a72", "a73", "a74", "a75", "a76",
        "a77", "a78", "a79", "a80", "a81", "a82", "a83", "a84", "a85", "a86", "a87", "a88", "a89", "a90", "a91", "a92",
        "a93", "a94", "a95", "a96", "a97", "a98", "a99", "a100", "a101", "a102", "a103", "a104", "a105", "a106", "a107", "a108",
        "a109", "a110", "a111", "a112", "a113", "a114", "a115", "a116", "a117", "a118", "a119", "a120", "a121", "a122", "a123", "a124",
        "a125", "a126", "a127", "a128", "a129", "a130", "a131", "a132", "a133", "a134", "a135", "a136", "a137", "a138", "a139", "a140",
        "a141", "a142", "a143", "a144", "a145", "a146", "a147", "a148", "a149", "a150", "a151", "a152", "a153", "a154", "a155", "a156",
        "a157", "a158", "a159", "a160", "a161", "a162", "a163", "a164", "a165", "a166", "a167", "a168", "a169", "a170", "a171", "a172",
        "a173", "a174", "a175", "a176", "a177", "a178", "a179", "a180", "a181", "a182", "a183", "a184", "a185", "a186", "a187", "a188",
        "a189", "a190", "a191", "a192", "a193", "a194", "a195", "a196", "a197", "a198", "a199", "a200", "a201", "a202", "a203", "a204",
        "a205", "a206", "a207", "a208", "a209", "a210", "a211", "a212", "a213", "a214", "a215", "a216", "a217", "a218", "a219", "a220",
        "a221", "a222", "a223", "a224", "a225", "a226", "a227", "a228", "a229", "a230", "a231", "a232", "a233", "a234", "a235", "a236",
        "a237", "a238", "a239", "a240", "a241", "a242", "a243", "a244", "a245", "a246", "a247", "a248", "a249", "a250", "a251", "a252",
        "a253", "a254", "a255");
}


extern "C" void kernel_launch(void* const* d_in, const int* in_sizes, int n_in, void* d_out, int out_size,
                              void* d_ws, size_t ws_size, hipStream_t stream) {
  (void)in_sizes; (void)n_in; (void)out_size; (void)ws_size; (void)d_ws;
  standalone__gqa_softmax__near_ridge_kernel<<<dim3(256), dim3(256), 0, stream>>>(
      d_in[0], d_in[1], d_in[2], d_out);
}
```
